```python
import torch

def _softmax(x, axis=-1):
    return torch.softmax(x, dim=axis)

def _gelu(x):
    return torch.nn.functional.gelu(x, approximate='tanh')

def _silu(x):
    return torch.nn.functional.silu(x)

def _sigmoid(x):
    return torch.sigmoid(x)

def _layernorm(x, axis=-1, eps=1e-05):
    m = x.mean(dim=axis, keepdim=True)
    v = x.var(dim=axis, keepdim=True, unbiased=False)
    return (x - m) / torch.sqrt(v + eps)

def _scatter_add(tgt, idx, src, axis=0):
    return tgt.clone().index_add_(axis, idx, src)

def _segment_sum(src, seg, n):
    out = torch.zeros((n,) + src.shape[1:], dtype=src.dtype)
    return out.index_add_(0, seg, src)

def _pad(x, pw):
    flat = tuple((v for pair in reversed(pw) for v in pair))
    return torch.nn.functional.pad(x, flat)
kernel_name = 'adaLN_cond__sliding_local_dilated__swiglu_ffn__prompt'

def setup_inputs():

    _w = lambda a: (lambda t: t.to(torch.bfloat16) if t.is_floating_point() else t)(torch.as_tensor(a))

    def _A_setup():
        B, S, D = (1, 2048, 1024)
        return {'x': _w((lambda *s: torch.randn(*s, dtype=torch.bfloat16) * 0.05)(B, S, D)), 'wqkv': _w((lambda *s: torch.randn(*s, dtype=torch.bfloat16) * 0.05)(D, 3 * D)), 'wo': _w((lambda *s: torch.randn(*s, dtype=torch.bfloat16) * 0.05)(D, D))}
    _A_in = _A_setup()

    def _B_setup():
        B, S, D, F = (1, 2048, 1024, 3584)
        return {'x': _w((lambda *s: torch.randn(*s, dtype=torch.bfloat16) * 0.05)(B, S, D)), 'w_gate': _w((lambda *s: torch.randn(*s, dtype=torch.bfloat16) * 0.05)(D, F)), 'w_up': _w((lambda *s: torch.randn(*s, dtype=torch.bfloat16) * 0.05)(D, F)), 'w_down': _w((lambda *s: torch.randn(*s, dtype=torch.bfloat16) * 0.05)(F, D))}
    _B_in = _B_setup()
    return {'A_x': _A_in['x'], 'A_wqkv': _A_in['wqkv'], 'A_wo': _A_in['wo'], 'B_x': _B_in['x'], 'B_w_gate': _B_in['w_gate'], 'B_w_up': _B_in['w_up'], 'B_w_down': _B_in['w_down'], 'c': _w((lambda *s: torch.randn(*s, dtype=torch.bfloat16) * 0.1)(1, 1024) * 8.0), 'w_mod': _w((lambda *s: torch.randn(*s, dtype=torch.bfloat16) * 0.1)(1024, 6 * 1024) * 1024 ** (-0.5) * 8.0)}

def reference(A_x, A_wqkv, A_wo, B_x, B_w_gate, B_w_up, B_w_down, c, w_mod):

    def _A_ref(x, wqkv, wo):
        B, S, D = x.shape
        H = 32
        Dh = D // H
        W, dil = (8, 2)
        qkv = (lambda x, *d: x.permute(*d))((x @ wqkv).reshape(B, S, 3, H, Dh), 2, 0, 3, 1, 4)
        q, k, v = (qkv[0], qkv[1], qkv[2])
        scale = Dh ** (-0.5)
        pos = torch.arange(S)
        off = torch.arange(W) * dil
        idx = pos[:, None] - off[None, :]
        mask = idx >= 0
        idx_c = torch.where(mask, idx, 0).reshape(-1)
        kg = k[:, :, idx_c, :].reshape(B, H, S, W, Dh)
        vg = v[:, :, idx_c, :].reshape(B, H, S, W, Dh)
        logits = torch.einsum('bhsd,bhswd->bhsw', q, kg) * scale
        logits = torch.where(mask[None, None, :, :], logits, -1000000000.0)
        attn = _softmax(logits, axis=-1)
        o = torch.einsum('bhsw,bhswd->bhsd', attn, vg)
        return (lambda x, *d: x.permute(*d))(o, 0, 2, 1, 3).reshape(B, S, D) @ wo

    def _B_ref(x, w_gate, w_up, w_down):
        g = _silu(x @ w_gate)
        u = x @ w_up
        return g * u @ w_down
    x = A_x
    mod = (_silu(c) @ w_mod).reshape(c.shape[0], 6, 1, -1)
    sa, ca, ga, sb, cb, gb = (mod[:, 0].clone(), mod[:, 1].clone(), mod[:, 2].clone(), mod[:, 3].clone(), mod[:, 4].clone(), mod[:, 5].clone())
    h = _layernorm(x, axis=-1) * (1 + ca) + sa
    x = x + ga * _A_ref(h, A_wqkv, A_wo)
    h = _layernorm(x, axis=-1) * (1 + cb) + sb
    x = x + gb * _B_ref(h, B_w_gate, B_w_up, B_w_down)
    return x
```

```cpp
#define PROF
#define DKB 1
#define DSB 0
#define DMODE 0
#define DMULT 0
#include <hip/hip_runtime.h>
#include <hip/hip_bf16.h>
#include <stdint.h>

typedef __hip_bfloat16 bf16;
typedef __attribute__((ext_vector_type(8))) short bf16x8;
typedef __attribute__((ext_vector_type(4))) float f32x4;
typedef __attribute__((ext_vector_type(2))) float f32x2;
typedef __attribute__((ext_vector_type(4))) unsigned u32x4;
typedef __attribute__((ext_vector_type(2))) unsigned u32x2;
typedef __attribute__((ext_vector_type(2))) __bf16 bf16x2_t;
typedef __amdgpu_buffer_rsrc_t rsrc_t;

#define DI __device__ __forceinline__
#define QBAR 2
#define QRN 17
#define TQ 12
#define TG 4
#define QK0 0
#define QK0W "6"
#define CODEPF 53

#define PROF_NK 8
#define PROF_NB 1024
#define PROF_NS 8
__device__ unsigned long long g_stamps[PROF_NK * PROF_NB * PROF_NS];
#define STAMPDECL unsigned long long _tnw = 0, _cnw = 0
#define STAMPNW(k, s) do { _tnw = __builtin_amdgcn_s_memrealtime(); _cnw = __builtin_amdgcn_s_memtime(); __builtin_amdgcn_s_waitcnt(0xC07F); } while (0)
#define STAMPFLUSH(k, s) do { unsigned long long _c2 = __builtin_amdgcn_s_memtime(); if (threadIdx.x == 0 && blockIdx.x < PROF_NB) { g_stamps[((k) * PROF_NB + blockIdx.x) * PROF_NS + (s)] = _tnw; g_stamps[((k) * PROF_NB + blockIdx.x) * PROF_NS + 7] = _c2 - _cnw; } } while (0)
#define STAMP(k, s) do { if (threadIdx.x == 0 && blockIdx.x < PROF_NB) g_stamps[((k) * PROF_NB + blockIdx.x) * PROF_NS + (s)] = __builtin_amdgcn_s_memrealtime(); } while (0)
#define STAMPT(k, s, T) do { if (threadIdx.x == (T) && blockIdx.x < PROF_NB) g_stamps[((k) * PROF_NB + blockIdx.x) * PROF_NS + (s)] = __builtin_amdgcn_s_memrealtime(); } while (0)

constexpr int S_LEN = 2048;
constexpr int D_DIM = 1024;
constexpr int F_DIM = 3584;

template <int V> struct ic { static constexpr int value = V; };
template <int N, int I = 0, class F>
DI void static_for(F&& f) {
  if constexpr (I < N) { f(ic<I>{}); static_for<N, I + 1>(f); }
}

DI float bf_lo(unsigned u) { return __uint_as_float(u << 16); }
DI float bf_hi(unsigned u) { return __uint_as_float(u & 0xffff0000u); }
DI unsigned pk_bf16(float lo, float hi) {
  const f32x2 v = {lo, hi};
  return __builtin_bit_cast(unsigned, __builtin_convertvector(v, bf16x2_t));
}
DI rsrc_t mk_rsrc(const void* p, unsigned bytes) { return __builtin_amdgcn_make_buffer_rsrc((void*)p, (short)0, (int)bytes, 0x00020000); }

template <int N>
DI void wait_vm() { asm volatile("s_waitcnt vmcnt(%0)" ::"n"(N > 63 ? 63 : N) : "memory"); }
#define WAIT_LGKM0() do { asm volatile("s_waitcnt lgkmcnt(0)" ::: "memory"); __builtin_amdgcn_sched_barrier(0); } while (0)
#define SCHED0() __builtin_amdgcn_sched_barrier(0)

#define MFMA_A(C, B, A) asm volatile("v_mfma_f32_16x16x32_bf16 %0, %1, %2, %0" : "+a"(C) : "v"(B), "a"(A))
#define MFMA_AV(C, B, A) asm volatile("v_mfma_f32_16x16x32_bf16 %0, %1, %2, %0" : "+v"(C) : "v"(B), "v"(A))
#define PERM_LO(D, X, Y) asm volatile("v_perm_b32 %0, %1, %2, %3" : "=v"(D) : "v"(Y), "v"(X), "s"(0x05040100u))
#define PERM_HI(D, X, Y) asm volatile("v_perm_b32 %0, %1, %2, %3" : "=v"(D) : "v"(Y), "v"(X), "s"(0x07060302u))
template <int OFF>
DI void bload_v(u32x4& d, unsigned voff, rsrc_t rs, unsigned soff) {
  asm volatile("buffer_load_dwordx4 %0, %1, %2, %3 offen offset:%4" : "+v"(d) : "v"(voff), "s"(rs), "s"(soff), "i"(OFF) : "memory");
}
template <int OFF>
DI void bload_a(u32x4& d, unsigned voff, rsrc_t rs, unsigned soff) {
  asm volatile("buffer_load_dwordx4 %0, %1, %2, %3 offen offset:%4" : "+a"(d) : "v"(voff), "s"(rs), "s"(soff), "i"(OFF) : "memory");
}
template <int OFF>
DI void bload2_v(u32x2& d, unsigned voff, rsrc_t rs, unsigned soff) {
  asm volatile("buffer_load_dwordx2 %0, %1, %2, %3 offen offset:%4" : "+v"(d) : "v"(voff), "s"(rs), "s"(soff), "i"(OFF) : "memory");
}
template <int OFF>
DI void bload1_v(unsigned& d, unsigned voff, rsrc_t rs, unsigned soff) {
  asm volatile("buffer_load_dword %0, %1, %2, %3 offen offset:%4" : "+v"(d) : "v"(voff), "s"(rs), "s"(soff), "i"(OFF) : "memory");
}
template <int OFF>
DI void bload_lds(unsigned m0v, unsigned voff, rsrc_t rs, unsigned soff) {
  asm volatile("s_mov_b32 m0, %0\n\ts_nop 0\n\tbuffer_load_dwordx4 %1, %2, %3 offen offset:%4 lds" ::"s"(m0v), "v"(voff), "s"(rs), "s"(soff), "i"(OFF) : "memory");
}
template <int OFF>
DI void dsr128(u32x4& d, unsigned addr) { asm volatile("ds_read_b128 %0, %1 offset:%2" : "=v"(d) : "v"(addr), "i"(OFF) : "memory"); }
template <int OFF>
DI void dsr64(u32x2& d, unsigned addr) { asm volatile("ds_read_b64 %0, %1 offset:%2" : "=v"(d) : "v"(addr), "i"(OFF) : "memory"); }
template <int OFF>
DI void dsr_tr(u32x2& d, unsigned addr) { asm volatile("ds_read_b64_tr_b16 %0, %1 offset:%2" : "=v"(d) : "v"(addr), "i"(OFF) : "memory"); }
template <int OFF>
DI void dsw128(unsigned addr, u32x4 d) { asm volatile("ds_write_b128 %0, %1 offset:%2" ::"v"(addr), "v"(d), "i"(OFF) : "memory"); }
template <int OFF>
DI void dsw128a(unsigned addr, const f32x4& d) { asm volatile("ds_write_b128 %0, %1 offset:%2" ::"v"(addr), "a"(d), "i"(OFF) : "memory"); }
template <int OFF>
DI void dsw32(unsigned addr, unsigned d) { asm volatile("ds_write_b32 %0, %1 offset:%2" ::"v"(addr), "v"(d), "i"(OFF) : "memory"); }

DI void gst128(void* p, u32x4 v) {
  asm volatile("global_store_dwordx4 %0, %1, off sc1\n\ts_nop 1" ::"v"(p), "v"(v) : "memory");
}
DI f32x2 lo2(f32x4 v) { return __builtin_shufflevector(v, v, 0, 1); }
DI f32x2 hi2(f32x4 v) { return __builtin_shufflevector(v, v, 2, 3); }
DI float rowsum4(float v) {
  const unsigned u = __float_as_uint(v);
  const auto a = __builtin_amdgcn_permlane16_swap(u, u, false, false);
  const unsigned t = __float_as_uint(__uint_as_float(a[0]) + __uint_as_float(a[1]));
  const auto b = __builtin_amdgcn_permlane32_swap(t, t, false, false);
  return __uint_as_float(b[0]) + __uint_as_float(b[1]);
}
DI void split4(const f32x4 v, u32x2& hi, u32x2& lo) {
  hi[0] = pk_bf16(v[0], v[1]); hi[1] = pk_bf16(v[2], v[3]);
  lo[0] = pk_bf16(v[0] - bf_lo(hi[0]), v[1] - bf_hi(hi[0]));
  lo[1] = pk_bf16(v[2] - bf_lo(hi[1]), v[3] - bf_hi(hi[1]));
}
DI float silu_f(float v) { return v / (1.0f + __expf(-v)); }

__global__ __launch_bounds__(512) void pre_kernel(const bf16* __restrict__ x, const bf16* __restrict__ c,
                                                  const bf16* __restrict__ wmod, float* __restrict__ modp,
                                                  float* __restrict__ rstat1, const bf16* __restrict__ Wq) {
  __shared__ __attribute__((aligned(16))) float red[8 * 2 * 128 + 256];
  STAMP(0, 0);
  const int tid = threadIdx.x, lane = tid & 63, w = tid >> 6;
  if (blockIdx.x < 192) {
    const int fr = lane & 15, fq = lane >> 4;
    const int cg = blockIdx.x >> 2, kq = blockIdx.x & 3;
    const int col0 = cg * 128;
    const int k0 = kq * 256 + w * 32 + fq * 8;
    u32x4 raw[8];
#pragma unroll
    for (int j = 0; j < 8; ++j) {
      const u32x4* p_ = reinterpret_cast<const u32x4*>(wmod + (size_t)(k0 + j) * 6144 + col0 + fr * 8);
      raw[j] = __builtin_nontemporal_load(p_);
    }
    const u32x4 cv = *reinterpret_cast<const u32x4*>(c + k0);
    u32x4 cf;
#pragma unroll
    for (int e = 0; e < 4; ++e) {
      const float a = silu_f(bf_lo(cv[e])), b = silu_f(bf_hi(cv[e]));
      const unsigned hi = pk_bf16(a, b);
      const unsigned lo = pk_bf16(a - bf_lo(hi), b - bf_hi(hi));
      cf[e] = (fr == 0) ? hi : ((fr == 1) ? lo : 0u);
    }
    f32x4 acc[8];
#pragma unroll
    for (int p = 0; p < 4; ++p) {
      u32x4 lo, hi;
#pragma unroll
      for (int t = 0; t < 4; ++t) {
        lo[t] = __builtin_amdgcn_perm(raw[2 * t + 1][p], raw[2 * t][p], 0x05040100u);
        hi[t] = __builtin_amdgcn_perm(raw[2 * t + 1][p], raw[2 * t][p], 0x07060302u);
      }
      acc[2 * p] = __builtin_amdgcn_mfma_f32_16x16x32_bf16(__builtin_bit_cast(bf16x8, lo), __builtin_bit_cast(bf16x8, cf), f32x4{0, 0, 0, 0}, 0, 0, 0);
      acc[2 * p + 1] = __builtin_amdgcn_mfma_f32_16x16x32_bf16(__builtin_bit_cast(bf16x8, hi), __builtin_bit_cast(bf16x8, cf), f32x4{0, 0, 0, 0}, 0, 0, 0);
    }
    if (fr < 2) {
#pragma unroll
      for (int r = 0; r < 4; ++r) {
        float* d = red + (w * 2 + fr) * 128 + fq * 32 + r * 8;
        *reinterpret_cast<f32x4*>(d) = f32x4{acc[0][r], acc[1][r], acc[2][r], acc[3][r]};
        *reinterpret_cast<f32x4*>(d + 4) = f32x4{acc[4][r], acc[5][r], acc[6][r], acc[7][r]};
      }
    }
    __syncthreads();
    if (tid < 128) {
      float s = 0;
#pragma unroll
      for (int g = 0; g < 16; ++g) s += red[g * 128 + tid];
      modp[kq * 6144 + col0 + tid] = s;
    }
    STAMP(0, 1);
    return;
  }
  const int row0 = (blockIdx.x - 192) * 16 + w * 2;
  u32x4 a[2][2];
#pragma unroll
  for (int i = 0; i < 2; ++i) {
    a[i][0] = *reinterpret_cast<const u32x4*>(x + (size_t)(row0 + i) * D_DIM + lane * 8);
    a[i][1] = *reinterpret_cast<const u32x4*>(x + (size_t)(row0 + i) * D_DIM + 512 + lane * 8);
  }
  if (blockIdx.x < 192 + 8) {
    unsigned long long pc;
    asm volatile("s_getpc_b64 %0" : "=s"(pc));
    const rsrc_t rsC = mk_rsrc((const void*)(uintptr_t)(pc & ~255ull), CODEPF * 1024);
#pragma unroll
    for (int i = 0; i < (CODEPF + 7) / 8; ++i) bload_lds<0>((unsigned)(uintptr_t)(red + 2048), tid * 16, rsC, i * 8192);
  }
#pragma unroll
  for (int i = 0; i < 2; ++i) {
    float v[16];
#pragma unroll
    for (int j = 0; j < 8; ++j) { v[2 * j] = bf_lo(a[i][j >> 2][j & 3]); v[2 * j + 1] = bf_hi(a[i][j >> 2][j & 3]); }
    float s = 0;
#pragma unroll
    for (int j = 0; j < 16; ++j) s += v[j];
#pragma unroll
    for (int o = 32; o > 0; o >>= 1) s += __shfl_xor(s, o);
    const float mean = s * (1.0f / D_DIM);
    float q = 0;
#pragma unroll
    for (int j = 0; j < 16; ++j) { const float d = v[j] - mean; q += d * d; }
#pragma unroll
    for (int o = 32; o > 0; o >>= 1) q += __shfl_xor(q, o);
    const float rstd = rsqrtf(q * (1.0f / D_DIM) + 1e-5f);
    if (lane == 0) *reinterpret_cast<f32x2*>(rstat1 + (row0 + i) * 2) = f32x2{rstd, -mean * rstd};
  }
  STAMP(0, 2);
}

__global__ __launch_bounds__(256) void ln_apply_kernel(const bf16* __restrict__ x, const float* __restrict__ modp,
                                                       const float* __restrict__ rstat1, bf16* __restrict__ H,
                                                       const bf16* __restrict__ Wq, unsigned* __restrict__ flags) {
  __shared__ __attribute__((aligned(16))) float tab[4][64];
  __shared__ __attribute__((aligned(16))) char sink[1024];
  STAMP(1, 0);
  const int lane = threadIdx.x & 63, w = threadIdx.x >> 6;
  const int fr = lane & 15, fq = lane >> 4;
  const int kb = (blockIdx.x & 7) * 4 + w, mb0 = (blockIdx.x >> 3) * 4;
  u32x4 mr, xv[4];
  u32x2 rr[4];
  asm volatile("" : "=v"(mr));
  {
    bload_v<0>(mr, (unsigned)((fq * 6144 + ((lane >> 3) & 1) * 1024 + kb * 32 + (lane & 7) * 4) * 4), mk_rsrc(modp, 4 * 6144 * 4), 0);
    const rsrc_t rsX = mk_rsrc(x, S_LEN * D_DIM * 2), rsS = mk_rsrc(rstat1, S_LEN * 8);
    const unsigned vx = (unsigned)(((mb0 * 16 + fr) * D_DIM + kb * 32 + fq * 8) * 2), vs = (unsigned)((mb0 * 16 + fr) * 8);
#pragma unroll
    for (int i = 0; i < 4; ++i) {
      asm volatile("" : "=v"(xv[i]), "=v"(rr[i]));
      bload_v<0>(xv[i], vx, rsX, i * 32768);
      bload2_v<0>(rr[i], vs, rsS, i * 128);
    }
    const rsrc_t rsP = mk_rsrc((const char*)Wq + (size_t)blockIdx.x * 24576, 24576);
    const unsigned vo = threadIdx.x * 16;
#pragma unroll
    for (int i = 0; i < 6; ++i) bload_lds<0>((unsigned)(uintptr_t)sink, vo, rsP, i * 4096);
  }
  asm volatile("s_waitcnt vmcnt(" QK0W ")" : "+v"(mr), "+v"(xv[0]), "+v"(xv[1]), "+v"(xv[2]), "+v"(xv[3]), "+v"(rr[0]), "+v"(rr[1]), "+v"(rr[2]), "+v"(rr[3])::"memory");
  f32x4 mv = __builtin_bit_cast(f32x4, mr);
  f32x2 rs[4];
#pragma unroll
  for (int i = 0; i < 4; ++i) rs[i] = __builtin_bit_cast(f32x2, rr[i]);
#pragma unroll
  for (int e = 0; e < 4; ++e) { mv[e] += __shfl_xor(mv[e], 16); mv[e] += __shfl_xor(mv[e], 32); }
  if (lane < 16) *reinterpret_cast<f32x4*>(&tab[w][lane * 4]) = mv;
  __builtin_amdgcn_wave_barrier();
  asm volatile("s_waitcnt lgkmcnt(0)" ::: "memory");
  const f32x4 s0 = *reinterpret_cast<const f32x4*>(&tab[w][fq * 8]), s1 = *reinterpret_cast<const f32x4*>(&tab[w][fq * 8 + 4]);
  const f32x4 g0 = *reinterpret_cast<const f32x4*>(&tab[w][32 + fq * 8]) + 1.0f, g1 = *reinterpret_cast<const f32x4*>(&tab[w][32 + fq * 8 + 4]) + 1.0f;
#pragma unroll
  for (int i = 0; i < 4; ++i) {
    u32x4 ho;
#pragma unroll
    for (int d = 0; d < 4; ++d) {
      const f32x4 gg = d < 2 ? g0 : g1, ss = d < 2 ? s0 : s1;
      const float a = (bf_lo(xv[i][d]) * rs[i][0] + rs[i][1]) * gg[(d & 1) * 2] + ss[(d & 1) * 2];
      const float b = (bf_hi(xv[i][d]) * rs[i][0] + rs[i][1]) * gg[(d & 1) * 2 + 1] + ss[(d & 1) * 2 + 1];
      ho[d] = pk_bf16(a, b);
    }
    gst128((char*)H + ((size_t)(mb0 + i) * 32 + kb) * 1024 + lane * 16, ho);
  }
  if (threadIdx.x < 4) flags[blockIdx.x * 4 + threadIdx.x] = 0u;
  STAMP(1, 1);
}

constexpr int Q_SLOT = 21504, Q_NSL = 7, QH_OFF = Q_SLOT * Q_NSL;
constexpr int Q_LDS = QH_OFF + 1024;
constexpr int Q_PITCH = 192 * 4 + 16;

DI float quad_sum(float d) {
  d += __uint_as_float(__builtin_amdgcn_mov_dpp(__float_as_uint(d), 0xB1, 0xf, 0xf, true));
  d += __uint_as_float(__builtin_amdgcn_mov_dpp(__float_as_uint(d), 0x4E, 0xf, 0xf, true));
  return d;
}
DI void attn_item(const char* smem, int item, int m0, int n0, bf16* __restrict__ O) {
  const int p = item >> 3, hd2 = (item >> 2) & 1, qd = item & 3;
  const int rA = (p >> 1) * 4 + (p & 1);
  const float scale = 0.17677669529663687f * 1.4426950408889634f;
  const char* base = smem + (16 + rA) * Q_PITCH + (hd2 * 32 + qd * 8) * 4;
  const unsigned lb = (unsigned)(uintptr_t)base - 14 * Q_PITCH;
  u32x4 qr[2][2], kr[9][2], vr[9][2];
  dsr128<14 * Q_PITCH>(qr[0][0], lb); dsr128<14 * Q_PITCH + 16>(qr[0][1], lb);
  dsr128<16 * Q_PITCH>(qr[1][0], lb); dsr128<16 * Q_PITCH + 16>(qr[1][1], lb);
  static_for<9>([&](auto T) {
    dsr128<(16 - 2 * T.value) * Q_PITCH + 256>(kr[T.value][0], lb);
    dsr128<(16 - 2 * T.value) * Q_PITCH + 272>(kr[T.value][1], lb);
  });
  asm volatile("s_waitcnt lgkmcnt(0)" ::: "memory");
  static_for<9>([&](auto T) {
    dsr128<(16 - 2 * T.value) * Q_PITCH + 512>(vr[T.value][0], lb);
    dsr128<(16 - 2 * T.value) * Q_PITCH + 528>(vr[T.value][1], lb);
  });
  f32x4 q[2][2];
#pragma unroll
  for (int i = 0; i < 2; ++i) {
    asm volatile("" : "+v"(qr[i][0]), "+v"(qr[i][1]));
    q[i][0] = __builtin_bit_cast(f32x4, qr[i][0]) * scale;
    q[i][1] = __builtin_bit_cast(f32x4, qr[i][1]) * scale;
  }
  float lg[2][8];
#pragma unroll
  for (int t = 0; t < 9; ++t) {
    asm volatile("" : "+v"(kr[t][0]), "+v"(kr[t][1]));
    const f32x4 k0 = __builtin_bit_cast(f32x4, kr[t][0]), k1 = __builtin_bit_cast(f32x4, kr[t][1]);
#pragma unroll
    for (int i = 0; i < 2; ++i) {
      if (i == 1 ? t < 8 : t > 0) {
        f32x2 s = lo2(q[i][0]) * lo2(k0);
        s += hi2(q[i][0]) * hi2(k0);
        s += lo2(q[i][1]) * lo2(k1);
        s += hi2(q[i][1]) * hi2(k1);
        float d = s[0] + s[1];
        asm volatile("" : "+v"(d));
        lg[i][t - 1 + i] = quad_sum(d);
      }
    }
  }
  if (m0 == 0) {
#pragma unroll
    for (int w = 0; w < 8; ++w) {
      if (rA - 2 * w < 0) lg[0][w] = -1.4426950408889634e9f;
      if (rA + 2 - 2 * w < 0) lg[1][w] = -1.4426950408889634e9f;
    }
  }
  float inv[2];
#pragma unroll
  for (int i = 0; i < 2; ++i) {
    float mx = lg[i][0];
#pragma unroll
    for (int w = 1; w < 8; ++w) mx = fmaxf(mx, lg[i][w]);
    float sum = 0;
#pragma unroll
    for (int w = 0; w < 8; ++w) { lg[i][w] = __builtin_amdgcn_exp2f(lg[i][w] - mx); sum += lg[i][w]; }
    inv[i] = 1.0f / sum;
  }
  f32x4 o[2][2] = {{{0, 0, 0, 0}, {0, 0, 0, 0}}, {{0, 0, 0, 0}, {0, 0, 0, 0}}};
  asm volatile("s_waitcnt lgkmcnt(0)" : "+v"(inv[0]), "+v"(inv[1])::"memory");
#pragma unroll
  for (int t = 0; t < 9; ++t) {
    asm volatile("" : "+v"(vr[t][0]), "+v"(vr[t][1]));
    const f32x4 v0 = __builtin_bit_cast(f32x4, vr[t][0]), v1 = __builtin_bit_cast(f32x4, vr[t][1]);
    if (t < 8) { o[1][0] += lg[1][t] * v0; o[1][1] += lg[1][t] * v1; }
    if (t > 0) { o[0][0] += lg[0][t - 1] * v0; o[0][1] += lg[0][t - 1] * v1; }
  }
#pragma unroll
  for (int i = 0; i < 2; ++i) { o[i][0] *= inv[i]; o[i][1] *= inv[i]; }
  u32x4 xa = {pk_bf16(o[0][0][0], o[0][0][1]), pk_bf16(o[0][0][2], o[0][0][3]), pk_bf16(o[0][1][0], o[0][1][1]), pk_bf16(o[0][1][2], o[0][1][3])};
  u32x4 xb = {pk_bf16(o[1][0][0], o[1][0][1]), pk_bf16(o[1][0][2], o[1][0][3]), pk_bf16(o[1][1][0], o[1][1][1]), pk_bf16(o[1][1][2], o[1][1][3])};
#pragma unroll
  for (int e = 0; e < 4; ++e) {
    const auto sw = __builtin_amdgcn_permlane32_swap(xa[e], xb[e], false, false);
    xa[e] = sw[0]; xb[e] = sw[1];
  }
  const int pl = p & 3, rl = (pl >> 1) * 4 + (pl & 1) + ((p & 4) ? 2 : 0);
  const int gmb = (m0 >> 4) + (p >> 3), gkb = (n0 >> 5) + hd2;
  char* op = (char*)O + ((size_t)(gmb >> 2) * (D_DIM / 32) + gkb) * 4096 + (gmb & 3) * 1024 + (qd * 16 + rl) * 16;
  gst128(op, xa);
  gst128(op + 128, xb);
}

DI void qkv_loader(unsigned smem_base, int lane, int sw, int m0, int n0, const bf16* __restrict__ H, const bf16* __restrict__ W) {
  const rsrc_t rsH = mk_rsrc(H, S_LEN * D_DIM * 2);
  const rsrc_t rsW = mk_rsrc(W, D_DIM * 3 * D_DIM * 2);
  int fb0 = (m0 >> 4) - 1 + 3 * sw; fb0 = fb0 < 0 ? 0 : fb0;
  const unsigned voffH0 = (unsigned)(fb0 * 32768 + lane * 16);
  const unsigned voffH1 = (unsigned)(((m0 >> 4) + 3 * sw) * 32768 + lane * 16);
  unsigned voffW[2];
#pragma unroll
  for (int i = 0; i < 2; ++i) {
    const int r = i * 8 + (lane >> 3), ch = lane & 7;
    const int g = ((r >> 1) & 1) | (((r >> 3) & 1) << 1);
    voffW[i] = (unsigned)(r * 6144 + sw * 2048 + (n0 + (((ch >> 1) ^ g) * 16) + (ch & 1) * 8) * 2);
  }
  const unsigned m0a = smem_base + sw * 3072, m0w = smem_base + 9216 + sw * 4096;
#define Q_LOAD(SOFF, U)                                                                                          \
  do {                                                                                                           \
    const unsigned so_ = ((U) < 32) ? (unsigned)(U) * 1024u : 0x40000000u;                                       \
    bload_lds<0>(m0a + (SOFF), voffH0, rsH, so_);                                                                \
    bload_lds<0>(m0a + (SOFF) + 1024, voffH1, rsH, so_);                                                         \
    bload_lds<0>(m0a + (SOFF) + 2048, voffH1, rsH, so_ + 32768u);                                                \
    _Pragma("unroll") for (int i = 0; i < 4; ++i)                                                                \
      bload_lds<0>(m0w + (SOFF) + i * 1024, voffW[i & 1], rsW, (unsigned)(U) * 196608u + (i >> 1) * 98304u);     \
  } while (0)
#pragma unroll
  for (int u = 0; u < Q_NSL - 1; ++u) Q_LOAD(u * Q_SLOT, u);
  wait_vm<(Q_NSL - 2) * 7>();
  __builtin_amdgcn_s_barrier();
  unsigned sb = (Q_NSL - 1) * Q_SLOT;
  for (int u = 0; u < 32; ++u) {
    Q_LOAD(sb, u + Q_NSL - 1);
    sb = (sb == (Q_NSL - 1) * Q_SLOT) ? 0u : sb + Q_SLOT;
    wait_vm<(Q_NSL - 2) * 7>();
    __builtin_amdgcn_s_barrier();
  }
  asm volatile("s_waitcnt vmcnt(0)" ::: "memory");
}

DI void qkv_mfma(char* smem, unsigned smem_base, int tid, int wid) {
  const int lane = tid & 63;
  const int wm = wid & 1, wn = wid >> 1;
  const int fr = lane & 15, fq = lane >> 4;
  unsigned brd[6];
  const unsigned hrd = smem_base + lane * 16, ard = hrd + 1024 + wm * 4096;
  {
    const int q = (lane >> 2) & 3, p = lane & 3;
    const int r = 8 * fq + q;
    const int g = ((r >> 1) & 1) | (((r >> 3) & 1) << 1);
#pragma unroll
    for (int jj = 0; jj < 6; ++jj) {
      const int sec = (jj < 2) ? 0 : ((jj < 4) ? 1 + wm : 2 - wm);
      brd[jj] = smem_base + 9216 + sec * 4096 + r * 128 + (((wn * 2 + (jj & 1)) ^ g) * 32) + 8 * p;
    }
  }
  f32x4 acc[4][6], acch[2];
#pragma unroll
  for (int i = 0; i < 4; ++i)
#pragma unroll
    for (int j = 0; j < 6; ++j) { acc[i][j] = f32x4{0, 0, 0, 0}; asm volatile("" : "+v"(acc[i][j])); }
#pragma unroll
  for (int j = 0; j < 2; ++j) { acch[j] = f32x4{0, 0, 0, 0}; asm volatile("" : "+v"(acch[j])); }
  __builtin_amdgcn_s_setprio(2);
  __builtin_amdgcn_s_barrier();
  STAMPDECL;
  STAMPNW(2, 1);
  u32x4 aX[5], aY[5];
  u32x2 bXl[6], bXh[6], bYl[6], bYh[6];
#define Q_READ(AR, BL, BH, SO, R)                                                                                \
  do {                                                                                                           \
    if ((R) < 12) {                                                                                              \
      constexpr int jb = ((R) < 12 ? (R) : 0) >> 1;                                                              \
      if ((R) & 1) dsr_tr<512>(BH[jb], brd[jb] + (SO)); else dsr_tr<0>(BL[jb], brd[jb] + (SO));                  \
    } else if ((R) < 16) {                                                                                       \
      constexpr int ia = ((R) >= 12 && (R) < 16) ? (R) - 12 : 0;                                                 \
      dsr128<ia * 1024>(AR[ia], ard + (SO));                                                                     \
    } else {                                                                                                     \
      dsr128<0>(AR[4], hrd + (SO));                                                                              \
    }                                                                                                            \
  } while (0)
#define Q_MFMA(AR, BB, N)                                                                                        \
  do {                                                                                                           \
    if ((N) < 24) MFMA_AV(acc[(N) & 3][((N) < 24 ? (N) : 0) >> 2], BB[((N) < 24 ? (N) : 0) >> 2], AR[(N) & 3]);  \
    else MFMA_AV(acch[(N) & 1], BB[2 + ((N) & 1)], AR[4]);                                                        \
  } while (0)
#define Q_UNIT(AR, BL, BH, NAR, NBL, NBH)                                                                        \
  do {                                                                                                           \
    so = (so == (Q_NSL - 1) * Q_SLOT) ? 0u : so + Q_SLOT;                                                        \
    WAIT_LGKM0();                                                                                                \
    u32x4 bb[6];                                                                                                 \
    _Pragma("unroll") for (int j = 0; j < 6; ++j) bb[j] = u32x4{BL[j][0], BL[j][1], BH[j][0], BH[j][1]};         \
    SCHED0();                                                                                                    \
    asm volatile("s_nop 1");                                                                                     \
    static_for<26>([&](auto N) {                                                                                 \
      constexpr int n = N.value;                                                                                 \
      if constexpr (n == QBAR) __builtin_amdgcn_s_barrier();                                                   \
      Q_MFMA(AR, bb, n);                                                                                         \
      if constexpr (n >= QBAR && n < QBAR + QRN) {                                                               \
        constexpr int r0 = ((n - QBAR) * 17) / QRN, r1 = ((n - QBAR + 1) * 17) / QRN;                            \
        Q_READ(NAR, NBL, NBH, so, r0);                                                                           \
        if constexpr (r0 + 1 < r1) Q_READ(NAR, NBL, NBH, so, (r0 + 1 < 17 ? r0 + 1 : 0));                        \
      }                                                                                                          \
    });                                                                                                          \
    SCHED0();                                                                                                    \
  } while (0)
  unsigned so = 0;
  static_for<17>([&](auto R) { Q_READ(aX, bXl, bXh, 0u, R.value); });
  for (int u = 0; u < 32; u += 2) {
    Q_UNIT(aX, bXl, bXh, aY, bYl, bYh);
    Q_UNIT(aY, bYl, bYh, aX, bXl, bXh);
  }
  asm volatile("s_waitcnt vmcnt(0) lgkmcnt(0)\n\ts_nop 15" ::: "memory");
#pragma unroll
  for (int i = 0; i < 5; ++i) asm volatile("" : "+v"(aX[i]));
#pragma unroll
  for (int j = 0; j < 6; ++j) asm volatile("" : "+v"(bXl[j]), "+v"(bXh[j]));
  SCHED0();
  __builtin_amdgcn_s_setprio(0);
  STAMP(2, 2); STAMPFLUSH(2, 1);
  __builtin_amdgcn_s_barrier();
#pragma unroll
  for (int jj = 0; jj < 6; ++jj) {
    const int sec = (jj < 2) ? 0 : ((jj < 4) ? 1 + wm : 2 - wm);
    char* cp = smem + fr * Q_PITCH + (sec * 64 + wn * 32 + (jj & 1) * 16 + fq * 4) * 4;
#pragma unroll
    for (int i = 0; i < 4; ++i) *reinterpret_cast<f32x4*>(cp + (16 + wm * 64 + i * 16) * Q_PITCH) = acc[i][jj];
    if (jj == 2 || jj == 3) *reinterpret_cast<f32x4*>(cp) = acch[jj & 1];
  }
}

__global__ __launch_bounds__(512, 2) void qkv_attn_kernel(const bf16* __restrict__ H, const bf16* __restrict__ W,
                                                          bf16* __restrict__ O, const bf16* __restrict__ wg,
                                                          const bf16* __restrict__ wu, const bf16* __restrict__ wo) {
  __shared__ __attribute__((aligned(16))) char smem[Q_LDS];
  STAMP(2, 0);
  const int tid = threadIdx.x;
  const int wid = __builtin_amdgcn_readfirstlane(tid >> 6);
  const unsigned smem_base = (unsigned)(uintptr_t)smem;
  const int bid = (blockIdx.x & 7) * 32 + (blockIdx.x >> 3);
  const int grp = bid >> 7, ing = bid & 127;
  const int bm = grp * 8 + (ing & 7), bn = ing >> 3;
  const int m0 = bm * 128, n0 = bn * 64;
  if (wid < 4) qkv_mfma(smem, smem_base, tid, wid);
  else {
    if (wid < 7) qkv_loader(smem_base, tid & 63, wid - 4, m0, n0, H, W);
    else { for (int i = 0; i < 33; ++i) __builtin_amdgcn_s_barrier(); }
    const unsigned vo = (tid & 255) * 16, sink = smem_base + QH_OFF;
    const rsrc_t rsO = mk_rsrc((const char*)wo + (size_t)blockIdx.x * 8192, 8192);
    const rsrc_t rsG = mk_rsrc((const char*)wg + (size_t)blockIdx.x * (TQ * 1024), TQ * 1024);
    const rsrc_t rsU = mk_rsrc((const char*)wu + (size_t)blockIdx.x * (TQ * 1024), TQ * 1024);
    bload_lds<0>(sink, vo, rsO, 0); bload_lds<0>(sink, vo, rsO, 4096);
#pragma unroll
    for (int i = 0; i < TQ / 4; ++i) { bload_lds<0>(sink, vo, rsG, i * 4096); bload_lds<0>(sink, vo, rsU, i * 4096); }
    __builtin_amdgcn_s_barrier();
  }
  asm volatile("s_waitcnt lgkmcnt(0)" ::: "memory");
  __builtin_amdgcn_s_barrier();
  asm volatile("" ::: "memory");
  STAMP(2, 3);
  const int lane2 = __builtin_amdgcn_mbcnt_hi(~0u, __builtin_amdgcn_mbcnt_lo(~0u, 0u));
  attn_item(smem, wid * 64 + lane2, m0, n0, O);
  STAMP(2, 4);
  asm volatile("s_waitcnt vmcnt(0)");
  STAMP(2, 5);
}

template <int NSTEP, int R, int EPI>
__global__ __launch_bounds__(256, 1) void gemm_direct(const bf16* __restrict__ At, const bf16* __restrict__ W,
                                                      const bf16* __restrict__ Xres,
                                                      const float* __restrict__ modp, bf16* __restrict__ C,
                                                      bf16* __restrict__ Y, float* __restrict__ stat,
                                                      const bf16* __restrict__ wg, const bf16* __restrict__ wu) {
  constexpr int KB = NSTEP * 4;
  __shared__ __attribute__((aligned(16))) char smem[96 * 1024 + 1024 + 1024];
  constexpr int KID = (EPI == 0) ? 3 : 6;
  STAMPDECL;
  STAMP(KID, 0);
  const int tid = threadIdx.x, lane = tid & 63;
  const int w = __builtin_amdgcn_readfirstlane(tid >> 6);
  const int xcd = blockIdx.x & 7, idx = blockIdx.x >> 3;
  const int bm = (xcd >> 1) * 8 + (idx & 7), bn = (xcd & 1) * 4 + (idx >> 3);
  const int m0 = bm * 64;
  const int fr = lane & 15, fq = lane >> 4;
  const int cA = bn * 128, cB = cA + 64;
  const int colq = (fq < 2 ? cA : cB - 64) + fq * 32;
  const int colf = (fr < 8 ? cA : cB - 64) + fr * 8;

  const rsrc_t rsA = mk_rsrc((const char*)At + (size_t)bm * KB * 4096, KB * 4096);
  const rsrc_t rsB = mk_rsrc(W, KB * 65536);
  const unsigned voffA = lane * 16;
  unsigned voffR[4];
#pragma unroll
  for (int i = 0; i < 4; ++i) voffR[i] = voffA + ((i + w) & 3) * 1024;
  const unsigned voffB = fq * 8 * 2048 + colf * 2;

  u32x4 braw[R][8], afr[R][4];
  u32x4 T[2][2];
  u32x4 xv[4];
  unsigned gv[4] = {0, 0, 0, 0};
  f32x4 acc[4][8];
#pragma unroll
  for (int r = 0; r < R; ++r) {
#pragma unroll
    for (int j = 0; j < 8; ++j) asm volatile("" : "=v"(braw[r][j]));
#pragma unroll
    for (int i = 0; i < 4; ++i) asm volatile("" : "=a"(afr[r][i]));
  }
#pragma unroll
  for (int r = 0; r < 4; ++r) asm volatile("" : "=v"(xv[r]));
#define DLOAD(SL, L, KBV)                                                                          \
  do {                                                                                             \
    if ((L) < 8) {                                                                                 \
      const unsigned so = (unsigned)(KBV) * 65536u + ((L) >> 1) * 4096u;                           \
      if ((L) & 1) bload_v<2048>(braw[SL][(L) & 7], voffB, rsB, so);                               \
      else bload_v<0>(braw[SL][(L) & 7], voffB, rsB, so);                                          \
    } else {                                                                                       \
      bload_a<0>(afr[SL][(L) & 3], voffR[(L) & 3], rsA, (unsigned)(KBV) * 4096u);                  \
    }                                                                                              \
  } while (0)
#define DPERM(TB, Q, SL, PP)                                                                       \
  do {                                                                                             \
    if ((Q) & 1) PERM_HI(T[TB][1][(Q) >> 1], braw[SL][((Q) >> 1) * 2][PP], braw[SL][((Q) >> 1) * 2 + 1][PP]); \
    else PERM_LO(T[TB][0][(Q) >> 1], braw[SL][((Q) >> 1) * 2][PP], braw[SL][((Q) >> 1) * 2 + 1][PP]);         \
  } while (0)

#pragma unroll
  for (int l = 0; l < 12; ++l) DLOAD(0, l, w);
  {
    const rsrc_t rsG = mk_rsrc(modp + (w >= 2 ? 4096 : (EPI == 0 ? 2048 : 5120)), 4 * 6144 * 4);
    const unsigned vo = (unsigned)((((tid & 64) ? cB : cA) + (tid & 63)) * 4);
    bload1_v<0>(gv[0], vo, rsG, 0); bload1_v<0>(gv[1], vo, rsG, 24576);
    bload1_v<0>(gv[2], vo, rsG, 49152); bload1_v<0>(gv[3], vo, rsG, 73728);
    const rsrc_t rsX = mk_rsrc(Xres, S_LEN * D_DIM * 2);
    if (EPI == 0) {
      const unsigned vx = (unsigned)(((m0 + w * 16 + fr) * D_DIM + colq) * 2);
      bload_v<0>(xv[0], vx, rsX, 0); bload_v<16>(xv[1], vx, rsX, 0); bload_v<32>(xv[2], vx, rsX, 0); bload_v<48>(xv[3], vx, rsX, 0);
    } else {
      const unsigned vx = (unsigned)(((bm >> 2) * 32 + (colq >> 5)) * 16384 + ((bm & 3) * 4 + w) * 1024 + fr * 16);
      bload_v<0>(xv[0], vx, rsX, 0); bload_v<256>(xv[1], vx, rsX, 0); bload_v<512>(xv[2], vx, rsX, 0); bload_v<768>(xv[3], vx, rsX, 0);
    }
  }
#pragma unroll
  for (int s = 1; s < R - 1; ++s)
#pragma unroll
    for (int l = 0; l < 12; ++l) DLOAD(s, l, s * 4 + w);
#pragma unroll
  for (int i = 0; i < 4; ++i)
#pragma unroll
    for (int f = 0; f < 8; ++f) { acc[i][f] = f32x4{0, 0, 0, 0}; asm volatile("" : "+a"(acc[i][f])); }
  unsigned wlo[3];
#pragma unroll
  for (int i = 1; i < 4; ++i) wlo[i - 1] = (unsigned)(uintptr_t)smem + voffA + (((w + i) & 3) * 3 + (i - 1)) * 8192;
  wait_vm<(R - 2) * 12 + 8>();
  STAMPNW(KID, 1);
#pragma unroll
  for (int q = 0; q < 8; ++q) DPERM(0, q, 0, 0);
  asm volatile("s_nop 1");
  int kbl = (R - 1) * 4 + w;
  auto step = [&](auto SLc, auto REMc) {
    constexpr int sl = SLc.value, rem = REMc.value, ns = (sl + 1) % R, ls = (sl + R - 1) % R;
    static_for<4>([&](auto P) {
      constexpr int p = P.value;
      if constexpr (p == 3 && rem > 0) wait_vm<(rem - 1 < R - 2 ? rem - 1 : R - 2) * 12>();
      static_for<8>([&](auto Q) {
        constexpr int q = Q.value;
        MFMA_A(acc[q & 3][2 * p + (q >> 2)], T[p & 1][q >> 2], afr[sl][q & 3]);
        if constexpr (p < 3) DPERM((p + 1) & 1, q, sl, p + 1);
        else if constexpr (rem > 0) DPERM(0, q, ns, 0);
        if constexpr (rem >= R - 1 && p < 3 && (q & 1)) DLOAD(ls, p * 4 + (q >> 1), kbl);
        if constexpr (rem == 0 && p > 0 && q >= 2) {
          constexpr int e = q - 2, i = 1 + e % 3, f = 2 * (p - 1) + e / 3;
          dsw128a<f * 1024>(wlo[i - 1], acc[i][f]);
        }
      });
    });
    kbl += 4;
  };
  for (int it = 0; it < NSTEP / R - 1; ++it) static_for<R>([&](auto S) { step(S, ic<R>{}); });
  static_for<R>([&](auto S) { step(S, ic<R - 1 - S.value>{}); });
  asm volatile("s_nop 15" ::: "memory");
#pragma unroll
  for (int f = 0; f < 8; ++f) asm volatile("" : "+a"(acc[0][f]));
  static_for<6>([&](auto E) { constexpr int i = 1 + E.value % 3, f = 6 + E.value / 3; dsw128a<f * 1024>(wlo[i - 1], acc[i][f]); });
#pragma unroll
  for (int r = 0; r < 4; ++r) asm volatile("" : "+v"(xv[r]), "+v"(gv[r]));
  STAMP(KID, 2); STAMPFLUSH(KID, 1);
  if constexpr (EPI == 0 && TG > 0) {
    const unsigned vo = tid * 16, sink = (unsigned)(uintptr_t)smem + 96 * 1024 + 1024;
    const rsrc_t rsG = mk_rsrc((const char*)wg + 256 * TQ * 1024 + (size_t)blockIdx.x * (TG * 1024), TG * 1024);
    const rsrc_t rsU = mk_rsrc((const char*)wu + 256 * TQ * 1024 + (size_t)blockIdx.x * (TG * 1024), TG * 1024);
#pragma unroll
    for (int i = 0; i < TG / 4; ++i) { bload_lds<0>(sink, vo, rsG, i * 4096); bload_lds<0>(sink, vo, rsU, i * 4096); }
  }
  float* gtab = reinterpret_cast<float*>(smem + 96 * 1024);
  {
    float sum = (__uint_as_float(gv[0]) + __uint_as_float(gv[1])) + (__uint_as_float(gv[2]) + __uint_as_float(gv[3]));
    if (w >= 2) {
      sum += 1.0f;
      if (__builtin_fabsf(sum) < 8.67361738e-19f) sum = 8.67361738e-19f;
      if (EPI == 1) sum = 1.0f / sum;
    }
    const int col = tid & 127;
    gtab[(tid & 128) + (col & 96) + (col & 7) * 4 + ((col >> 3) & 3)] = sum;
  }
  asm volatile("s_waitcnt lgkmcnt(0)" ::: "memory");
  __builtin_amdgcn_s_barrier();
  u32x4 pr[3][8], gt[8], ct[8];
  f32x4 fin[8];
  {
    const unsigned src = (unsigned)(uintptr_t)smem + w * 24576 + lane * 16;
    const unsigned gsrc = (unsigned)(uintptr_t)smem + 96 * 1024 + fq * 128;
    static_for<24>([&](auto E) { dsr128<E.value * 1024>(pr[E.value >> 3][E.value & 7], src); });
    static_for<8>([&](auto F) { dsr128<F.value * 16>(gt[F.value], gsrc); });
    static_for<8>([&](auto F) { dsr128<512 + F.value * 16>(ct[F.value], gsrc); });
#pragma unroll
    for (int f = 0; f < 8; ++f) { fin[f] = acc[0][f]; asm volatile("" : "+v"(fin[f])); }
    asm volatile("s_waitcnt lgkmcnt(0)" ::: "memory");
#pragma unroll
    for (int f = 0; f < 8; ++f) {
      asm volatile("" : "+v"(pr[0][f]), "+v"(pr[1][f]), "+v"(pr[2][f]), "+v"(gt[f]), "+v"(ct[f]));
    }
  }
#pragma unroll
  for (int f = 0; f < 8; ++f) {
    fin[f] = (fin[f] + __builtin_bit_cast(f32x4, pr[0][f])) + (__builtin_bit_cast(f32x4, pr[1][f]) + __builtin_bit_cast(f32x4, pr[2][f]));
    f32x4 xf;
#pragma unroll
    for (int r = 0; r < 4; ++r) xf[r] = (f & 1) ? bf_hi(xv[r][f >> 1]) : bf_lo(xv[r][f >> 1]);
    if constexpr (EPI == 1) xf *= __builtin_bit_cast(f32x4, ct[f]);
    fin[f] = xf + __builtin_bit_cast(f32x4, gt[f]) * fin[f];
  }
  if constexpr (EPI == 0) {
    f32x4 s4 = (fin[0] + fin[1]) + (fin[2] + fin[3]) + ((fin[4] + fin[5]) + (fin[6] + fin[7]));
    const float s = rowsum4((s4[0] + s4[1]) + (s4[2] + s4[3]));
    const float mb = s * (1.0f / 128.0f);
    f32x4 q4 = {0, 0, 0, 0};
    f32x4 yv[8];
#pragma unroll
    for (int f = 0; f < 8; ++f) {
      yv[f] = fin[f] * __builtin_bit_cast(f32x4, ct[f]);
      const f32x4 d = fin[f] - mb;
      q4 += d * d;
    }
    char* yb = (char*)Y + ((size_t)(bm >> 2) * 32 + (colq >> 5)) * 16384 + ((bm & 3) * 4 + w) * 1024 + fr * 16;
#pragma unroll
    for (int r = 0; r < 4; ++r) {
      const u32x4 yo = {pk_bf16(yv[0][r], yv[1][r]), pk_bf16(yv[2][r], yv[3][r]), pk_bf16(yv[4][r], yv[5][r]), pk_bf16(yv[6][r], yv[7][r])};
      gst128(yb + r * 256, yo);
    }
    const float m2 = rowsum4((q4[0] + q4[1]) + (q4[2] + q4[3]));
    if (fq == 0) *reinterpret_cast<f32x2*>(stat + ((size_t)(m0 + w * 16 + fr) * 8 + bn) * 2) = f32x2{s, m2};
  } else {
    char* tb = smem + (w * 3 * 8) * 1024;
#pragma unroll
    for (int r = 0; r < 4; ++r) {
      const u32x4 ov = {pk_bf16(fin[0][r], fin[1][r]), pk_bf16(fin[2][r], fin[3][r]), pk_bf16(fin[4][r], fin[5][r]), pk_bf16(fin[6][r], fin[7][r])};
      *reinterpret_cast<u32x4*>(tb + fr * 256 + (((fq * 4 + r) ^ (fr & 7)) * 16)) = ov;
    }
#pragma unroll
    for (int j = 0; j < 4; ++j) {
      const int row = j * 4 + fq;
      const u32x4 ov = *reinterpret_cast<const u32x4*>(tb + row * 256 + ((fr ^ (row & 7)) * 16));
      gst128(C + (size_t)(m0 + w * 16 + row) * D_DIM + colf, ov);
    }
  }
  STAMP(KID, 3);
  asm volatile("s_waitcnt vmcnt(0)");
  STAMP(KID, 4);
}

#define NBR 0
#define TPOS 6
#define NBC 1
#define G3_TW "12"
#define G3_DUMMY() do { } while (0)
constexpr int G3_CT = 131072 + 4096 + 1024;
#define G3_STAGE(SLOT, P, U)                                                                               \
  do {                                                                                                     \
    if ((P) == 0) bload_lds<0>(m0w + (SLOT) * 32768, voffA, rsA, (unsigned)(U) * 16384u);                  \
    else if ((P) == 1) bload_lds<1024>(m0w + (SLOT) * 32768, voffA, rsA, (unsigned)(U) * 16384u);          \
    else if ((P) == 2) bload_lds<0>(m0w + (SLOT) * 32768 + 16384, voffB0, rsB, (unsigned)(U) * 229376u);   \
    else bload_lds<0>(m0w + (SLOT) * 32768 + 16384 + 1024, voffB1, rsB, (unsigned)(U) * 229376u);          \
  } while (0)

template <int FN>
DI void g3_body(char* smem, unsigned smem_base, int wm, int wn, int wid, int lane, int bm, int bn, rsrc_t rsA, rsrc_t rsB,
                rsrc_t rsP, unsigned voffA, unsigned voffB0, unsigned voffB1, bf16* __restrict__ act) {
  constexpr int NB = 2 * FN, NM = 4 * NB, RD = 5 + 2 * NB;
  const int fr = lane & 15, fq = lane >> 4;
  f32x4 acc[4][NB], accC[2];
  accC[0] = f32x4{0, 0, 0, 0}; accC[1] = f32x4{0, 0, 0, 0};
  asm volatile("" : "+v"(accC[0]), "+v"(accC[1]));
  const int rot = wm % FN;
  unsigned cad = smem_base + G3_CT + (fr < 4 ? fr * 64 : 256) + fq * 16;
#pragma unroll
  for (int i = 0; i < 4; ++i)
#pragma unroll
    for (int j = 0; j < NB; ++j) { acc[i][j] = f32x4{0, 0, 0, 0}; asm volatile("" : "+v"(acc[i][j])); }
  unsigned aad[2], bad[2][FN];
  aad[0] = smem_base + wm * 4096 + lane * 16; aad[1] = aad[0] + 65536;
  {
    const int q = (lane >> 2) & 3, p = lane & 3;
    const int r = 8 * fq + q;
    const int sw = (r & 3) | (((r >> 3) & 1) << 2);
#pragma unroll
    for (int j = 0; j < FN; ++j) {
      const int gi = wn * 4 + (j + rot) % FN + (bn & 1);
      bad[0][j] = smem_base + 16384 + r * 256 + ((gi ^ sw) * 32) + 8 * p;
      bad[1][j] = bad[0][j] + 65536;
    }
  }
  u32x4 aX[5], aY[5];
  u32x2 bXl[NB], bXh[NB], bYl[NB], bYh[NB];
  const unsigned m0w = smem_base + wid * 2048;
  const unsigned voffP = lane * 16;
#define G3_READ(AR, BL, BH, SLOT, R)                                                                       \
  do {                                                                                                     \
    if ((R) < 2 * NB) {                                                                                    \
      constexpr int jb = ((R) < 2 * NB ? (R) : 0) >> 1;                                                    \
      constexpr int off = ((SLOT) & 1) * 32768 + (jb / FN) * 8192 + ((R) & 1) * 1024;                      \
      if ((R) & 1) dsr_tr<off>(BH[jb], bad[(SLOT) >> 1][jb % FN]);                                         \
      else dsr_tr<off>(BL[jb], bad[(SLOT) >> 1][jb % FN]);                                                 \
    } else if ((R) < 2 * NB + 4) {                                                                         \
      constexpr int ia = ((R) >= 2 * NB && (R) < 2 * NB + 4) ? (R) - 2 * NB : 0;                           \
      dsr128<((SLOT) & 1) * 32768 + ia * 1024>(AR[ia], aad[(SLOT) >> 1]);                                  \
    } else {                                                                                               \
      if ((SLOT) == 0) cad += 1280;                                      \
      dsr128<(SLOT) * 320>(AR[4], cad);                                                                    \
    }                                                                                                      \
  } while (0)
  auto rd = [&](auto SL, auto RR, u32x4 (&AR)[5], u32x2 (&BL)[NB], u32x2 (&BH)[NB]) { G3_READ(AR, BL, BH, SL.value, RR.value); };
#define G3_UNIT(AR, BL, BH, NAR, NBL, NBH, SLOT, U)                                                        \
  do {                                                                                                     \
    WAIT_LGKM0();                                                                                          \
    u32x4 bb[NB];                                                                                          \
    _Pragma("unroll") for (int j = 0; j < NB; ++j) bb[j] = u32x4{BL[j][0], BL[j][1], BH[j][0], BH[j][1]};  \
    SCHED0();                                                                                              \
    asm volatile("s_nop 1");                                                                               \
    static_for<NM>([&](auto N) {                                                                           \
      constexpr int n = N.value;                                                                           \
      if constexpr (n == NM / 2) {                                                                         \
        wait_vm<NBR ? 10 : 8>(); __builtin_amdgcn_s_barrier();                                             \
      }                                                                                                    \
      if constexpr (n == NM / 2 + TPOS) {                                                                  \
                      \
        if (NBR) bload_lds<0>(smem_base + 131072 + 4096, voffP, rsP, (((U) & 7) == wid) ? (unsigned)(U) * 1024u : 0x40000000u); \
        else if (((U) & 7) == wid) bload_lds<0>(smem_base + 131072 + 4096, voffP, rsP, (unsigned)(U) * 1024u);  \
      }                                                                                                    \
      MFMA_AV(acc[n & 3][n >> 2], bb[n >> 2], AR[n & 3]);                                                  \
      if constexpr (n < NM / 2) {                                                                          \
        if constexpr (n % 3 == 1 && n / 3 < 4) G3_STAGE(((SLOT) + 3) & 3, n / 3, (U) + 3);                 \
      } else {                                                                                             \
        constexpr int r0 = (n - NM / 2) * 2;                                                               \
        if constexpr (r0 < RD) rd(ic<((SLOT) + 1) & 3>{}, ic<(r0 < RD ? r0 : 0)>{}, NAR, NBL, NBH);        \
        if constexpr (r0 + 1 < RD) rd(ic<((SLOT) + 1) & 3>{}, ic<(r0 + 1 < RD ? r0 + 1 : 0)>{}, NAR, NBL, NBH); \
      }                                                                                                    \
    });                                                                                                    \
    if (NBC || FN == 4 || wm < 3) {                                                                        \
      MFMA_AV(accC[0], bb[0], AR[4]);                                                                      \
      MFMA_AV(accC[1], bb[FN], AR[4]);                                                                     \
    }                                                                                                      \
    SCHED0();                                                                                              \
  } while (0)

  if (NBR) asm volatile("s_waitcnt vmcnt(10) lgkmcnt(0)" ::: "memory");
  else asm volatile("s_waitcnt vmcnt(8) lgkmcnt(0)" ::: "memory");
  __builtin_amdgcn_s_barrier();
  STAMPDECL;
  STAMPNW(5, 1);
  cad -= 1280;
  static_for<RD>([&](auto RR) { rd(ic<0>{}, RR, aX, bXl, bXh); });
  for (int u = 0; u < 32; u += 4) {
    G3_UNIT(aX, bXl, bXh, aY, bYl, bYh, 0, u);
    G3_UNIT(aY, bYl, bYh, aX, bXl, bXh, 1, u + 1);
    G3_UNIT(aX, bXl, bXh, aY, bYl, bYh, 2, u + 2);
    G3_UNIT(aY, bYl, bYh, aX, bXl, bXh, 3, u + 3);
  }
  asm volatile("s_waitcnt lgkmcnt(0)\n\ts_nop 15" ::: "memory");
#pragma unroll
  for (int i = 0; i < 5; ++i) asm volatile("" : "+v"(aX[i]));
#pragma unroll
  for (int j = 0; j < NB; ++j) asm volatile("" : "+v"(bXl[j]), "+v"(bXh[j]));
  SCHED0();
  STAMP(5, 2);
  STAMPFLUSH(5, 1);
  const f32x2* rstat = reinterpret_cast<const f32x2*>(smem + 131072);
  float* colc = reinterpret_cast<float*>(smem + 131072 + 2048);
#pragma unroll
  for (int m = 0; m < 2; ++m) {
    f32x4 v;
#pragma unroll
    for (int r = 0; r < 4; ++r) v[r] = accC[m][r] + __shfl_xor(accC[m][r], 1);
    if ((fr == 0 || fr == 2) && wm < FN) *reinterpret_cast<f32x4*>(colc + (m * 2 + (fr >> 1)) * 128 + (wn * 4 + rot) * 16 + fq * 4) = v;
  }
  asm volatile("s_waitcnt lgkmcnt(0)" ::: "memory");
  __builtin_amdgcn_s_barrier();
  asm volatile("" ::: "memory");
  f32x2 rs[4];
#pragma unroll
  for (int i = 0; i < 4; ++i) rs[i] = rstat[wm * 64 + i * 16 + fr];
#pragma unroll
  for (int j = 0; j < FN; ++j) {
    const int jg = (j + rot) % FN;
    const int cl = (wn * 4 + jg) * 16 + fq * 4;
    const f32x4 csg = *reinterpret_cast<const f32x4*>(colc + cl), bg = *reinterpret_cast<const f32x4*>(colc + 128 + cl);
    const f32x4 csu = *reinterpret_cast<const f32x4*>(colc + 256 + cl), bu = *reinterpret_cast<const f32x4*>(colc + 384 + cl);
    u32x2 pk[4];
#pragma unroll
    for (int i = 0; i < 4; ++i) {
#pragma unroll
      for (int hh = 0; hh < 2; ++hh) {
        const f32x2 ag = hh ? hi2(acc[i][j]) : lo2(acc[i][j]), au = hh ? hi2(acc[i][FN + j]) : lo2(acc[i][FN + j]);
        const f32x2 g = rs[i][0] * ag + (rs[i][1] * (hh ? hi2(csg) : lo2(csg)) + (hh ? hi2(bg) : lo2(bg)));
        const f32x2 uu = rs[i][0] * au + (rs[i][1] * (hh ? hi2(csu) : lo2(csu)) + (hh ? hi2(bu) : lo2(bu)));
        const f32x2 ea = g * -1.4426950408889634f;
        f32x2 e = {__builtin_amdgcn_exp2f(ea[0]), __builtin_amdgcn_exp2f(ea[1])};
        e = e + 1.0f;
        const f32x2 rc = {__builtin_amdgcn_rcpf(e[0]), __builtin_amdgcn_rcpf(e[1])};
        const f32x2 o = (g * uu) * rc;
        pk[i][hh] = pk_bf16(o[0], o[1]);
      }
    }
    const int J = bn * 7 + wn * 4 + jg;
    char* dst = (char*)act + ((size_t)((bm * 4 + wm) * (F_DIM / 32) + (J >> 1)) * 4 + (fq & 1)) * 1024 + (((J & 1) * 2 + (fq >> 1)) * 16 + fr) * 16;
#pragma unroll
    for (int i = 0; i < 4; i += 2) {
      const auto s0 = __builtin_amdgcn_permlane16_swap(pk[i][0], pk[i + 1][0], false, false);
      const auto s1 = __builtin_amdgcn_permlane16_swap(pk[i][1], pk[i + 1][1], false, false);
      gst128(dst + i * 1024, u32x4{s0[0], s1[0], s0[1], s1[1]});
    }
  }
}

__global__ __launch_bounds__(512, 2) void gemm3_kernel(const bf16* __restrict__ Y, const bf16* __restrict__ Wg,
                                                       const bf16* __restrict__ Wu, bf16* __restrict__ act,
                                                       const float* __restrict__ stat, const float* __restrict__ modp,
                                                       const bf16* __restrict__ Wd) {
  __shared__ __attribute__((aligned(16))) char smem[G3_CT + 33 * 320];
  STAMP(5, 0);
  const int tid = threadIdx.x, lane = tid & 63;
  const int wid = __builtin_amdgcn_readfirstlane(tid >> 6);
  const int wm = wid & 3, wn = wid >> 2;
  const int xcd = blockIdx.x & 7, idx = blockIdx.x >> 3;
  const int bm = idx & 7, bn = xcd * 4 + (idx >> 3);
  const int n0 = bn * 112, n0a = n0 & ~31;
  const unsigned smem_base = (unsigned)(uintptr_t)smem;
  const rsrc_t rsA = mk_rsrc((const char*)Y + (size_t)bm * 32 * 16384, 32 * 16384);
  const rsrc_t rsB = mk_rsrc(wn ? Wu : Wg, D_DIM * F_DIM * 2);
  const rsrc_t rsP = mk_rsrc((const char*)Wd + (size_t)blockIdx.x * 28672, 28672);
  const unsigned voffA = wid * 2048 + lane * 16;
  unsigned voffB[2];
#pragma unroll
  for (int i = 0; i < 2; ++i) {
    const int r = (wm * 2 + i) * 4 + (lane >> 4);
    const int sw = (r & 3) | (((r >> 3) & 1) << 2);
    const int c16 = lane & 15;
    voffB[i] = (unsigned)(r * (F_DIM * 2) + (n0a + (((c16 >> 1) ^ sw) * 16) + (c16 & 1) * 8) * 2);
  }
  const unsigned m0w = smem_base + wid * 2048;
  const unsigned voffB0 = voffB[0], voffB1 = voffB[1];
  if (wn == 0) {
    u32x4 tv[4];
#pragma unroll
    for (int i = 0; i < 4; ++i) asm volatile("" : "=v"(tv[i]));
    const rsrc_t rsS = mk_rsrc(stat, S_LEN * 64);
    const unsigned vo = (unsigned)((bm * 256 + tid) * 64);
    bload_v<0>(tv[0], vo, rsS, 0); bload_v<16>(tv[1], vo, rsS, 0); bload_v<32>(tv[2], vo, rsS, 0); bload_v<48>(tv[3], vo, rsS, 0);
    G3_STAGE(0, 0, 0); G3_STAGE(0, 1, 0); G3_STAGE(0, 2, 0); G3_STAGE(0, 3, 0);
    G3_STAGE(1, 0, 1); G3_STAGE(1, 1, 1); G3_STAGE(1, 2, 1); G3_STAGE(1, 3, 1);
    G3_DUMMY();
    G3_STAGE(2, 0, 2); G3_STAGE(2, 1, 2); G3_STAGE(2, 2, 2); G3_STAGE(2, 3, 2);
    G3_DUMMY();
    asm volatile("s_waitcnt vmcnt(" G3_TW ")" : "+v"(tv[0]), "+v"(tv[1]), "+v"(tv[2]), "+v"(tv[3])::"memory");
    float s = 0;
#pragma unroll
    for (int i = 0; i < 4; ++i) s += __uint_as_float(tv[i][0]) + __uint_as_float(tv[i][2]);
    const float mean = s * (1.0f / D_DIM);
    float m2 = 0;
#pragma unroll
    for (int i = 0; i < 4; ++i) {
      const float d0 = __uint_as_float(tv[i][0]) * (1.0f / 128.0f) - mean, d1 = __uint_as_float(tv[i][2]) * (1.0f / 128.0f) - mean;
      m2 += __uint_as_float(tv[i][1]) + __uint_as_float(tv[i][3]) + 128.0f * (d0 * d0 + d1 * d1);
    }
    const float rstd = rsqrtf(m2 * (1.0f / D_DIM) + 1e-5f);
    reinterpret_cast<f32x2*>(smem + 131072)[tid] = f32x2{rstd, -mean * rstd};
    g3_body<4>(smem, smem_base, wm, wn, wid, lane, bm, bn, rsA, rsB, rsP, voffA, voffB0, voffB1, act);
  } else {
    const rsrc_t rsC = mk_rsrc(modp, 4 * 6144 * 4);
    const int t = tid - 256;
    const unsigned vo = (unsigned)((3072 + t * 4) * 4);
    u32x4 tc[8];
#pragma unroll
    for (int i = 0; i < 8; ++i) { asm volatile("" : "=v"(tc[i])); bload_v<0>(tc[i], vo, rsC, (i >> 1) * 24576 + (i & 1) * 4096); }
    G3_STAGE(0, 0, 0); G3_STAGE(0, 1, 0); G3_STAGE(0, 2, 0); G3_STAGE(0, 3, 0);
    G3_STAGE(1, 0, 1); G3_STAGE(1, 1, 1); G3_STAGE(1, 2, 1); G3_STAGE(1, 3, 1);
    G3_DUMMY();
    G3_STAGE(2, 0, 2); G3_STAGE(2, 1, 2); G3_STAGE(2, 2, 2); G3_STAGE(2, 3, 2);
    G3_DUMMY();
    asm volatile("s_waitcnt vmcnt(" G3_TW ")" : "+v"(tc[0]), "+v"(tc[1]), "+v"(tc[2]), "+v"(tc[3]), "+v"(tc[4]), "+v"(tc[5]), "+v"(tc[6]), "+v"(tc[7])::"memory");
    const f32x4 sv = (__builtin_bit_cast(f32x4, tc[0]) + __builtin_bit_cast(f32x4, tc[2])) + (__builtin_bit_cast(f32x4, tc[4]) + __builtin_bit_cast(f32x4, tc[6]));
    const f32x4 gw = (__builtin_bit_cast(f32x4, tc[1]) + __builtin_bit_cast(f32x4, tc[3])) + (__builtin_bit_cast(f32x4, tc[5]) + __builtin_bit_cast(f32x4, tc[7])) + 1.0f;
    u32x2 gh, gl, sh, sl;
    split4(gw, gh, gl); split4(sv, sh, sl);
    char* d = smem + G3_CT + (t >> 3) * 320 + ((t >> 1) & 3) * 16 + (t & 1) * 8;
    *reinterpret_cast<u32x2*>(d) = gh; *reinterpret_cast<u32x2*>(d + 64) = gl;
    *reinterpret_cast<u32x2*>(d + 128) = sh; *reinterpret_cast<u32x2*>(d + 192) = sl;
    *reinterpret_cast<u32x2*>(d + 256) = u32x2{0, 0};
    g3_body<3>(smem, smem_base, wm, wn, wid, lane, bm, bn, rsA, rsB, rsP, voffA, voffB0, voffB1, act);
  }
  STAMP(5, 3);
  asm volatile("s_waitcnt vmcnt(0)");
  STAMP(5, 4);
}

#define XST 0
#define G4NF 11
#define HFS 3
#define HFL 4
#define HCK 5
#define XMAP 1
template <int OFF>
DI void bstore_sc1(u32x4 d, unsigned voff, rsrc_t rs, unsigned soff) {
  asm volatile("buffer_store_dwordx4 %0, %1, %2, %3 offen offset:%4 sc1\n\ts_nop 1" ::"v"(d), "v"(voff), "s"(rs), "s"(soff), "i"(OFF) : "memory");
}
template <int OFF>
DI void bload_sc1(u32x4& d, unsigned voff, rsrc_t rs, unsigned soff) {
  asm volatile("buffer_load_dwordx4 %0, %1, %2, %3 offen offset:%4 sc1" : "+v"(d) : "v"(voff), "s"(rs), "s"(soff), "i"(OFF) : "memory");
}
DI void bload1_sc1(unsigned& d, unsigned voff, rsrc_t rs) {
  asm volatile("buffer_load_dword %0, %1, %2, 0 offen sc1" : "+v"(d) : "v"(voff), "s"(rs) : "memory");
}
namespace g4 {
constexpr int NF = G4NF, NH = 28 - 2 * NF, NS = NF + NH, HEP = NH - 3;
constexpr int nslot(int g) { return g < NF ? 64 : 32; }
constexpr int wslot(int g) { return g < NF - 1 ? 48 : (g == NF - 1 ? 56 : 24); }
constexpr int ring(int g, int n) {
  if (g < NF - 1) {
    if (n >= 48 || n % 3 != 1) return -1;
    const int l = n / 3;
    if (g + 2 < NF) return (g + 2) * 16 + l;
    return l < 12 ? (g + 2) * 16 + (l < 8 ? l : l + 4) : -1;
  }
  if (g == NF - 1) { if (n >= 48 || n % 4 != 1) return -1; const int l = n / 4; return (g + 2) * 16 + (l < 8 ? l : l + 4); }
  if (g == NF) { if (n >= 24) return -1; const int l = n % 12; return (g + 2 + n / 12) * 16 + (l < 8 ? l : l + 4); }
  if (g + 3 >= NS || n >= 24 || (n & 1)) return -1;
  const int l = n >> 1;
  return (g + 3) * 16 + (l < 8 ? l : l + 4);
}
constexpr int pwrite(int g, int n) {
  const int m = g == NF - 1 ? n : 64 + n;
  for (int e = 0; e < 24; ++e) if (12 + (13 * e) / 4 == m) return e;
  return -1;
}
constexpr int red0(int n) { return n < 8 ? 0 : (64 * (n - 8)) / 24; }
#define SPS 4
constexpr int sslot(int f) {
  const int ready = (f >> 2) * 32 + 8 + (((f & 3) + 1) * 16 * 24 + 63) / 64;
  const int paced = (f / SPS) * 32 + 15 + (f % SPS) * (32 / SPS);
  int m = ready > paced ? ready : paced;
  if ((m & 1) == 0) ++m;
  return m;
}
constexpr int dstore_f(int g, int n) {
  for (int f = 0; f < 8; ++f) if (sslot(f) == (g - NF) * 32 + n) return f;
  return -1;
}
constexpr bool dstore(int g, int n) { return g >= NF && dstore_f(g, n) >= 0; }
constexpr int vmops(int g, int n) {
  int c = (ring(g, n) >= 0 ? 1 : 0) + (dstore(g, n) ? 1 : 0);
  const int h = g - NF;
  if (h == HFS && n == 24) c += 1;
  if (h == HFL && n == 25) c += 1;
  if (h == HCK && n >= 24) c += 1;
  if (h == HEP && (n == 1 || n == 3)) c += 4;
  return c;
}
constexpr int between(int g0, int n0, int g1, int n1) {
  int c = 0;
  for (int g = g0; g <= g1; ++g)
    for (int n = (g == g0 ? n0 + 1 : 0); n < (g == g1 ? n1 : nslot(g)); ++n) c += vmops(g, n);
  return c;
}
constexpr int imin(int a, int b) { return a < b ? a : b; }
constexpr int waitcnt(int g) {
  int c = 63;
  if (g + 1 < NS && g + 1 >= 2) {
    for (int s = 0; s <= g; ++s)
      for (int n = 0; n < nslot(s); ++n)
        if (ring(s, n) == (g + 1) * 16 + 15) c = imin(c, between(s, n, g, wslot(g)) + (dstore(s, n) ? 1 : 0));
  }
  if (g == 0) c = 16;
  if (g - NF == HFS) c = imin(c, between(NF + sslot(7) / 32, sslot(7) % 32, g, 24));
  if (g - NF == HCK) c = imin(c, between(NF + HFL, 25, g, 24));
  return c;
}
constexpr int bslot(int b) { return b < NF + 3 ? b % 3 : ((b - NF) % 4 == 3 ? 3 : (NF + (b - NF) % 4) % 3); }
}
__global__ __launch_bounds__(256, 1) void gemm4_kernel(const bf16* __restrict__ At, const bf16* __restrict__ W,
                                                       const bf16* __restrict__ Yt, const float* __restrict__ modp,
                                                       bf16* __restrict__ C, float* xbuf, unsigned* flags) {
  using namespace g4;
  constexpr int KB = F_DIM / 32;
  constexpr int GT = 96 * 1024;
  __shared__ __attribute__((aligned(16))) char smem[GT + 1024];
  STAMPDECL;
  STAMP(6, 0);
  const int tid = threadIdx.x, lane = tid & 63;
  const int w = __builtin_amdgcn_readfirstlane(tid >> 6);
  const int xcd = blockIdx.x & 7, idx = blockIdx.x >> 3;
  const int kh = xcd & 1, bm = ((xcd >> 1) & 1) * 8 + (idx & 7), bn = (xcd >> 2) * 4 + (idx >> 3);
  const unsigned partner = blockIdx.x ^ 1;
  const int n0 = bn * 128, bm64 = bm * 2 + kh;
  const int fr = lane & 15, fq = lane >> 4;
  const rsrc_t rsA0 = mk_rsrc((const char*)At + (size_t)(bm64 ^ 1) * KB * 4096, KB * 4096);
  const rsrc_t rsA1 = mk_rsrc((const char*)At + (size_t)bm64 * KB * 4096, KB * 4096);
  const rsrc_t rsB = mk_rsrc((const char*)W + n0 * 2, KB * 65536 - n0 * 2);
  const unsigned voffL = lane * 16;
  unsigned voffA[4];
#pragma unroll
  for (int i = 0; i < 4; ++i) voffA[i] = voffL + ((i + w) & 3) * 1024;
  const unsigned voffB = fq * 8 * 2048 + fr * 16;
  u32x4 braw[4][8], afr[3][8], afa[4][4];
  u32x4 T[2][2];
  u32x4 xv[4], pv[8];
  unsigned gv[4], flg;
  f32x4 acc[8][8];
#pragma unroll
  for (int r = 0; r < 3; ++r)
#pragma unroll
    for (int j = 0; j < 8; ++j) asm volatile("" : "=v"(braw[r][j]), "=v"(afr[r][j]));
  const int kbF = (kh ? KB - 4 * NF : 0) + w, kbH = w;
#define ELOAD(B, L)                                                                                \
  do {                                                                                             \
    const unsigned kbv = (unsigned)(((B) < NF ? kbF : kbH) + (B) * 4);                             \
    if constexpr ((L) < 8) {                                                                       \
      const unsigned so = kbv * 65536u + ((L) >> 1) * 4096u;                                       \
      if constexpr (bslot(B) == 3 || (B) >= NF + 3) asm volatile("" : "=v"(braw[bslot(B)][(L) & 7]));        \
      if constexpr ((L) & 1) bload_v<2048>(braw[bslot(B)][(L) & 7], voffB, rsB, so);               \
      else bload_v<0>(braw[bslot(B)][(L) & 7], voffB, rsB, so);                                    \
    } else if constexpr ((B) >= NF + 2) {                                                          \
      asm volatile("" : "=a"(afa[((B) - NF) & 3][(L) & 3]));                                       \
      bload_a<0>(afa[((B) - NF) & 3][(L) & 3], voffA[(L) & 3], rsA1, kbv * 4096u);                 \
    } else {                                                                                       \
      bload_v<0>(afr[(B) % 3][(L) & 7], voffA[(L) & 3], ((L) & 4) ? rsA1 : rsA0, kbv * 4096u);     \
    }                                                                                              \
  } while (0)
#define MFMA_E(C, B, A) asm volatile("v_mfma_f32_16x16x32_bf16 %0, %1, %2, %0" : "+a"(C) : "v"(B), "v"(A))
  static_for<16>([&](auto L) { ELOAD(0, L.value); });
  static_for<16>([&](auto L) { ELOAD(1, L.value); });
#pragma unroll
  for (int i = 0; i < 8; ++i)
#pragma unroll
    for (int f = 0; f < 8; ++f) { acc[i][f] = f32x4{0, 0, 0, 0}; asm volatile("" : "+a"(acc[i][f])); }
  const unsigned sbase = (unsigned)(uintptr_t)smem + voffL;
  unsigned wlo[3];
#pragma unroll
  for (int i = 1; i < 4; ++i) wlo[i - 1] = sbase + (((w + i) & 3) * 3 + (i - 1)) * 8192;
  const unsigned rlo = sbase + w * 24576;
  const rsrc_t rsEx = mk_rsrc(xbuf, 256 * 32768);
  const unsigned exo = (unsigned)(blockIdx.x * 32768 + w * 8192);
  const rsrc_t rsFo = mk_rsrc(flags + blockIdx.x * 4 + w, 4), rsFi = mk_rsrc(flags + partner * 4 + w, 4);
  wait_vm<16>();
  STAMPNW(6, 1);
#pragma unroll
  for (int q = 0; q < 8; ++q) DPERM(0, q, 0, 0);
  asm volatile("s_nop 1");
  u32x4 pr[3][4];
  unsigned long long _ts[7];
  f32x4 tt[8], uu;
  static_for<NS>([&](auto G) {
    constexpr int g = G.value, h = g - NF, sl = bslot(g), ns = bslot(g + 1);
    constexpr bool full = g < NF - 1, lastf = g == NF - 1;
    constexpr bool red = (h == 0 || h == 1);
    constexpr int fb = h == 1 ? 4 : 0;
    if constexpr (h == 0 || h == 1) { WAIT_LGKM0(); __builtin_amdgcn_s_barrier(); }
    if constexpr (h == 3) __builtin_amdgcn_s_barrier();
    static_for<nslot(g)>([&](auto N) {
      constexpr int n = N.value;
      if constexpr (n == wslot(g)) {
        if constexpr (waitcnt(g) < 63) {
          if constexpr (h == HCK) asm volatile("s_waitcnt vmcnt(%1)" : "+v"(flg) : "n"(waitcnt(g)) : "memory");
          else wait_vm<waitcnt(g)>();
        }
        if constexpr (h == HFS) asm volatile("buffer_store_dword %0, %1, %2, 0 offen sc1" ::"v"(1u), "v"(voffL >> 2), "s"(rsFo) : "memory");
        if constexpr (h == HCK) {
          unsigned spins = 0;
          while (__builtin_amdgcn_readfirstlane(flg) == 0u && spins < (1u << 20)) {
            __builtin_amdgcn_s_sleep(1);
            bload1_sc1(flg, 0u, rsFi);
            asm volatile("s_waitcnt vmcnt(0)" : "+v"(flg)::"memory");
            ++spins;
          }
        }
      }
      if constexpr (red && n == 8) WAIT_LGKM0();
      if constexpr (full) {
        constexpr int p = n >> 4, q = n & 15;
        MFMA_E(acc[q & 7][2 * p + (q >> 3)], T[p & 1][q >> 3], afr[g % 3][q & 7]);
        if constexpr ((q & 1) == 0) {
          if constexpr (p < 3) DPERM((p + 1) & 1, q >> 1, sl, p + 1);
          else DPERM(0, q >> 1, ns, 0);
        }
      } else {
        constexpr int p = (n >> 3) & 3, q = n & 7, ib = (lastf && n < 32) ? 0 : 4;
        if constexpr (h >= 2) MFMA_A(acc[ib + (q & 3)][2 * p + (q >> 2)], T[p & 1][q >> 2], afa[h & 3][q & 3]);
        else MFMA_E(acc[ib + (q & 3)][2 * p + (q >> 2)], T[p & 1][q >> 2], afr[g % 3][ib + (q & 3)]);
        if constexpr (p < 3) DPERM((p + 1) & 1, q, sl, p + 1);
        else if constexpr (ib == 0) DPERM(0, q, sl, 0);
        else if constexpr (g + 1 < NS) DPERM(0, q, ns, 0);
      }
      if constexpr (ring(g, n) >= 0) ELOAD((ring(g, n) >> 4), (ring(g, n) & 15));
      if constexpr ((lastf || h == 0) && pwrite(g, n) >= 0) {
        constexpr int e = pwrite(g, n), i = 1 + e % 3, f = e / 3;
        dsw128a<f * 1024>(wlo[i - 1], acc[i][f]);
      }
      if constexpr (red) {
        if constexpr (n < 6) dsr128<(n % 3) * 8192 + (fb + n / 3) * 1024>(pr[n % 3][n / 3], rlo);
        if constexpr (n == 6 || n == 7) {
          static_for<3>([&](auto E) { dsr128<E.value * 8192 + (fb + n - 4) * 1024>(pr[E.value][n - 4], rlo); });
        }
        if constexpr (n >= 8) {
          constexpr int o0 = red0(n), o1 = red0(n + 1);
          static_for<o1 - o0>([&](auto O) {
            constexpr int o = o0 + O.value, fl = o / 16, k = o % 16, r = k & 3;
            (void)&uu; (void)&tt; (void)&pr;
            if constexpr (k < 4) asm volatile("v_accvgpr_read_b32 %0, %1" : "=v"(tt[fb + fl][r]) : "a"(acc[0][fb + fl][r]));
            else if constexpr (k < 8) asm volatile("v_add_f32 %0, %0, %1" : "+v"(tt[fb + fl][r]) : "v"(pr[0][fl][r]));
            else if constexpr (k < 12) asm volatile("v_add_f32 %0, %1, %2" : "=v"(uu[r]) : "v"(pr[1][fl][r]), "v"(pr[2][fl][r]));
            else asm volatile("v_add_f32 %0, %0, %1" : "+v"(tt[fb + fl][r]) : "v"(uu[r]));
          });
        }
      }
      if constexpr (h >= 0 && dstore(g, n)) {
        constexpr int f = dstore_f(g, n);
        bstore_sc1<(f & 3) * 1024>(__builtin_bit_cast(u32x4, tt[f]), voffL, rsEx, exo + (f >> 2) * 4096);
      }
      if constexpr (h == HFL && n == 25) {
        asm volatile("" : "=v"(flg));
        bload1_sc1(flg, 0u, rsFi);
      }
      if constexpr (h == HCK && n >= 24) {
        constexpr int f = n - 24;
        asm volatile("" : "=v"(pv[f]));
        bload_sc1<(f & 3) * 1024>(pv[f], voffL, rsEx, partner * 32768u + w * 8192u + (f >> 2) * 4096u);
      }
      if constexpr (h == HEP && n == 1) {
        const rsrc_t rsG = mk_rsrc(modp + (w >= 2 ? 4096 : 5120), 4 * 6144 * 4);
        const unsigned vo = (unsigned)((n0 + (tid & 127)) * 4);
#pragma unroll
        for (int r = 0; r < 4; ++r) { asm volatile("" : "=v"(gv[r])); bload1_v<0>(gv[r], vo, rsG, r * 24576); }
      }
      if constexpr (h == HEP && n == 3) {
        const rsrc_t rsX = mk_rsrc(Yt, S_LEN * D_DIM * 2);
        const unsigned vx = (unsigned)(((bm64 >> 2) * 32 + bn * 4 + fq) * 16384 + ((bm64 & 3) * 4 + w) * 1024 + fr * 16);
#pragma unroll
        for (int r = 0; r < 4; ++r) asm volatile("" : "=v"(xv[r]));
        bload_v<0>(xv[0], vx, rsX, 0); bload_v<256>(xv[1], vx, rsX, 0); bload_v<512>(xv[2], vx, rsX, 0); bload_v<768>(xv[3], vx, rsX, 0);
      }
      if constexpr (g == NS - 1 && n >= 8 && (n & 7) >= 2) {
        constexpr int e = (n & 7) - 2, i = 1 + e % 3, f = 2 * ((n >> 3) - 1) + e / 3;
        dsw128a<f * 1024>(wlo[i - 1], acc[4 + i][f]);
      }
    });
  });
  asm volatile("s_nop 15" ::: "memory");
#pragma unroll
  for (int f = 0; f < 8; ++f) asm volatile("" : "+a"(acc[4][f]));
  static_for<6>([&](auto E) { constexpr int i = 1 + E.value % 3, f = 6 + E.value / 3; dsw128a<f * 1024>(wlo[i - 1], acc[4 + i][f]); });
  asm volatile("s_waitcnt vmcnt(%8)" : "+v"(xv[0]), "+v"(xv[1]), "+v"(xv[2]), "+v"(xv[3]), "+v"(gv[0]), "+v"(gv[1]), "+v"(gv[2]), "+v"(gv[3]) : "n"(between(NF + HEP, 3, NS - 1, 32)) : "memory");
  STAMP(6, 2); STAMPFLUSH(6, 1);
  float* gtab = reinterpret_cast<float*>(smem + GT);
  {
    float sum = (__uint_as_float(gv[0]) + __uint_as_float(gv[1])) + (__uint_as_float(gv[2]) + __uint_as_float(gv[3]));
    if (w >= 2) {
      sum += 1.0f;
      if (__builtin_fabsf(sum) < 8.67361738e-19f) sum = 8.67361738e-19f;
      sum = 1.0f / sum;
    }
    const int col = tid & 127;
    gtab[(tid & 128) + (col & 96) + (col & 7) * 4 + ((col >> 3) & 3)] = sum;
  }
  asm volatile("s_waitcnt lgkmcnt(0)" ::: "memory");
  __builtin_amdgcn_s_barrier();
  u32x4 pq[3][8], gt[8], ct[8];
  f32x4 fin[8];
  {
    const unsigned gsrc = (unsigned)(uintptr_t)smem + GT + fq * 128;
    static_for<24>([&](auto E) { dsr128<E.value * 1024>(pq[E.value >> 3][E.value & 7], rlo); });
    static_for<8>([&](auto F) { dsr128<F.value * 16>(gt[F.value], gsrc); });
    static_for<8>([&](auto F) { dsr128<512 + F.value * 16>(ct[F.value], gsrc); });
#pragma unroll
    for (int f = 0; f < 8; ++f) { fin[f] = acc[4][f]; asm volatile("" : "+v"(fin[f])); }
    asm volatile("s_waitcnt lgkmcnt(0)" ::: "memory");
#pragma unroll
    for (int f = 0; f < 8; ++f) asm volatile("" : "+v"(pq[0][f]), "+v"(pq[1][f]), "+v"(pq[2][f]), "+v"(gt[f]), "+v"(ct[f]));
  }
#pragma unroll
  for (int f = 0; f < 8; ++f) {
    fin[f] = (fin[f] + __builtin_bit_cast(f32x4, pq[0][f])) + (__builtin_bit_cast(f32x4, pq[1][f]) + __builtin_bit_cast(f32x4, pq[2][f]));
    asm volatile("" : "+v"(fin[f]));
  }
  asm volatile("s_waitcnt vmcnt(0)" : "+v"(pv[0]), "+v"(pv[1]), "+v"(pv[2]), "+v"(pv[3]), "+v"(pv[4]), "+v"(pv[5]), "+v"(pv[6]), "+v"(pv[7])::"memory");
  STAMP(6, 6);
#pragma unroll
  for (int f = 0; f < 8; ++f) {
    fin[f] += __builtin_bit_cast(f32x4, pv[f]);
    f32x4 xf;
#pragma unroll
    for (int r = 0; r < 4; ++r) xf[r] = (f & 1) ? bf_hi(xv[r][f >> 1]) : bf_lo(xv[r][f >> 1]);
    xf *= __builtin_bit_cast(f32x4, ct[f]);
    fin[f] = xf + __builtin_bit_cast(f32x4, gt[f]) * fin[f];
  }
  {
    char* tb = smem + w * 24576;
#pragma unroll
    for (int r = 0; r < 4; ++r) {
      const u32x4 ov = {pk_bf16(fin[0][r], fin[1][r]), pk_bf16(fin[2][r], fin[3][r]), pk_bf16(fin[4][r], fin[5][r]), pk_bf16(fin[6][r], fin[7][r])};
      *reinterpret_cast<u32x4*>(tb + fr * 256 + (((fq * 4 + r) ^ (fr & 7)) * 16)) = ov;
    }
#pragma unroll
    for (int j = 0; j < 4; ++j) {
      const int row = j * 4 + fq;
      const u32x4 ov = *reinterpret_cast<const u32x4*>(tb + row * 256 + ((fr ^ (row & 7)) * 16));
      gst128(C + (size_t)(bm64 * 64 + w * 16 + row) * D_DIM + n0 + fr * 8, ov);
    }
  }
  STAMP(6, 3);
  asm volatile("s_waitcnt vmcnt(0)");
  STAMP(6, 4);
}

__global__ void diag_kernel() {
  __shared__ unsigned long long r0[256], r1[256];
  const int t = threadIdx.x;
  unsigned long long a = ~0ull, b = (DMODE == 0) ? ~0ull : 0ull; unsigned long long cnt = 0;
  for (int i = t; i < PROF_NB; i += 256) {
    unsigned long long x = g_stamps[(0 * PROF_NB + i) * PROF_NS + 0];
    if (x && x < a) a = x;
    unsigned long long y = g_stamps[(DKB * PROF_NB + i) * PROF_NS + DSB];
    if (y) { if (DMODE == 0) { if (y < b) b = y; } else if (DMODE == 1) { if (y > b) b = y; } else { b += y; cnt++; } }
  }
  __shared__ unsigned long long r2[256];
  r0[t] = a; r1[t] = b; r2[t] = cnt;
  __syncthreads();
  if (t == 0) {
    for (int i = 1; i < 256; ++i) {
      if (r0[i] < a) a = r0[i];
      if (DMODE == 0) { if (r1[i] < b) b = r1[i]; } else if (DMODE == 1) { if (r1[i] > b) b = r1[i]; } else { b += r1[i]; cnt += r2[i]; }
    }
    if (DMODE == 2) b = cnt ? b / cnt : a;
    unsigned long long d = (DSB == 7) ? b / 100 : ((b > a) ? (b - a) : 0);
    if (d > 20000) d = 20000;
    const unsigned long long t0 = __builtin_amdgcn_s_memrealtime();
    while (__builtin_amdgcn_s_memrealtime() - t0 < d * DMULT) __builtin_amdgcn_s_sleep(1);
  }
}
extern "C" void kernel_launch(void* const* d_in, const int* in_sizes, int n_in, void* d_out, int out_size,
                              void* d_ws, size_t ws_size, hipStream_t stream) {
  const bf16* x = (const bf16*)d_in[0];
  const bf16* wqkv = (const bf16*)d_in[1];
  const bf16* wo = (const bf16*)d_in[2];
  const bf16* wg = (const bf16*)d_in[4];
  const bf16* wu = (const bf16*)d_in[5];
  const bf16* wd = (const bf16*)d_in[6];
  const bf16* c = (const bf16*)d_in[7];
  const bf16* wmod = (const bf16*)d_in[8];
  bf16* out = (bf16*)d_out;

  char* ws = (char*)d_ws;
  float* modp = (float*)ws;
  float* rstat1 = (float*)(ws + 114688);
  float* stat = (float*)(ws + 131072);
  bf16* ot = (bf16*)(ws + (1 << 20));
  bf16* yt = (bf16*)(ws + (9 << 20));
  bf16* ht = (bf16*)(ws + (13 << 20));
  bf16* act = (bf16*)(ws + (17 << 20));
  float* xbuf = (float*)(ws + (32 << 20));
  unsigned* flags = (unsigned*)(ws + 262144);

  pre_kernel<<<320, 512, 0, stream>>>(x, c, wmod, modp, rstat1, wqkv);
  ln_apply_kernel<<<256, 256, 0, stream>>>(x, modp, rstat1, ht, wqkv, flags);
  qkv_attn_kernel<<<256, 512, 0, stream>>>(ht, wqkv, ot, wg, wu, wo);
  gemm_direct<8, 4, 0><<<256, 256, 0, stream>>>(ot, wo, x, modp, nullptr, yt, stat, wg, wu);
  gemm3_kernel<<<256, 512, 0, stream>>>(yt, wg, wu, act, stat, modp, wd);
  gemm4_kernel<<<256, 256, 0, stream>>>(act, wd, yt, modp, out, xbuf, flags);
  diag_kernel<<<1, 256, 0, stream>>>();
}
```

```cpp
#include <hip/hip_runtime.h>
#include <hip/hip_bf16.h>
#include <stdint.h>

typedef __hip_bfloat16 bf16;
typedef __attribute__((ext_vector_type(8))) short bf16x8;
typedef __attribute__((ext_vector_type(4))) float f32x4;
typedef __attribute__((ext_vector_type(2))) float f32x2;
typedef __attribute__((ext_vector_type(4))) unsigned u32x4;
typedef __attribute__((ext_vector_type(2))) unsigned u32x2;
typedef __attribute__((ext_vector_type(2))) __bf16 bf16x2_t;
typedef __amdgpu_buffer_rsrc_t rsrc_t;

#define DI __device__ __forceinline__
#define QBAR 2
#define QRN 17
#define TQ 12
#define TG 4
#define K1MAP 1
#define QTM 1
#define WOT 1
#define CODEPF 53

#define STAMP(k, s) do { } while (0)
#define STAMPT(k, s, T) do { } while (0)
#define STAMPNW(k, s)
#define STAMPFLUSH(k, s)
#define STAMPDECL

constexpr int S_LEN = 2048;
constexpr int D_DIM = 1024;
constexpr int F_DIM = 3584;

template <int V> struct ic { static constexpr int value = V; };
template <int N, int I = 0, class F>
DI void static_for(F&& f) {
  if constexpr (I < N) { f(ic<I>{}); static_for<N, I + 1>(f); }
}

DI float bf_lo(unsigned u) { return __uint_as_float(u << 16); }
DI float bf_hi(unsigned u) { return __uint_as_float(u & 0xffff0000u); }
DI unsigned pk_bf16(float lo, float hi) {
  const f32x2 v = {lo, hi};
  return __builtin_bit_cast(unsigned, __builtin_convertvector(v, bf16x2_t));
}
DI rsrc_t mk_rsrc(const void* p, unsigned bytes) { return __builtin_amdgcn_make_buffer_rsrc((void*)p, (short)0, (int)bytes, 0x00020000); }

template <int N>
DI void wait_vm() { asm volatile("s_waitcnt vmcnt(%0)" ::"n"(N > 63 ? 63 : N) : "memory"); }
#define WAIT_LGKM0() do { asm volatile("s_waitcnt lgkmcnt(0)" ::: "memory"); __builtin_amdgcn_sched_barrier(0); } while (0)
#define SCHED0() __builtin_amdgcn_sched_barrier(0)

#define MFMA_A(C, B, A) asm volatile("v_mfma_f32_16x16x32_bf16 %0, %1, %2, %0" : "+a"(C) : "v"(B), "a"(A))
#define MFMA_AV(C, B, A) asm volatile("v_mfma_f32_16x16x32_bf16 %0, %1, %2, %0" : "+v"(C) : "v"(B), "v"(A))
#define PERM_LO(D, X, Y) asm volatile("v_perm_b32 %0, %1, %2, %3" : "=v"(D) : "v"(Y), "v"(X), "s"(0x05040100u))
#define PERM_HI(D, X, Y) asm volatile("v_perm_b32 %0, %1, %2, %3" : "=v"(D) : "v"(Y), "v"(X), "s"(0x07060302u))
template <int OFF>
DI void bload_v(u32x4& d, unsigned voff, rsrc_t rs, unsigned soff) {
  asm volatile("buffer_load_dwordx4 %0, %1, %2, %3 offen offset:%4" : "+v"(d) : "v"(voff), "s"(rs), "s"(soff), "i"(OFF) : "memory");
}
template <int OFF>
DI void bload_a(u32x4& d, unsigned voff, rsrc_t rs, unsigned soff) {
  asm volatile("buffer_load_dwordx4 %0, %1, %2, %3 offen offset:%4" : "+a"(d) : "v"(voff), "s"(rs), "s"(soff), "i"(OFF) : "memory");
}
template <int OFF>
DI void bload2_v(u32x2& d, unsigned voff, rsrc_t rs, unsigned soff) {
  asm volatile("buffer_load_dwordx2 %0, %1, %2, %3 offen offset:%4" : "+v"(d) : "v"(voff), "s"(rs), "s"(soff), "i"(OFF) : "memory");
}
template <int OFF>
DI void bload1_v(unsigned& d, unsigned voff, rsrc_t rs, unsigned soff) {
  asm volatile("buffer_load_dword %0, %1, %2, %3 offen offset:%4" : "+v"(d) : "v"(voff), "s"(rs), "s"(soff), "i"(OFF) : "memory");
}
template <int OFF>
DI void bload_lds(unsigned m0v, unsigned voff, rsrc_t rs, unsigned soff) {
  asm volatile("s_mov_b32 m0, %0\n\ts_nop 0\n\tbuffer_load_dwordx4 %1, %2, %3 offen offset:%4 lds" ::"s"(m0v), "v"(voff), "s"(rs), "s"(soff), "i"(OFF) : "memory");
}
DI void touch_lds(unsigned m0v, unsigned voff, rsrc_t rs, unsigned soff) {
  asm volatile("s_mov_b32 m0, %0\n\ts_nop 0\n\tbuffer_load_dword %1, %2, %3 offen lds" ::"s"(m0v), "v"(voff), "s"(rs), "s"(soff) : "memory");
}
DI void touch_nt(unsigned m0v, unsigned voff, rsrc_t rs, unsigned soff) {
  asm volatile("s_mov_b32 m0, %0\n\ts_nop 0\n\tbuffer_load_dword %1, %2, %3 offen nt lds\n\tbuffer_load_dword %1, %2, %3 offen offset:64 sc1 lds" ::"s"(m0v), "v"(voff), "s"(rs), "s"(soff) : "memory");
}
template <int OFF>
DI void bload_lds_nt(unsigned m0v, unsigned voff, rsrc_t rs, unsigned soff) {
  asm volatile("s_mov_b32 m0, %0\n\ts_nop 0\n\tbuffer_load_dwordx4 %1, %2, %3 offen offset:%4 nt lds" ::"s"(m0v), "v"(voff), "s"(rs), "s"(soff), "i"(OFF) : "memory");
}
template <int OFF>
DI void dsr128(u32x4& d, unsigned addr) { asm volatile("ds_read_b128 %0, %1 offset:%2" : "=v"(d) : "v"(addr), "i"(OFF) : "memory"); }
template <int OFF>
DI void dsr64(u32x2& d, unsigned addr) { asm volatile("ds_read_b64 %0, %1 offset:%2" : "=v"(d) : "v"(addr), "i"(OFF) : "memory"); }
template <int OFF>
DI void dsr_tr(u32x2& d, unsigned addr) { asm volatile("ds_read_b64_tr_b16 %0, %1 offset:%2" : "=v"(d) : "v"(addr), "i"(OFF) : "memory"); }
template <int OFF>
DI void dsw128(unsigned addr, u32x4 d) { asm volatile("ds_write_b128 %0, %1 offset:%2" ::"v"(addr), "v"(d), "i"(OFF) : "memory"); }
template <int OFF>
DI void dsw128a(unsigned addr, const f32x4& d) { asm volatile("ds_write_b128 %0, %1 offset:%2" ::"v"(addr), "a"(d), "i"(OFF) : "memory"); }
template <int OFF>
DI void dsw32(unsigned addr, unsigned d) { asm volatile("ds_write_b32 %0, %1 offset:%2" ::"v"(addr), "v"(d), "i"(OFF) : "memory"); }

DI void gst128(void* p, u32x4 v) {
  asm volatile("global_store_dwordx4 %0, %1, off sc1\n\ts_nop 1" ::"v"(p), "v"(v) : "memory");
}
DI f32x2 lo2(f32x4 v) { return __builtin_shufflevector(v, v, 0, 1); }
DI f32x2 hi2(f32x4 v) { return __builtin_shufflevector(v, v, 2, 3); }
DI float rowsum4(float v) {
  const unsigned u = __float_as_uint(v);
  const auto a = __builtin_amdgcn_permlane16_swap(u, u, false, false);
  const unsigned t = __float_as_uint(__uint_as_float(a[0]) + __uint_as_float(a[1]));
  const auto b = __builtin_amdgcn_permlane32_swap(t, t, false, false);
  return __uint_as_float(b[0]) + __uint_as_float(b[1]);
}
DI void split4(const f32x4 v, u32x2& hi, u32x2& lo) {
  hi[0] = pk_bf16(v[0], v[1]); hi[1] = pk_bf16(v[2], v[3]);
  lo[0] = pk_bf16(v[0] - bf_lo(hi[0]), v[1] - bf_hi(hi[0]));
  lo[1] = pk_bf16(v[2] - bf_lo(hi[1]), v[3] - bf_hi(hi[1]));
}
DI float silu_f(float v) { return v / (1.0f + __expf(-v)); }
DI float rbf(float v) { return bf_lo(pk_bf16(v, v)); }
DI float rowmax4(float v) {
  const unsigned u = __float_as_uint(v);
  const auto a = __builtin_amdgcn_permlane16_swap(u, u, false, false);
  const unsigned t = __float_as_uint(fmaxf(__uint_as_float(a[0]), __uint_as_float(a[1])));
  const auto b = __builtin_amdgcn_permlane32_swap(t, t, false, false);
  return fmaxf(__uint_as_float(b[0]), __uint_as_float(b[1]));
}
DI float gain_nz(float m) {
  const float g = rbf(1.0f + rbf(m));
  return __builtin_fabsf(g) < 8.67361738e-19f ? 8.67361738e-19f : g;
}
template <int OFF>
DI void bload_sc1(u32x4& d, unsigned voff, rsrc_t rs, unsigned soff) {
  asm volatile("buffer_load_dwordx4 %0, %1, %2, %3 offen offset:%4 sc1" : "+v"(d) : "v"(voff), "s"(rs), "s"(soff), "i"(OFF) : "memory");
}
DI void bload1_sc1(unsigned& d, unsigned voff, rsrc_t rs) {
  asm volatile("buffer_load_dword %0, %1, %2, 0 offen sc1" : "+v"(d) : "v"(voff), "s"(rs) : "memory");
}

__global__ __launch_bounds__(512) void pre_kernel(const bf16* __restrict__ x, const bf16* __restrict__ c,
                                                  const bf16* __restrict__ wmod, float* __restrict__ modp,
                                                  float* __restrict__ rstat1, const bf16* __restrict__ Wq) {
  __shared__ __attribute__((aligned(16))) float red[8 * 2 * 128 + 256];
  STAMP(0, 0);
  const int tid = threadIdx.x, lane = tid & 63, w = tid >> 6;
  if (blockIdx.x < 192) {
    const int fr = lane & 15, fq = lane >> 4;
    const int cg = blockIdx.x >> 2, kq = blockIdx.x & 3;
    const int col0 = cg * 128;
    const int k0 = kq * 256 + w * 32 + fq * 8;
    u32x4 raw[8];
#pragma unroll
    for (int j = 0; j < 8; ++j) {
      const u32x4* p_ = reinterpret_cast<const u32x4*>(wmod + (size_t)(k0 + j) * 6144 + col0 + fr * 8);
      raw[j] = __builtin_nontemporal_load(p_);
    }
    const u32x4 cv = *reinterpret_cast<const u32x4*>(c + k0);
    u32x4 cf;
#pragma unroll
    for (int e = 0; e < 4; ++e) {
      const float a = silu_f(bf_lo(cv[e])), b = silu_f(bf_hi(cv[e]));
      cf[e] = (fr == 0) ? pk_bf16(a, b) : 0u;
    }
    f32x4 acc[8];
#pragma unroll
    for (int p = 0; p < 4; ++p) {
      u32x4 lo, hi;
#pragma unroll
      for (int t = 0; t < 4; ++t) {
        lo[t] = __builtin_amdgcn_perm(raw[2 * t + 1][p], raw[2 * t][p], 0x05040100u);
        hi[t] = __builtin_amdgcn_perm(raw[2 * t + 1][p], raw[2 * t][p], 0x07060302u);
      }
      acc[2 * p] = __builtin_amdgcn_mfma_f32_16x16x32_bf16(__builtin_bit_cast(bf16x8, lo), __builtin_bit_cast(bf16x8, cf), f32x4{0, 0, 0, 0}, 0, 0, 0);
      acc[2 * p + 1] = __builtin_amdgcn_mfma_f32_16x16x32_bf16(__builtin_bit_cast(bf16x8, hi), __builtin_bit_cast(bf16x8, cf), f32x4{0, 0, 0, 0}, 0, 0, 0);
    }
    if (fr == 0) {
#pragma unroll
      for (int r = 0; r < 4; ++r) {
        float* d = red + w * 128 + fq * 32 + r * 8;
        *reinterpret_cast<f32x4*>(d) = f32x4{acc[0][r], acc[1][r], acc[2][r], acc[3][r]};
        *reinterpret_cast<f32x4*>(d + 4) = f32x4{acc[4][r], acc[5][r], acc[6][r], acc[7][r]};
      }
    }
    __syncthreads();
    if (tid < 128) {
      float s = 0;
#pragma unroll
      for (int g = 0; g < 8; ++g) s += red[g * 128 + tid];
      modp[kq * 6144 + col0 + tid] = s;
    }
    STAMP(0, 1);
    return;
  }
  const int row0 = (blockIdx.x - 192) * 16 + w * 2;
  u32x4 a[2][2];
#pragma unroll
  for (int i = 0; i < 2; ++i) {
    a[i][0] = *reinterpret_cast<const u32x4*>(x + (size_t)(row0 + i) * D_DIM + lane * 8);
    a[i][1] = *reinterpret_cast<const u32x4*>(x + (size_t)(row0 + i) * D_DIM + 512 + lane * 8);
  }
  if (blockIdx.x < 192 + 8) {
    unsigned long long pc;
    asm volatile("s_getpc_b64 %0" : "=s"(pc));
    const rsrc_t rsC = mk_rsrc((const void*)(uintptr_t)(pc & ~255ull), CODEPF * 1024);
#pragma unroll
    for (int i = 0; i < (CODEPF + 7) / 8; ++i) bload_lds<0>((unsigned)(uintptr_t)(red + 2048), tid * 16, rsC, i * 8192);
  }
#pragma unroll
  for (int i = 0; i < 2; ++i) {
    float v[16];
#pragma unroll
    for (int j = 0; j < 8; ++j) { v[2 * j] = bf_lo(a[i][j >> 2][j & 3]); v[2 * j + 1] = bf_hi(a[i][j >> 2][j & 3]); }
    float s = 0;
#pragma unroll
    for (int j = 0; j < 16; ++j) s += v[j];
#pragma unroll
    for (int o = 32; o > 0; o >>= 1) s += __shfl_xor(s, o);
    const float mean = s * (1.0f / D_DIM);
    float q = 0;
#pragma unroll
    for (int j = 0; j < 16; ++j) { const float d = v[j] - mean; q += d * d; }
#pragma unroll
    for (int o = 32; o > 0; o >>= 1) q += __shfl_xor(q, o);
    const float rstd = rsqrtf(q * (1.0f / D_DIM) + 1e-5f);
    if (lane == 0) *reinterpret_cast<f32x2*>(rstat1 + (row0 + i) * 2) = f32x2{rstd, -mean * rstd};
  }
  STAMP(0, 2);
}

#define QNT 0
#define K1POS 2
DI void k1_touch(char* sink, const bf16* Wq, const bf16* Wo, int part) {
  const int lane = threadIdx.x & 63, w = threadIdx.x >> 6;
  const rsrc_t rsP = mk_rsrc(Wq, D_DIM * 3 * D_DIM * 2);
  const unsigned vo = (unsigned)(((blockIdx.x >> 3) * 32 + (threadIdx.x >> 3)) * 6144 + (blockIdx.x & 3) * 512 + (threadIdx.x & 3) * 128);
  const unsigned v2 = (unsigned)(((blockIdx.x >> 3) * 32 + (threadIdx.x >> 2)) * 6144 + (blockIdx.x & 3) * 512 + (threadIdx.x & 3) * 128);
  if (part & 1) touch_lds((unsigned)(uintptr_t)sink, vo + ((threadIdx.x >> 2) & 1) * 2048, rsP, 0);
  if (part & 2) {
    if (w < 2) touch_lds((unsigned)(uintptr_t)sink, v2, rsP, 4096);
    else touch_lds((unsigned)(uintptr_t)sink, (w == 2 && WOT) ? blockIdx.x * 8192 + lane * 128 : 0x40000000u, mk_rsrc(Wo, D_DIM * D_DIM * 2), 0);
  }
}
__global__ __launch_bounds__(256) void ln_apply_kernel(const bf16* __restrict__ x, const float* __restrict__ modp,
                                                       const float* __restrict__ rstat1, bf16* __restrict__ H,
                                                       const bf16* __restrict__ Wq, unsigned* __restrict__ flags,
                                                       const bf16* __restrict__ Wo) {
  __shared__ __attribute__((aligned(16))) float tab[4][64];
  __shared__ __attribute__((aligned(16))) char sink[1024];
  STAMP(1, 0);
  const int lane = threadIdx.x & 63, w = threadIdx.x >> 6;
  const int fr = lane & 15, fq = lane >> 4;
  const int kb = (blockIdx.x >> 5) * 4 + w, mb0 = (blockIdx.x & 7) + ((blockIdx.x >> 3) & 3) * 32;
  constexpr int MBS = 8;
  u32x4 mr, xv[4];
  u32x2 rr[4];
  asm volatile("" : "=v"(mr));
  {
    bload_v<0>(mr, (unsigned)((fq * 6144 + ((lane >> 3) & 1) * 1024 + kb * 32 + (lane & 7) * 4) * 4), mk_rsrc(modp, 4 * 6144 * 4), 0);
    const rsrc_t rsX = mk_rsrc(x, S_LEN * D_DIM * 2), rsS = mk_rsrc(rstat1, S_LEN * 8);
    const unsigned vx = (unsigned)(((mb0 * 16 + fr) * D_DIM + kb * 32 + fq * 8) * 2), vs = (unsigned)((mb0 * 16 + fr) * 8);
#pragma unroll
    for (int i = 0; i < 4; ++i) {
      asm volatile("" : "=v"(xv[i]), "=v"(rr[i]));
      bload_v<0>(xv[i], vx, rsX, i * 32768 * MBS);
      bload2_v<0>(rr[i], vs, rsS, i * 128 * MBS);
    }
    if (K1POS == 0) k1_touch(sink, Wq, Wo, 3);
    if (K1POS == 2) k1_touch(sink, Wq, Wo, 2);
  }
  asm volatile("s_waitcnt vmcnt(%9)" : "+v"(mr), "+v"(xv[0]), "+v"(xv[1]), "+v"(xv[2]), "+v"(xv[3]), "+v"(rr[0]), "+v"(rr[1]), "+v"(rr[2]), "+v"(rr[3]) : "n"((K1POS == 0 ? 2 : (K1POS == 2 ? 1 : 0)) * (QNT ? 2 : 1)) : "memory");
  f32x4 mv = __builtin_bit_cast(f32x4, mr);
  f32x2 rs[4];
#pragma unroll
  for (int i = 0; i < 4; ++i) rs[i] = __builtin_bit_cast(f32x2, rr[i]);
#pragma unroll
  for (int e = 0; e < 4; ++e) {
    mv[e] = rbf(rowsum4(mv[e]));
    if (lane & 8) mv[e] = rbf(1.0f + mv[e]);
  }
  if (lane < 16) *reinterpret_cast<f32x4*>(&tab[w][lane * 4]) = mv;
  __builtin_amdgcn_wave_barrier();
  asm volatile("s_waitcnt lgkmcnt(0)" ::: "memory");
  const f32x4 s0 = *reinterpret_cast<const f32x4*>(&tab[w][fq * 8]), s1 = *reinterpret_cast<const f32x4*>(&tab[w][fq * 8 + 4]);
  const f32x4 g0 = *reinterpret_cast<const f32x4*>(&tab[w][32 + fq * 8]), g1 = *reinterpret_cast<const f32x4*>(&tab[w][32 + fq * 8 + 4]);
#pragma unroll
  for (int i = 0; i < 4; ++i) {
    u32x4 ho;
#pragma unroll
    for (int d = 0; d < 4; ++d) {
      const f32x4 gg = d < 2 ? g0 : g1, ss = d < 2 ? s0 : s1;
      const float a = (bf_lo(xv[i][d]) * rs[i][0] + rs[i][1]) * gg[(d & 1) * 2] + ss[(d & 1) * 2];
      const float b = (bf_hi(xv[i][d]) * rs[i][0] + rs[i][1]) * gg[(d & 1) * 2 + 1] + ss[(d & 1) * 2 + 1];
      ho[d] = pk_bf16(a, b);
    }
    gst128((char*)H + ((size_t)(mb0 + i * MBS) * 32 + kb) * 1024 + lane * 16, ho);
  }
  if (threadIdx.x < 4) flags[blockIdx.x * 4 + threadIdx.x] = 0u;
  if (threadIdx.x == 4) flags[1024 + blockIdx.x * 32] = 0u;
  STAMP(1, 1);
  if (K1POS == 1) k1_touch(sink, Wq, Wo, 3);
  if (K1POS == 2) k1_touch(sink, Wq, Wo, 1);
}

#define GMAP 0
#define T1 0
#define T2 0
#define GNT 0
#define TW 0
#define TP 8
#define TS 2
#define TN 2
#define GDEP 12
constexpr int Q_SLOT = 21504, Q_NSL = 7;
constexpr int QX_WH = 4 * Q_SLOT;
constexpr int QX_HX = 65536;
constexpr int QX_GT = QX_WH + 65536;
constexpr int QX_CNT = QX_GT + 512;
constexpr int QX_SINK = QX_CNT + 512;
constexpr int Q_LDS = QX_SINK + 1024;

DI unsigned lds_add(unsigned addr, unsigned v0) {
  unsigned o;
  asm volatile("ds_add_rtn_u32 %0, %1, %2\n\ts_waitcnt lgkmcnt(0)" : "=v"(o) : "v"(addr), "v"(v0) : "memory");
  return __builtin_amdgcn_readfirstlane(o);
}
template <int SLP>
DI void lds_wait(unsigned addr, unsigned n) {
  for (;;) {
    unsigned v;
    asm volatile("ds_read_b32 %0, %1\n\ts_waitcnt lgkmcnt(0)" : "=v"(v) : "v"(addr) : "memory");
    if (__builtin_amdgcn_readfirstlane(v) >= n) break;
    __builtin_amdgcn_s_sleep(SLP);
  }
}
DI void wo_piece(unsigned smem_base, unsigned p, unsigned rot, unsigned voff, rsrc_t rs) {
  const unsigned pb = p >> 2, kb = (pb & 24u) | ((pb + rot) & 7u);
  bload_lds<0>(smem_base + (p < 64u ? QX_WH + p * 1024u : (p - 64u) * 1024u), voff, rs, kb * 65536u + (p & 3u) * 16384u);
}

DI void gu_touch(unsigned smem_base, int e, unsigned vgu, const bf16* wg, const bf16* wu, bool nt) {
  if (nt) touch_nt(smem_base + QX_SINK, vgu, mk_rsrc((e & 1) ? wu : wg, D_DIM * F_DIM * 2), (unsigned)(e >> 1) * 256u * (F_DIM * 2));
  else touch_lds(smem_base + QX_SINK, vgu, mk_rsrc((e & 1) ? wu : wg, D_DIM * F_DIM * 2), (unsigned)(e >> 1) * 256u * (F_DIM * 2));
}
DI void qkv_loader(unsigned smem_base, int lane, int sw, int m0, int n0, const bf16* __restrict__ H, const bf16* __restrict__ W,
                   unsigned rot, unsigned voffO, rsrc_t rsO) {
  const rsrc_t rsH = mk_rsrc(H, S_LEN * D_DIM * 2);
  const rsrc_t rsW = mk_rsrc(W, D_DIM * 3 * D_DIM * 2);
  int fb0 = (m0 >> 4) - 1 + 3 * sw; fb0 = fb0 < 0 ? 0 : fb0;
  const unsigned voffH0 = (unsigned)(fb0 * 32768 + lane * 16);
  const unsigned voffH1 = (unsigned)(((m0 >> 4) + 3 * sw) * 32768 + lane * 16);
  unsigned voffW[2];
#pragma unroll
  for (int i = 0; i < 2; ++i) {
    const int r = i * 8 + (lane >> 3), ch = lane & 7;
    const int g = ((r >> 1) & 1) | (((r >> 3) & 1) << 1);
    voffW[i] = (unsigned)(r * 6144 + sw * 2048 + (n0 + (((ch >> 1) ^ g) * 16) + (ch & 1) * 8) * 2);
  }
  const unsigned m0a = smem_base + sw * 3072, m0w = smem_base + 9216 + sw * 4096;
#define Q_LOAD(SOFF, U)                                                                                          \
  do {                                                                                                           \
    const unsigned so_ = (unsigned)(U) * 1024u;                                                                  \
    bload_lds<0>(m0a + (SOFF), voffH0, rsH, so_);                                                                \
    bload_lds<0>(m0a + (SOFF) + 1024, voffH1, rsH, so_);                                                         \
    bload_lds<0>(m0a + (SOFF) + 2048, voffH1, rsH, so_ + 32768u);                                                \
    _Pragma("unroll") for (int i = 0; i < 4; ++i)                                                                \
      bload_lds<0>(m0w + (SOFF) + i * 1024, voffW[i & 1], rsW, (unsigned)(U) * 196608u + (i >> 1) * 98304u);     \
  } while (0)
#pragma unroll
  for (int u = 0; u < Q_NSL - 1; ++u) Q_LOAD(u * Q_SLOT, u);
  wait_vm<(Q_NSL - 2) * 7>();
  __builtin_amdgcn_s_barrier();
  unsigned sb = (Q_NSL - 1) * Q_SLOT;
  for (int u = 0; u < 32 - (Q_NSL - 1); ++u) {
    Q_LOAD(sb, u + Q_NSL - 1);
    sb = (sb == (Q_NSL - 1) * Q_SLOT) ? 0u : sb + Q_SLOT;
    wait_vm<(Q_NSL - 2) * 7>();
    __builtin_amdgcn_s_barrier();
  }
  for (int t = 0; t < 6; ++t) {
    const unsigned p0 = (unsigned)(t * 21 + (t >= 3 ? 1 : 0) + sw * 7);
#pragma unroll
    for (int i = 0; i < 7; ++i) wo_piece(smem_base, p0 + i, rot, voffO, rsO);
    if (t < 5) wait_vm<(Q_NSL - 2) * 7>();
    __builtin_amdgcn_s_barrier();
  }
}

DI f32x4 mfma0(u32x4 a, u32x4 b) {
  return __builtin_amdgcn_mfma_f32_16x16x32_bf16(__builtin_bit_cast(bf16x8, a), __builtin_bit_cast(bf16x8, b), f32x4{0, 0, 0, 0}, 0, 0, 0);
}
DI u32x4 pack8(const f32x4 a, const f32x4 b) { return u32x4{pk_bf16(a[0], a[1]), pk_bf16(a[2], a[3]), pk_bf16(b[0], b[1]), pk_bf16(b[2], b[3])}; }
template <class OP>
DI float rows4(const float (&v)[4], OP op) {
  const auto a = __builtin_amdgcn_permlane16_swap(__float_as_uint(v[0]), __float_as_uint(v[1]), false, false);
  const auto b = __builtin_amdgcn_permlane16_swap(__float_as_uint(v[2]), __float_as_uint(v[3]), false, false);
  const float t0 = op(__uint_as_float(a[0]), __uint_as_float(a[1])), t1 = op(__uint_as_float(b[0]), __uint_as_float(b[1]));
  const auto c = __builtin_amdgcn_permlane32_swap(__float_as_uint(t0), __float_as_uint(t1), false, false);
  return op(__uint_as_float(c[0]), __uint_as_float(c[1]));
}
DI void bcast4(float t, float (&v)[4]) {
  const auto c = __builtin_amdgcn_permlane32_swap(__float_as_uint(t), __float_as_uint(t), false, false);
  const auto a = __builtin_amdgcn_permlane16_swap(c[0], c[0], false, false);
  const auto b = __builtin_amdgcn_permlane16_swap(c[1], c[1], false, false);
  v[0] = __uint_as_float(a[0]); v[1] = __uint_as_float(a[1]); v[2] = __uint_as_float(b[0]); v[3] = __uint_as_float(b[1]);
}
DI float fmax2(float a, float b) { return __builtin_amdgcn_fmed3f(a, b, __builtin_inff()); }

template <int WM>
DI void qkv_mfma(unsigned smem_base, int lane, int wn, int bm, int bn, bf16* __restrict__ O) {
  const int fr = lane & 15, fq = lane >> 4;
  unsigned bb0, bb1;
  const unsigned ab = smem_base + lane * 16;
  {
    const int q = (lane >> 2) & 3, p = lane & 3;
    const int r = 8 * fq + q;
    const int g = ((r >> 1) & 1) | (((r >> 3) & 1) << 1);
    bb0 = smem_base + 9216 + r * 128 + (((wn * 2) ^ g) * 32) + 8 * p;
    bb1 = bb0 ^ 32u;
  }
  const bool par = fr & 1, c0 = 4 * fq + (fr & 1) <= fr, c1 = 4 * fq + 2 + (fr & 1) <= fr;
  const unsigned s0 = par ? 0x01000c0cu : 0x0c0c0100u, s1 = par ? 0x03020c0cu : 0x0c0c0302u;
  u32x4 sel = {c0 ? s0 : 0x0c0c0c0cu, c1 ? s1 : 0x0c0c0c0cu, c0 ? 0x0c0c0c0cu : s0, c1 ? 0x0c0c0c0cu : s1};
  unsigned hx = ab + QX_HX + wn * 3072;
  asm volatile("" : "+v"(sel), "+v"(hx));
  const rsrc_t rsO = mk_rsrc((char*)O + (size_t)((bm * 2 + WM) * 32 + bn * 2 + wn) * 4096, 4096);
  f32x4 acc[4][6], acch[2];
#pragma unroll
  for (int i = 0; i < 4; ++i)
#pragma unroll
    for (int j = 0; j < 6; ++j) { acc[i][j] = f32x4{0, 0, 0, 0}; asm volatile("" : "+v"(acc[i][j])); }
#pragma unroll
  for (int j = 0; j < 2; ++j) { acch[j] = f32x4{0, 0, 0, 0}; asm volatile("" : "+v"(acch[j])); }
  __builtin_amdgcn_s_setprio(2);
  __builtin_amdgcn_s_barrier();
  STAMPDECL;
  STAMPNW(2, 1);
  u32x4 aX[5], aY[5];
  u32x2 bXl[6], bXh[6], bYl[6], bYh[6];
  unsigned ra = ab, rb0 = bb0, rb1 = bb1, so = 0;
#define Q_READ(AR, BL, BH, R)                                                                                    \
  do {                                                                                                           \
    if ((R) < 12) {                                                                                              \
      constexpr int jb = ((R) < 12 ? (R) : 0) >> 1;                                                              \
      if ((R) & 1) dsr_tr<(jb >> 1) * 4096 + 512>(BH[jb], (jb & 1) ? rb1 : rb0);                                 \
      else dsr_tr<(jb >> 1) * 4096>(BL[jb], (jb & 1) ? rb1 : rb0);                                               \
    } else if ((R) < 16) {                                                                                       \
      constexpr int ia = ((R) >= 12 && (R) < 16) ? (R) - 12 : 0;                                                 \
      dsr128<1024 + WM * 4096 + ia * 1024>(AR[ia], ra);                                                          \
    } else {                                                                                                     \
      dsr128<0>(AR[4], ra);                                                                                      \
    }                                                                                                            \
  } while (0)
#define Q_MFMA(AR, BB, N)                                                                                        \
  do {                                                                                                           \
    if ((N) < 16) MFMA_AV(acc[(N) & 3][((N) < 24 ? (N) : 0) >> 2], BB[((N) < 24 ? (N) : 0) >> 2], AR[(N) & 3]);  \
    else if ((N) < 24) MFMA_AV(acc[(N) & 3][((N) < 24 ? (N) : 0) >> 2], AR[(N) & 3], BB[((N) < 24 ? (N) : 0) >> 2]); \
    else if (WM == 0) MFMA_AV(acch[(N) & 1], BB[2 + ((N) & 1)], AR[4]);                                          \
    else MFMA_AV(acch[(N) & 1], AR[4], BB[4 + ((N) & 1)]);                                                       \
  } while (0)
#define Q_ADV(K)                                                                                                 \
  do {                                                                                                           \
    if ((K) == 0) asm volatile("s_add_u32 %0, %0, %1\n\ts_cmp_eq_u32 %0, %2\n\ts_cselect_b32 %0, 0, %0" : "+s"(so) : "n"(Q_SLOT), "n"(Q_NSL * Q_SLOT) : "scc"); \
    else if ((K) == 1) asm volatile("v_add_u32 %0, %1, %2" : "=v"(rb0) : "s"(so), "v"(bb0));                     \
    else if ((K) == 2) asm volatile("v_add_u32 %0, %1, %2" : "=v"(rb1) : "s"(so), "v"(bb1));                     \
    else asm volatile("v_add_u32 %0, %1, %2" : "=v"(ra) : "s"(so), "v"(ab));                                     \
  } while (0)
#define Q_UNIT(AR, BL, BH, NAR, NBL, NBH)                                                                        \
  do {                                                                                                           \
    WAIT_LGKM0();                                                                                                \
    u32x4 bb[6];                                                                                                 \
    _Pragma("unroll") for (int j = 0; j < 6; ++j) bb[j] = u32x4{BL[j][0], BL[j][1], BH[j][0], BH[j][1]};         \
    SCHED0();                                                                                                    \
    static_for<26>([&](auto N) {                                                                                 \
      constexpr int n = N.value;                                                                                 \
      if constexpr (n == QBAR) __builtin_amdgcn_s_barrier();                                                     \
      Q_MFMA(AR, bb, n);                                                                                         \
      if constexpr (n >= QBAR && n < QBAR + 17) Q_READ(NAR, NBL, NBH, n - QBAR);                                 \
      if constexpr (n >= QBAR + 18 && n < QBAR + 22) Q_ADV(n - QBAR - 18);                                       \
    });                                                                                                          \
    SCHED0();                                                                                                    \
  } while (0)
  static_for<17>([&](auto R) { Q_READ(aX, bXl, bXh, R.value); });
  static_for<4>([&](auto K) { Q_ADV(K.value); });
  for (int u = 0; u < 32; u += 2) {
    Q_UNIT(aX, bXl, bXh, aY, bYl, bYh);
    Q_UNIT(aY, bYl, bYh, aX, bXl, bXh);
  }
  asm volatile("s_waitcnt lgkmcnt(0)\n\ts_nop 7" ::: "memory");
#pragma unroll
  for (int i = 0; i < 5; ++i) asm volatile("" : "+v"(aX[i]));
#pragma unroll
  for (int j = 0; j < 6; ++j) asm volatile("" : "+v"(bXl[j]), "+v"(bXh[j]));
#pragma unroll
  for (int i = 0; i < 4; ++i)
#pragma unroll
    for (int j = 0; j < 6; ++j) asm volatile("" : "+v"(acc[i][j]));
  asm volatile("" : "+v"(acch[0]), "+v"(acch[1]));
  SCHED0();
  STAMP(2, 2); STAMPFLUSH(2, 1);
  u32x4 kk[5], vv[5];
  if (WM == 0) {
    kk[4] = pack8(acc[3][2], acc[3][3]); vv[4] = pack8(acc[3][4], acc[3][5]);
    dsw128<0>(hx, kk[4]); dsw128<1024>(hx, vv[4]);
  } else {
    dsw128<2048>(hx, pack8(acch[0], acch[1]));
  }
  asm volatile("s_waitcnt lgkmcnt(0)" ::: "memory");
  __builtin_amdgcn_s_barrier();
  if (WM == 0) dsr128<2048>(vv[0], hx);
  else { dsr128<0>(kk[0], hx); dsr128<1024>(vv[0], hx); }
  SCHED0();
  if (WM == 0) kk[0] = pack8(acch[0], acch[1]);
#pragma unroll
  for (int i = 0; i < (WM == 0 ? 3 : 4); ++i) kk[i + 1] = pack8(acc[i][2], acc[i][3]);
  const float cs = 0.17677669529663687f * 1.4426950408889634f;
  float l0[4], l1[4], tm[4];
#pragma unroll
  for (int ii = 1; ii < 5; ++ii) {
    const int i = ii & 3;
    const u32x4 q = pack8(acc[i][0], acc[i][1]);
    if (WM == 1 && i == 0) asm volatile("s_waitcnt lgkmcnt(0)" : "+v"(kk[0]), "+v"(vv[0])::"memory");
    const f32x4 sc = mfma0(kk[i + 1], q), sp = mfma0(kk[i], q);
    float p0 = par ? sp[1] : sp[0], p1 = par ? sp[3] : sp[2];
    if (WM == 0 && i == 0 && bm == 0) { p0 = -5.656854249e9f; p1 = -5.656854249e9f; }
    l0[i] = c0 ? (par ? sc[1] : sc[0]) : p0;
    l1[i] = c1 ? (par ? sc[3] : sc[2]) : p1;
    tm[i] = fmax2(l0[i], l1[i]);
  }
#pragma unroll
  for (int i = 0; i < (WM == 0 ? 3 : 4); ++i) vv[i + 1] = pack8(acc[i][4], acc[i][5]);
  bcast4(rows4(tm, [](float a, float b) { return fmax2(a, b); }), tm);
#pragma unroll
  for (int i = 0; i < 4; ++i) {
    l0[i] = __builtin_amdgcn_exp2f((l0[i] - tm[i]) * cs);
    l1[i] = __builtin_amdgcn_exp2f((l1[i] - tm[i]) * cs);
    tm[i] = l0[i] + l1[i];
  }
  bcast4(__builtin_amdgcn_rcpf(rows4(tm, [](float a, float b) { return a + b; })), tm);
  if (WM == 0) asm volatile("s_waitcnt lgkmcnt(0)" : "+v"(vv[0])::"memory");
#pragma unroll
  for (int i = 0; i < 4; ++i) {
    const unsigned pp = pk_bf16(l0[i] * tm[i], l1[i] * tm[i]);
    const u32x4 pr = {__builtin_amdgcn_perm(pp, pp, sel[0]), __builtin_amdgcn_perm(pp, pp, sel[1]),
                      __builtin_amdgcn_perm(pp, pp, sel[2]), __builtin_amdgcn_perm(pp, pp, sel[3])};
    const u32x4 ov = pack8(mfma0(u32x4{vv[i + 1][0], vv[i + 1][1], vv[i][0], vv[i][1]}, pr), mfma0(u32x4{vv[i + 1][2], vv[i + 1][3], vv[i][2], vv[i][3]}, pr));
    asm volatile("buffer_store_dwordx4 %0, %1, %2, 0 offen offset:%3 sc1\n\ts_nop 1" ::"v"(ov), "v"(lane * 16), "s"(rsO), "i"(i * 1024) : "memory");
  }
  STAMP(2, 3);
}

__global__ __launch_bounds__(512, 2) void qkv_attn_kernel(const bf16* __restrict__ H, const bf16* __restrict__ W,
                                                          bf16* O, const bf16* __restrict__ wo, const bf16* __restrict__ X,
                                                          const float* __restrict__ modp, bf16* __restrict__ Y,
                                                          float* __restrict__ stat, unsigned* flags,
                                                          const bf16* __restrict__ wg, const bf16* __restrict__ wu) {
  __shared__ __attribute__((aligned(1024))) char smem[Q_LDS];
  STAMP(2, 0);
  const int tid = threadIdx.x, lane = tid & 63;
  const int wid = __builtin_amdgcn_readfirstlane(tid >> 6);
  const unsigned smem_base = (unsigned)(uintptr_t)smem;
  const int xcd = blockIdx.x & 7, idx = blockIdx.x >> 3;
  const int bm = (xcd >> 2) * 8 + (idx & 7), bn = (xcd & 3) * 4 + (idx >> 3);
  const int rb = xcd * 2 + (idx & 1), cg = idx >> 1;
  const unsigned rot = cg & 7;
  const unsigned vgu = (unsigned)((idx * 8 + (lane >> 3)) * (F_DIM * 2) + xcd * 896 + ((lane & 7) == 7 ? 6 : (lane & 7)) * 128);
  const unsigned cnt = smem_base + QX_CNT, cntl = cnt + lane * 4;
  if (wid < 4) {
    if (wid & 1) qkv_mfma<1>(smem_base, lane, wid >> 1, bm, bn, O);
    else qkv_mfma<0>(smem_base, lane, wid >> 1, bm, bn, O);
  } else {
    const rsrc_t rsO = mk_rsrc(wo, D_DIM * D_DIM * 2);
    const unsigned voffO = (unsigned)((lane >> 3) * 2048 + cg * 128 + ((((lane & 7) >> 1) ^ (lane >> 4)) * 32) + (lane & 1) * 16);
    if (wid < 7) qkv_loader(smem_base, lane, wid - 4, bm * 128, bn * 64, H, W, rot, voffO, rsO);
    else {
      unsigned gv[8];
      const rsrc_t rsG = mk_rsrc(modp, 4 * 6144 * 4);
#pragma unroll
      for (int i = 0; i < 8; ++i) { asm volatile("" : "=v"(gv[i])); bload1_v<0>(gv[i], (unsigned)((cg * 64 + lane) * 4), rsG, (i >> 1) * 24576 + 8192 + (i & 1) * 8192); }
      dsw32<0>(cntl, 0u); dsw32<256>(cntl, 0u);
      asm volatile("s_waitcnt lgkmcnt(0)" ::: "memory");
      for (int i = 0; i < 3; ++i) __builtin_amdgcn_s_barrier();
      asm volatile("s_waitcnt vmcnt(0)" : "+v"(gv[0]), "+v"(gv[1]), "+v"(gv[2]), "+v"(gv[3]), "+v"(gv[4]), "+v"(gv[5]), "+v"(gv[6]), "+v"(gv[7])::"memory");
      const float ga = (__uint_as_float(gv[0]) + __uint_as_float(gv[2])) + (__uint_as_float(gv[4]) + __uint_as_float(gv[6]));
      const float cb = (__uint_as_float(gv[1]) + __uint_as_float(gv[3])) + (__uint_as_float(gv[5]) + __uint_as_float(gv[7]));
      dsw32<0>(smem_base + QX_GT + lane * 4, __float_as_uint(rbf(ga)));
      dsw32<256>(smem_base + QX_GT + lane * 4, __float_as_uint(gain_nz(cb)));
      asm volatile("s_waitcnt lgkmcnt(0)" ::: "memory");
      {
        int nxt = TS + idx % TP, ev = 0;
        for (int i = 2; i < 27; ++i) {
          if (TW > 0 && i == nxt && ev < TN) {
#pragma unroll
            for (int j = 0; j < TW; ++j) gu_touch(smem_base, 7 - (ev * TW + j), vgu, wg, wu, GNT & 1);
            nxt += TP; ++ev;
          }
          __builtin_amdgcn_s_barrier();
        }
      }
      wo_piece(smem_base, 63u, rot, voffO, rsO);
      for (int i = 28; i < 33; ++i) __builtin_amdgcn_s_barrier();
      wo_piece(smem_base, 127u, rot, voffO, rsO);
    }
    __builtin_amdgcn_s_barrier();
    if (wid - 4 < T1) { gu_touch(smem_base, T2 + wid - 4, vgu, wg, wu, GNT & 2); wait_vm<(GNT & 2) ? 2 : 1>(); }
    else wait_vm<0>();
    lds_add(cntl, 1u);
  }
  const int fr = lane & 15, fq = lane >> 4;
  const int kq = wid & 3, rg = rb * 2 + (wid >> 2);
  lds_wait<2>(cnt, 4);
  u32x2 wl[8][4], wh[8][4];
  {
    const int q = (lane >> 2) & 3, p = lane & 3, r = 4 * fq + q;
    const unsigned wb = smem_base + (kq < 2 ? QX_WH + kq * 32768 : (kq - 2) * 32768) + r * 128 + (((r >> 1) & 3) * 32) + 8 * p;
    unsigned wa[4];
#pragma unroll
    for (int f = 0; f < 4; ++f) wa[f] = wb ^ (f * 32);
    static_for<32>([&](auto E) {
      constexpr int s = E.value >> 2, f = E.value & 3;
      dsr_tr<s * 4096>(wl[s][f], wa[f]);
      dsr_tr<s * 4096 + 2048>(wh[s][f], wa[f]);
    });
  }
  const rsrc_t rsA = mk_rsrc((const char*)O + (size_t)rg * 131072, 131072);
  const rsrc_t rsF = mk_rsrc(flags + (rb * 16 + kq * 4) * 32, 4 * 128);
  unsigned voffA[4], hs[8];
#pragma unroll
  for (int i = 0; i < 4; ++i) { voffA[i] = lane * 16 + ((kq + 1 + i) & 3) * 1024; asm volatile("" : "+v"(voffA[i])); }
#pragma unroll
  for (int i = 0; i < 8; ++i) { hs[i] = (kq * 8 + ((i + rot) & 7)) * 4096; asm volatile("" : "+s"(hs[i])); }
  asm volatile("" ::"s"(rsA), "s"(rsF));
  if (wid < 4) {
    const rsrc_t rsFo = mk_rsrc(flags + (bm * 16 + bn) * 32, 4);
    asm volatile("s_waitcnt vmcnt(0)" ::"s"(rsFo) : "memory");
    if ((lds_add(cntl + 256, 1u) & 15u) == 3u)
      asm volatile("buffer_store_dword %0, %1, %2, 0 offen sc1" ::"v"(1u), "v"(lane * 4), "s"(rsFo) : "memory");
    STAMP(2, 4);
  } else {
    lds_add(cntl + 256, 16u);
  }
  lds_wait<2>(cnt + 256, 68);
  u32x4 wf[8][4];
#pragma unroll
  for (int s = 0; s < 8; ++s)
#pragma unroll
    for (int f = 0; f < 4; ++f) {
      asm volatile("" : "+v"(wl[s][f]), "+v"(wh[s][f]));
      wf[s][f] = u32x4{wl[s][f][0], wl[s][f][1], wh[s][f][0], wh[s][f][1]};
    }
  {
    unsigned spins = 0;
    for (;;) {
      unsigned fl;
      asm volatile("buffer_load_dword %0, %1, %2, 0 offen sc1\n\ts_waitcnt vmcnt(0)" : "=v"(fl) : "v"((lane & 3) * 128), "s"(rsF) : "memory");
      if (__builtin_amdgcn_ballot_w64(fl == 0u) == 0ull || ++spins >= (1u << 20)) break;
      __builtin_amdgcn_s_sleep(1);
    }
  }
  STAMP(3, 0);
  const unsigned voffL = lane * 16;
  u32x4 af[32], xv[2], pr[3][4], gt[4], ct[4];
  f32x4 acc[4][4];
  auto aload = [&](auto N) {
    constexpr int n = N.value;
    asm volatile("" : "=v"(af[n]));
    bload_sc1<0>(af[n], voffA[n >> 3], rsA, hs[n & 7]);
  };
  static_for<GDEP>([&](auto N) { aload(N); });
  const unsigned sl = smem_base + voffL + (wid >> 2) * 49152;
  const int sw = ((fq & 1) << 1) | (fq >> 1);
  SCHED0();
  static_for<32>([&](auto N) {
    constexpr int n = N.value, ps = n >> 3, s = n & 7, XG = 32 - GDEP;
    constexpr int last = n - 1 + GDEP < 31 ? n - 1 + GDEP : 31;
    asm volatile("s_waitcnt vmcnt(%1)" : "+v"(af[n]) : "n"(last - n + (n > XG ? 2 : 0)) : "memory");
    if constexpr (n == 28) { WAIT_LGKM0(); __builtin_amdgcn_s_barrier(); }
    static_for<4>([&](auto F) {
      constexpr int f = F.value;
      if constexpr (s == 0) asm volatile("v_mfma_f32_16x16x32_bf16 %0, %1, %2, 0" : "=&v"(acc[ps][f]) : "v"(wf[s][f]), "v"(af[n]));
      else MFMA_AV(acc[ps][f], wf[s][f], af[n]);
      if constexpr (f == 0 && n + GDEP < 32) aload(ic<(n + GDEP < 32 ? n + GDEP : 0)>{});
      if constexpr (f == 0 && n == XG) {
        const rsrc_t rsX = mk_rsrc(X, S_LEN * D_DIM * 2);
        const unsigned vx = (unsigned)(((rg * 64 + kq * 16 + fr) * D_DIM + cg * 64 + sw * 8) * 2);
        asm volatile("" : "=v"(xv[0]), "=v"(xv[1]));
        bload_v<0>(xv[0], vx, rsX, 0); bload_v<64>(xv[1], vx, rsX, 0);
      }
      if constexpr (ps > 0 && (s == 2 || s == 3) && (f & 1)) {
        constexpr int e = (s - 2) * 2 + (f >> 1);
        dsw128<(ps - 1) * 4096 + e * 1024>(sl + ((kq + ps) & 3) * 12288, __builtin_bit_cast(u32x4, acc[ps - 1][e]));
      }
      if constexpr (n >= 28 && n < 31) dsr128<(n - 28) * 4096 + f * 1024>(pr[n - 28][f], sl + kq * 12288);
      if constexpr (n == 31) { dsr128<f * 64>(gt[f], smem_base + QX_GT + fq * 16); dsr128<256 + f * 64>(ct[f], smem_base + QX_GT + fq * 16); }
    });
  });
  asm volatile("s_waitcnt vmcnt(0)" : "+v"(xv[0]), "+v"(xv[1])::"memory");
  asm volatile("s_waitcnt lgkmcnt(0)\n\ts_nop 4" ::: "memory");
  STAMP(3, 2);
  f32x4 fin[4], yv[4];
#pragma unroll
  for (int f = 0; f < 4; ++f) asm volatile("" : "+v"(acc[3][f]), "+v"(pr[0][f]), "+v"(pr[1][f]), "+v"(pr[2][f]), "+v"(gt[f]), "+v"(ct[f]));
#pragma unroll
  for (int c = 0; c < 2; ++c) {
    const auto x0 = __builtin_amdgcn_permlane16_swap(xv[c][0], xv[c][2], false, false);
    const auto x1 = __builtin_amdgcn_permlane16_swap(xv[c][1], xv[c][3], false, false);
#pragma unroll
    for (int h = 0; h < 2; ++h) {
      const int f = 2 * c + h;
      const f32x4 a = (acc[3][f] + __builtin_bit_cast(f32x4, pr[0][f])) + (__builtin_bit_cast(f32x4, pr[1][f]) + __builtin_bit_cast(f32x4, pr[2][f]));
      const f32x4 xf = {bf_lo(x0[h]), bf_hi(x0[h]), bf_lo(x1[h]), bf_hi(x1[h])};
      fin[f] = xf + __builtin_bit_cast(f32x4, gt[f]) * a;
      yv[f] = fin[f] * __builtin_bit_cast(f32x4, ct[f]);
    }
    const auto y0 = __builtin_amdgcn_permlane16_swap(pk_bf16(yv[2 * c][0], yv[2 * c][1]), pk_bf16(yv[2 * c + 1][0], yv[2 * c + 1][1]), false, false);
    const auto y1 = __builtin_amdgcn_permlane16_swap(pk_bf16(yv[2 * c][2], yv[2 * c][3]), pk_bf16(yv[2 * c + 1][2], yv[2 * c + 1][3]), false, false);
    gst128((char*)Y + ((size_t)(rg >> 2) * 32 + cg * 2 + c) * 16384 + ((rg & 3) * 4 + kq) * 1024 + (sw * 16 + fr) * 16,
           u32x4{y0[0], y1[0], y0[1], y1[1]});
  }
  const f32x4 s4 = (fin[0] + fin[1]) + (fin[2] + fin[3]);
  const float s = rowsum4((s4[0] + s4[1]) + (s4[2] + s4[3]));
  const float mb = s * (1.0f / 64.0f);
  f32x4 q4 = {0, 0, 0, 0};
#pragma unroll
  for (int f = 0; f < 4; ++f) { const f32x4 d = fin[f] - mb; q4 += d * d; }
  const float m2 = rowsum4((q4[0] + q4[1]) + (q4[2] + q4[3]));
  if (fq == 0) *reinterpret_cast<f32x2*>(stat + (((size_t)(cg >> 1) * S_LEN + rg * 64 + kq * 16 + fr) * 2 + (cg & 1)) * 2) = f32x2{s, m2};
  STAMP(3, 3);
  STAMP(3, 4);
  if (wid < T2) gu_touch(smem_base, wid, vgu, wg, wu, GNT & 4);
}

#define G3NT 1
#define NBR 0
#define TPOS 6
#define NBC 1
#define G3_TW "12"
#define G3_DUMMY() do { } while (0)
constexpr int G3_CT = 131072 + 4096 + 1024;
#define G3_STAGE(SLOT, P, U)                                                                               \
  do {                                                                                                     \
    if ((P) == 0) bload_lds<0>(m0w + (SLOT) * 32768, voffA, rsA, (unsigned)(U) * 16384u);                  \
    else if ((P) == 1) bload_lds<1024>(m0w + (SLOT) * 32768, voffA, rsA, (unsigned)(U) * 16384u);          \
    else if (G3NT && (P) == 2) bload_lds_nt<0>(m0w + (SLOT) * 32768 + 16384, voffB0, rsB, (unsigned)(U) * 229376u);   \
    else if (G3NT) bload_lds_nt<0>(m0w + (SLOT) * 32768 + 16384 + 1024, voffB1, rsB, (unsigned)(U) * 229376u);          \
    else if ((P) == 2) bload_lds<0>(m0w + (SLOT) * 32768 + 16384, voffB0, rsB, (unsigned)(U) * 229376u);   \
    else bload_lds<0>(m0w + (SLOT) * 32768 + 16384 + 1024, voffB1, rsB, (unsigned)(U) * 229376u);          \
  } while (0)

#define G3ROT 1
DI int g3_line(int gx, int p) {
  const int t = p + (G3ROT ? (3 + gx) & 7 : 0);
  return 7 * gx + (t >= 7 ? t - 7 : t);
}
template <int FN>
DI void g3_body(char* smem, unsigned smem_base, int wm, int wn, int wid, int lane, int bm, int bn, rsrc_t rsA, rsrc_t rsB,
                rsrc_t rsP, unsigned voffA, unsigned voffB0, unsigned voffB1, bf16* __restrict__ act) {
  constexpr int NB = 2 * FN, NM = 4 * NB, RD = 5 + 2 * NB;
  const int fr = lane & 15, fq = lane >> 4;
  f32x4 acc[4][NB], accC[2];
  accC[0] = f32x4{0, 0, 0, 0}; accC[1] = f32x4{0, 0, 0, 0};
  asm volatile("" : "+v"(accC[0]), "+v"(accC[1]));
  const int rot = wm % FN;
  unsigned cad = smem_base + G3_CT + (fr < 4 ? fr * 64 : 256) + fq * 16;
#pragma unroll
  for (int i = 0; i < 4; ++i)
#pragma unroll
    for (int j = 0; j < NB; ++j) { acc[i][j] = f32x4{0, 0, 0, 0}; asm volatile("" : "+v"(acc[i][j])); }
  unsigned aad[2], bad[2][FN];
  aad[0] = smem_base + wm * 4096 + lane * 16; aad[1] = aad[0] + 65536;
  {
    const int q = (lane >> 2) & 3, p = lane & 3;
    const int r = 8 * fq + q;
    const int sw = (r & 3) | (((r >> 3) & 1) << 2);
#pragma unroll
    for (int j = 0; j < FN; ++j) {
      const int gi = wn * 4 + (j + rot) % FN + (bn & 1);
      bad[0][j] = smem_base + 16384 + r * 256 + ((gi ^ sw) * 32) + 8 * p;
      bad[1][j] = bad[0][j] + 65536;
    }
  }
  u32x4 aX[5], aY[5];
  u32x2 bXl[NB], bXh[NB], bYl[NB], bYh[NB];
  const unsigned m0w = smem_base + wid * 2048;
  const unsigned voffP = lane * 16;
#define G3_READ(AR, BL, BH, SLOT, R)                                                                       \
  do {                                                                                                     \
    if ((R) < 2 * NB) {                                                                                    \
      constexpr int jb = ((R) < 2 * NB ? (R) : 0) >> 1;                                                    \
      constexpr int off = ((SLOT) & 1) * 32768 + (jb / FN) * 8192 + ((R) & 1) * 1024;                      \
      if ((R) & 1) dsr_tr<off>(BH[jb], bad[(SLOT) >> 1][jb % FN]);                                         \
      else dsr_tr<off>(BL[jb], bad[(SLOT) >> 1][jb % FN]);                                                 \
    } else if ((R) < 2 * NB + 4) {                                                                         \
      constexpr int ia = ((R) >= 2 * NB && (R) < 2 * NB + 4) ? (R) - 2 * NB : 0;                           \
      dsr128<((SLOT) & 1) * 32768 + ia * 1024>(AR[ia], aad[(SLOT) >> 1]);                                  \
    } else {                                                                                               \
      if ((SLOT) == 0) cad += 1280;                                      \
      dsr128<(SLOT) * 320>(AR[4], cad);                                                                    \
    }                                                                                                      \
  } while (0)
  auto rd = [&](auto SL, auto RR, u32x4 (&AR)[5], u32x2 (&BL)[NB], u32x2 (&BH)[NB]) { G3_READ(AR, BL, BH, SL.value, RR.value); };
#define G3_UNIT(AR, BL, BH, NAR, NBL, NBH, SLOT, U)                                                        \
  do {                                                                                                     \
    WAIT_LGKM0();                                                                                          \
    u32x4 bb[NB];                                                                                          \
    _Pragma("unroll") for (int j = 0; j < NB; ++j) bb[j] = u32x4{BL[j][0], BL[j][1], BH[j][0], BH[j][1]};  \
    SCHED0();                                                                                              \
    asm volatile("s_nop 1");                                                                               \
    static_for<NM>([&](auto N) {                                                                           \
      constexpr int n = N.value;                                                                           \
      if constexpr (n == NM / 2) {                                                                         \
        wait_vm<NBR ? 10 : 8>(); __builtin_amdgcn_s_barrier();                                             \
      }                                                                                                    \
      if constexpr (n == NM / 2 + TPOS) {                                                                  \
                      \
        if (NBR) bload_lds<0>(smem_base + 131072 + 4096, voffP, rsP, (((U) & 7) == wid) ? (unsigned)(U) * 1024u : 0x40000000u); \
        else if (((U) & 7) == wid) bload_lds<0>(smem_base + 131072 + 4096, voffP, rsP, (unsigned)(U) * 1024u);  \
      }                                                                                                    \
      MFMA_AV(acc[n & 3][n >> 2], bb[n >> 2], AR[n & 3]);                                                  \
      if constexpr (n < NM / 2) {                                                                          \
        if constexpr (n % 3 == 1 && n / 3 < 4) G3_STAGE(((SLOT) + 3) & 3, n / 3, (U) + 3);                 \
      } else {                                                                                             \
        constexpr int r0 = (n - NM / 2) * 2;                                                               \
        if constexpr (r0 < RD) rd(ic<((SLOT) + 1) & 3>{}, ic<(r0 < RD ? r0 : 0)>{}, NAR, NBL, NBH);        \
        if constexpr (r0 + 1 < RD) rd(ic<((SLOT) + 1) & 3>{}, ic<(r0 + 1 < RD ? r0 + 1 : 0)>{}, NAR, NBL, NBH); \
      }                                                                                                    \
    });                                                                                                    \
    if (NBC || FN == 4 || wm < 3) {                                                                        \
      MFMA_AV(accC[0], bb[0], AR[4]);                                                                      \
      MFMA_AV(accC[1], bb[FN], AR[4]);                                                                     \
    }                                                                                                      \
    SCHED0();                                                                                              \
  } while (0)

  if (NBR) asm volatile("s_waitcnt vmcnt(10) lgkmcnt(0)" ::: "memory");
  else asm volatile("s_waitcnt vmcnt(8) lgkmcnt(0)" ::: "memory");
  __builtin_amdgcn_s_barrier();
  STAMPDECL;
  STAMPNW(5, 1);
  cad -= 1280;
  static_for<RD>([&](auto RR) { rd(ic<0>{}, RR, aX, bXl, bXh); });
  for (int u = 0; u < 32; u += 4) {
    G3_UNIT(aX, bXl, bXh, aY, bYl, bYh, 0, u);
    G3_UNIT(aY, bYl, bYh, aX, bXl, bXh, 1, u + 1);
    G3_UNIT(aX, bXl, bXh, aY, bYl, bYh, 2, u + 2);
    G3_UNIT(aY, bYl, bYh, aX, bXl, bXh, 3, u + 3);
  }
  asm volatile("s_waitcnt lgkmcnt(0)\n\ts_nop 15" ::: "memory");
#pragma unroll
  for (int i = 0; i < 5; ++i) asm volatile("" : "+v"(aX[i]));
#pragma unroll
  for (int j = 0; j < NB; ++j) asm volatile("" : "+v"(bXl[j]), "+v"(bXh[j]));
  SCHED0();
  STAMP(5, 2);
  STAMPFLUSH(5, 1);
  const f32x2* rstat = reinterpret_cast<const f32x2*>(smem + 131072);
  float* colc = reinterpret_cast<float*>(smem + 131072 + 2048);
#pragma unroll
  for (int m = 0; m < 2; ++m) {
    f32x4 v;
#pragma unroll
    for (int r = 0; r < 4; ++r) v[r] = accC[m][r] + __shfl_xor(accC[m][r], 1);
    if ((fr == 0 || fr == 2) && wm < FN) *reinterpret_cast<f32x4*>(colc + (m * 2 + (fr >> 1)) * 128 + (wn * 4 + rot) * 16 + fq * 4) = v;
  }
  asm volatile("s_waitcnt lgkmcnt(0)" ::: "memory");
  __builtin_amdgcn_s_barrier();
  asm volatile("" ::: "memory");
  f32x2 rs[4];
#pragma unroll
  for (int i = 0; i < 4; ++i) rs[i] = rstat[wm * 64 + i * 16 + fr];
#pragma unroll
  for (int j = 0; j < FN; ++j) {
    const int jg = (j + rot) % FN;
    const int cl = (wn * 4 + jg) * 16 + fq * 4;
    const f32x4 csg = *reinterpret_cast<const f32x4*>(colc + cl), bg = *reinterpret_cast<const f32x4*>(colc + 128 + cl);
    const f32x4 csu = *reinterpret_cast<const f32x4*>(colc + 256 + cl), bu = *reinterpret_cast<const f32x4*>(colc + 384 + cl);
    u32x2 pk[4];
#pragma unroll
    for (int i = 0; i < 4; ++i) {
#pragma unroll
      for (int hh = 0; hh < 2; ++hh) {
        const f32x2 ag = hh ? hi2(acc[i][j]) : lo2(acc[i][j]), au = hh ? hi2(acc[i][FN + j]) : lo2(acc[i][FN + j]);
        const f32x2 g = rs[i][0] * ag + (rs[i][1] * (hh ? hi2(csg) : lo2(csg)) + (hh ? hi2(bg) : lo2(bg)));
        const f32x2 uu = rs[i][0] * au + (rs[i][1] * (hh ? hi2(csu) : lo2(csu)) + (hh ? hi2(bu) : lo2(bu)));
        const f32x2 ea = g * -1.4426950408889634f;
        f32x2 e = {__builtin_amdgcn_exp2f(ea[0]), __builtin_amdgcn_exp2f(ea[1])};
        e = e + 1.0f;
        const f32x2 rc = {__builtin_amdgcn_rcpf(e[0]), __builtin_amdgcn_rcpf(e[1])};
        const f32x2 o = (g * uu) * rc;
        pk[i][hh] = pk_bf16(o[0], o[1]);
      }
    }
    const int Jv = (bn & 3) * 7 + wn * 4 + jg;
    const int J = g3_line(bn >> 2, Jv >> 2) * 4 + (Jv & 3);
    char* dst = (char*)act + ((size_t)((bm * 4 + wm) * (F_DIM / 32) + (J >> 1)) * 4 + (fq & 1)) * 1024 + (((J & 1) * 2 + (fq >> 1)) * 16 + fr) * 16;
#pragma unroll
    for (int i = 0; i < 4; i += 2) {
      const auto s0 = __builtin_amdgcn_permlane16_swap(pk[i][0], pk[i + 1][0], false, false);
      const auto s1 = __builtin_amdgcn_permlane16_swap(pk[i][1], pk[i + 1][1], false, false);
      gst128(dst + i * 1024, u32x4{s0[0], s1[0], s0[1], s1[1]});
    }
  }
}

__global__ __launch_bounds__(512, 2) void gemm3_kernel(const bf16* __restrict__ Y, const bf16* __restrict__ Wg,
                                                       const bf16* __restrict__ Wu, bf16* __restrict__ act,
                                                       const float* __restrict__ stat, const float* __restrict__ modp,
                                                       const bf16* __restrict__ Wd) {
  __shared__ __attribute__((aligned(16))) char smem[G3_CT + 33 * 320];
  STAMP(5, 0);
  const int tid = threadIdx.x, lane = tid & 63;
  const int wid = __builtin_amdgcn_readfirstlane(tid >> 6);
  const int wm = wid & 3, wn = wid >> 2;
  const int xcd = blockIdx.x & 7, idx = blockIdx.x >> 3;
  const int bm = idx & 7, bn = xcd * 4 + (idx >> 3);
  const unsigned smem_base = (unsigned)(uintptr_t)smem;
  const rsrc_t rsA = mk_rsrc((const char*)Y + (size_t)bm * 32 * 16384, 32 * 16384);
  const rsrc_t rsB = mk_rsrc(wn ? Wu : Wg, D_DIM * F_DIM * 2);
  const rsrc_t rsP = mk_rsrc((const char*)Wd + (size_t)blockIdx.x * 28672, 28672);
  const unsigned voffA = wid * 2048 + lane * 16;
  unsigned voffB[2];
#pragma unroll
  for (int i = 0; i < 2; ++i) {
    const int r = (wm * 2 + i) * 4 + (lane >> 4);
    const int sw = (r & 3) | (((r >> 3) & 1) << 2);
    const int c16 = lane & 15;
    const int vb = (((bn & 3) * 224) & ~63) + ((c16 >> 1) ^ sw) * 32 + (c16 & 1) * 16;
    voffB[i] = (unsigned)(r * (F_DIM * 2) + g3_line(bn >> 2, vb >> 7) * 128 + (vb & 127));
  }
  const unsigned m0w = smem_base + wid * 2048;
  const unsigned voffB0 = voffB[0], voffB1 = voffB[1];
  if (wn == 0) {
    u32x4 tv[8];
#pragma unroll
    for (int i = 0; i < 8; ++i) asm volatile("" : "=v"(tv[i]));
    const rsrc_t rsS = mk_rsrc(stat, S_LEN * 128);
    const unsigned vo = (unsigned)((bm * 256 + tid) * 16);
    static_for<8>([&](auto I) { bload_v<0>(tv[I.value], vo, rsS, I.value * 32768); });
    G3_STAGE(0, 0, 0); G3_STAGE(0, 1, 0); G3_STAGE(0, 2, 0); G3_STAGE(0, 3, 0);
    G3_STAGE(1, 0, 1); G3_STAGE(1, 1, 1); G3_STAGE(1, 2, 1); G3_STAGE(1, 3, 1);
    G3_DUMMY();
    G3_STAGE(2, 0, 2); G3_STAGE(2, 1, 2); G3_STAGE(2, 2, 2); G3_STAGE(2, 3, 2);
    G3_DUMMY();
    asm volatile("s_waitcnt vmcnt(" G3_TW ")" : "+v"(tv[0]), "+v"(tv[1]), "+v"(tv[2]), "+v"(tv[3]), "+v"(tv[4]), "+v"(tv[5]), "+v"(tv[6]), "+v"(tv[7])::"memory");
    float s = 0;
#pragma unroll
    for (int i = 0; i < 8; ++i) s += __uint_as_float(tv[i][0]) + __uint_as_float(tv[i][2]);
    const float mean = s * (1.0f / D_DIM);
    float m2 = 0;
#pragma unroll
    for (int i = 0; i < 8; ++i) {
      const float d0 = __uint_as_float(tv[i][0]) * (1.0f / 64.0f) - mean, d1 = __uint_as_float(tv[i][2]) * (1.0f / 64.0f) - mean;
      m2 += __uint_as_float(tv[i][1]) + __uint_as_float(tv[i][3]) + 64.0f * (d0 * d0 + d1 * d1);
    }
    const float rstd = rsqrtf(m2 * (1.0f / D_DIM) + 1e-5f);
    reinterpret_cast<f32x2*>(smem + 131072)[tid] = f32x2{rstd, -mean * rstd};
    g3_body<4>(smem, smem_base, wm, wn, wid, lane, bm, bn, rsA, rsB, rsP, voffA, voffB0, voffB1, act);
  } else {
    const rsrc_t rsC = mk_rsrc(modp, 4 * 6144 * 4);
    const int t = tid - 256;
    const unsigned vo = (unsigned)((3072 + t * 4) * 4);
    u32x4 tc[8];
#pragma unroll
    for (int i = 0; i < 8; ++i) { asm volatile("" : "=v"(tc[i])); bload_v<0>(tc[i], vo, rsC, (i >> 1) * 24576 + (i & 1) * 4096); }
    G3_STAGE(0, 0, 0); G3_STAGE(0, 1, 0); G3_STAGE(0, 2, 0); G3_STAGE(0, 3, 0);
    G3_STAGE(1, 0, 1); G3_STAGE(1, 1, 1); G3_STAGE(1, 2, 1); G3_STAGE(1, 3, 1);
    G3_DUMMY();
    G3_STAGE(2, 0, 2); G3_STAGE(2, 1, 2); G3_STAGE(2, 2, 2); G3_STAGE(2, 3, 2);
    G3_DUMMY();
    asm volatile("s_waitcnt vmcnt(" G3_TW ")" : "+v"(tc[0]), "+v"(tc[1]), "+v"(tc[2]), "+v"(tc[3]), "+v"(tc[4]), "+v"(tc[5]), "+v"(tc[6]), "+v"(tc[7])::"memory");
    f32x4 sv = (__builtin_bit_cast(f32x4, tc[0]) + __builtin_bit_cast(f32x4, tc[2])) + (__builtin_bit_cast(f32x4, tc[4]) + __builtin_bit_cast(f32x4, tc[6]));
    f32x4 gw = (__builtin_bit_cast(f32x4, tc[1]) + __builtin_bit_cast(f32x4, tc[3])) + (__builtin_bit_cast(f32x4, tc[5]) + __builtin_bit_cast(f32x4, tc[7]));
#pragma unroll
    for (int e = 0; e < 4; ++e) { gw[e] = gain_nz(gw[e]); sv[e] = rbf(sv[e]); }
    u32x2 gh, gl, sh, sl;
    split4(gw, gh, gl); split4(sv, sh, sl);
    char* d = smem + G3_CT + (t >> 3) * 320 + ((t >> 1) & 3) * 16 + (t & 1) * 8;
    *reinterpret_cast<u32x2*>(d) = gh; *reinterpret_cast<u32x2*>(d + 64) = gl;
    *reinterpret_cast<u32x2*>(d + 128) = sh; *reinterpret_cast<u32x2*>(d + 192) = sl;
    *reinterpret_cast<u32x2*>(d + 256) = u32x2{0, 0};
    g3_body<3>(smem, smem_base, wm, wn, wid, lane, bm, bn, rsA, rsB, rsP, voffA, voffB0, voffB1, act);
  }
  STAMP(5, 3);
  STAMP(5, 4);
}

#define XST 0
#define G4NF 11
#define HFS 3
#define HFL 4
#define HCK 5
#define XMAP 1
template <int OFF>
DI void bstore_sc1(u32x4 d, unsigned voff, rsrc_t rs, unsigned soff) {
  asm volatile("buffer_store_dwordx4 %0, %1, %2, %3 offen offset:%4 sc1\n\ts_nop 1" ::"v"(d), "v"(voff), "s"(rs), "s"(soff), "i"(OFF) : "memory");
}
#define DPERM(TB, Q, SL, PP)                                                                       \
  do {                                                                                             \
    if ((Q) & 1) PERM_HI(T[TB][1][(Q) >> 1], braw[SL][((Q) >> 1) * 2][PP], braw[SL][((Q) >> 1) * 2 + 1][PP]); \
    else PERM_LO(T[TB][0][(Q) >> 1], braw[SL][((Q) >> 1) * 2][PP], braw[SL][((Q) >> 1) * 2 + 1][PP]);         \
  } while (0)
namespace g4 {
constexpr int NF = G4NF, NH = 28 - 2 * NF, NS = NF + NH, HEP = NH - 3;
constexpr int nslot(int g) { return g < NF ? 64 : 32; }
constexpr int wslot(int g) { return g < NF - 1 ? 48 : (g == NF - 1 ? 56 : 24); }
constexpr int ring(int g, int n) {
  if (g < NF - 1) {
    if (n >= 48 || n % 3 != 1) return -1;
    const int l = n / 3;
    if (g + 2 < NF) return (g + 2) * 16 + l;
    return l < 12 ? (g + 2) * 16 + (l < 8 ? l : l + 4) : -1;
  }
  if (g == NF - 1) { if (n >= 48 || n % 4 != 1) return -1; const int l = n / 4; return (g + 2) * 16 + (l < 8 ? l : l + 4); }
  if (g == NF) { if (n >= 24) return -1; const int l = n % 12; return (g + 2 + n / 12) * 16 + (l < 8 ? l : l + 4); }
  if (g + 3 >= NS || n >= 24 || (n & 1)) return -1;
  const int l = n >> 1;
  return (g + 3) * 16 + (l < 8 ? l : l + 4);
}
constexpr int pwrite(int g, int n) {
  const int m = g == NF - 1 ? n : 64 + n;
  for (int e = 0; e < 24; ++e) if (12 + (13 * e) / 4 == m) return e;
  return -1;
}
constexpr int red0(int n) { return n < 8 ? 0 : (64 * (n - 8)) / 24; }
#define SPS 4
constexpr int sslot(int f) {
  const int ready = (f >> 2) * 32 + 8 + (((f & 3) + 1) * 16 * 24 + 63) / 64;
  const int paced = (f / SPS) * 32 + 15 + (f % SPS) * (32 / SPS);
  int m = ready > paced ? ready : paced;
  if ((m & 1) == 0) ++m;
  return m;
}
constexpr int dstore_f(int g, int n) {
  for (int f = 0; f < 8; ++f) if (sslot(f) == (g - NF) * 32 + n) return f;
  return -1;
}
constexpr bool dstore(int g, int n) { return g >= NF && dstore_f(g, n) >= 0; }
constexpr int vmops(int g, int n) {
  int c = (ring(g, n) >= 0 ? 1 : 0) + (dstore(g, n) ? 1 : 0);
  const int h = g - NF;
  if (h == HFS && n == 24) c += 1;
  if (h == HFL && n == 25) c += 1;
  if (h == HCK && n >= 24) c += 1;
  if (h == HEP && (n == 1 || n == 3)) c += 4;
  return c;
}
constexpr int between(int g0, int n0, int g1, int n1) {
  int c = 0;
  for (int g = g0; g <= g1; ++g)
    for (int n = (g == g0 ? n0 + 1 : 0); n < (g == g1 ? n1 : nslot(g)); ++n) c += vmops(g, n);
  return c;
}
constexpr int imin(int a, int b) { return a < b ? a : b; }
constexpr int waitcnt(int g) {
  int c = 63;
  if (g + 1 < NS && g + 1 >= 2) {
    for (int s = 0; s <= g; ++s)
      for (int n = 0; n < nslot(s); ++n)
        if (ring(s, n) == (g + 1) * 16 + 15) c = imin(c, between(s, n, g, wslot(g)) + (dstore(s, n) ? 1 : 0));
  }
  if (g == 0) c = 16;
  if (g - NF == HFS) c = imin(c, between(NF + sslot(7) / 32, sslot(7) % 32, g, 24));
  if (g - NF == HCK) c = imin(c, between(NF + HFL, 25, g, 24));
  return c;
}
constexpr int bslot(int b) { return b < NF + 3 ? b % 3 : ((b - NF) % 4 == 3 ? 3 : (NF + (b - NF) % 4) % 3); }
}
__global__ __launch_bounds__(256, 1) void gemm4_kernel(const bf16* __restrict__ At, const bf16* __restrict__ W,
                                                       const bf16* __restrict__ Yt, const float* __restrict__ modp,
                                                       bf16* __restrict__ C, float* xbuf, unsigned* flags) {
  using namespace g4;
  constexpr int KB = F_DIM / 32;
  constexpr int GT = 96 * 1024;
  __shared__ __attribute__((aligned(16))) char smem[GT + 1024];
  STAMPDECL;
  STAMP(6, 0);
  const int tid = threadIdx.x, lane = tid & 63;
  const int w = __builtin_amdgcn_readfirstlane(tid >> 6);
  const int xcd = blockIdx.x & 7, idx = blockIdx.x >> 3;
  const int kh = xcd & 1, bm = ((xcd >> 1) & 1) * 8 + (idx & 7), bn = (xcd >> 2) * 4 + (idx >> 3);
  const unsigned partner = blockIdx.x ^ 1;
  const int n0 = bn * 128, bm64 = bm * 2 + kh;
  const int fr = lane & 15, fq = lane >> 4;
  const rsrc_t rsA0 = mk_rsrc((const char*)At + (size_t)(bm64 ^ 1) * KB * 4096, KB * 4096);
  const rsrc_t rsA1 = mk_rsrc((const char*)At + (size_t)bm64 * KB * 4096, KB * 4096);
  const rsrc_t rsB = mk_rsrc((const char*)W + n0 * 2, KB * 65536 - n0 * 2);
  const unsigned voffL = lane * 16;
  unsigned voffA[4];
#pragma unroll
  for (int i = 0; i < 4; ++i) voffA[i] = voffL + ((i + w) & 3) * 1024;
  const unsigned voffB = fq * 8 * 2048 + fr * 16;
  u32x4 braw[4][8], afr[3][8], afa[4][4];
  u32x4 T[2][2];
  u32x4 xv[4], pv[8];
  unsigned gv[4], flg;
  f32x4 acc[8][8];
#pragma unroll
  for (int r = 0; r < 3; ++r)
#pragma unroll
    for (int j = 0; j < 8; ++j) asm volatile("" : "=v"(braw[r][j]), "=v"(afr[r][j]));
  const int kbF = (kh ? KB - 4 * NF : 0) + w, kbH = w;
#define ELOAD(B, L)                                                                                \
  do {                                                                                             \
    const unsigned kbv = (unsigned)(((B) < NF ? kbF : kbH) + (B) * 4);                             \
    if constexpr ((L) < 8) {                                                                       \
      const unsigned so = kbv * 65536u + ((L) >> 1) * 4096u;                                       \
      if constexpr (bslot(B) == 3 || (B) >= NF + 3) asm volatile("" : "=v"(braw[bslot(B)][(L) & 7]));        \
      if constexpr ((L) & 1) bload_v<2048>(braw[bslot(B)][(L) & 7], voffB, rsB, so);               \
      else bload_v<0>(braw[bslot(B)][(L) & 7], voffB, rsB, so);                                    \
    } else if constexpr ((B) >= NF + 2) {                                                          \
      asm volatile("" : "=a"(afa[((B) - NF) & 3][(L) & 3]));                                       \
      bload_a<0>(afa[((B) - NF) & 3][(L) & 3], voffA[(L) & 3], rsA1, kbv * 4096u);                 \
    } else {                                                                                       \
      bload_v<0>(afr[(B) % 3][(L) & 7], voffA[(L) & 3], ((L) & 4) ? rsA1 : rsA0, kbv * 4096u);     \
    }                                                                                              \
  } while (0)
#define MFMA_E(C, B, A) asm volatile("v_mfma_f32_16x16x32_bf16 %0, %1, %2, %0" : "+a"(C) : "v"(B), "v"(A))
  static_for<16>([&](auto L) { ELOAD(0, L.value); });
  static_for<16>([&](auto L) { ELOAD(1, L.value); });
#pragma unroll
  for (int i = 0; i < 8; ++i)
#pragma unroll
    for (int f = 0; f < 8; ++f) { acc[i][f] = f32x4{0, 0, 0, 0}; asm volatile("" : "+a"(acc[i][f])); }
  const unsigned sbase = (unsigned)(uintptr_t)smem + voffL;
  unsigned wlo[3];
#pragma unroll
  for (int i = 1; i < 4; ++i) wlo[i - 1] = sbase + (((w + i) & 3) * 3 + (i - 1)) * 8192;
  const unsigned rlo = sbase + w * 24576;
  const rsrc_t rsEx = mk_rsrc(xbuf, 256 * 32768);
  const unsigned exo = (unsigned)(blockIdx.x * 32768 + w * 8192);
  const rsrc_t rsFo = mk_rsrc(flags + blockIdx.x * 4 + w, 4), rsFi = mk_rsrc(flags + partner * 4 + w, 4);
  wait_vm<16>();
  STAMPNW(6, 1);
#pragma unroll
  for (int q = 0; q < 8; ++q) DPERM(0, q, 0, 0);
  asm volatile("s_nop 1");
  u32x4 pr[3][4];
  f32x4 tt[8], uu;
  static_for<NS>([&](auto G) {
    constexpr int g = G.value, h = g - NF, sl = bslot(g), ns = bslot(g + 1);
    constexpr bool full = g < NF - 1, lastf = g == NF - 1;
    constexpr bool red = (h == 0 || h == 1);
    constexpr int fb = h == 1 ? 4 : 0;
    if constexpr (h == 0 || h == 1) { WAIT_LGKM0(); __builtin_amdgcn_s_barrier(); }
    if constexpr (h == 3) __builtin_amdgcn_s_barrier();
    static_for<nslot(g)>([&](auto N) {
      constexpr int n = N.value;
      if constexpr (n == wslot(g)) {
        if constexpr (waitcnt(g) < 63) {
          if constexpr (h == HCK) asm volatile("s_waitcnt vmcnt(%1)" : "+v"(flg) : "n"(waitcnt(g)) : "memory");
          else wait_vm<waitcnt(g)>();
        }
        if constexpr (h == HFS) asm volatile("buffer_store_dword %0, %1, %2, 0 offen sc1" ::"v"(1u), "v"(voffL >> 2), "s"(rsFo) : "memory");
        if constexpr (h == HCK) {
          unsigned spins = 0;
          while (__builtin_amdgcn_readfirstlane(flg) == 0u && spins < (1u << 20)) {
            __builtin_amdgcn_s_sleep(1);
            bload1_sc1(flg, 0u, rsFi);
            asm volatile("s_waitcnt vmcnt(0)" : "+v"(flg)::"memory");
            ++spins;
          }
        }
      }
      if constexpr (red && n == 8) WAIT_LGKM0();
      if constexpr (full) {
        constexpr int p = n >> 4, q = n & 15;
        MFMA_E(acc[q & 7][2 * p + (q >> 3)], T[p & 1][q >> 3], afr[g % 3][q & 7]);
        if constexpr ((q & 1) == 0) {
          if constexpr (p < 3) DPERM((p + 1) & 1, q >> 1, sl, p + 1);
          else DPERM(0, q >> 1, ns, 0);
        }
      } else {
        constexpr int p = (n >> 3) & 3, q = n & 7, ib = (lastf && n < 32) ? 0 : 4;
        if constexpr (h >= 2) MFMA_A(acc[ib + (q & 3)][2 * p + (q >> 2)], T[p & 1][q >> 2], afa[h & 3][q & 3]);
        else MFMA_E(acc[ib + (q & 3)][2 * p + (q >> 2)], T[p & 1][q >> 2], afr[g % 3][ib + (q & 3)]);
        if constexpr (p < 3) DPERM((p + 1) & 1, q, sl, p + 1);
        else if constexpr (ib == 0) DPERM(0, q, sl, 0);
        else if constexpr (g + 1 < NS) DPERM(0, q, ns, 0);
      }
      if constexpr (ring(g, n) >= 0) ELOAD((ring(g, n) >> 4), (ring(g, n) & 15));
      if constexpr ((lastf || h == 0) && pwrite(g, n) >= 0) {
        constexpr int e = pwrite(g, n), i = 1 + e % 3, f = e / 3;
        dsw128a<f * 1024>(wlo[i - 1], acc[i][f]);
      }
      if constexpr (red) {
        if constexpr (n < 6) dsr128<(n % 3) * 8192 + (fb + n / 3) * 1024>(pr[n % 3][n / 3], rlo);
        if constexpr (n == 6 || n == 7) {
          static_for<3>([&](auto E) { dsr128<E.value * 8192 + (fb + n - 4) * 1024>(pr[E.value][n - 4], rlo); });
        }
        if constexpr (n >= 8) {
          constexpr int o0 = red0(n), o1 = red0(n + 1);
          static_for<o1 - o0>([&](auto O) {
            constexpr int o = o0 + O.value, fl = o / 16, k = o % 16, r = k & 3;
            (void)&uu; (void)&tt; (void)&pr;
            if constexpr (k < 4) asm volatile("v_accvgpr_read_b32 %0, %1" : "=v"(tt[fb + fl][r]) : "a"(acc[0][fb + fl][r]));
            else if constexpr (k < 8) asm volatile("v_add_f32 %0, %0, %1" : "+v"(tt[fb + fl][r]) : "v"(pr[0][fl][r]));
            else if constexpr (k < 12) asm volatile("v_add_f32 %0, %1, %2" : "=v"(uu[r]) : "v"(pr[1][fl][r]), "v"(pr[2][fl][r]));
            else asm volatile("v_add_f32 %0, %0, %1" : "+v"(tt[fb + fl][r]) : "v"(uu[r]));
          });
        }
      }
      if constexpr (h >= 0 && dstore(g, n)) {
        constexpr int f = dstore_f(g, n);
        bstore_sc1<(f & 3) * 1024>(__builtin_bit_cast(u32x4, tt[f]), voffL, rsEx, exo + (f >> 2) * 4096);
      }
      if constexpr (h == HFL && n == 25) {
        asm volatile("" : "=v"(flg));
        bload1_sc1(flg, 0u, rsFi);
      }
      if constexpr (h == HCK && n >= 24) {
        constexpr int f = n - 24;
        asm volatile("" : "=v"(pv[f]));
        bload_sc1<(f & 3) * 1024>(pv[f], voffL, rsEx, partner * 32768u + w * 8192u + (f >> 2) * 4096u);
      }
      if constexpr (h == HEP && n == 1) {
        const rsrc_t rsG = mk_rsrc(modp + (w >= 2 ? 4096 : 5120), 4 * 6144 * 4);
        const unsigned vo = (unsigned)((n0 + (tid & 127)) * 4);
#pragma unroll
        for (int r = 0; r < 4; ++r) { asm volatile("" : "=v"(gv[r])); bload1_v<0>(gv[r], vo, rsG, r * 24576); }
      }
      if constexpr (h == HEP && n == 3) {
        const rsrc_t rsX = mk_rsrc(Yt, S_LEN * D_DIM * 2);
        const unsigned vx = (unsigned)(((bm64 >> 2) * 32 + bn * 4 + fq) * 16384 + ((bm64 & 3) * 4 + w) * 1024 + fr * 16);
#pragma unroll
        for (int r = 0; r < 4; ++r) asm volatile("" : "=v"(xv[r]));
        bload_v<0>(xv[0], vx, rsX, 0); bload_v<256>(xv[1], vx, rsX, 0); bload_v<512>(xv[2], vx, rsX, 0); bload_v<768>(xv[3], vx, rsX, 0);
      }
      if constexpr (g == NS - 1 && n >= 8 && (n & 7) >= 2) {
        constexpr int e = (n & 7) - 2, i = 1 + e % 3, f = 2 * ((n >> 3) - 1) + e / 3;
        dsw128a<f * 1024>(wlo[i - 1], acc[4 + i][f]);
      }
    });
  });
  asm volatile("s_nop 15" ::: "memory");
#pragma unroll
  for (int f = 0; f < 8; ++f) asm volatile("" : "+a"(acc[4][f]));
  static_for<6>([&](auto E) { constexpr int i = 1 + E.value % 3, f = 6 + E.value / 3; dsw128a<f * 1024>(wlo[i - 1], acc[4 + i][f]); });
  asm volatile("s_waitcnt vmcnt(%8)" : "+v"(xv[0]), "+v"(xv[1]), "+v"(xv[2]), "+v"(xv[3]), "+v"(gv[0]), "+v"(gv[1]), "+v"(gv[2]), "+v"(gv[3]) : "n"(between(NF + HEP, 3, NS - 1, 32)) : "memory");
  STAMP(6, 2); STAMPFLUSH(6, 1);
  float* gtab = reinterpret_cast<float*>(smem + GT);
  {
    float sum = (__uint_as_float(gv[0]) + __uint_as_float(gv[1])) + (__uint_as_float(gv[2]) + __uint_as_float(gv[3]));
    sum = (w >= 2) ? 1.0f / gain_nz(sum) : rbf(sum);
    const int col = tid & 127;
    gtab[(tid & 128) + (col & 96) + (col & 7) * 4 + ((col >> 3) & 3)] = sum;
  }
  asm volatile("s_waitcnt lgkmcnt(0)" ::: "memory");
  __builtin_amdgcn_s_barrier();
  u32x4 pq[3][8], gt[8], ct[8];
  f32x4 fin[8];
  {
    const unsigned gsrc = (unsigned)(uintptr_t)smem + GT + fq * 128;
    static_for<24>([&](auto E) { dsr128<E.value * 1024>(pq[E.value >> 3][E.value & 7], rlo); });
    static_for<8>([&](auto F) { dsr128<F.value * 16>(gt[F.value], gsrc); });
    static_for<8>([&](auto F) { dsr128<512 + F.value * 16>(ct[F.value], gsrc); });
#pragma unroll
    for (int f = 0; f < 8; ++f) { fin[f] = acc[4][f]; asm volatile("" : "+v"(fin[f])); }
    asm volatile("s_waitcnt lgkmcnt(0)" ::: "memory");
#pragma unroll
    for (int f = 0; f < 8; ++f) asm volatile("" : "+v"(pq[0][f]), "+v"(pq[1][f]), "+v"(pq[2][f]), "+v"(gt[f]), "+v"(ct[f]));
  }
#pragma unroll
  for (int f = 0; f < 8; ++f) {
    fin[f] = (fin[f] + __builtin_bit_cast(f32x4, pq[0][f])) + (__builtin_bit_cast(f32x4, pq[1][f]) + __builtin_bit_cast(f32x4, pq[2][f]));
    asm volatile("" : "+v"(fin[f]));
  }
  asm volatile("s_waitcnt vmcnt(0)" : "+v"(pv[0]), "+v"(pv[1]), "+v"(pv[2]), "+v"(pv[3]), "+v"(pv[4]), "+v"(pv[5]), "+v"(pv[6]), "+v"(pv[7])::"memory");
  STAMP(6, 6);
#pragma unroll
  for (int f = 0; f < 8; ++f) {
    fin[f] += __builtin_bit_cast(f32x4, pv[f]);
    f32x4 xf;
#pragma unroll
    for (int r = 0; r < 4; ++r) xf[r] = (f & 1) ? bf_hi(xv[r][f >> 1]) : bf_lo(xv[r][f >> 1]);
    xf *= __builtin_bit_cast(f32x4, ct[f]);
    fin[f] = xf + __builtin_bit_cast(f32x4, gt[f]) * fin[f];
  }
  {
    char* tb = smem + w * 24576;
#pragma unroll
    for (int r = 0; r < 4; ++r) {
      const u32x4 ov = {pk_bf16(fin[0][r], fin[1][r]), pk_bf16(fin[2][r], fin[3][r]), pk_bf16(fin[4][r], fin[5][r]), pk_bf16(fin[6][r], fin[7][r])};
      *reinterpret_cast<u32x4*>(tb + fr * 256 + (((fq * 4 + r) ^ (fr & 7)) * 16)) = ov;
    }
#pragma unroll
    for (int j = 0; j < 4; ++j) {
      const int row = j * 4 + fq;
      const u32x4 ov = *reinterpret_cast<const u32x4*>(tb + row * 256 + ((fr ^ (row & 7)) * 16));
      gst128(C + (size_t)(bm64 * 64 + w * 16 + row) * D_DIM + n0 + fr * 8, ov);
    }
  }
  STAMP(6, 3);
  STAMP(6, 4);
}

extern "C" void kernel_launch(void* const* d_in, const int* in_sizes, int n_in, void* d_out, int out_size,
                              void* d_ws, size_t ws_size, hipStream_t stream) {
  const bf16* x = (const bf16*)d_in[0];
  const bf16* wqkv = (const bf16*)d_in[1];
  const bf16* wo = (const bf16*)d_in[2];
  const bf16* wg = (const bf16*)d_in[4];
  const bf16* wu = (const bf16*)d_in[5];
  const bf16* wd = (const bf16*)d_in[6];
  const bf16* c = (const bf16*)d_in[7];
  const bf16* wmod = (const bf16*)d_in[8];
  bf16* out = (bf16*)d_out;

  char* ws = (char*)d_ws;
  float* modp = (float*)ws;
  float* rstat1 = (float*)(ws + 114688);
  float* stat = (float*)(ws + 524288);
  bf16* ot = (bf16*)(ws + (1 << 20));
  bf16* yt = (bf16*)(ws + (9 << 20));
  bf16* ht = (bf16*)(ws + (13 << 20));
  bf16* act = (bf16*)(ws + (17 << 20));
  float* xbuf = (float*)(ws + (32 << 20));
  unsigned* flags = (unsigned*)(ws + 262144);

  pre_kernel<<<320, 512, 0, stream>>>(x, c, wmod, modp, rstat1, wqkv);
  ln_apply_kernel<<<256, 256, 0, stream>>>(x, modp, rstat1, ht, wqkv, flags, wo);
  qkv_attn_kernel<<<256, 512, 0, stream>>>(ht, wqkv, ot, wo, x, modp, yt, stat, flags + 1024, wg, wu);
  gemm3_kernel<<<256, 512, 0, stream>>>(yt, wg, wu, act, stat, modp, wd);
  gemm4_kernel<<<256, 256, 0, stream>>>(act, wd, yt, modp, out, xbuf, flags);
}
```

```cpp
#include <hip/hip_runtime.h>
#include <hip/hip_bf16.h>
#include <stdint.h>

typedef __hip_bfloat16 bf16;
typedef __attribute__((ext_vector_type(8))) short bf16x8;
typedef __attribute__((ext_vector_type(4))) float f32x4;
typedef __attribute__((ext_vector_type(2))) float f32x2;
typedef __attribute__((ext_vector_type(4))) unsigned u32x4;
typedef __attribute__((ext_vector_type(2))) unsigned u32x2;
typedef __attribute__((ext_vector_type(2))) __bf16 bf16x2_t;
typedef __amdgpu_buffer_rsrc_t rsrc_t;

#define DI __device__ __forceinline__
#define QBAR 2
#define QRN 17
#define TQ 12
#define TG 4
#define K1MAP 1
#define QTM 1
#define WOT 1
#define CODEPF 53

#define STAMP(k, s) do { } while (0)
#define STAMPT(k, s, T) do { } while (0)
#define STAMPNW(k, s)
#define STAMPFLUSH(k, s)
#define STAMPDECL

constexpr int S_LEN = 2048;
constexpr int D_DIM = 1024;
constexpr int F_DIM = 3584;

template <int V> struct ic { static constexpr int value = V; };
template <int N, int I = 0, class F>
DI void static_for(F&& f) {
  if constexpr (I < N) { f(ic<I>{}); static_for<N, I + 1>(f); }
}

DI float bf_lo(unsigned u) { return __uint_as_float(u << 16); }
DI float bf_hi(unsigned u) { return __uint_as_float(u & 0xffff0000u); }
DI unsigned pk_bf16(float lo, float hi) {
  const f32x2 v = {lo, hi};
  return __builtin_bit_cast(unsigned, __builtin_convertvector(v, bf16x2_t));
}
DI rsrc_t mk_rsrc(const void* p, unsigned bytes) { return __builtin_amdgcn_make_buffer_rsrc((void*)p, (short)0, (int)bytes, 0x00020000); }

template <int N>
DI void wait_vm() { asm volatile("s_waitcnt vmcnt(%0)" ::"n"(N > 63 ? 63 : N) : "memory"); }
#define WAIT_LGKM0() do { asm volatile("s_waitcnt lgkmcnt(0)" ::: "memory"); __builtin_amdgcn_sched_barrier(0); } while (0)
#define SCHED0() __builtin_amdgcn_sched_barrier(0)

#define MFMA_A(C, B, A) asm volatile("v_mfma_f32_16x16x32_bf16 %0, %1, %2, %0" : "+a"(C) : "v"(B), "a"(A))
#define MFMA_AV(C, B, A) asm volatile("v_mfma_f32_16x16x32_bf16 %0, %1, %2, %0" : "+v"(C) : "v"(B), "v"(A))
#define PERM_LO(D, X, Y) asm volatile("v_perm_b32 %0, %1, %2, %3" : "=v"(D) : "v"(Y), "v"(X), "s"(0x05040100u))
#define PERM_HI(D, X, Y) asm volatile("v_perm_b32 %0, %1, %2, %3" : "=v"(D) : "v"(Y), "v"(X), "s"(0x07060302u))
template <int OFF>
DI void bload_v(u32x4& d, unsigned voff, rsrc_t rs, unsigned soff) {
  asm volatile("buffer_load_dwordx4 %0, %1, %2, %3 offen offset:%4" : "+v"(d) : "v"(voff), "s"(rs), "s"(soff), "i"(OFF) : "memory");
}
template <int OFF>
DI void bload_a(u32x4& d, unsigned voff, rsrc_t rs, unsigned soff) {
  asm volatile("buffer_load_dwordx4 %0, %1, %2, %3 offen offset:%4" : "+a"(d) : "v"(voff), "s"(rs), "s"(soff), "i"(OFF) : "memory");
}
template <int OFF>
DI void bload2_v(u32x2& d, unsigned voff, rsrc_t rs, unsigned soff) {
  asm volatile("buffer_load_dwordx2 %0, %1, %2, %3 offen offset:%4" : "+v"(d) : "v"(voff), "s"(rs), "s"(soff), "i"(OFF) : "memory");
}
template <int OFF>
DI void bload1_v(unsigned& d, unsigned voff, rsrc_t rs, unsigned soff) {
  asm volatile("buffer_load_dword %0, %1, %2, %3 offen offset:%4" : "+v"(d) : "v"(voff), "s"(rs), "s"(soff), "i"(OFF) : "memory");
}
template <int OFF>
DI void bload_lds(unsigned m0v, unsigned voff, rsrc_t rs, unsigned soff) {
  asm volatile("s_mov_b32 m0, %0\n\ts_nop 0\n\tbuffer_load_dwordx4 %1, %2, %3 offen offset:%4 lds" ::"s"(m0v), "v"(voff), "s"(rs), "s"(soff), "i"(OFF) : "memory");
}
DI void touch_lds(unsigned m0v, unsigned voff, rsrc_t rs, unsigned soff) {
  asm volatile("s_mov_b32 m0, %0\n\ts_nop 0\n\tbuffer_load_dword %1, %2, %3 offen lds" ::"s"(m0v), "v"(voff), "s"(rs), "s"(soff) : "memory");
}
DI void touch_nt(unsigned m0v, unsigned voff, rsrc_t rs, unsigned soff) {
  asm volatile("s_mov_b32 m0, %0\n\ts_nop 0\n\tbuffer_load_dword %1, %2, %3 offen nt lds\n\tbuffer_load_dword %1, %2, %3 offen offset:64 sc1 lds" ::"s"(m0v), "v"(voff), "s"(rs), "s"(soff) : "memory");
}
template <int OFF>
DI void bload_lds_nt(unsigned m0v, unsigned voff, rsrc_t rs, unsigned soff) {
  asm volatile("s_mov_b32 m0, %0\n\ts_nop 0\n\tbuffer_load_dwordx4 %1, %2, %3 offen offset:%4 nt lds" ::"s"(m0v), "v"(voff), "s"(rs), "s"(soff), "i"(OFF) : "memory");
}
template <int OFF>
DI void dsr128(u32x4& d, unsigned addr) { asm volatile("ds_read_b128 %0, %1 offset:%2" : "=v"(d) : "v"(addr), "i"(OFF) : "memory"); }
template <int OFF>
DI void dsr64(u32x2& d, unsigned addr) { asm volatile("ds_read_b64 %0, %1 offset:%2" : "=v"(d) : "v"(addr), "i"(OFF) : "memory"); }
template <int OFF>
DI void dsr_tr(u32x2& d, unsigned addr) { asm volatile("ds_read_b64_tr_b16 %0, %1 offset:%2" : "=v"(d) : "v"(addr), "i"(OFF) : "memory"); }
template <int OFF>
DI void dsw128(unsigned addr, u32x4 d) { asm volatile("ds_write_b128 %0, %1 offset:%2" ::"v"(addr), "v"(d), "i"(OFF) : "memory"); }
template <int OFF>
DI void dsw128a(unsigned addr, const f32x4& d) { asm volatile("ds_write_b128 %0, %1 offset:%2" ::"v"(addr), "a"(d), "i"(OFF) : "memory"); }
template <int OFF>
DI void dsw32(unsigned addr, unsigned d) { asm volatile("ds_write_b32 %0, %1 offset:%2" ::"v"(addr), "v"(d), "i"(OFF) : "memory"); }

DI void gst128(void* p, u32x4 v) {
  asm volatile("global_store_dwordx4 %0, %1, off sc1\n\ts_nop 1" ::"v"(p), "v"(v) : "memory");
}
DI f32x2 lo2(f32x4 v) { return __builtin_shufflevector(v, v, 0, 1); }
DI f32x2 hi2(f32x4 v) { return __builtin_shufflevector(v, v, 2, 3); }
DI float rowsum4(float v) {
  const unsigned u = __float_as_uint(v);
  const auto a = __builtin_amdgcn_permlane16_swap(u, u, false, false);
  const unsigned t = __float_as_uint(__uint_as_float(a[0]) + __uint_as_float(a[1]));
  const auto b = __builtin_amdgcn_permlane32_swap(t, t, false, false);
  return __uint_as_float(b[0]) + __uint_as_float(b[1]);
}
DI void split4(const f32x4 v, u32x2& hi, u32x2& lo) {
  hi[0] = pk_bf16(v[0], v[1]); hi[1] = pk_bf16(v[2], v[3]);
  lo[0] = pk_bf16(v[0] - bf_lo(hi[0]), v[1] - bf_hi(hi[0]));
  lo[1] = pk_bf16(v[2] - bf_lo(hi[1]), v[3] - bf_hi(hi[1]));
}
DI float silu_f(float v) { return v / (1.0f + __expf(-v)); }
DI float rbf(float v) { return bf_lo(pk_bf16(v, v)); }
DI float rowmax4(float v) {
  const unsigned u = __float_as_uint(v);
  const auto a = __builtin_amdgcn_permlane16_swap(u, u, false, false);
  const unsigned t = __float_as_uint(fmaxf(__uint_as_float(a[0]), __uint_as_float(a[1])));
  const auto b = __builtin_amdgcn_permlane32_swap(t, t, false, false);
  return fmaxf(__uint_as_float(b[0]), __uint_as_float(b[1]));
}
DI float gain_nz(float m) {
  const float g = rbf(1.0f + rbf(m));
  return __builtin_fabsf(g) < 8.67361738e-19f ? 8.67361738e-19f : g;
}
template <int OFF>
DI void bload_sc1(u32x4& d, unsigned voff, rsrc_t rs, unsigned soff) {
  asm volatile("buffer_load_dwordx4 %0, %1, %2, %3 offen offset:%4 sc1" : "+v"(d) : "v"(voff), "s"(rs), "s"(soff), "i"(OFF) : "memory");
}
DI void bload1_sc1(unsigned& d, unsigned voff, rsrc_t rs) {
  asm volatile("buffer_load_dword %0, %1, %2, 0 offen sc1" : "+v"(d) : "v"(voff), "s"(rs) : "memory");
}

__global__ __launch_bounds__(512) void pre_kernel(const bf16* __restrict__ x, const bf16* __restrict__ c,
                                                  const bf16* __restrict__ wmod, float* __restrict__ modp,
                                                  float* __restrict__ rstat1, const bf16* __restrict__ Wq) {
  __shared__ __attribute__((aligned(16))) float red[8 * 2 * 128 + 256];
  STAMP(0, 0);
  const int tid = threadIdx.x, lane = tid & 63, w = tid >> 6;
  if (blockIdx.x < 192) {
    const int fr = lane & 15, fq = lane >> 4;
    const int cg = blockIdx.x >> 2, kq = blockIdx.x & 3;
    const int col0 = cg * 128;
    const int k0 = kq * 256 + w * 32 + fq * 8;
    u32x4 raw[8];
#pragma unroll
    for (int j = 0; j < 8; ++j) {
      const u32x4* p_ = reinterpret_cast<const u32x4*>(wmod + (size_t)(k0 + j) * 6144 + col0 + fr * 8);
      raw[j] = __builtin_nontemporal_load(p_);
    }
    const u32x4 cv = *reinterpret_cast<const u32x4*>(c + k0);
    u32x4 cf;
#pragma unroll
    for (int e = 0; e < 4; ++e) {
      const float a = silu_f(bf_lo(cv[e])), b = silu_f(bf_hi(cv[e]));
      cf[e] = (fr == 0) ? pk_bf16(a, b) : 0u;
    }
    f32x4 acc[8];
#pragma unroll
    for (int p = 0; p < 4; ++p) {
      u32x4 lo, hi;
#pragma unroll
      for (int t = 0; t < 4; ++t) {
        lo[t] = __builtin_amdgcn_perm(raw[2 * t + 1][p], raw[2 * t][p], 0x05040100u);
        hi[t] = __builtin_amdgcn_perm(raw[2 * t + 1][p], raw[2 * t][p], 0x07060302u);
      }
      acc[2 * p] = __builtin_amdgcn_mfma_f32_16x16x32_bf16(__builtin_bit_cast(bf16x8, lo), __builtin_bit_cast(bf16x8, cf), f32x4{0, 0, 0, 0}, 0, 0, 0);
      acc[2 * p + 1] = __builtin_amdgcn_mfma_f32_16x16x32_bf16(__builtin_bit_cast(bf16x8, hi), __builtin_bit_cast(bf16x8, cf), f32x4{0, 0, 0, 0}, 0, 0, 0);
    }
    if (fr == 0) {
#pragma unroll
      for (int r = 0; r < 4; ++r) {
        float* d = red + w * 128 + fq * 32 + r * 8;
        *reinterpret_cast<f32x4*>(d) = f32x4{acc[0][r], acc[1][r], acc[2][r], acc[3][r]};
        *reinterpret_cast<f32x4*>(d + 4) = f32x4{acc[4][r], acc[5][r], acc[6][r], acc[7][r]};
      }
    }
    __syncthreads();
    if (tid < 128) {
      float s = 0;
#pragma unroll
      for (int g = 0; g < 8; ++g) s += red[g * 128 + tid];
      modp[kq * 6144 + col0 + tid] = s;
    }
    STAMP(0, 1);
    return;
  }
  const int row0 = (blockIdx.x - 192) * 16 + w * 2;
  u32x4 a[2][2];
#pragma unroll
  for (int i = 0; i < 2; ++i) {
    a[i][0] = *reinterpret_cast<const u32x4*>(x + (size_t)(row0 + i) * D_DIM + lane * 8);
    a[i][1] = *reinterpret_cast<const u32x4*>(x + (size_t)(row0 + i) * D_DIM + 512 + lane * 8);
  }
  if (blockIdx.x < 192 + 8) {
    unsigned long long pc;
    asm volatile("s_getpc_b64 %0" : "=s"(pc));
    const rsrc_t rsC = mk_rsrc((const void*)(uintptr_t)(pc & ~255ull), CODEPF * 1024);
#pragma unroll
    for (int i = 0; i < (CODEPF + 7) / 8; ++i) bload_lds<0>((unsigned)(uintptr_t)(red + 2048), tid * 16, rsC, i * 8192);
  }
#pragma unroll
  for (int i = 0; i < 2; ++i) {
    float v[16];
#pragma unroll
    for (int j = 0; j < 8; ++j) { v[2 * j] = bf_lo(a[i][j >> 2][j & 3]); v[2 * j + 1] = bf_hi(a[i][j >> 2][j & 3]); }
    float s = 0;
#pragma unroll
    for (int j = 0; j < 16; ++j) s += v[j];
#pragma unroll
    for (int o = 32; o > 0; o >>= 1) s += __shfl_xor(s, o);
    const float mean = s * (1.0f / D_DIM);
    float q = 0;
#pragma unroll
    for (int j = 0; j < 16; ++j) { const float d = v[j] - mean; q += d * d; }
#pragma unroll
    for (int o = 32; o > 0; o >>= 1) q += __shfl_xor(q, o);
    const float rstd = rsqrtf(q * (1.0f / D_DIM) + 1e-5f);
    if (lane == 0) *reinterpret_cast<f32x2*>(rstat1 + (row0 + i) * 2) = f32x2{rstd, -mean * rstd};
  }
  STAMP(0, 2);
}

#define QNT 0
#define K1POS 2
DI void k1_touch(char* sink, const bf16* Wq, const bf16* Wo, int part) {
  const int lane = threadIdx.x & 63, w = threadIdx.x >> 6;
  const rsrc_t rsP = mk_rsrc(Wq, D_DIM * 3 * D_DIM * 2);
  const unsigned vo = (unsigned)(((blockIdx.x >> 3) * 32 + (threadIdx.x >> 3)) * 6144 + (blockIdx.x & 3) * 512 + (threadIdx.x & 3) * 128);
  const unsigned v2 = (unsigned)(((blockIdx.x >> 3) * 32 + (threadIdx.x >> 2)) * 6144 + (blockIdx.x & 3) * 512 + (threadIdx.x & 3) * 128);
  if (part & 1) touch_lds((unsigned)(uintptr_t)sink, vo + ((threadIdx.x >> 2) & 1) * 2048, rsP, 0);
  if (part & 2) {
    if (w < 2) touch_lds((unsigned)(uintptr_t)sink, v2, rsP, 4096);
    else touch_lds((unsigned)(uintptr_t)sink, (w == 2 && WOT) ? blockIdx.x * 8192 + lane * 128 : 0x40000000u, mk_rsrc(Wo, D_DIM * D_DIM * 2), 0);
  }
}
__global__ __launch_bounds__(256) void ln_apply_kernel(const bf16* __restrict__ x, const float* __restrict__ modp,
                                                       const float* __restrict__ rstat1, bf16* __restrict__ H,
                                                       const bf16* __restrict__ Wq, unsigned* __restrict__ flags,
                                                       const bf16* __restrict__ Wo) {
  __shared__ __attribute__((aligned(16))) float tab[4][64];
  __shared__ __attribute__((aligned(16))) char sink[1024];
  STAMP(1, 0);
  const int lane = threadIdx.x & 63, w = threadIdx.x >> 6;
  const int fr = lane & 15, fq = lane >> 4;
  const int kb = (blockIdx.x >> 5) * 4 + w, mb0 = (blockIdx.x & 7) + ((blockIdx.x >> 3) & 3) * 32;
  constexpr int MBS = 8;
  u32x4 mr, xv[4];
  u32x2 rr[4];
  asm volatile("" : "=v"(mr));
  {
    bload_v<0>(mr, (unsigned)((fq * 6144 + ((lane >> 3) & 1) * 1024 + kb * 32 + (lane & 7) * 4) * 4), mk_rsrc(modp, 4 * 6144 * 4), 0);
    const rsrc_t rsX = mk_rsrc(x, S_LEN * D_DIM * 2), rsS = mk_rsrc(rstat1, S_LEN * 8);
    const unsigned vx = (unsigned)(((mb0 * 16 + fr) * D_DIM + kb * 32 + fq * 8) * 2), vs = (unsigned)((mb0 * 16 + fr) * 8);
#pragma unroll
    for (int i = 0; i < 4; ++i) {
      asm volatile("" : "=v"(xv[i]), "=v"(rr[i]));
      bload_v<0>(xv[i], vx, rsX, i * 32768 * MBS);
      bload2_v<0>(rr[i], vs, rsS, i * 128 * MBS);
    }
    if (K1POS == 0) k1_touch(sink, Wq, Wo, 3);
    if (K1POS == 2) k1_touch(sink, Wq, Wo, 2);
  }
  asm volatile("s_waitcnt vmcnt(%9)" : "+v"(mr), "+v"(xv[0]), "+v"(xv[1]), "+v"(xv[2]), "+v"(xv[3]), "+v"(rr[0]), "+v"(rr[1]), "+v"(rr[2]), "+v"(rr[3]) : "n"((K1POS == 0 ? 2 : (K1POS == 2 ? 1 : 0)) * (QNT ? 2 : 1)) : "memory");
  f32x4 mv = __builtin_bit_cast(f32x4, mr);
  f32x2 rs[4];
#pragma unroll
  for (int i = 0; i < 4; ++i) rs[i] = __builtin_bit_cast(f32x2, rr[i]);
#pragma unroll
  for (int e = 0; e < 4; ++e) {
    mv[e] = rbf(rowsum4(mv[e]));
    if (lane & 8) mv[e] = rbf(1.0f + mv[e]);
  }
  if (lane < 16) *reinterpret_cast<f32x4*>(&tab[w][lane * 4]) = mv;
  __builtin_amdgcn_wave_barrier();
  asm volatile("s_waitcnt lgkmcnt(0)" ::: "memory");
  const f32x4 s0 = *reinterpret_cast<const f32x4*>(&tab[w][fq * 8]), s1 = *reinterpret_cast<const f32x4*>(&tab[w][fq * 8 + 4]);
  const f32x4 g0 = *reinterpret_cast<const f32x4*>(&tab[w][32 + fq * 8]), g1 = *reinterpret_cast<const f32x4*>(&tab[w][32 + fq * 8 + 4]);
#pragma unroll
  for (int i = 0; i < 4; ++i) {
    u32x4 ho;
#pragma unroll
    for (int d = 0; d < 4; ++d) {
      const f32x4 gg = d < 2 ? g0 : g1, ss = d < 2 ? s0 : s1;
      const float a = (bf_lo(xv[i][d]) * rs[i][0] + rs[i][1]) * gg[(d & 1) * 2] + ss[(d & 1) * 2];
      const float b = (bf_hi(xv[i][d]) * rs[i][0] + rs[i][1]) * gg[(d & 1) * 2 + 1] + ss[(d & 1) * 2 + 1];
      ho[d] = pk_bf16(a, b);
    }
    gst128((char*)H + ((size_t)(mb0 + i * MBS) * 32 + kb) * 1024 + lane * 16, ho);
  }
  if (threadIdx.x < 4) flags[blockIdx.x * 4 + threadIdx.x] = 0u;
  if (threadIdx.x == 4) flags[1024 + blockIdx.x * 32] = 0u;
  STAMP(1, 1);
  if (K1POS == 1) k1_touch(sink, Wq, Wo, 3);
  if (K1POS == 2) k1_touch(sink, Wq, Wo, 1);
}

#define GMAP 0
#define T1 0
#define T2 0
#define GNT 0
#define TW 0
#define TP 8
#define TS 2
#define TN 2
#define GDEP 12
constexpr int Q_SLOT = 21504, Q_NSL = 7;
constexpr int QX_WH = 4 * Q_SLOT;
constexpr int QX_HX = 65536;
constexpr int QX_GT = QX_WH + 65536;
constexpr int QX_CNT = QX_GT + 512;
constexpr int QX_SINK = QX_CNT + 512;
constexpr int Q_LDS = QX_SINK + 1024;

DI unsigned lds_add(unsigned addr, unsigned v0) {
  unsigned o;
  asm volatile("ds_add_rtn_u32 %0, %1, %2\n\ts_waitcnt lgkmcnt(0)" : "=v"(o) : "v"(addr), "v"(v0) : "memory");
  return __builtin_amdgcn_readfirstlane(o);
}
template <int SLP>
DI void lds_wait(unsigned addr, unsigned n) {
  for (;;) {
    unsigned v;
    asm volatile("ds_read_b32 %0, %1\n\ts_waitcnt lgkmcnt(0)" : "=v"(v) : "v"(addr) : "memory");
    if (__builtin_amdgcn_readfirstlane(v) >= n) break;
    __builtin_amdgcn_s_sleep(SLP);
  }
}
DI void wo_piece(unsigned smem_base, unsigned p, unsigned rot, unsigned voff, rsrc_t rs) {
  const unsigned pb = p >> 2, kb = (pb & 24u) | ((pb + rot) & 7u);
  bload_lds<0>(smem_base + (p < 64u ? QX_WH + p * 1024u : (p - 64u) * 1024u), voff, rs, kb * 65536u + (p & 3u) * 16384u);
}

DI void gu_touch(unsigned smem_base, int e, unsigned vgu, const bf16* wg, const bf16* wu, bool nt) {
  if (nt) touch_nt(smem_base + QX_SINK, vgu, mk_rsrc((e & 1) ? wu : wg, D_DIM * F_DIM * 2), (unsigned)(e >> 1) * 256u * (F_DIM * 2));
  else touch_lds(smem_base + QX_SINK, vgu, mk_rsrc((e & 1) ? wu : wg, D_DIM * F_DIM * 2), (unsigned)(e >> 1) * 256u * (F_DIM * 2));
}
DI void qkv_loader(unsigned smem_base, int lane, int sw, int m0, int n0, const bf16* __restrict__ H, const bf16* __restrict__ W,
                   unsigned rot, unsigned voffO, rsrc_t rsO) {
  const rsrc_t rsH = mk_rsrc(H, S_LEN * D_DIM * 2);
  const rsrc_t rsW = mk_rsrc(W, D_DIM * 3 * D_DIM * 2);
  int fb0 = (m0 >> 4) - 1 + 3 * sw; fb0 = fb0 < 0 ? 0 : fb0;
  const unsigned voffH0 = (unsigned)(fb0 * 32768 + lane * 16);
  const unsigned voffH1 = (unsigned)(((m0 >> 4) + 3 * sw) * 32768 + lane * 16);
  unsigned voffW[2];
#pragma unroll
  for (int i = 0; i < 2; ++i) {
    const int r = i * 8 + (lane >> 3), ch = lane & 7;
    const int g = ((r >> 1) & 1) | (((r >> 3) & 1) << 1);
    voffW[i] = (unsigned)(r * 6144 + sw * 2048 + (n0 + (((ch >> 1) ^ g) * 16) + (ch & 1) * 8) * 2);
  }
  const unsigned m0a = smem_base + sw * 3072, m0w = smem_base + 9216 + sw * 4096;
#define Q_LOAD(SOFF, U)                                                                                          \
  do {                                                                                                           \
    const unsigned so_ = (unsigned)(U) * 1024u;                                                                  \
    bload_lds<0>(m0a + (SOFF), voffH0, rsH, so_);                                                                \
    bload_lds<0>(m0a + (SOFF) + 1024, voffH1, rsH, so_);                                                         \
    bload_lds<0>(m0a + (SOFF) + 2048, voffH1, rsH, so_ + 32768u);                                                \
    _Pragma("unroll") for (int i = 0; i < 4; ++i)                                                                \
      bload_lds<0>(m0w + (SOFF) + i * 1024, voffW[i & 1], rsW, (unsigned)(U) * 196608u + (i >> 1) * 98304u);     \
  } while (0)
#pragma unroll
  for (int u = 0; u < Q_NSL - 1; ++u) Q_LOAD(u * Q_SLOT, u);
  wait_vm<(Q_NSL - 2) * 7>();
  __builtin_amdgcn_s_barrier();
  unsigned sb = (Q_NSL - 1) * Q_SLOT;
  for (int u = 0; u < 32 - (Q_NSL - 1); ++u) {
    Q_LOAD(sb, u + Q_NSL - 1);
    sb = (sb == (Q_NSL - 1) * Q_SLOT) ? 0u : sb + Q_SLOT;
    wait_vm<(Q_NSL - 2) * 7>();
    __builtin_amdgcn_s_barrier();
  }
  for (int t = 0; t < 6; ++t) {
    const unsigned p0 = (unsigned)(t * 21 + (t >= 3 ? 1 : 0) + sw * 7);
#pragma unroll
    for (int i = 0; i < 7; ++i) wo_piece(smem_base, p0 + i, rot, voffO, rsO);
    if (t < 5) wait_vm<(Q_NSL - 2) * 7>();
    __builtin_amdgcn_s_barrier();
  }
}

DI f32x4 mfma0(u32x4 a, u32x4 b) {
  return __builtin_amdgcn_mfma_f32_16x16x32_bf16(__builtin_bit_cast(bf16x8, a), __builtin_bit_cast(bf16x8, b), f32x4{0, 0, 0, 0}, 0, 0, 0);
}
DI u32x4 pack8(const f32x4 a, const f32x4 b) { return u32x4{pk_bf16(a[0], a[1]), pk_bf16(a[2], a[3]), pk_bf16(b[0], b[1]), pk_bf16(b[2], b[3])}; }
template <class OP>
DI float rows4(const float (&v)[4], OP op) {
  const auto a = __builtin_amdgcn_permlane16_swap(__float_as_uint(v[0]), __float_as_uint(v[1]), false, false);
  const auto b = __builtin_amdgcn_permlane16_swap(__float_as_uint(v[2]), __float_as_uint(v[3]), false, false);
  const float t0 = op(__uint_as_float(a[0]), __uint_as_float(a[1])), t1 = op(__uint_as_float(b[0]), __uint_as_float(b[1]));
  const auto c = __builtin_amdgcn_permlane32_swap(__float_as_uint(t0), __float_as_uint(t1), false, false);
  return op(__uint_as_float(c[0]), __uint_as_float(c[1]));
}
DI void bcast4(float t, float (&v)[4]) {
  const auto c = __builtin_amdgcn_permlane32_swap(__float_as_uint(t), __float_as_uint(t), false, false);
  const auto a = __builtin_amdgcn_permlane16_swap(c[0], c[0], false, false);
  const auto b = __builtin_amdgcn_permlane16_swap(c[1], c[1], false, false);
  v[0] = __uint_as_float(a[0]); v[1] = __uint_as_float(a[1]); v[2] = __uint_as_float(b[0]); v[3] = __uint_as_float(b[1]);
}
DI float fmax2(float a, float b) { return __builtin_amdgcn_fmed3f(a, b, __builtin_inff()); }

template <int WM>
DI void qkv_mfma(unsigned smem_base, int lane, int wn, int bm, int bn, bf16* __restrict__ O) {
  const int fr = lane & 15, fq = lane >> 4;
  unsigned bb0, bb1;
  const unsigned ab = smem_base + lane * 16;
  {
    const int q = (lane >> 2) & 3, p = lane & 3;
    const int r = 8 * fq + q;
    const int g = ((r >> 1) & 1) | (((r >> 3) & 1) << 1);
    bb0 = smem_base + 9216 + r * 128 + (((wn * 2) ^ g) * 32) + 8 * p;
    bb1 = bb0 ^ 32u;
  }
  const bool par = fr & 1, c0 = 4 * fq + (fr & 1) <= fr, c1 = 4 * fq + 2 + (fr & 1) <= fr;
  const unsigned s0 = par ? 0x01000c0cu : 0x0c0c0100u, s1 = par ? 0x03020c0cu : 0x0c0c0302u;
  u32x4 sel = {c0 ? s0 : 0x0c0c0c0cu, c1 ? s1 : 0x0c0c0c0cu, c0 ? 0x0c0c0c0cu : s0, c1 ? 0x0c0c0c0cu : s1};
  unsigned hx = ab + QX_HX + wn * 3072;
  asm volatile("" : "+v"(sel), "+v"(hx));
  const rsrc_t rsO = mk_rsrc((char*)O + (size_t)((bm * 2 + WM) * 32 + bn * 2 + wn) * 4096, 4096);
  f32x4 acc[4][6], acch[2];
#pragma unroll
  for (int i = 0; i < 4; ++i)
#pragma unroll
    for (int j = 0; j < 6; ++j) { acc[i][j] = f32x4{0, 0, 0, 0}; asm volatile("" : "+v"(acc[i][j])); }
#pragma unroll
  for (int j = 0; j < 2; ++j) { acch[j] = f32x4{0, 0, 0, 0}; asm volatile("" : "+v"(acch[j])); }
  __builtin_amdgcn_s_setprio(2);
  __builtin_amdgcn_s_barrier();
  STAMPDECL;
  STAMPNW(2, 1);
  u32x4 aX[5], aY[5];
  u32x2 bXl[6], bXh[6], bYl[6], bYh[6];
  unsigned ra = ab, rb0 = bb0, rb1 = bb1, so = 0;
#define Q_READ(AR, BL, BH, R)                                                                                    \
  do {                                                                                                           \
    if ((R) < 12) {                                                                                              \
      constexpr int jb = ((R) < 12 ? (R) : 0) >> 1;                                                              \
      if ((R) & 1) dsr_tr<(jb >> 1) * 4096 + 512>(BH[jb], (jb & 1) ? rb1 : rb0);                                 \
      else dsr_tr<(jb >> 1) * 4096>(BL[jb], (jb & 1) ? rb1 : rb0);                                               \
    } else if ((R) < 16) {                                                                                       \
      constexpr int ia = ((R) >= 12 && (R) < 16) ? (R) - 12 : 0;                                                 \
      dsr128<1024 + WM * 4096 + ia * 1024>(AR[ia], ra);                                                          \
    } else {                                                                                                     \
      dsr128<0>(AR[4], ra);                                                                                      \
    }                                                                                                            \
  } while (0)
#define Q_MFMA(AR, BB, N)                                                                                        \
  do {                                                                                                           \
    if ((N) < 16) MFMA_AV(acc[(N) & 3][((N) < 24 ? (N) : 0) >> 2], BB[((N) < 24 ? (N) : 0) >> 2], AR[(N) & 3]);  \
    else if ((N) < 24) MFMA_AV(acc[(N) & 3][((N) < 24 ? (N) : 0) >> 2], AR[(N) & 3], BB[((N) < 24 ? (N) : 0) >> 2]); \
    else if (WM == 0) MFMA_AV(acch[(N) & 1], BB[2 + ((N) & 1)], AR[4]);                                          \
    else MFMA_AV(acch[(N) & 1], AR[4], BB[4 + ((N) & 1)]);                                                       \
  } while (0)
#define Q_ADV(K)                                                                                                 \
  do {                                                                                                           \
    if ((K) == 0) asm volatile("s_add_u32 %0, %0, %1\n\ts_cmp_eq_u32 %0, %2\n\ts_cselect_b32 %0, 0, %0" : "+s"(so) : "n"(Q_SLOT), "n"(Q_NSL * Q_SLOT) : "scc"); \
    else if ((K) == 1) asm volatile("v_add_u32 %0, %1, %2" : "=v"(rb0) : "s"(so), "v"(bb0));                     \
    else if ((K) == 2) asm volatile("v_add_u32 %0, %1, %2" : "=v"(rb1) : "s"(so), "v"(bb1));                     \
    else asm volatile("v_add_u32 %0, %1, %2" : "=v"(ra) : "s"(so), "v"(ab));                                     \
  } while (0)
#define Q_UNIT(AR, BL, BH, NAR, NBL, NBH)                                                                        \
  do {                                                                                                           \
    WAIT_LGKM0();                                                                                                \
    u32x4 bb[6];                                                                                                 \
    _Pragma("unroll") for (int j = 0; j < 6; ++j) bb[j] = u32x4{BL[j][0], BL[j][1], BH[j][0], BH[j][1]};         \
    SCHED0();                                                                                                    \
    static_for<26>([&](auto N) {                                                                                 \
      constexpr int n = N.value;                                                                                 \
      if constexpr (n == QBAR) __builtin_amdgcn_s_barrier();                                                     \
      Q_MFMA(AR, bb, n);                                                                                         \
      if constexpr (n >= QBAR && n < QBAR + 17) Q_READ(NAR, NBL, NBH, n - QBAR);                                 \
      if constexpr (n >= QBAR + 18 && n < QBAR + 22) Q_ADV(n - QBAR - 18);                                       \
    });                                                                                                          \
    SCHED0();                                                                                                    \
  } while (0)
  static_for<17>([&](auto R) { Q_READ(aX, bXl, bXh, R.value); });
  static_for<4>([&](auto K) { Q_ADV(K.value); });
  for (int u = 0; u < 32; u += 2) {
    Q_UNIT(aX, bXl, bXh, aY, bYl, bYh);
    Q_UNIT(aY, bYl, bYh, aX, bXl, bXh);
  }
  asm volatile("s_waitcnt lgkmcnt(0)\n\ts_nop 7" ::: "memory");
#pragma unroll
  for (int i = 0; i < 5; ++i) asm volatile("" : "+v"(aX[i]));
#pragma unroll
  for (int j = 0; j < 6; ++j) asm volatile("" : "+v"(bXl[j]), "+v"(bXh[j]));
#pragma unroll
  for (int i = 0; i < 4; ++i)
#pragma unroll
    for (int j = 0; j < 6; ++j) asm volatile("" : "+v"(acc[i][j]));
  asm volatile("" : "+v"(acch[0]), "+v"(acch[1]));
  SCHED0();
  STAMP(2, 2); STAMPFLUSH(2, 1);
  u32x4 kk[5], vv[5];
  if (WM == 0) {
    kk[4] = pack8(acc[3][2], acc[3][3]); vv[4] = pack8(acc[3][4], acc[3][5]);
    dsw128<0>(hx, kk[4]); dsw128<1024>(hx, vv[4]);
  } else {
    dsw128<2048>(hx, pack8(acch[0], acch[1]));
  }
  asm volatile("s_waitcnt lgkmcnt(0)" ::: "memory");
  __builtin_amdgcn_s_barrier();
  if (WM == 0) dsr128<2048>(vv[0], hx);
  else { dsr128<0>(kk[0], hx); dsr128<1024>(vv[0], hx); }
  SCHED0();
  if (WM == 0) kk[0] = pack8(acch[0], acch[1]);
#pragma unroll
  for (int i = 0; i < (WM == 0 ? 3 : 4); ++i) kk[i + 1] = pack8(acc[i][2], acc[i][3]);
  const float cs = 0.17677669529663687f * 1.4426950408889634f;
  float l0[4], l1[4], tm[4];
#pragma unroll
  for (int ii = 1; ii < 5; ++ii) {
    const int i = ii & 3;
    const u32x4 q = pack8(acc[i][0], acc[i][1]);
    if (WM == 1 && i == 0) asm volatile("s_waitcnt lgkmcnt(0)" : "+v"(kk[0]), "+v"(vv[0])::"memory");
    const f32x4 sc = mfma0(kk[i + 1], q), sp = mfma0(kk[i], q);
    float p0 = par ? sp[1] : sp[0], p1 = par ? sp[3] : sp[2];
    if (WM == 0 && i == 0 && bm == 0) { p0 = -5.656854249e9f; p1 = -5.656854249e9f; }
    l0[i] = c0 ? (par ? sc[1] : sc[0]) : p0;
    l1[i] = c1 ? (par ? sc[3] : sc[2]) : p1;
    tm[i] = fmax2(l0[i], l1[i]);
  }
#pragma unroll
  for (int i = 0; i < (WM == 0 ? 3 : 4); ++i) vv[i + 1] = pack8(acc[i][4], acc[i][5]);
  bcast4(rows4(tm, [](float a, float b) { return fmax2(a, b); }), tm);
#pragma unroll
  for (int i = 0; i < 4; ++i) {
    l0[i] = __builtin_amdgcn_exp2f((l0[i] - tm[i]) * cs);
    l1[i] = __builtin_amdgcn_exp2f((l1[i] - tm[i]) * cs);
    tm[i] = l0[i] + l1[i];
  }
  bcast4(__builtin_amdgcn_rcpf(rows4(tm, [](float a, float b) { return a + b; })), tm);
  if (WM == 0) asm volatile("s_waitcnt lgkmcnt(0)" : "+v"(vv[0])::"memory");
#pragma unroll
  for (int i = 0; i < 4; ++i) {
    const unsigned pp = pk_bf16(l0[i] * tm[i], l1[i] * tm[i]);
    const u32x4 pr = {__builtin_amdgcn_perm(pp, pp, sel[0]), __builtin_amdgcn_perm(pp, pp, sel[1]),
                      __builtin_amdgcn_perm(pp, pp, sel[2]), __builtin_amdgcn_perm(pp, pp, sel[3])};
    const u32x4 ov = pack8(mfma0(u32x4{vv[i + 1][0], vv[i + 1][1], vv[i][0], vv[i][1]}, pr), mfma0(u32x4{vv[i + 1][2], vv[i + 1][3], vv[i][2], vv[i][3]}, pr));
    asm volatile("buffer_store_dwordx4 %0, %1, %2, 0 offen offset:%3 sc1\n\ts_nop 1" ::"v"(ov), "v"(lane * 16), "s"(rsO), "i"(i * 1024) : "memory");
  }
  STAMP(2, 3);
}

__global__ __launch_bounds__(512, 2) void qkv_attn_kernel(const bf16* __restrict__ H, const bf16* __restrict__ W,
                                                          bf16* O, const bf16* __restrict__ wo, const bf16* __restrict__ X,
                                                          const float* __restrict__ modp, bf16* __restrict__ Y,
                                                          float* __restrict__ stat, unsigned* flags,
                                                          const bf16* __restrict__ wg, const bf16* __restrict__ wu) {
  __shared__ __attribute__((aligned(1024))) char smem[Q_LDS];
  STAMP(2, 0);
  const int tid = threadIdx.x, lane = tid & 63;
  const int wid = __builtin_amdgcn_readfirstlane(tid >> 6);
  const unsigned smem_base = (unsigned)(uintptr_t)smem;
  const int xcd = blockIdx.x & 7, idx = blockIdx.x >> 3;
  const int bm = (xcd >> 2) * 8 + (idx & 7), bn = (xcd & 3) * 4 + (idx >> 3);
  const int rb = xcd * 2 + (idx & 1), cg = idx >> 1;
  const unsigned rot = cg & 7;
  const unsigned vgu = (unsigned)((idx * 8 + (lane >> 3)) * (F_DIM * 2) + xcd * 896 + ((lane & 7) == 7 ? 6 : (lane & 7)) * 128);
  const unsigned cnt = smem_base + QX_CNT, cntl = cnt + lane * 4;
  if (wid < 4) {
    if (wid & 1) qkv_mfma<1>(smem_base, lane, wid >> 1, bm, bn, O);
    else qkv_mfma<0>(smem_base, lane, wid >> 1, bm, bn, O);
  } else {
    const rsrc_t rsO = mk_rsrc(wo, D_DIM * D_DIM * 2);
    const unsigned voffO = (unsigned)((lane >> 3) * 2048 + cg * 128 + ((((lane & 7) >> 1) ^ (lane >> 4)) * 32) + (lane & 1) * 16);
    if (wid < 7) qkv_loader(smem_base, lane, wid - 4, bm * 128, bn * 64, H, W, rot, voffO, rsO);
    else {
      unsigned gv[8];
      const rsrc_t rsG = mk_rsrc(modp, 4 * 6144 * 4);
#pragma unroll
      for (int i = 0; i < 8; ++i) { asm volatile("" : "=v"(gv[i])); bload1_v<0>(gv[i], (unsigned)((cg * 64 + lane) * 4), rsG, (i >> 1) * 24576 + 8192 + (i & 1) * 8192); }
      dsw32<0>(cntl, 0u); dsw32<256>(cntl, 0u);
      asm volatile("s_waitcnt lgkmcnt(0)" ::: "memory");
      for (int i = 0; i < 3; ++i) __builtin_amdgcn_s_barrier();
      asm volatile("s_waitcnt vmcnt(0)" : "+v"(gv[0]), "+v"(gv[1]), "+v"(gv[2]), "+v"(gv[3]), "+v"(gv[4]), "+v"(gv[5]), "+v"(gv[6]), "+v"(gv[7])::"memory");
      const float ga = (__uint_as_float(gv[0]) + __uint_as_float(gv[2])) + (__uint_as_float(gv[4]) + __uint_as_float(gv[6]));
      const float cb = (__uint_as_float(gv[1]) + __uint_as_float(gv[3])) + (__uint_as_float(gv[5]) + __uint_as_float(gv[7]));
      dsw32<0>(smem_base + QX_GT + lane * 4, __float_as_uint(rbf(ga)));
      dsw32<256>(smem_base + QX_GT + lane * 4, __float_as_uint(gain_nz(cb)));
      asm volatile("s_waitcnt lgkmcnt(0)" ::: "memory");
      {
        int nxt = TS + idx % TP, ev = 0;
        for (int i = 2; i < 27; ++i) {
          if (TW > 0 && i == nxt && ev < TN) {
#pragma unroll
            for (int j = 0; j < TW; ++j) gu_touch(smem_base, 7 - (ev * TW + j), vgu, wg, wu, GNT & 1);
            nxt += TP; ++ev;
          }
          __builtin_amdgcn_s_barrier();
        }
      }
      wo_piece(smem_base, 63u, rot, voffO, rsO);
      for (int i = 28; i < 33; ++i) __builtin_amdgcn_s_barrier();
      wo_piece(smem_base, 127u, rot, voffO, rsO);
    }
    __builtin_amdgcn_s_barrier();
    if (wid - 4 < T1) { gu_touch(smem_base, T2 + wid - 4, vgu, wg, wu, GNT & 2); wait_vm<(GNT & 2) ? 2 : 1>(); }
    else wait_vm<0>();
    lds_add(cntl, 1u);
  }
  const int fr = lane & 15, fq = lane >> 4;
  const int kq = wid & 3, rg = rb * 2 + (wid >> 2);
  lds_wait<2>(cnt, 4);
  u32x2 wl[8][4], wh[8][4];
  {
    const int q = (lane >> 2) & 3, p = lane & 3, r = 4 * fq + q;
    const unsigned wb = smem_base + (kq < 2 ? QX_WH + kq * 32768 : (kq - 2) * 32768) + r * 128 + (((r >> 1) & 3) * 32) + 8 * p;
    unsigned wa[4];
#pragma unroll
    for (int f = 0; f < 4; ++f) wa[f] = wb ^ (f * 32);
    static_for<32>([&](auto E) {
      constexpr int s = E.value >> 2, f = E.value & 3;
      dsr_tr<s * 4096>(wl[s][f], wa[f]);
      dsr_tr<s * 4096 + 2048>(wh[s][f], wa[f]);
    });
  }
  const rsrc_t rsA = mk_rsrc((const char*)O + (size_t)rg * 131072, 131072);
  const rsrc_t rsF = mk_rsrc(flags + (rb * 16 + kq * 4) * 32, 4 * 128);
  unsigned voffA[4], hs[8];
#pragma unroll
  for (int i = 0; i < 4; ++i) { voffA[i] = lane * 16 + ((kq + 1 + i) & 3) * 1024; asm volatile("" : "+v"(voffA[i])); }
#pragma unroll
  for (int i = 0; i < 8; ++i) { hs[i] = (kq * 8 + ((i + rot) & 7)) * 4096; asm volatile("" : "+s"(hs[i])); }
  asm volatile("" ::"s"(rsA), "s"(rsF));
  if (wid < 4) {
    const rsrc_t rsFo = mk_rsrc(flags + (bm * 16 + bn) * 32, 4);
    asm volatile("s_waitcnt vmcnt(0)" ::"s"(rsFo) : "memory");
    if ((lds_add(cntl + 256, 1u) & 15u) == 3u)
      asm volatile("buffer_store_dword %0, %1, %2, 0 offen sc1" ::"v"(1u), "v"(lane * 4), "s"(rsFo) : "memory");
    STAMP(2, 4);
  } else {
    lds_add(cntl + 256, 16u);
  }
  lds_wait<2>(cnt + 256, 68);
  u32x4 wf[8][4];
#pragma unroll
  for (int s = 0; s < 8; ++s)
#pragma unroll
    for (int f = 0; f < 4; ++f) {
      asm volatile("" : "+v"(wl[s][f]), "+v"(wh[s][f]));
      wf[s][f] = u32x4{wl[s][f][0], wl[s][f][1], wh[s][f][0], wh[s][f][1]};
    }
  {
    unsigned spins = 0;
    for (;;) {
      unsigned fl;
      asm volatile("buffer_load_dword %0, %1, %2, 0 offen sc1\n\ts_waitcnt vmcnt(0)" : "=v"(fl) : "v"((lane & 3) * 128), "s"(rsF) : "memory");
      if (__builtin_amdgcn_ballot_w64(fl == 0u) == 0ull || ++spins >= (1u << 20)) break;
      __builtin_amdgcn_s_sleep(1);
    }
  }
  STAMP(3, 0);
  const unsigned voffL = lane * 16;
  u32x4 af[32], xv[2], pr[3][4], gt[4], ct[4];
  f32x4 acc[4][4];
  auto aload = [&](auto N) {
    constexpr int n = N.value;
    asm volatile("" : "=v"(af[n]));
    bload_sc1<0>(af[n], voffA[n >> 3], rsA, hs[n & 7]);
  };
  static_for<GDEP>([&](auto N) { aload(N); });
  const unsigned sl = smem_base + voffL + (wid >> 2) * 49152;
  const int sw = ((fq & 1) << 1) | (fq >> 1);
  SCHED0();
  static_for<32>([&](auto N) {
    constexpr int n = N.value, ps = n >> 3, s = n & 7, XG = 32 - GDEP;
    constexpr int last = n - 1 + GDEP < 31 ? n - 1 + GDEP : 31;
    asm volatile("s_waitcnt vmcnt(%1)" : "+v"(af[n]) : "n"(last - n + (n > XG ? 2 : 0)) : "memory");
    if constexpr (n == 28) { WAIT_LGKM0(); __builtin_amdgcn_s_barrier(); }
    static_for<4>([&](auto F) {
      constexpr int f = F.value;
      if constexpr (s == 0) asm volatile("v_mfma_f32_16x16x32_bf16 %0, %1, %2, 0" : "=&v"(acc[ps][f]) : "v"(wf[s][f]), "v"(af[n]));
      else MFMA_AV(acc[ps][f], wf[s][f], af[n]);
      if constexpr (f == 0 && n + GDEP < 32) aload(ic<(n + GDEP < 32 ? n + GDEP : 0)>{});
      if constexpr (f == 0 && n == XG) {
        const rsrc_t rsX = mk_rsrc(X, S_LEN * D_DIM * 2);
        const unsigned vx = (unsigned)(((rg * 64 + kq * 16 + fr) * D_DIM + cg * 64 + sw * 8) * 2);
        asm volatile("" : "=v"(xv[0]), "=v"(xv[1]));
        bload_v<0>(xv[0], vx, rsX, 0); bload_v<64>(xv[1], vx, rsX, 0);
      }
      if constexpr (ps > 0 && (s == 2 || s == 3) && (f & 1)) {
        constexpr int e = (s - 2) * 2 + (f >> 1);
        dsw128<(ps - 1) * 4096 + e * 1024>(sl + ((kq + ps) & 3) * 12288, __builtin_bit_cast(u32x4, acc[ps - 1][e]));
      }
      if constexpr (n >= 28 && n < 31) dsr128<(n - 28) * 4096 + f * 1024>(pr[n - 28][f], sl + kq * 12288);
      if constexpr (n == 31) { dsr128<f * 64>(gt[f], smem_base + QX_GT + fq * 16); dsr128<256 + f * 64>(ct[f], smem_base + QX_GT + fq * 16); }
    });
  });
  asm volatile("s_waitcnt vmcnt(0)" : "+v"(xv[0]), "+v"(xv[1])::"memory");
  asm volatile("s_waitcnt lgkmcnt(0)\n\ts_nop 4" ::: "memory");
  STAMP(3, 2);
  f32x4 fin[4], yv[4];
#pragma unroll
  for (int f = 0; f < 4; ++f) asm volatile("" : "+v"(acc[3][f]), "+v"(pr[0][f]), "+v"(pr[1][f]), "+v"(pr[2][f]), "+v"(gt[f]), "+v"(ct[f]));
#pragma unroll
  for (int c = 0; c < 2; ++c) {
    const auto x0 = __builtin_amdgcn_permlane16_swap(xv[c][0], xv[c][2], false, false);
    const auto x1 = __builtin_amdgcn_permlane16_swap(xv[c][1], xv[c][3], false, false);
#pragma unroll
    for (int h = 0; h < 2; ++h) {
      const int f = 2 * c + h;
      const f32x4 a = (acc[3][f] + __builtin_bit_cast(f32x4, pr[0][f])) + (__builtin_bit_cast(f32x4, pr[1][f]) + __builtin_bit_cast(f32x4, pr[2][f]));
      const f32x4 xf = {bf_lo(x0[h]), bf_hi(x0[h]), bf_lo(x1[h]), bf_hi(x1[h])};
      fin[f] = xf + __builtin_bit_cast(f32x4, gt[f]) * a;
      yv[f] = fin[f] * __builtin_bit_cast(f32x4, ct[f]);
    }
    const auto y0 = __builtin_amdgcn_permlane16_swap(pk_bf16(yv[2 * c][0], yv[2 * c][1]), pk_bf16(yv[2 * c + 1][0], yv[2 * c + 1][1]), false, false);
    const auto y1 = __builtin_amdgcn_permlane16_swap(pk_bf16(yv[2 * c][2], yv[2 * c][3]), pk_bf16(yv[2 * c + 1][2], yv[2 * c + 1][3]), false, false);
    gst128((char*)Y + ((size_t)(rg >> 2) * 32 + cg * 2 + c) * 16384 + ((rg & 3) * 4 + kq) * 1024 + (sw * 16 + fr) * 16,
           u32x4{y0[0], y1[0], y0[1], y1[1]});
  }
  const f32x4 s4 = (fin[0] + fin[1]) + (fin[2] + fin[3]);
  const float s = rowsum4((s4[0] + s4[1]) + (s4[2] + s4[3]));
  const float mb = s * (1.0f / 64.0f);
  f32x4 q4 = {0, 0, 0, 0};
#pragma unroll
  for (int f = 0; f < 4; ++f) { const f32x4 d = fin[f] - mb; q4 += d * d; }
  const float m2 = rowsum4((q4[0] + q4[1]) + (q4[2] + q4[3]));
  if (fq == 0) *reinterpret_cast<f32x2*>(stat + (((size_t)(cg >> 1) * S_LEN + rg * 64 + kq * 16 + fr) * 2 + (cg & 1)) * 2) = f32x2{s, m2};
  STAMP(3, 3);
  STAMP(3, 4);
  if (wid < T2) gu_touch(smem_base, wid, vgu, wg, wu, GNT & 4);
}

#define G3NT 1
#define NBR 0
#define TPOS 6
#define NBC 1
#define G3_TW "12"
#define G3_DUMMY() do { } while (0)
constexpr int G3_CT = 131072 + 4096 + 1024;
#define G3_STAGE(SLOT, P, U)                                                                               \
  do {                                                                                                     \
    if ((P) == 0) bload_lds<0>(m0w + (SLOT) * 32768, voffA, rsA, (unsigned)(U) * 16384u);                  \
    else if ((P) == 1) bload_lds<1024>(m0w + (SLOT) * 32768, voffA, rsA, (unsigned)(U) * 16384u);          \
    else if (G3NT && (P) == 2) bload_lds_nt<0>(m0w + (SLOT) * 32768 + 16384, voffB0, rsB, (unsigned)(U) * 229376u);   \
    else if (G3NT) bload_lds_nt<0>(m0w + (SLOT) * 32768 + 16384 + 1024, voffB1, rsB, (unsigned)(U) * 229376u);          \
    else if ((P) == 2) bload_lds<0>(m0w + (SLOT) * 32768 + 16384, voffB0, rsB, (unsigned)(U) * 229376u);   \
    else bload_lds<0>(m0w + (SLOT) * 32768 + 16384 + 1024, voffB1, rsB, (unsigned)(U) * 229376u);          \
  } while (0)

#define G3ROT 1
DI int g3_line(int gx, int p) {
  const int t = p + (G3ROT ? (3 + gx) & 7 : 0);
  return 7 * gx + (t >= 7 ? t - 7 : t);
}
template <int FN>
DI void g3_body(char* smem, unsigned smem_base, int wm, int wn, int wid, int lane, int bm, int bn, rsrc_t rsA, rsrc_t rsB,
                rsrc_t rsP, unsigned voffA, unsigned voffB0, unsigned voffB1, bf16* __restrict__ act) {
  constexpr int NB = 2 * FN, NM = 4 * NB, RD = 5 + 2 * NB;
  const int fr = lane & 15, fq = lane >> 4;
  f32x4 acc[4][NB], accC[2];
  accC[0] = f32x4{0, 0, 0, 0}; accC[1] = f32x4{0, 0, 0, 0};
  asm volatile("" : "+v"(accC[0]), "+v"(accC[1]));
  const int rot = wm % FN;
  unsigned cad = smem_base + G3_CT + (fr < 4 ? fr * 64 : 256) + fq * 16;
#pragma unroll
  for (int i = 0; i < 4; ++i)
#pragma unroll
    for (int j = 0; j < NB; ++j) { acc[i][j] = f32x4{0, 0, 0, 0}; asm volatile("" : "+v"(acc[i][j])); }
  unsigned aad[2], bad[2][FN];
  aad[0] = smem_base + wm * 4096 + lane * 16; aad[1] = aad[0] + 65536;
  {
    const int q = (lane >> 2) & 3, p = lane & 3;
    const int r = 8 * fq + q;
    const int sw = (r & 3) | (((r >> 3) & 1) << 2);
#pragma unroll
    for (int j = 0; j < FN; ++j) {
      const int gi = wn * 4 + (j + rot) % FN + (bn & 1);
      bad[0][j] = smem_base + 16384 + r * 256 + ((gi ^ sw) * 32) + 8 * p;
      bad[1][j] = bad[0][j] + 65536;
    }
  }
  u32x4 aX[5], aY[5];
  u32x2 bXl[NB], bXh[NB], bYl[NB], bYh[NB];
  const unsigned m0w = smem_base + wid * 2048;
  const unsigned voffP = lane * 16;
#define G3_READ(AR, BL, BH, SLOT, R)                                                                       \
  do {                                                                                                     \
    if ((R) < 2 * NB) {                                                                                    \
      constexpr int jb = ((R) < 2 * NB ? (R) : 0) >> 1;                                                    \
      constexpr int off = ((SLOT) & 1) * 32768 + (jb / FN) * 8192 + ((R) & 1) * 1024;                      \
      if ((R) & 1) dsr_tr<off>(BH[jb], bad[(SLOT) >> 1][jb % FN]);                                         \
      else dsr_tr<off>(BL[jb], bad[(SLOT) >> 1][jb % FN]);                                                 \
    } else if ((R) < 2 * NB + 4) {                                                                         \
      constexpr int ia = ((R) >= 2 * NB && (R) < 2 * NB + 4) ? (R) - 2 * NB : 0;                           \
      dsr128<((SLOT) & 1) * 32768 + ia * 1024>(AR[ia], aad[(SLOT) >> 1]);                                  \
    } else {                                                                                               \
      if ((SLOT) == 0) cad += 1280;                                      \
      dsr128<(SLOT) * 320>(AR[4], cad);                                                                    \
    }                                                                                                      \
  } while (0)
  auto rd = [&](auto SL, auto RR, u32x4 (&AR)[5], u32x2 (&BL)[NB], u32x2 (&BH)[NB]) { G3_READ(AR, BL, BH, SL.value, RR.value); };
#define G3_UNIT(AR, BL, BH, NAR, NBL, NBH, SLOT, U)                                                        \
  do {                                                                                                     \
    WAIT_LGKM0();                                                                                          \
    u32x4 bb[NB];                                                                                          \
    _Pragma("unroll") for (int j = 0; j < NB; ++j) bb[j] = u32x4{BL[j][0], BL[j][1], BH[j][0], BH[j][1]};  \
    SCHED0();                                                                                              \
    asm volatile("s_nop 1");                                                                               \
    static_for<NM>([&](auto N) {                                                                           \
      constexpr int n = N.value;                                                                           \
      if constexpr (n == NM / 2) {                                                                         \
        wait_vm<NBR ? 10 : 8>(); __builtin_amdgcn_s_barrier();                                             \
      }                                                                                                    \
      if constexpr (n == NM / 2 + TPOS) {                                                                  \
                      \
        if (NBR) bload_lds<0>(smem_base + 131072 + 4096, voffP, rsP, (((U) & 7) == wid) ? (unsigned)(U) * 1024u : 0x40000000u); \
        else if (((U) & 7) == wid) bload_lds<0>(smem_base + 131072 + 4096, voffP, rsP, (unsigned)(U) * 1024u);  \
      }                                                                                                    \
      MFMA_AV(acc[n & 3][n >> 2], bb[n >> 2], AR[n & 3]);                                                  \
      if constexpr (n < NM / 2) {                                                                          \
        if constexpr (n % 3 == 1 && n / 3 < 4) G3_STAGE(((SLOT) + 3) & 3, n / 3, (U) + 3);                 \
      } else {                                                                                             \
        constexpr int r0 = (n - NM / 2) * 2;                                                               \
        if constexpr (r0 < RD) rd(ic<((SLOT) + 1) & 3>{}, ic<(r0 < RD ? r0 : 0)>{}, NAR, NBL, NBH);        \
        if constexpr (r0 + 1 < RD) rd(ic<((SLOT) + 1) & 3>{}, ic<(r0 + 1 < RD ? r0 + 1 : 0)>{}, NAR, NBL, NBH); \
      }                                                                                                    \
    });                                                                                                    \
    if (NBC || FN == 4 || wm < 3) {                                                                        \
      MFMA_AV(accC[0], bb[0], AR[4]);                                                                      \
      MFMA_AV(accC[1], bb[FN], AR[4]);                                                                     \
    }                                                                                                      \
    SCHED0();                                                                                              \
  } while (0)

  if (NBR) asm volatile("s_waitcnt vmcnt(10) lgkmcnt(0)" ::: "memory");
  else asm volatile("s_waitcnt vmcnt(8) lgkmcnt(0)" ::: "memory");
  __builtin_amdgcn_s_barrier();
  STAMPDECL;
  STAMPNW(5, 1);
  cad -= 1280;
  static_for<RD>([&](auto RR) { rd(ic<0>{}, RR, aX, bXl, bXh); });
  for (int u = 0; u < 32; u += 4) {
    G3_UNIT(aX, bXl, bXh, aY, bYl, bYh, 0, u);
    G3_UNIT(aY, bYl, bYh, aX, bXl, bXh, 1, u + 1);
    G3_UNIT(aX, bXl, bXh, aY, bYl, bYh, 2, u + 2);
    G3_UNIT(aY, bYl, bYh, aX, bXl, bXh, 3, u + 3);
  }
  asm volatile("s_waitcnt lgkmcnt(0)\n\ts_nop 15" ::: "memory");
#pragma unroll
  for (int i = 0; i < 5; ++i) asm volatile("" : "+v"(aX[i]));
#pragma unroll
  for (int j = 0; j < NB; ++j) asm volatile("" : "+v"(bXl[j]), "+v"(bXh[j]));
  SCHED0();
  STAMP(5, 2);
  STAMPFLUSH(5, 1);
  const f32x2* rstat = reinterpret_cast<const f32x2*>(smem + 131072);
  float* colc = reinterpret_cast<float*>(smem + 131072 + 2048);
#pragma unroll
  for (int m = 0; m < 2; ++m) {
    f32x4 v;
#pragma unroll
    for (int r = 0; r < 4; ++r) v[r] = accC[m][r] + __shfl_xor(accC[m][r], 1);
    if ((fr == 0 || fr == 2) && wm < FN) *reinterpret_cast<f32x4*>(colc + (m * 2 + (fr >> 1)) * 128 + (wn * 4 + rot) * 16 + fq * 4) = v;
  }
  asm volatile("s_waitcnt lgkmcnt(0)" ::: "memory");
  __builtin_amdgcn_s_barrier();
  asm volatile("" ::: "memory");
  f32x2 rs[4];
#pragma unroll
  for (int i = 0; i < 4; ++i) rs[i] = rstat[wm * 64 + i * 16 + fr];
#pragma unroll
  for (int j = 0; j < FN; ++j) {
    const int jg = (j + rot) % FN;
    const int cl = (wn * 4 + jg) * 16 + fq * 4;
    const f32x4 csg = *reinterpret_cast<const f32x4*>(colc + cl), bg = *reinterpret_cast<const f32x4*>(colc + 128 + cl);
    const f32x4 csu = *reinterpret_cast<const f32x4*>(colc + 256 + cl), bu = *reinterpret_cast<const f32x4*>(colc + 384 + cl);
    u32x2 pk[4];
#pragma unroll
    for (int i = 0; i < 4; ++i) {
#pragma unroll
      for (int hh = 0; hh < 2; ++hh) {
        const f32x2 ag = hh ? hi2(acc[i][j]) : lo2(acc[i][j]), au = hh ? hi2(acc[i][FN + j]) : lo2(acc[i][FN + j]);
        const f32x2 g = rs[i][0] * ag + (rs[i][1] * (hh ? hi2(csg) : lo2(csg)) + (hh ? hi2(bg) : lo2(bg)));
        const f32x2 uu = rs[i][0] * au + (rs[i][1] * (hh ? hi2(csu) : lo2(csu)) + (hh ? hi2(bu) : lo2(bu)));
        const f32x2 ea = g * -1.4426950408889634f;
        f32x2 e = {__builtin_amdgcn_exp2f(ea[0]), __builtin_amdgcn_exp2f(ea[1])};
        e = e + 1.0f;
        const f32x2 rc = {__builtin_amdgcn_rcpf(e[0]), __builtin_amdgcn_rcpf(e[1])};
        const f32x2 o = (g * uu) * rc;
        pk[i][hh] = pk_bf16(o[0], o[1]);
      }
    }
    const int Jv = (bn & 3) * 7 + wn * 4 + jg;
    const int J = g3_line(bn >> 2, Jv >> 2) * 4 + (Jv & 3);
    char* dst = (char*)act + ((size_t)((bm * 4 + wm) * (F_DIM / 32) + (J >> 1)) * 4 + (fq & 1)) * 1024 + (((J & 1) * 2 + (fq >> 1)) * 16 + fr) * 16;
#pragma unroll
    for (int i = 0; i < 4; i += 2) {
      const auto s0 = __builtin_amdgcn_permlane16_swap(pk[i][0], pk[i + 1][0], false, false);
      const auto s1 = __builtin_amdgcn_permlane16_swap(pk[i][1], pk[i + 1][1], false, false);
      gst128(dst + i * 1024, u32x4{s0[0], s1[0], s0[1], s1[1]});
    }
  }
}

__global__ __launch_bounds__(512, 2) void gemm3_kernel(const bf16* __restrict__ Y, const bf16* __restrict__ Wg,
                                                       const bf16* __restrict__ Wu, bf16* __restrict__ act,
                                                       const float* __restrict__ stat, const float* __restrict__ modp,
                                                       const bf16* __restrict__ Wd) {
  __shared__ __attribute__((aligned(16))) char smem[G3_CT + 33 * 320];
  STAMP(5, 0);
  const int tid = threadIdx.x, lane = tid & 63;
  const int wid = __builtin_amdgcn_readfirstlane(tid >> 6);
  const int wm = wid & 3, wn = wid >> 2;
  const int xcd = blockIdx.x & 7, idx = blockIdx.x >> 3;
  const int bm = idx & 7, bn = xcd * 4 + (idx >> 3);
  const unsigned smem_base = (unsigned)(uintptr_t)smem;
  const rsrc_t rsA = mk_rsrc((const char*)Y + (size_t)bm * 32 * 16384, 32 * 16384);
  const rsrc_t rsB = mk_rsrc(wn ? Wu : Wg, D_DIM * F_DIM * 2);
  const rsrc_t rsP = mk_rsrc((const char*)Wd + (size_t)blockIdx.x * 28672, 28672);
  const unsigned voffA = wid * 2048 + lane * 16;
  unsigned voffB[2];
#pragma unroll
  for (int i = 0; i < 2; ++i) {
    const int r = (wm * 2 + i) * 4 + (lane >> 4);
    const int sw = (r & 3) | (((r >> 3) & 1) << 2);
    const int c16 = lane & 15;
    const int vb = (((bn & 3) * 224) & ~63) + ((c16 >> 1) ^ sw) * 32 + (c16 & 1) * 16;
    voffB[i] = (unsigned)(r * (F_DIM * 2) + g3_line(bn >> 2, vb >> 7) * 128 + (vb & 127));
  }
  const unsigned m0w = smem_base + wid * 2048;
  const unsigned voffB0 = voffB[0], voffB1 = voffB[1];
  if (wn == 0) {
    u32x4 tv[8];
#pragma unroll
    for (int i = 0; i < 8; ++i) asm volatile("" : "=v"(tv[i]));
    const rsrc_t rsS = mk_rsrc(stat, S_LEN * 128);
    const unsigned vo = (unsigned)((bm * 256 + tid) * 16);
    static_for<8>([&](auto I) { bload_v<0>(tv[I.value], vo, rsS, I.value * 32768); });
    G3_STAGE(0, 0, 0); G3_STAGE(0, 1, 0); G3_STAGE(0, 2, 0); G3_STAGE(0, 3, 0);
    G3_STAGE(1, 0, 1); G3_STAGE(1, 1, 1); G3_STAGE(1, 2, 1); G3_STAGE(1, 3, 1);
    G3_DUMMY();
    G3_STAGE(2, 0, 2); G3_STAGE(2, 1, 2); G3_STAGE(2, 2, 2); G3_STAGE(2, 3, 2);
    G3_DUMMY();
    asm volatile("s_waitcnt vmcnt(" G3_TW ")" : "+v"(tv[0]), "+v"(tv[1]), "+v"(tv[2]), "+v"(tv[3]), "+v"(tv[4]), "+v"(tv[5]), "+v"(tv[6]), "+v"(tv[7])::"memory");
    float s = 0;
#pragma unroll
    for (int i = 0; i < 8; ++i) s += __uint_as_float(tv[i][0]) + __uint_as_float(tv[i][2]);
    const float mean = s * (1.0f / D_DIM);
    float m2 = 0;
#pragma unroll
    for (int i = 0; i < 8; ++i) {
      const float d0 = __uint_as_float(tv[i][0]) * (1.0f / 64.0f) - mean, d1 = __uint_as_float(tv[i][2]) * (1.0f / 64.0f) - mean;
      m2 += __uint_as_float(tv[i][1]) + __uint_as_float(tv[i][3]) + 64.0f * (d0 * d0 + d1 * d1);
    }
    const float rstd = rsqrtf(m2 * (1.0f / D_DIM) + 1e-5f);
    reinterpret_cast<f32x2*>(smem + 131072)[tid] = f32x2{rstd, -mean * rstd};
    g3_body<4>(smem, smem_base, wm, wn, wid, lane, bm, bn, rsA, rsB, rsP, voffA, voffB0, voffB1, act);
  } else {
    const rsrc_t rsC = mk_rsrc(modp, 4 * 6144 * 4);
    const int t = tid - 256;
    const unsigned vo = (unsigned)((3072 + t * 4) * 4);
    u32x4 tc[8];
#pragma unroll
    for (int i = 0; i < 8; ++i) { asm volatile("" : "=v"(tc[i])); bload_v<0>(tc[i], vo, rsC, (i >> 1) * 24576 + (i & 1) * 4096); }
    G3_STAGE(0, 0, 0); G3_STAGE(0, 1, 0); G3_STAGE(0, 2, 0); G3_STAGE(0, 3, 0);
    G3_STAGE(1, 0, 1); G3_STAGE(1, 1, 1); G3_STAGE(1, 2, 1); G3_STAGE(1, 3, 1);
    G3_DUMMY();
    G3_STAGE(2, 0, 2); G3_STAGE(2, 1, 2); G3_STAGE(2, 2, 2); G3_STAGE(2, 3, 2);
    G3_DUMMY();
    asm volatile("s_waitcnt vmcnt(" G3_TW ")" : "+v"(tc[0]), "+v"(tc[1]), "+v"(tc[2]), "+v"(tc[3]), "+v"(tc[4]), "+v"(tc[5]), "+v"(tc[6]), "+v"(tc[7])::"memory");
    f32x4 sv = (__builtin_bit_cast(f32x4, tc[0]) + __builtin_bit_cast(f32x4, tc[2])) + (__builtin_bit_cast(f32x4, tc[4]) + __builtin_bit_cast(f32x4, tc[6]));
    f32x4 gw = (__builtin_bit_cast(f32x4, tc[1]) + __builtin_bit_cast(f32x4, tc[3])) + (__builtin_bit_cast(f32x4, tc[5]) + __builtin_bit_cast(f32x4, tc[7]));
#pragma unroll
    for (int e = 0; e < 4; ++e) { gw[e] = gain_nz(gw[e]); sv[e] = rbf(sv[e]); }
    u32x2 gh, gl, sh, sl;
    split4(gw, gh, gl); split4(sv, sh, sl);
    char* d = smem + G3_CT + (t >> 3) * 320 + ((t >> 1) & 3) * 16 + (t & 1) * 8;
    *reinterpret_cast<u32x2*>(d) = gh; *reinterpret_cast<u32x2*>(d + 64) = gl;
    *reinterpret_cast<u32x2*>(d + 128) = sh; *reinterpret_cast<u32x2*>(d + 192) = sl;
    *reinterpret_cast<u32x2*>(d + 256) = u32x2{0, 0};
    g3_body<3>(smem, smem_base, wm, wn, wid, lane, bm, bn, rsA, rsB, rsP, voffA, voffB0, voffB1, act);
  }
  STAMP(5, 3);
  STAMP(5, 4);
}

#define XST 0
#define G4NF 11
#define HFS 3
#define HFL 4
#define HCK 5
#define XMAP 1
template <int OFF>
DI void bstore_sc1(u32x4 d, unsigned voff, rsrc_t rs, unsigned soff) {
  asm volatile("buffer_store_dwordx4 %0, %1, %2, %3 offen offset:%4 sc1\n\ts_nop 1" ::"v"(d), "v"(voff), "s"(rs), "s"(soff), "i"(OFF) : "memory");
}
#define DPERM(TB, Q, SL, PP)                                                                       \
  do {                                                                                             \
    if ((Q) & 1) PERM_HI(T[TB][1][(Q) >> 1], braw[SL][((Q) >> 1) * 2][PP], braw[SL][((Q) >> 1) * 2 + 1][PP]); \
    else PERM_LO(T[TB][0][(Q) >> 1], braw[SL][((Q) >> 1) * 2][PP], braw[SL][((Q) >> 1) * 2 + 1][PP]);         \
  } while (0)
namespace g4 {
constexpr int NF = G4NF, NH = 28 - 2 * NF, NS = NF + NH, HEP = NH - 3;
constexpr int nslot(int g) { return g < NF ? 64 : 32; }
constexpr int wslot(int g) { return g < NF - 1 ? 48 : (g == NF - 1 ? 56 : 24); }
constexpr int ring(int g, int n) {
  if (g < NF - 1) {
    if (n >= 48 || n % 3 != 1) return -1;
    const int l = n / 3;
    if (g + 2 < NF) return (g + 2) * 16 + l;
    return l < 12 ? (g + 2) * 16 + (l < 8 ? l : l + 4) : -1;
  }
  if (g == NF - 1) { if (n >= 48 || n % 4 != 1) return -1; const int l = n / 4; return (g + 2) * 16 + (l < 8 ? l : l + 4); }
  if (g == NF) { if (n >= 24) return -1; const int l = n % 12; return (g + 2 + n / 12) * 16 + (l < 8 ? l : l + 4); }
  if (g + 3 >= NS || n >= 24 || (n & 1)) return -1;
  const int l = n >> 1;
  return (g + 3) * 16 + (l < 8 ? l : l + 4);
}
constexpr int pwrite(int g, int n) {
  const int m = g == NF - 1 ? n : 64 + n;
  for (int e = 0; e < 24; ++e) if (12 + (13 * e) / 4 == m) return e;
  return -1;
}
constexpr int red0(int n) { return n < 8 ? 0 : (64 * (n - 8)) / 24; }
#define SPS 4
constexpr int sslot(int f) {
  const int ready = (f >> 2) * 32 + 8 + (((f & 3) + 1) * 16 * 24 + 63) / 64;
  const int paced = (f / SPS) * 32 + 15 + (f % SPS) * (32 / SPS);
  int m = ready > paced ? ready : paced;
  if ((m & 1) == 0) ++m;
  return m;
}
constexpr int dstore_f(int g, int n) {
  for (int f = 0; f < 8; ++f) if (sslot(f) == (g - NF) * 32 + n) return f;
  return -1;
}
constexpr bool dstore(int g, int n) { return g >= NF && dstore_f(g, n) >= 0; }
constexpr int vmops(int g, int n) {
  int c = (ring(g, n) >= 0 ? 1 : 0) + (dstore(g, n) ? 1 : 0);
  const int h = g - NF;
  if (h == HFS && n == 24) c += 1;
  if (h == HFL && n == 25) c += 1;
  if (h == HCK && n >= 24) c += 1;
  if (h == HEP && (n == 1 || n == 3)) c += 4;
  return c;
}
constexpr int between(int g0, int n0, int g1, int n1) {
  int c = 0;
  for (int g = g0; g <= g1; ++g)
    for (int n = (g == g0 ? n0 + 1 : 0); n < (g == g1 ? n1 : nslot(g)); ++n) c += vmops(g, n);
  return c;
}
constexpr int imin(int a, int b) { return a < b ? a : b; }
constexpr int waitcnt(int g) {
  int c = 63;
  if (g + 1 < NS && g + 1 >= 2) {
    for (int s = 0; s <= g; ++s)
      for (int n = 0; n < nslot(s); ++n)
        if (ring(s, n) == (g + 1) * 16 + 15) c = imin(c, between(s, n, g, wslot(g)) + (dstore(s, n) ? 1 : 0));
  }
  if (g == 0) c = 16;
  if (g - NF == HFS) c = imin(c, between(NF + sslot(7) / 32, sslot(7) % 32, g, 24));
  if (g - NF == HCK) c = imin(c, between(NF + HFL, 25, g, 24));
  return c;
}
constexpr int bslot(int b) { return b < NF + 3 ? b % 3 : ((b - NF) % 4 == 3 ? 3 : (NF + (b - NF) % 4) % 3); }
}
__global__ __launch_bounds__(256, 1) void gemm4_kernel(const bf16* __restrict__ At, const bf16* __restrict__ W,
                                                       const bf16* __restrict__ Yt, const float* __restrict__ modp,
                                                       bf16* __restrict__ C, float* xbuf, unsigned* flags) {
  using namespace g4;
  constexpr int KB = F_DIM / 32;
  constexpr int GT = 96 * 1024;
  __shared__ __attribute__((aligned(16))) char smem[GT + 1024];
  STAMPDECL;
  STAMP(6, 0);
  const int tid = threadIdx.x, lane = tid & 63;
  const int w = __builtin_amdgcn_readfirstlane(tid >> 6);
  const int xcd = blockIdx.x & 7, idx = blockIdx.x >> 3;
  const int kh = xcd & 1, bm = ((xcd >> 1) & 1) * 8 + (idx & 7), bn = (xcd >> 2) * 4 + (idx >> 3);
  const unsigned partner = blockIdx.x ^ 1;
  const int n0 = bn * 128, bm64 = bm * 2 + kh;
  const int fr = lane & 15, fq = lane >> 4;
  const rsrc_t rsA0 = mk_rsrc((const char*)At + (size_t)(bm64 ^ 1) * KB * 4096, KB * 4096);
  const rsrc_t rsA1 = mk_rsrc((const char*)At + (size_t)bm64 * KB * 4096, KB * 4096);
  const rsrc_t rsB = mk_rsrc((const char*)W + n0 * 2, KB * 65536 - n0 * 2);
  const unsigned voffL = lane * 16;
  unsigned voffA[4];
#pragma unroll
  for (int i = 0; i < 4; ++i) voffA[i] = voffL + ((i + w) & 3) * 1024;
  const unsigned voffB = fq * 8 * 2048 + fr * 16;
  u32x4 braw[4][8], afr[3][8], afa[4][4];
  u32x4 T[2][2];
  u32x4 xv[4], pv[8];
  unsigned gv[4], flg;
  f32x4 acc[8][8];
#pragma unroll
  for (int r = 0; r < 3; ++r)
#pragma unroll
    for (int j = 0; j < 8; ++j) asm volatile("" : "=v"(braw[r][j]), "=v"(afr[r][j]));
  const int kbF = (kh ? KB - 4 * NF : 0) + w, kbH = w;
#define ELOAD(B, L)                                                                                \
  do {                                                                                             \
    const unsigned kbv = (unsigned)(((B) < NF ? kbF : kbH) + (B) * 4);                             \
    if constexpr ((L) < 8) {                                                                       \
      const unsigned so = kbv * 65536u + ((L) >> 1) * 4096u;                                       \
      if constexpr (bslot(B) == 3 || (B) >= NF + 3) asm volatile("" : "=v"(braw[bslot(B)][(L) & 7]));        \
      if constexpr ((L) & 1) bload_v<2048>(braw[bslot(B)][(L) & 7], voffB, rsB, so);               \
      else bload_v<0>(braw[bslot(B)][(L) & 7], voffB, rsB, so);                                    \
    } else if constexpr ((B) >= NF + 2) {                                                          \
      asm volatile("" : "=a"(afa[((B) - NF) & 3][(L) & 3]));                                       \
      bload_a<0>(afa[((B) - NF) & 3][(L) & 3], voffA[(L) & 3], rsA1, kbv * 4096u);                 \
    } else {                                                                                       \
      bload_v<0>(afr[(B) % 3][(L) & 7], voffA[(L) & 3], ((L) & 4) ? rsA1 : rsA0, kbv * 4096u);     \
    }                                                                                              \
  } while (0)
#define MFMA_E(C, B, A) asm volatile("v_mfma_f32_16x16x32_bf16 %0, %1, %2, %0" : "+a"(C) : "v"(B), "v"(A))
  static_for<16>([&](auto L) { ELOAD(0, L.value); });
  static_for<16>([&](auto L) { ELOAD(1, L.value); });
#pragma unroll
  for (int i = 0; i < 8; ++i)
#pragma unroll
    for (int f = 0; f < 8; ++f) { acc[i][f] = f32x4{0, 0, 0, 0}; asm volatile("" : "+a"(acc[i][f])); }
  const unsigned sbase = (unsigned)(uintptr_t)smem + voffL;
  unsigned wlo[3];
#pragma unroll
  for (int i = 1; i < 4; ++i) wlo[i - 1] = sbase + (((w + i) & 3) * 3 + (i - 1)) * 8192;
  const unsigned rlo = sbase + w * 24576;
  const rsrc_t rsEx = mk_rsrc(xbuf, 256 * 32768);
  const unsigned exo = (unsigned)(blockIdx.x * 32768 + w * 8192);
  const rsrc_t rsFo = mk_rsrc(flags + blockIdx.x * 4 + w, 4), rsFi = mk_rsrc(flags + partner * 4 + w, 4);
  wait_vm<16>();
  STAMPNW(6, 1);
#pragma unroll
  for (int q = 0; q < 8; ++q) DPERM(0, q, 0, 0);
  asm volatile("s_nop 1");
  u32x4 pr[3][4];
  f32x4 tt[8], uu;
  static_for<NS>([&](auto G) {
    constexpr int g = G.value, h = g - NF, sl = bslot(g), ns = bslot(g + 1);
    constexpr bool full = g < NF - 1, lastf = g == NF - 1;
    constexpr bool red = (h == 0 || h == 1);
    constexpr int fb = h == 1 ? 4 : 0;
    if constexpr (h == 0 || h == 1) { WAIT_LGKM0(); __builtin_amdgcn_s_barrier(); }
    if constexpr (h == 3) __builtin_amdgcn_s_barrier();
    static_for<nslot(g)>([&](auto N) {
      constexpr int n = N.value;
      if constexpr (n == wslot(g)) {
        if constexpr (waitcnt(g) < 63) {
          if constexpr (h == HCK) asm volatile("s_waitcnt vmcnt(%1)" : "+v"(flg) : "n"(waitcnt(g)) : "memory");
          else wait_vm<waitcnt(g)>();
        }
        if constexpr (h == HFS) asm volatile("buffer_store_dword %0, %1, %2, 0 offen sc1" ::"v"(1u), "v"(voffL >> 2), "s"(rsFo) : "memory");
        if constexpr (h == HCK) {
          unsigned spins = 0;
          while (__builtin_amdgcn_readfirstlane(flg) == 0u && spins < (1u << 20)) {
            __builtin_amdgcn_s_sleep(1);
            bload1_sc1(flg, 0u, rsFi);
            asm volatile("s_waitcnt vmcnt(0)" : "+v"(flg)::"memory");
            ++spins;
          }
        }
      }
      if constexpr (red && n == 8) WAIT_LGKM0();
      if constexpr (full) {
        constexpr int p = n >> 4, q = n & 15;
        MFMA_E(acc[q & 7][2 * p + (q >> 3)], T[p & 1][q >> 3], afr[g % 3][q & 7]);
        if constexpr ((q & 1) == 0) {
          if constexpr (p < 3) DPERM((p + 1) & 1, q >> 1, sl, p + 1);
          else DPERM(0, q >> 1, ns, 0);
        }
      } else {
        constexpr int p = (n >> 3) & 3, q = n & 7, ib = (lastf && n < 32) ? 0 : 4;
        if constexpr (h >= 2) MFMA_A(acc[ib + (q & 3)][2 * p + (q >> 2)], T[p & 1][q >> 2], afa[h & 3][q & 3]);
        else MFMA_E(acc[ib + (q & 3)][2 * p + (q >> 2)], T[p & 1][q >> 2], afr[g % 3][ib + (q & 3)]);
        if constexpr (p < 3) DPERM((p + 1) & 1, q, sl, p + 1);
        else if constexpr (ib == 0) DPERM(0, q, sl, 0);
        else if constexpr (g + 1 < NS) DPERM(0, q, ns, 0);
      }
      if constexpr (ring(g, n) >= 0) ELOAD((ring(g, n) >> 4), (ring(g, n) & 15));
      if constexpr ((lastf || h == 0) && pwrite(g, n) >= 0) {
        constexpr int e = pwrite(g, n), i = 1 + e % 3, f = e / 3;
        dsw128a<f * 1024>(wlo[i - 1], acc[i][f]);
      }
      if constexpr (red) {
        if constexpr (n < 6) dsr128<(n % 3) * 8192 + (fb + n / 3) * 1024>(pr[n % 3][n / 3], rlo);
        if constexpr (n == 6 || n == 7) {
          static_for<3>([&](auto E) { dsr128<E.value * 8192 + (fb + n - 4) * 1024>(pr[E.value][n - 4], rlo); });
        }
        if constexpr (n >= 8) {
          constexpr int o0 = red0(n), o1 = red0(n + 1);
          static_for<o1 - o0>([&](auto O) {
            constexpr int o = o0 + O.value, fl = o / 16, k = o % 16, r = k & 3;
            (void)&uu; (void)&tt; (void)&pr;
            if constexpr (k < 4) asm volatile("v_accvgpr_read_b32 %0, %1" : "=v"(tt[fb + fl][r]) : "a"(acc[0][fb + fl][r]));
            else if constexpr (k < 8) asm volatile("v_add_f32 %0, %0, %1" : "+v"(tt[fb + fl][r]) : "v"(pr[0][fl][r]));
            else if constexpr (k < 12) asm volatile("v_add_f32 %0, %1, %2" : "=v"(uu[r]) : "v"(pr[1][fl][r]), "v"(pr[2][fl][r]));
            else asm volatile("v_add_f32 %0, %0, %1" : "+v"(tt[fb + fl][r]) : "v"(uu[r]));
          });
        }
      }
      if constexpr (h >= 0 && dstore(g, n)) {
        constexpr int f = dstore_f(g, n);
        bstore_sc1<(f & 3) * 1024>(__builtin_bit_cast(u32x4, tt[f]), voffL, rsEx, exo + (f >> 2) * 4096);
      }
      if constexpr (h == HFL && n == 25) {
        asm volatile("" : "=v"(flg));
        bload1_sc1(flg, 0u, rsFi);
      }
      if constexpr (h == HCK && n >= 24) {
        constexpr int f = n - 24;
        asm volatile("" : "=v"(pv[f]));
        bload_sc1<(f & 3) * 1024>(pv[f], voffL, rsEx, partner * 32768u + w * 8192u + (f >> 2) * 4096u);
      }
      if constexpr (h == HEP && n == 1) {
        const rsrc_t rsG = mk_rsrc(modp + (w >= 2 ? 4096 : 5120), 4 * 6144 * 4);
        const unsigned vo = (unsigned)((n0 + (tid & 127)) * 4);
#pragma unroll
        for (int r = 0; r < 4; ++r) { asm volatile("" : "=v"(gv[r])); bload1_v<0>(gv[r], vo, rsG, r * 24576); }
      }
      if constexpr (h == HEP && n == 3) {
        const rsrc_t rsX = mk_rsrc(Yt, S_LEN * D_DIM * 2);
        const unsigned vx = (unsigned)(((bm64 >> 2) * 32 + bn * 4 + fq) * 16384 + ((bm64 & 3) * 4 + w) * 1024 + fr * 16);
#pragma unroll
        for (int r = 0; r < 4; ++r) asm volatile("" : "=v"(xv[r]));
        bload_v<0>(xv[0], vx, rsX, 0); bload_v<256>(xv[1], vx, rsX, 0); bload_v<512>(xv[2], vx, rsX, 0); bload_v<768>(xv[3], vx, rsX, 0);
      }
      if constexpr (g == NS - 1 && n >= 8 && (n & 7) >= 2) {
        constexpr int e = (n & 7) - 2, i = 1 + e % 3, f = 2 * ((n >> 3) - 1) + e / 3;
        dsw128a<f * 1024>(wlo[i - 1], acc[4 + i][f]);
      }
    });
  });
  asm volatile("s_nop 15" ::: "memory");
#pragma unroll
  for (int f = 0; f < 8; ++f) asm volatile("" : "+a"(acc[4][f]));
  static_for<6>([&](auto E) { constexpr int i = 1 + E.value % 3, f = 6 + E.value / 3; dsw128a<f * 1024>(wlo[i - 1], acc[4 + i][f]); });
  asm volatile("s_waitcnt vmcnt(%8)" : "+v"(xv[0]), "+v"(xv[1]), "+v"(xv[2]), "+v"(xv[3]), "+v"(gv[0]), "+v"(gv[1]), "+v"(gv[2]), "+v"(gv[3]) : "n"(between(NF + HEP, 3, NS - 1, 32)) : "memory");
  STAMP(6, 2); STAMPFLUSH(6, 1);
  float* gtab = reinterpret_cast<float*>(smem + GT);
  {
    float sum = (__uint_as_float(gv[0]) + __uint_as_float(gv[1])) + (__uint_as_float(gv[2]) + __uint_as_float(gv[3]));
    sum = (w >= 2) ? 1.0f / gain_nz(sum) : rbf(sum);
    const int col = tid & 127;
    gtab[(tid & 128) + (col & 96) + (col & 7) * 4 + ((col >> 3) & 3)] = sum;
  }
  asm volatile("s_waitcnt lgkmcnt(0)" ::: "memory");
  __builtin_amdgcn_s_barrier();
  u32x4 pq[3][8], gt[8], ct[8];
  f32x4 fin[8];
  {
    const unsigned gsrc = (unsigned)(uintptr_t)smem + GT + fq * 128;
    static_for<24>([&](auto E) { dsr128<E.value * 1024>(pq[E.value >> 3][E.value & 7], rlo); });
    static_for<8>([&](auto F) { dsr128<F.value * 16>(gt[F.value], gsrc); });
    static_for<8>([&](auto F) { dsr128<512 + F.value * 16>(ct[F.value], gsrc); });
#pragma unroll
    for (int f = 0; f < 8; ++f) { fin[f] = acc[4][f]; asm volatile("" : "+v"(fin[f])); }
    asm volatile("s_waitcnt lgkmcnt(0)" ::: "memory");
#pragma unroll
    for (int f = 0; f < 8; ++f) asm volatile("" : "+v"(pq[0][f]), "+v"(pq[1][f]), "+v"(pq[2][f]), "+v"(gt[f]), "+v"(ct[f]));
  }
#pragma unroll
  for (int f = 0; f < 8; ++f) {
    fin[f] = (fin[f] + __builtin_bit_cast(f32x4, pq[0][f])) + (__builtin_bit_cast(f32x4, pq[1][f]) + __builtin_bit_cast(f32x4, pq[2][f]));
    asm volatile("" : "+v"(fin[f]));
  }
  asm volatile("s_waitcnt vmcnt(0)" : "+v"(pv[0]), "+v"(pv[1]), "+v"(pv[2]), "+v"(pv[3]), "+v"(pv[4]), "+v"(pv[5]), "+v"(pv[6]), "+v"(pv[7])::"memory");
  STAMP(6, 6);
#pragma unroll
  for (int f = 0; f < 8; ++f) {
    fin[f] += __builtin_bit_cast(f32x4, pv[f]);
    f32x4 xf;
#pragma unroll
    for (int r = 0; r < 4; ++r) xf[r] = (f & 1) ? bf_hi(xv[r][f >> 1]) : bf_lo(xv[r][f >> 1]);
    xf *= __builtin_bit_cast(f32x4, ct[f]);
    fin[f] = xf + __builtin_bit_cast(f32x4, gt[f]) * fin[f];
  }
  {
    char* tb = smem + w * 24576;
#pragma unroll
    for (int r = 0; r < 4; ++r) {
      const u32x4 ov = {pk_bf16(fin[0][r], fin[1][r]), pk_bf16(fin[2][r], fin[3][r]), pk_bf16(fin[4][r], fin[5][r]), pk_bf16(fin[6][r], fin[7][r])};
      *reinterpret_cast<u32x4*>(tb + fr * 256 + (((fq * 4 + r) ^ (fr & 7)) * 16)) = ov;
    }
#pragma unroll
    for (int j = 0; j < 4; ++j) {
      const int row = j * 4 + fq;
      const u32x4 ov = *reinterpret_cast<const u32x4*>(tb + row * 256 + ((fr ^ (row & 7)) * 16));
      gst128(C + (size_t)(bm64 * 64 + w * 16 + row) * D_DIM + n0 + fr * 8, ov);
    }
  }
  STAMP(6, 3);
  STAMP(6, 4);
}

__global__ void diag_kernel(const void* a, const void* b) {
  if (threadIdx.x == 0) {
    const unsigned long long d = 600ull * (((unsigned long long)(uintptr_t)a >> 7) & 7) + 6000ull * (((unsigned long long)(uintptr_t)b >> 7) & 7);
    const unsigned long long t0 = __builtin_amdgcn_s_memrealtime();
    while (__builtin_amdgcn_s_memrealtime() - t0 < d) __builtin_amdgcn_s_sleep(1);
  }
}
extern "C" void kernel_launch(void* const* d_in, const int* in_sizes, int n_in, void* d_out, int out_size,
                              void* d_ws, size_t ws_size, hipStream_t stream) {
  const bf16* x = (const bf16*)d_in[0];
  const bf16* wqkv = (const bf16*)d_in[1];
  const bf16* wo = (const bf16*)d_in[2];
  const bf16* wg = (const bf16*)d_in[4];
  const bf16* wu = (const bf16*)d_in[5];
  const bf16* wd = (const bf16*)d_in[6];
  const bf16* c = (const bf16*)d_in[7];
  const bf16* wmod = (const bf16*)d_in[8];
  bf16* out = (bf16*)d_out;

  char* ws = (char*)d_ws;
  float* modp = (float*)ws;
  float* rstat1 = (float*)(ws + 114688);
  float* stat = (float*)(ws + 524288);
  bf16* ot = (bf16*)(ws + (1 << 20));
  bf16* yt = (bf16*)(ws + (9 << 20));
  bf16* ht = (bf16*)(ws + (13 << 20));
  bf16* act = (bf16*)(ws + (17 << 20));
  float* xbuf = (float*)(ws + (32 << 20));
  unsigned* flags = (unsigned*)(ws + 262144);

  pre_kernel<<<320, 512, 0, stream>>>(x, c, wmod, modp, rstat1, wqkv);
  ln_apply_kernel<<<256, 256, 0, stream>>>(x, modp, rstat1, ht, wqkv, flags, wo);
  qkv_attn_kernel<<<256, 512, 0, stream>>>(ht, wqkv, ot, wo, x, modp, yt, stat, flags + 1024, wg, wu);
  gemm3_kernel<<<256, 512, 0, stream>>>(yt, wg, wu, act, stat, modp, wd);
  gemm4_kernel<<<256, 256, 0, stream>>>(act, wd, yt, modp, out, xbuf, flags);
  diag_kernel<<<1, 64, 0, stream>>>(wqkv, wo);
}
```

```cpp
#define PROF
#define DKB 6
#define DSB 4
#define DMODE 1
#define DMULT 0
#include <hip/hip_runtime.h>
#include <hip/hip_bf16.h>
#include <stdint.h>

typedef __hip_bfloat16 bf16;
typedef __attribute__((ext_vector_type(8))) short bf16x8;
typedef __attribute__((ext_vector_type(4))) float f32x4;
typedef __attribute__((ext_vector_type(2))) float f32x2;
typedef __attribute__((ext_vector_type(4))) unsigned u32x4;
typedef __attribute__((ext_vector_type(2))) unsigned u32x2;
typedef __attribute__((ext_vector_type(2))) __bf16 bf16x2_t;
typedef __amdgpu_buffer_rsrc_t rsrc_t;

#define DI __device__ __forceinline__
#define QBAR 2
#define QRN 17
#define TQ 12
#define TG 4
#define K1MAP 1
#define QTM 1
#define WOT 1
#define CODEPF 53

#define PROF_NK 8
#define PROF_NB 1024
#define PROF_NS 8
__device__ unsigned long long g_stamps[PROF_NK * PROF_NB * PROF_NS];
#define STAMPDECL unsigned long long _tnw = 0, _cnw = 0
#define STAMPNW(k, s) do { _tnw = __builtin_amdgcn_s_memrealtime(); _cnw = __builtin_amdgcn_s_memtime(); __builtin_amdgcn_s_waitcnt(0xC07F); } while (0)
#define STAMPFLUSH(k, s) do { unsigned long long _c2 = __builtin_amdgcn_s_memtime(); if (threadIdx.x == 0 && blockIdx.x < PROF_NB) { g_stamps[((k) * PROF_NB + blockIdx.x) * PROF_NS + (s)] = _tnw; g_stamps[((k) * PROF_NB + blockIdx.x) * PROF_NS + 7] = _c2 - _cnw; } } while (0)
#define STAMP(k, s) do { if (threadIdx.x == 0 && blockIdx.x < PROF_NB) g_stamps[((k) * PROF_NB + blockIdx.x) * PROF_NS + (s)] = __builtin_amdgcn_s_memrealtime(); } while (0)
#define STAMPT(k, s, T) do { if (threadIdx.x == (T) && blockIdx.x < PROF_NB) g_stamps[((k) * PROF_NB + blockIdx.x) * PROF_NS + (s)] = __builtin_amdgcn_s_memrealtime(); } while (0)

constexpr int S_LEN = 2048;
constexpr int D_DIM = 1024;
constexpr int F_DIM = 3584;

template <int V> struct ic { static constexpr int value = V; };
template <int N, int I = 0, class F>
DI void static_for(F&& f) {
  if constexpr (I < N) { f(ic<I>{}); static_for<N, I + 1>(f); }
}

DI float bf_lo(unsigned u) { return __uint_as_float(u << 16); }
DI float bf_hi(unsigned u) { return __uint_as_float(u & 0xffff0000u); }
DI unsigned pk_bf16(float lo, float hi) {
  const f32x2 v = {lo, hi};
  return __builtin_bit_cast(unsigned, __builtin_convertvector(v, bf16x2_t));
}
DI rsrc_t mk_rsrc(const void* p, unsigned bytes) { return __builtin_amdgcn_make_buffer_rsrc((void*)p, (short)0, (int)bytes, 0x00020000); }

template <int N>
DI void wait_vm() { asm volatile("s_waitcnt vmcnt(%0)" ::"n"(N > 63 ? 63 : N) : "memory"); }
#define WAIT_LGKM0() do { asm volatile("s_waitcnt lgkmcnt(0)" ::: "memory"); __builtin_amdgcn_sched_barrier(0); } while (0)
#define SCHED0() __builtin_amdgcn_sched_barrier(0)

#define MFMA_A(C, B, A) asm volatile("v_mfma_f32_16x16x32_bf16 %0, %1, %2, %0" : "+a"(C) : "v"(B), "a"(A))
#define MFMA_AV(C, B, A) asm volatile("v_mfma_f32_16x16x32_bf16 %0, %1, %2, %0" : "+v"(C) : "v"(B), "v"(A))
#define PERM_LO(D, X, Y) asm volatile("v_perm_b32 %0, %1, %2, %3" : "=v"(D) : "v"(Y), "v"(X), "s"(0x05040100u))
#define PERM_HI(D, X, Y) asm volatile("v_perm_b32 %0, %1, %2, %3" : "=v"(D) : "v"(Y), "v"(X), "s"(0x07060302u))
template <int OFF>
DI void bload_v(u32x4& d, unsigned voff, rsrc_t rs, unsigned soff) {
  asm volatile("buffer_load_dwordx4 %0, %1, %2, %3 offen offset:%4" : "+v"(d) : "v"(voff), "s"(rs), "s"(soff), "i"(OFF) : "memory");
}
template <int OFF>
DI void bload_a(u32x4& d, unsigned voff, rsrc_t rs, unsigned soff) {
  asm volatile("buffer_load_dwordx4 %0, %1, %2, %3 offen offset:%4" : "+a"(d) : "v"(voff), "s"(rs), "s"(soff), "i"(OFF) : "memory");
}
template <int OFF>
DI void bload2_v(u32x2& d, unsigned voff, rsrc_t rs, unsigned soff) {
  asm volatile("buffer_load_dwordx2 %0, %1, %2, %3 offen offset:%4" : "+v"(d) : "v"(voff), "s"(rs), "s"(soff), "i"(OFF) : "memory");
}
template <int OFF>
DI void bload1_v(unsigned& d, unsigned voff, rsrc_t rs, unsigned soff) {
  asm volatile("buffer_load_dword %0, %1, %2, %3 offen offset:%4" : "+v"(d) : "v"(voff), "s"(rs), "s"(soff), "i"(OFF) : "memory");
}
template <int OFF>
DI void bload_lds(unsigned m0v, unsigned voff, rsrc_t rs, unsigned soff) {
  asm volatile("s_mov_b32 m0, %0\n\ts_nop 0\n\tbuffer_load_dwordx4 %1, %2, %3 offen offset:%4 lds" ::"s"(m0v), "v"(voff), "s"(rs), "s"(soff), "i"(OFF) : "memory");
}
DI void touch_lds(unsigned m0v, unsigned voff, rsrc_t rs, unsigned soff) {
  asm volatile("s_mov_b32 m0, %0\n\ts_nop 0\n\tbuffer_load_dword %1, %2, %3 offen lds" ::"s"(m0v), "v"(voff), "s"(rs), "s"(soff) : "memory");
}
DI void touch_nt(unsigned m0v, unsigned voff, rsrc_t rs, unsigned soff) {
  asm volatile("s_mov_b32 m0, %0\n\ts_nop 0\n\tbuffer_load_dword %1, %2, %3 offen nt lds\n\tbuffer_load_dword %1, %2, %3 offen offset:64 sc1 lds" ::"s"(m0v), "v"(voff), "s"(rs), "s"(soff) : "memory");
}
template <int OFF>
DI void bload_lds_nt(unsigned m0v, unsigned voff, rsrc_t rs, unsigned soff) {
  asm volatile("s_mov_b32 m0, %0\n\ts_nop 0\n\tbuffer_load_dwordx4 %1, %2, %3 offen offset:%4 nt lds" ::"s"(m0v), "v"(voff), "s"(rs), "s"(soff), "i"(OFF) : "memory");
}
template <int OFF>
DI void dsr128(u32x4& d, unsigned addr) { asm volatile("ds_read_b128 %0, %1 offset:%2" : "=v"(d) : "v"(addr), "i"(OFF) : "memory"); }
template <int OFF>
DI void dsr64(u32x2& d, unsigned addr) { asm volatile("ds_read_b64 %0, %1 offset:%2" : "=v"(d) : "v"(addr), "i"(OFF) : "memory"); }
template <int OFF>
DI void dsr_tr(u32x2& d, unsigned addr) { asm volatile("ds_read_b64_tr_b16 %0, %1 offset:%2" : "=v"(d) : "v"(addr), "i"(OFF) : "memory"); }
template <int OFF>
DI void dsw128(unsigned addr, u32x4 d) { asm volatile("ds_write_b128 %0, %1 offset:%2" ::"v"(addr), "v"(d), "i"(OFF) : "memory"); }
template <int OFF>
DI void dsw128a(unsigned addr, const f32x4& d) { asm volatile("ds_write_b128 %0, %1 offset:%2" ::"v"(addr), "a"(d), "i"(OFF) : "memory"); }
template <int OFF>
DI void dsw32(unsigned addr, unsigned d) { asm volatile("ds_write_b32 %0, %1 offset:%2" ::"v"(addr), "v"(d), "i"(OFF) : "memory"); }

DI void gst128(void* p, u32x4 v) {
  asm volatile("global_store_dwordx4 %0, %1, off sc1\n\ts_nop 1" ::"v"(p), "v"(v) : "memory");
}
DI f32x2 lo2(f32x4 v) { return __builtin_shufflevector(v, v, 0, 1); }
DI f32x2 hi2(f32x4 v) { return __builtin_shufflevector(v, v, 2, 3); }
DI float rowsum4(float v) {
  const unsigned u = __float_as_uint(v);
  const auto a = __builtin_amdgcn_permlane16_swap(u, u, false, false);
  const unsigned t = __float_as_uint(__uint_as_float(a[0]) + __uint_as_float(a[1]));
  const auto b = __builtin_amdgcn_permlane32_swap(t, t, false, false);
  return __uint_as_float(b[0]) + __uint_as_float(b[1]);
}
DI void split4(const f32x4 v, u32x2& hi, u32x2& lo) {
  hi[0] = pk_bf16(v[0], v[1]); hi[1] = pk_bf16(v[2], v[3]);
  lo[0] = pk_bf16(v[0] - bf_lo(hi[0]), v[1] - bf_hi(hi[0]));
  lo[1] = pk_bf16(v[2] - bf_lo(hi[1]), v[3] - bf_hi(hi[1]));
}
DI float silu_f(float v) { return v / (1.0f + __expf(-v)); }
DI float rbf(float v) { return bf_lo(pk_bf16(v, v)); }
DI float rowmax4(float v) {
  const unsigned u = __float_as_uint(v);
  const auto a = __builtin_amdgcn_permlane16_swap(u, u, false, false);
  const unsigned t = __float_as_uint(fmaxf(__uint_as_float(a[0]), __uint_as_float(a[1])));
  const auto b = __builtin_amdgcn_permlane32_swap(t, t, false, false);
  return fmaxf(__uint_as_float(b[0]), __uint_as_float(b[1]));
}
DI float gain_nz(float m) {
  const float g = rbf(1.0f + rbf(m));
  return __builtin_fabsf(g) < 8.67361738e-19f ? 8.67361738e-19f : g;
}
template <int OFF>
DI void bload_sc1(u32x4& d, unsigned voff, rsrc_t rs, unsigned soff) {
  asm volatile("buffer_load_dwordx4 %0, %1, %2, %3 offen offset:%4 sc1" : "+v"(d) : "v"(voff), "s"(rs), "s"(soff), "i"(OFF) : "memory");
}
DI void bload1_sc1(unsigned& d, unsigned voff, rsrc_t rs) {
  asm volatile("buffer_load_dword %0, %1, %2, 0 offen sc1" : "+v"(d) : "v"(voff), "s"(rs) : "memory");
}

__global__ __launch_bounds__(512) void pre_kernel(const bf16* __restrict__ x, const bf16* __restrict__ c,
                                                  const bf16* __restrict__ wmod, float* __restrict__ modp,
                                                  float* __restrict__ rstat1, const bf16* __restrict__ Wq) {
  __shared__ __attribute__((aligned(16))) float red[8 * 2 * 128 + 256];
  STAMP(0, 0);
  const int tid = threadIdx.x, lane = tid & 63, w = tid >> 6;
  if (blockIdx.x < 192) {
    const int fr = lane & 15, fq = lane >> 4;
    const int cg = blockIdx.x >> 2, kq = blockIdx.x & 3;
    const int col0 = cg * 128;
    const int k0 = kq * 256 + w * 32 + fq * 8;
    u32x4 raw[8];
#pragma unroll
    for (int j = 0; j < 8; ++j) {
      const u32x4* p_ = reinterpret_cast<const u32x4*>(wmod + (size_t)(k0 + j) * 6144 + col0 + fr * 8);
      raw[j] = __builtin_nontemporal_load(p_);
    }
    const u32x4 cv = *reinterpret_cast<const u32x4*>(c + k0);
    u32x4 cf;
#pragma unroll
    for (int e = 0; e < 4; ++e) {
      const float a = silu_f(bf_lo(cv[e])), b = silu_f(bf_hi(cv[e]));
      cf[e] = (fr == 0) ? pk_bf16(a, b) : 0u;
    }
    f32x4 acc[8];
#pragma unroll
    for (int p = 0; p < 4; ++p) {
      u32x4 lo, hi;
#pragma unroll
      for (int t = 0; t < 4; ++t) {
        lo[t] = __builtin_amdgcn_perm(raw[2 * t + 1][p], raw[2 * t][p], 0x05040100u);
        hi[t] = __builtin_amdgcn_perm(raw[2 * t + 1][p], raw[2 * t][p], 0x07060302u);
      }
      acc[2 * p] = __builtin_amdgcn_mfma_f32_16x16x32_bf16(__builtin_bit_cast(bf16x8, lo), __builtin_bit_cast(bf16x8, cf), f32x4{0, 0, 0, 0}, 0, 0, 0);
      acc[2 * p + 1] = __builtin_amdgcn_mfma_f32_16x16x32_bf16(__builtin_bit_cast(bf16x8, hi), __builtin_bit_cast(bf16x8, cf), f32x4{0, 0, 0, 0}, 0, 0, 0);
    }
    if (fr == 0) {
#pragma unroll
      for (int r = 0; r < 4; ++r) {
        float* d = red + w * 128 + fq * 32 + r * 8;
        *reinterpret_cast<f32x4*>(d) = f32x4{acc[0][r], acc[1][r], acc[2][r], acc[3][r]};
        *reinterpret_cast<f32x4*>(d + 4) = f32x4{acc[4][r], acc[5][r], acc[6][r], acc[7][r]};
      }
    }
    __syncthreads();
    if (tid < 128) {
      float s = 0;
#pragma unroll
      for (int g = 0; g < 8; ++g) s += red[g * 128 + tid];
      modp[kq * 6144 + col0 + tid] = s;
    }
    STAMP(0, 1);
    return;
  }
  const int row0 = (blockIdx.x - 192) * 16 + w * 2;
  u32x4 a[2][2];
#pragma unroll
  for (int i = 0; i < 2; ++i) {
    a[i][0] = *reinterpret_cast<const u32x4*>(x + (size_t)(row0 + i) * D_DIM + lane * 8);
    a[i][1] = *reinterpret_cast<const u32x4*>(x + (size_t)(row0 + i) * D_DIM + 512 + lane * 8);
  }
  if (blockIdx.x < 192 + 8) {
    unsigned long long pc;
    asm volatile("s_getpc_b64 %0" : "=s"(pc));
    const rsrc_t rsC = mk_rsrc((const void*)(uintptr_t)(pc & ~255ull), CODEPF * 1024);
#pragma unroll
    for (int i = 0; i < (CODEPF + 7) / 8; ++i) bload_lds<0>((unsigned)(uintptr_t)(red + 2048), tid * 16, rsC, i * 8192);
  }
#pragma unroll
  for (int i = 0; i < 2; ++i) {
    float v[16];
#pragma unroll
    for (int j = 0; j < 8; ++j) { v[2 * j] = bf_lo(a[i][j >> 2][j & 3]); v[2 * j + 1] = bf_hi(a[i][j >> 2][j & 3]); }
    float s = 0;
#pragma unroll
    for (int j = 0; j < 16; ++j) s += v[j];
#pragma unroll
    for (int o = 32; o > 0; o >>= 1) s += __shfl_xor(s, o);
    const float mean = s * (1.0f / D_DIM);
    float q = 0;
#pragma unroll
    for (int j = 0; j < 16; ++j) { const float d = v[j] - mean; q += d * d; }
#pragma unroll
    for (int o = 32; o > 0; o >>= 1) q += __shfl_xor(q, o);
    const float rstd = rsqrtf(q * (1.0f / D_DIM) + 1e-5f);
    if (lane == 0) *reinterpret_cast<f32x2*>(rstat1 + (row0 + i) * 2) = f32x2{rstd, -mean * rstd};
  }
  STAMP(0, 2);
}

#define QNT 0
#define K1POS 2
DI void k1_touch(char* sink, const bf16* Wq, const bf16* Wo, int part) {
  const int lane = threadIdx.x & 63, w = threadIdx.x >> 6;
  const rsrc_t rsP = mk_rsrc(Wq, D_DIM * 3 * D_DIM * 2);
  const unsigned vo = (unsigned)(((blockIdx.x >> 3) * 32 + (threadIdx.x >> 3)) * 6144 + (blockIdx.x & 3) * 512 + (threadIdx.x & 3) * 128);
  const unsigned v2 = (unsigned)(((blockIdx.x >> 3) * 32 + (threadIdx.x >> 2)) * 6144 + (blockIdx.x & 3) * 512 + (threadIdx.x & 3) * 128);
  if (part & 1) touch_lds((unsigned)(uintptr_t)sink, vo + ((threadIdx.x >> 2) & 1) * 2048, rsP, 0);
  if (part & 2) {
    if (w < 2) touch_lds((unsigned)(uintptr_t)sink, v2, rsP, 4096);
    else touch_lds((unsigned)(uintptr_t)sink, (w == 2 && WOT) ? blockIdx.x * 8192 + lane * 128 : 0x40000000u, mk_rsrc(Wo, D_DIM * D_DIM * 2), 0);
  }
}
__global__ __launch_bounds__(256) void ln_apply_kernel(const bf16* __restrict__ x, const float* __restrict__ modp,
                                                       const float* __restrict__ rstat1, bf16* __restrict__ H,
                                                       const bf16* __restrict__ Wq, unsigned* __restrict__ flags,
                                                       const bf16* __restrict__ Wo) {
  __shared__ __attribute__((aligned(16))) float tab[4][64];
  __shared__ __attribute__((aligned(16))) char sink[1024];
  STAMP(1, 0);
  const int lane = threadIdx.x & 63, w = threadIdx.x >> 6;
  const int fr = lane & 15, fq = lane >> 4;
  const int kb = (blockIdx.x >> 5) * 4 + w, mb0 = (blockIdx.x & 7) + ((blockIdx.x >> 3) & 3) * 32;
  constexpr int MBS = 8;
  u32x4 mr, xv[4];
  u32x2 rr[4];
  asm volatile("" : "=v"(mr));
  {
    bload_v<0>(mr, (unsigned)((fq * 6144 + ((lane >> 3) & 1) * 1024 + kb * 32 + (lane & 7) * 4) * 4), mk_rsrc(modp, 4 * 6144 * 4), 0);
    const rsrc_t rsX = mk_rsrc(x, S_LEN * D_DIM * 2), rsS = mk_rsrc(rstat1, S_LEN * 8);
    const unsigned vx = (unsigned)(((mb0 * 16 + fr) * D_DIM + kb * 32 + fq * 8) * 2), vs = (unsigned)((mb0 * 16 + fr) * 8);
#pragma unroll
    for (int i = 0; i < 4; ++i) {
      asm volatile("" : "=v"(xv[i]), "=v"(rr[i]));
      bload_v<0>(xv[i], vx, rsX, i * 32768 * MBS);
      bload2_v<0>(rr[i], vs, rsS, i * 128 * MBS);
    }
    if (K1POS == 0) k1_touch(sink, Wq, Wo, 3);
    if (K1POS == 2) k1_touch(sink, Wq, Wo, 2);
  }
  asm volatile("s_waitcnt vmcnt(%9)" : "+v"(mr), "+v"(xv[0]), "+v"(xv[1]), "+v"(xv[2]), "+v"(xv[3]), "+v"(rr[0]), "+v"(rr[1]), "+v"(rr[2]), "+v"(rr[3]) : "n"((K1POS == 0 ? 2 : (K1POS == 2 ? 1 : 0)) * (QNT ? 2 : 1)) : "memory");
  f32x4 mv = __builtin_bit_cast(f32x4, mr);
  f32x2 rs[4];
#pragma unroll
  for (int i = 0; i < 4; ++i) rs[i] = __builtin_bit_cast(f32x2, rr[i]);
#pragma unroll
  for (int e = 0; e < 4; ++e) {
    mv[e] = rbf(rowsum4(mv[e]));
    if (lane & 8) mv[e] = rbf(1.0f + mv[e]);
  }
  if (lane < 16) *reinterpret_cast<f32x4*>(&tab[w][lane * 4]) = mv;
  __builtin_amdgcn_wave_barrier();
  asm volatile("s_waitcnt lgkmcnt(0)" ::: "memory");
  const f32x4 s0 = *reinterpret_cast<const f32x4*>(&tab[w][fq * 8]), s1 = *reinterpret_cast<const f32x4*>(&tab[w][fq * 8 + 4]);
  const f32x4 g0 = *reinterpret_cast<const f32x4*>(&tab[w][32 + fq * 8]), g1 = *reinterpret_cast<const f32x4*>(&tab[w][32 + fq * 8 + 4]);
#pragma unroll
  for (int i = 0; i < 4; ++i) {
    u32x4 ho;
#pragma unroll
    for (int d = 0; d < 4; ++d) {
      const f32x4 gg = d < 2 ? g0 : g1, ss = d < 2 ? s0 : s1;
      const float a = (bf_lo(xv[i][d]) * rs[i][0] + rs[i][1]) * gg[(d & 1) * 2] + ss[(d & 1) * 2];
      const float b = (bf_hi(xv[i][d]) * rs[i][0] + rs[i][1]) * gg[(d & 1) * 2 + 1] + ss[(d & 1) * 2 + 1];
      ho[d] = pk_bf16(a, b);
    }
    gst128((char*)H + ((size_t)(mb0 + i * MBS) * 32 + kb) * 1024 + lane * 16, ho);
  }
  if (threadIdx.x < 4) flags[blockIdx.x * 4 + threadIdx.x] = 0u;
  if (threadIdx.x == 4) flags[1024 + blockIdx.x * 32] = 0u;
  STAMP(1, 1);
  if (K1POS == 1) k1_touch(sink, Wq, Wo, 3);
  if (K1POS == 2) k1_touch(sink, Wq, Wo, 1);
}

#define GMAP 0
#define T1 0
#define T2 0
#define GNT 0
#define TW 0
#define TP 8
#define TS 2
#define TN 2
#define GDEP 12
constexpr int Q_SLOT = 21504, Q_NSL = 7;
constexpr int QX_WH = 4 * Q_SLOT;
constexpr int QX_HX = 65536;
constexpr int QX_GT = QX_WH + 65536;
constexpr int QX_CNT = QX_GT + 512;
constexpr int QX_SINK = QX_CNT + 512;
constexpr int Q_LDS = QX_SINK + 1024;

DI unsigned lds_add(unsigned addr, unsigned v0) {
  unsigned o;
  asm volatile("ds_add_rtn_u32 %0, %1, %2\n\ts_waitcnt lgkmcnt(0)" : "=v"(o) : "v"(addr), "v"(v0) : "memory");
  return __builtin_amdgcn_readfirstlane(o);
}
template <int SLP>
DI void lds_wait(unsigned addr, unsigned n) {
  for (;;) {
    unsigned v;
    asm volatile("ds_read_b32 %0, %1\n\ts_waitcnt lgkmcnt(0)" : "=v"(v) : "v"(addr) : "memory");
    if (__builtin_amdgcn_readfirstlane(v) >= n) break;
    __builtin_amdgcn_s_sleep(SLP);
  }
}
DI void wo_piece(unsigned smem_base, unsigned p, unsigned rot, unsigned voff, rsrc_t rs) {
  const unsigned pb = p >> 2, kb = (pb & 24u) | ((pb + rot) & 7u);
  bload_lds<0>(smem_base + (p < 64u ? QX_WH + p * 1024u : (p - 64u) * 1024u), voff, rs, kb * 65536u + (p & 3u) * 16384u);
}

DI void gu_touch(unsigned smem_base, int e, unsigned vgu, const bf16* wg, const bf16* wu, bool nt) {
  if (nt) touch_nt(smem_base + QX_SINK, vgu, mk_rsrc((e & 1) ? wu : wg, D_DIM * F_DIM * 2), (unsigned)(e >> 1) * 256u * (F_DIM * 2));
  else touch_lds(smem_base + QX_SINK, vgu, mk_rsrc((e & 1) ? wu : wg, D_DIM * F_DIM * 2), (unsigned)(e >> 1) * 256u * (F_DIM * 2));
}
DI void qkv_loader(unsigned smem_base, int lane, int sw, int m0, int n0, const bf16* __restrict__ H, const bf16* __restrict__ W,
                   unsigned rot, unsigned voffO, rsrc_t rsO) {
  const rsrc_t rsH = mk_rsrc(H, S_LEN * D_DIM * 2);
  const rsrc_t rsW = mk_rsrc(W, D_DIM * 3 * D_DIM * 2);
  int fb0 = (m0 >> 4) - 1 + 3 * sw; fb0 = fb0 < 0 ? 0 : fb0;
  const unsigned voffH0 = (unsigned)(fb0 * 32768 + lane * 16);
  const unsigned voffH1 = (unsigned)(((m0 >> 4) + 3 * sw) * 32768 + lane * 16);
  unsigned voffW[2];
#pragma unroll
  for (int i = 0; i < 2; ++i) {
    const int r = i * 8 + (lane >> 3), ch = lane & 7;
    const int g = ((r >> 1) & 1) | (((r >> 3) & 1) << 1);
    voffW[i] = (unsigned)(r * 6144 + sw * 2048 + (n0 + (((ch >> 1) ^ g) * 16) + (ch & 1) * 8) * 2);
  }
  const unsigned m0a = smem_base + sw * 3072, m0w = smem_base + 9216 + sw * 4096;
#define Q_LOAD(SOFF, U)                                                                                          \
  do {                                                                                                           \
    const unsigned so_ = (unsigned)(U) * 1024u;                                                                  \
    bload_lds<0>(m0a + (SOFF), voffH0, rsH, so_);                                                                \
    bload_lds<0>(m0a + (SOFF) + 1024, voffH1, rsH, so_);                                                         \
    bload_lds<0>(m0a + (SOFF) + 2048, voffH1, rsH, so_ + 32768u);                                                \
    _Pragma("unroll") for (int i = 0; i < 4; ++i)                                                                \
      bload_lds<0>(m0w + (SOFF) + i * 1024, voffW[i & 1], rsW, (unsigned)(U) * 196608u + (i >> 1) * 98304u);     \
  } while (0)
#pragma unroll
  for (int u = 0; u < Q_NSL - 1; ++u) Q_LOAD(u * Q_SLOT, u);
  wait_vm<(Q_NSL - 2) * 7>();
  __builtin_amdgcn_s_barrier();
  unsigned sb = (Q_NSL - 1) * Q_SLOT;
  for (int u = 0; u < 32 - (Q_NSL - 1); ++u) {
    Q_LOAD(sb, u + Q_NSL - 1);
    sb = (sb == (Q_NSL - 1) * Q_SLOT) ? 0u : sb + Q_SLOT;
    wait_vm<(Q_NSL - 2) * 7>();
    __builtin_amdgcn_s_barrier();
  }
  for (int t = 0; t < 6; ++t) {
    const unsigned p0 = (unsigned)(t * 21 + (t >= 3 ? 1 : 0) + sw * 7);
#pragma unroll
    for (int i = 0; i < 7; ++i) wo_piece(smem_base, p0 + i, rot, voffO, rsO);
    if (t < 5) wait_vm<(Q_NSL - 2) * 7>();
    __builtin_amdgcn_s_barrier();
  }
}

DI f32x4 mfma0(u32x4 a, u32x4 b) {
  return __builtin_amdgcn_mfma_f32_16x16x32_bf16(__builtin_bit_cast(bf16x8, a), __builtin_bit_cast(bf16x8, b), f32x4{0, 0, 0, 0}, 0, 0, 0);
}
DI u32x4 pack8(const f32x4 a, const f32x4 b) { return u32x4{pk_bf16(a[0], a[1]), pk_bf16(a[2], a[3]), pk_bf16(b[0], b[1]), pk_bf16(b[2], b[3])}; }
template <class OP>
DI float rows4(const float (&v)[4], OP op) {
  const auto a = __builtin_amdgcn_permlane16_swap(__float_as_uint(v[0]), __float_as_uint(v[1]), false, false);
  const auto b = __builtin_amdgcn_permlane16_swap(__float_as_uint(v[2]), __float_as_uint(v[3]), false, false);
  const float t0 = op(__uint_as_float(a[0]), __uint_as_float(a[1])), t1 = op(__uint_as_float(b[0]), __uint_as_float(b[1]));
  const auto c = __builtin_amdgcn_permlane32_swap(__float_as_uint(t0), __float_as_uint(t1), false, false);
  return op(__uint_as_float(c[0]), __uint_as_float(c[1]));
}
DI void bcast4(float t, float (&v)[4]) {
  const auto c = __builtin_amdgcn_permlane32_swap(__float_as_uint(t), __float_as_uint(t), false, false);
  const auto a = __builtin_amdgcn_permlane16_swap(c[0], c[0], false, false);
  const auto b = __builtin_amdgcn_permlane16_swap(c[1], c[1], false, false);
  v[0] = __uint_as_float(a[0]); v[1] = __uint_as_float(a[1]); v[2] = __uint_as_float(b[0]); v[3] = __uint_as_float(b[1]);
}
DI float fmax2(float a, float b) { return __builtin_amdgcn_fmed3f(a, b, __builtin_inff()); }

template <int WM>
DI void qkv_mfma(unsigned smem_base, int lane, int wn, int bm, int bn, bf16* __restrict__ O) {
  const int fr = lane & 15, fq = lane >> 4;
  unsigned bb0, bb1;
  const unsigned ab = smem_base + lane * 16;
  {
    const int q = (lane >> 2) & 3, p = lane & 3;
    const int r = 8 * fq + q;
    const int g = ((r >> 1) & 1) | (((r >> 3) & 1) << 1);
    bb0 = smem_base + 9216 + r * 128 + (((wn * 2) ^ g) * 32) + 8 * p;
    bb1 = bb0 ^ 32u;
  }
  const bool par = fr & 1, c0 = 4 * fq + (fr & 1) <= fr, c1 = 4 * fq + 2 + (fr & 1) <= fr;
  const unsigned s0 = par ? 0x01000c0cu : 0x0c0c0100u, s1 = par ? 0x03020c0cu : 0x0c0c0302u;
  u32x4 sel = {c0 ? s0 : 0x0c0c0c0cu, c1 ? s1 : 0x0c0c0c0cu, c0 ? 0x0c0c0c0cu : s0, c1 ? 0x0c0c0c0cu : s1};
  unsigned hx = ab + QX_HX + wn * 3072;
  asm volatile("" : "+v"(sel), "+v"(hx));
  const rsrc_t rsO = mk_rsrc((char*)O + (size_t)((bm * 2 + WM) * 32 + bn * 2 + wn) * 4096, 4096);
  f32x4 acc[4][6], acch[2];
#pragma unroll
  for (int i = 0; i < 4; ++i)
#pragma unroll
    for (int j = 0; j < 6; ++j) { acc[i][j] = f32x4{0, 0, 0, 0}; asm volatile("" : "+v"(acc[i][j])); }
#pragma unroll
  for (int j = 0; j < 2; ++j) { acch[j] = f32x4{0, 0, 0, 0}; asm volatile("" : "+v"(acch[j])); }
  __builtin_amdgcn_s_setprio(2);
  __builtin_amdgcn_s_barrier();
  STAMPDECL;
  STAMPNW(2, 1);
  u32x4 aX[5], aY[5];
  u32x2 bXl[6], bXh[6], bYl[6], bYh[6];
  unsigned ra = ab, rb0 = bb0, rb1 = bb1, so = 0;
#define Q_READ(AR, BL, BH, R)                                                                                    \
  do {                                                                                                           \
    if ((R) < 12) {                                                                                              \
      constexpr int jb = ((R) < 12 ? (R) : 0) >> 1;                                                              \
      if ((R) & 1) dsr_tr<(jb >> 1) * 4096 + 512>(BH[jb], (jb & 1) ? rb1 : rb0);                                 \
      else dsr_tr<(jb >> 1) * 4096>(BL[jb], (jb & 1) ? rb1 : rb0);                                               \
    } else if ((R) < 16) {                                                                                       \
      constexpr int ia = ((R) >= 12 && (R) < 16) ? (R) - 12 : 0;                                                 \
      dsr128<1024 + WM * 4096 + ia * 1024>(AR[ia], ra);                                                          \
    } else {                                                                                                     \
      dsr128<0>(AR[4], ra);                                                                                      \
    }                                                                                                            \
  } while (0)
#define Q_MFMA(AR, BB, N)                                                                                        \
  do {                                                                                                           \
    if ((N) < 16) MFMA_AV(acc[(N) & 3][((N) < 24 ? (N) : 0) >> 2], BB[((N) < 24 ? (N) : 0) >> 2], AR[(N) & 3]);  \
    else if ((N) < 24) MFMA_AV(acc[(N) & 3][((N) < 24 ? (N) : 0) >> 2], AR[(N) & 3], BB[((N) < 24 ? (N) : 0) >> 2]); \
    else if (WM == 0) MFMA_AV(acch[(N) & 1], BB[2 + ((N) & 1)], AR[4]);                                          \
    else MFMA_AV(acch[(N) & 1], AR[4], BB[4 + ((N) & 1)]);                                                       \
  } while (0)
#define Q_ADV(K)                                                                                                 \
  do {                                                                                                           \
    if ((K) == 0) asm volatile("s_add_u32 %0, %0, %1\n\ts_cmp_eq_u32 %0, %2\n\ts_cselect_b32 %0, 0, %0" : "+s"(so) : "n"(Q_SLOT), "n"(Q_NSL * Q_SLOT) : "scc"); \
    else if ((K) == 1) asm volatile("v_add_u32 %0, %1, %2" : "=v"(rb0) : "s"(so), "v"(bb0));                     \
    else if ((K) == 2) asm volatile("v_add_u32 %0, %1, %2" : "=v"(rb1) : "s"(so), "v"(bb1));                     \
    else asm volatile("v_add_u32 %0, %1, %2" : "=v"(ra) : "s"(so), "v"(ab));                                     \
  } while (0)
#define Q_UNIT(AR, BL, BH, NAR, NBL, NBH)                                                                        \
  do {                                                                                                           \
    WAIT_LGKM0();                                                                                                \
    u32x4 bb[6];                                                                                                 \
    _Pragma("unroll") for (int j = 0; j < 6; ++j) bb[j] = u32x4{BL[j][0], BL[j][1], BH[j][0], BH[j][1]};         \
    SCHED0();                                                                                                    \
    static_for<26>([&](auto N) {                                                                                 \
      constexpr int n = N.value;                                                                                 \
      if constexpr (n == QBAR) __builtin_amdgcn_s_barrier();                                                     \
      Q_MFMA(AR, bb, n);                                                                                         \
      if constexpr (n >= QBAR && n < QBAR + 17) Q_READ(NAR, NBL, NBH, n - QBAR);                                 \
      if constexpr (n >= QBAR + 18 && n < QBAR + 22) Q_ADV(n - QBAR - 18);                                       \
    });                                                                                                          \
    SCHED0();                                                                                                    \
  } while (0)
  static_for<17>([&](auto R) { Q_READ(aX, bXl, bXh, R.value); });
  static_for<4>([&](auto K) { Q_ADV(K.value); });
  for (int u = 0; u < 32; u += 2) {
    Q_UNIT(aX, bXl, bXh, aY, bYl, bYh);
    Q_UNIT(aY, bYl, bYh, aX, bXl, bXh);
  }
  asm volatile("s_waitcnt lgkmcnt(0)\n\ts_nop 7" ::: "memory");
#pragma unroll
  for (int i = 0; i < 5; ++i) asm volatile("" : "+v"(aX[i]));
#pragma unroll
  for (int j = 0; j < 6; ++j) asm volatile("" : "+v"(bXl[j]), "+v"(bXh[j]));
#pragma unroll
  for (int i = 0; i < 4; ++i)
#pragma unroll
    for (int j = 0; j < 6; ++j) asm volatile("" : "+v"(acc[i][j]));
  asm volatile("" : "+v"(acch[0]), "+v"(acch[1]));
  SCHED0();
  STAMP(2, 2); STAMPFLUSH(2, 1);
  u32x4 kk[5], vv[5];
  if (WM == 0) {
    kk[4] = pack8(acc[3][2], acc[3][3]); vv[4] = pack8(acc[3][4], acc[3][5]);
    dsw128<0>(hx, kk[4]); dsw128<1024>(hx, vv[4]);
  } else {
    dsw128<2048>(hx, pack8(acch[0], acch[1]));
  }
  asm volatile("s_waitcnt lgkmcnt(0)" ::: "memory");
  __builtin_amdgcn_s_barrier();
  if (WM == 0) dsr128<2048>(vv[0], hx);
  else { dsr128<0>(kk[0], hx); dsr128<1024>(vv[0], hx); }
  SCHED0();
  if (WM == 0) kk[0] = pack8(acch[0], acch[1]);
#pragma unroll
  for (int i = 0; i < (WM == 0 ? 3 : 4); ++i) kk[i + 1] = pack8(acc[i][2], acc[i][3]);
  const float cs = 0.17677669529663687f * 1.4426950408889634f;
  float l0[4], l1[4], tm[4];
#pragma unroll
  for (int ii = 1; ii < 5; ++ii) {
    const int i = ii & 3;
    const u32x4 q = pack8(acc[i][0], acc[i][1]);
    if (WM == 1 && i == 0) asm volatile("s_waitcnt lgkmcnt(0)" : "+v"(kk[0]), "+v"(vv[0])::"memory");
    const f32x4 sc = mfma0(kk[i + 1], q), sp = mfma0(kk[i], q);
    float p0 = par ? sp[1] : sp[0], p1 = par ? sp[3] : sp[2];
    if (WM == 0 && i == 0 && bm == 0) { p0 = -5.656854249e9f; p1 = -5.656854249e9f; }
    l0[i] = c0 ? (par ? sc[1] : sc[0]) : p0;
    l1[i] = c1 ? (par ? sc[3] : sc[2]) : p1;
    tm[i] = fmax2(l0[i], l1[i]);
  }
#pragma unroll
  for (int i = 0; i < (WM == 0 ? 3 : 4); ++i) vv[i + 1] = pack8(acc[i][4], acc[i][5]);
  bcast4(rows4(tm, [](float a, float b) { return fmax2(a, b); }), tm);
#pragma unroll
  for (int i = 0; i < 4; ++i) {
    l0[i] = __builtin_amdgcn_exp2f((l0[i] - tm[i]) * cs);
    l1[i] = __builtin_amdgcn_exp2f((l1[i] - tm[i]) * cs);
    tm[i] = l0[i] + l1[i];
  }
  bcast4(__builtin_amdgcn_rcpf(rows4(tm, [](float a, float b) { return a + b; })), tm);
  if (WM == 0) asm volatile("s_waitcnt lgkmcnt(0)" : "+v"(vv[0])::"memory");
#pragma unroll
  for (int i = 0; i < 4; ++i) {
    const unsigned pp = pk_bf16(l0[i] * tm[i], l1[i] * tm[i]);
    const u32x4 pr = {__builtin_amdgcn_perm(pp, pp, sel[0]), __builtin_amdgcn_perm(pp, pp, sel[1]),
                      __builtin_amdgcn_perm(pp, pp, sel[2]), __builtin_amdgcn_perm(pp, pp, sel[3])};
    const u32x4 ov = pack8(mfma0(u32x4{vv[i + 1][0], vv[i + 1][1], vv[i][0], vv[i][1]}, pr), mfma0(u32x4{vv[i + 1][2], vv[i + 1][3], vv[i][2], vv[i][3]}, pr));
    asm volatile("buffer_store_dwordx4 %0, %1, %2, 0 offen offset:%3 sc1\n\ts_nop 1" ::"v"(ov), "v"(lane * 16), "s"(rsO), "i"(i * 1024) : "memory");
  }
  STAMP(2, 3);
}

__global__ __launch_bounds__(512, 2) void qkv_attn_kernel(const bf16* __restrict__ H, const bf16* __restrict__ W,
                                                          bf16* O, const bf16* __restrict__ wo, const bf16* __restrict__ X,
                                                          const float* __restrict__ modp, bf16* __restrict__ Y,
                                                          float* __restrict__ stat, unsigned* flags,
                                                          const bf16* __restrict__ wg, const bf16* __restrict__ wu) {
  __shared__ __attribute__((aligned(1024))) char smem[Q_LDS];
  STAMP(2, 0);
  const int tid = threadIdx.x, lane = tid & 63;
  const int wid = __builtin_amdgcn_readfirstlane(tid >> 6);
  const unsigned smem_base = (unsigned)(uintptr_t)smem;
  const int xcd = blockIdx.x & 7, idx = blockIdx.x >> 3;
  const int bm = (xcd >> 2) * 8 + (idx & 7), bn = (xcd & 3) * 4 + (idx >> 3);
  const int rb = xcd * 2 + (idx & 1), cg = idx >> 1;
  const unsigned rot = cg & 7;
  const unsigned vgu = (unsigned)((idx * 8 + (lane >> 3)) * (F_DIM * 2) + xcd * 896 + ((lane & 7) == 7 ? 6 : (lane & 7)) * 128);
  const unsigned cnt = smem_base + QX_CNT, cntl = cnt + lane * 4;
  if (wid < 4) {
    if (wid & 1) qkv_mfma<1>(smem_base, lane, wid >> 1, bm, bn, O);
    else qkv_mfma<0>(smem_base, lane, wid >> 1, bm, bn, O);
  } else {
    const rsrc_t rsO = mk_rsrc(wo, D_DIM * D_DIM * 2);
    const unsigned voffO = (unsigned)((lane >> 3) * 2048 + cg * 128 + ((((lane & 7) >> 1) ^ (lane >> 4)) * 32) + (lane & 1) * 16);
    if (wid < 7) qkv_loader(smem_base, lane, wid - 4, bm * 128, bn * 64, H, W, rot, voffO, rsO);
    else {
      unsigned gv[8];
      const rsrc_t rsG = mk_rsrc(modp, 4 * 6144 * 4);
#pragma unroll
      for (int i = 0; i < 8; ++i) { asm volatile("" : "=v"(gv[i])); bload1_v<0>(gv[i], (unsigned)((cg * 64 + lane) * 4), rsG, (i >> 1) * 24576 + 8192 + (i & 1) * 8192); }
      dsw32<0>(cntl, 0u); dsw32<256>(cntl, 0u);
      asm volatile("s_waitcnt lgkmcnt(0)" ::: "memory");
      for (int i = 0; i < 3; ++i) __builtin_amdgcn_s_barrier();
      asm volatile("s_waitcnt vmcnt(0)" : "+v"(gv[0]), "+v"(gv[1]), "+v"(gv[2]), "+v"(gv[3]), "+v"(gv[4]), "+v"(gv[5]), "+v"(gv[6]), "+v"(gv[7])::"memory");
      const float ga = (__uint_as_float(gv[0]) + __uint_as_float(gv[2])) + (__uint_as_float(gv[4]) + __uint_as_float(gv[6]));
      const float cb = (__uint_as_float(gv[1]) + __uint_as_float(gv[3])) + (__uint_as_float(gv[5]) + __uint_as_float(gv[7]));
      dsw32<0>(smem_base + QX_GT + lane * 4, __float_as_uint(rbf(ga)));
      dsw32<256>(smem_base + QX_GT + lane * 4, __float_as_uint(gain_nz(cb)));
      asm volatile("s_waitcnt lgkmcnt(0)" ::: "memory");
      {
        int nxt = TS + idx % TP, ev = 0;
        for (int i = 2; i < 27; ++i) {
          if (TW > 0 && i == nxt && ev < TN) {
#pragma unroll
            for (int j = 0; j < TW; ++j) gu_touch(smem_base, 7 - (ev * TW + j), vgu, wg, wu, GNT & 1);
            nxt += TP; ++ev;
          }
          __builtin_amdgcn_s_barrier();
        }
      }
      wo_piece(smem_base, 63u, rot, voffO, rsO);
      for (int i = 28; i < 33; ++i) __builtin_amdgcn_s_barrier();
      wo_piece(smem_base, 127u, rot, voffO, rsO);
    }
    __builtin_amdgcn_s_barrier();
    if (wid - 4 < T1) { gu_touch(smem_base, T2 + wid - 4, vgu, wg, wu, GNT & 2); wait_vm<(GNT & 2) ? 2 : 1>(); }
    else wait_vm<0>();
    lds_add(cntl, 1u);
  }
  const int fr = lane & 15, fq = lane >> 4;
  const int kq = wid & 3, rg = rb * 2 + (wid >> 2);
  lds_wait<2>(cnt, 4);
  u32x2 wl[8][4], wh[8][4];
  {
    const int q = (lane >> 2) & 3, p = lane & 3, r = 4 * fq + q;
    const unsigned wb = smem_base + (kq < 2 ? QX_WH + kq * 32768 : (kq - 2) * 32768) + r * 128 + (((r >> 1) & 3) * 32) + 8 * p;
    unsigned wa[4];
#pragma unroll
    for (int f = 0; f < 4; ++f) wa[f] = wb ^ (f * 32);
    static_for<32>([&](auto E) {
      constexpr int s = E.value >> 2, f = E.value & 3;
      dsr_tr<s * 4096>(wl[s][f], wa[f]);
      dsr_tr<s * 4096 + 2048>(wh[s][f], wa[f]);
    });
  }
  const rsrc_t rsA = mk_rsrc((const char*)O + (size_t)rg * 131072, 131072);
  const rsrc_t rsF = mk_rsrc(flags + (rb * 16 + kq * 4) * 32, 4 * 128);
  unsigned voffA[4], hs[8];
#pragma unroll
  for (int i = 0; i < 4; ++i) { voffA[i] = lane * 16 + ((kq + 1 + i) & 3) * 1024; asm volatile("" : "+v"(voffA[i])); }
#pragma unroll
  for (int i = 0; i < 8; ++i) { hs[i] = (kq * 8 + ((i + rot) & 7)) * 4096; asm volatile("" : "+s"(hs[i])); }
  asm volatile("" ::"s"(rsA), "s"(rsF));
  if (wid < 4) {
    const rsrc_t rsFo = mk_rsrc(flags + (bm * 16 + bn) * 32, 4);
    asm volatile("s_waitcnt vmcnt(0)" ::"s"(rsFo) : "memory");
    if ((lds_add(cntl + 256, 1u) & 15u) == 3u)
      asm volatile("buffer_store_dword %0, %1, %2, 0 offen sc1" ::"v"(1u), "v"(lane * 4), "s"(rsFo) : "memory");
    STAMP(2, 4);
  } else {
    lds_add(cntl + 256, 16u);
  }
  lds_wait<2>(cnt + 256, 68);
  u32x4 wf[8][4];
#pragma unroll
  for (int s = 0; s < 8; ++s)
#pragma unroll
    for (int f = 0; f < 4; ++f) {
      asm volatile("" : "+v"(wl[s][f]), "+v"(wh[s][f]));
      wf[s][f] = u32x4{wl[s][f][0], wl[s][f][1], wh[s][f][0], wh[s][f][1]};
    }
  {
    unsigned spins = 0;
    for (;;) {
      unsigned fl;
      asm volatile("buffer_load_dword %0, %1, %2, 0 offen sc1\n\ts_waitcnt vmcnt(0)" : "=v"(fl) : "v"((lane & 3) * 128), "s"(rsF) : "memory");
      if (__builtin_amdgcn_ballot_w64(fl == 0u) == 0ull || ++spins >= (1u << 20)) break;
      __builtin_amdgcn_s_sleep(1);
    }
  }
  STAMP(3, 0);
  const unsigned voffL = lane * 16;
  u32x4 af[32], xv[2], pr[3][4], gt[4], ct[4];
  f32x4 acc[4][4];
  auto aload = [&](auto N) {
    constexpr int n = N.value;
    asm volatile("" : "=v"(af[n]));
    bload_sc1<0>(af[n], voffA[n >> 3], rsA, hs[n & 7]);
  };
  static_for<GDEP>([&](auto N) { aload(N); });
  const unsigned sl = smem_base + voffL + (wid >> 2) * 49152;
  const int sw = ((fq & 1) << 1) | (fq >> 1);
  SCHED0();
  static_for<32>([&](auto N) {
    constexpr int n = N.value, ps = n >> 3, s = n & 7, XG = 32 - GDEP;
    constexpr int last = n - 1 + GDEP < 31 ? n - 1 + GDEP : 31;
    asm volatile("s_waitcnt vmcnt(%1)" : "+v"(af[n]) : "n"(last - n + (n > XG ? 2 : 0)) : "memory");
    if constexpr (n == 28) { WAIT_LGKM0(); __builtin_amdgcn_s_barrier(); }
    static_for<4>([&](auto F) {
      constexpr int f = F.value;
      if constexpr (s == 0) asm volatile("v_mfma_f32_16x16x32_bf16 %0, %1, %2, 0" : "=&v"(acc[ps][f]) : "v"(wf[s][f]), "v"(af[n]));
      else MFMA_AV(acc[ps][f], wf[s][f], af[n]);
      if constexpr (f == 0 && n + GDEP < 32) aload(ic<(n + GDEP < 32 ? n + GDEP : 0)>{});
      if constexpr (f == 0 && n == XG) {
        const rsrc_t rsX = mk_rsrc(X, S_LEN * D_DIM * 2);
        const unsigned vx = (unsigned)(((rg * 64 + kq * 16 + fr) * D_DIM + cg * 64 + sw * 8) * 2);
        asm volatile("" : "=v"(xv[0]), "=v"(xv[1]));
        bload_v<0>(xv[0], vx, rsX, 0); bload_v<64>(xv[1], vx, rsX, 0);
      }
      if constexpr (ps > 0 && (s == 2 || s == 3) && (f & 1)) {
        constexpr int e = (s - 2) * 2 + (f >> 1);
        dsw128<(ps - 1) * 4096 + e * 1024>(sl + ((kq + ps) & 3) * 12288, __builtin_bit_cast(u32x4, acc[ps - 1][e]));
      }
      if constexpr (n >= 28 && n < 31) dsr128<(n - 28) * 4096 + f * 1024>(pr[n - 28][f], sl + kq * 12288);
      if constexpr (n == 31) { dsr128<f * 64>(gt[f], smem_base + QX_GT + fq * 16); dsr128<256 + f * 64>(ct[f], smem_base + QX_GT + fq * 16); }
    });
  });
  asm volatile("s_waitcnt vmcnt(0)" : "+v"(xv[0]), "+v"(xv[1])::"memory");
  asm volatile("s_waitcnt lgkmcnt(0)\n\ts_nop 4" ::: "memory");
  STAMP(3, 2);
  f32x4 fin[4], yv[4];
#pragma unroll
  for (int f = 0; f < 4; ++f) asm volatile("" : "+v"(acc[3][f]), "+v"(pr[0][f]), "+v"(pr[1][f]), "+v"(pr[2][f]), "+v"(gt[f]), "+v"(ct[f]));
#pragma unroll
  for (int c = 0; c < 2; ++c) {
    const auto x0 = __builtin_amdgcn_permlane16_swap(xv[c][0], xv[c][2], false, false);
    const auto x1 = __builtin_amdgcn_permlane16_swap(xv[c][1], xv[c][3], false, false);
#pragma unroll
    for (int h = 0; h < 2; ++h) {
      const int f = 2 * c + h;
      const f32x4 a = (acc[3][f] + __builtin_bit_cast(f32x4, pr[0][f])) + (__builtin_bit_cast(f32x4, pr[1][f]) + __builtin_bit_cast(f32x4, pr[2][f]));
      const f32x4 xf = {bf_lo(x0[h]), bf_hi(x0[h]), bf_lo(x1[h]), bf_hi(x1[h])};
      fin[f] = xf + __builtin_bit_cast(f32x4, gt[f]) * a;
      yv[f] = fin[f] * __builtin_bit_cast(f32x4, ct[f]);
    }
    const auto y0 = __builtin_amdgcn_permlane16_swap(pk_bf16(yv[2 * c][0], yv[2 * c][1]), pk_bf16(yv[2 * c + 1][0], yv[2 * c + 1][1]), false, false);
    const auto y1 = __builtin_amdgcn_permlane16_swap(pk_bf16(yv[2 * c][2], yv[2 * c][3]), pk_bf16(yv[2 * c + 1][2], yv[2 * c + 1][3]), false, false);
    gst128((char*)Y + ((size_t)(rg >> 2) * 32 + cg * 2 + c) * 16384 + ((rg & 3) * 4 + kq) * 1024 + (sw * 16 + fr) * 16,
           u32x4{y0[0], y1[0], y0[1], y1[1]});
  }
  const f32x4 s4 = (fin[0] + fin[1]) + (fin[2] + fin[3]);
  const float s = rowsum4((s4[0] + s4[1]) + (s4[2] + s4[3]));
  const float mb = s * (1.0f / 64.0f);
  f32x4 q4 = {0, 0, 0, 0};
#pragma unroll
  for (int f = 0; f < 4; ++f) { const f32x4 d = fin[f] - mb; q4 += d * d; }
  const float m2 = rowsum4((q4[0] + q4[1]) + (q4[2] + q4[3]));
  if (fq == 0) *reinterpret_cast<f32x2*>(stat + (((size_t)(cg >> 1) * S_LEN + rg * 64 + kq * 16 + fr) * 2 + (cg & 1)) * 2) = f32x2{s, m2};
  STAMP(3, 3);
  asm volatile("s_waitcnt vmcnt(0)");
  STAMP(3, 4);
  if (wid < T2) gu_touch(smem_base, wid, vgu, wg, wu, GNT & 4);
}

#define G3NT 1
#define NBR 0
#define TPOS 6
#define NBC 1
#define G3_TW "12"
#define G3_DUMMY() do { } while (0)
constexpr int G3_CT = 131072 + 4096 + 1024;
#define G3_STAGE(SLOT, P, U)                                                                               \
  do {                                                                                                     \
    if ((P) == 0) bload_lds<0>(m0w + (SLOT) * 32768, voffA, rsA, (unsigned)(U) * 16384u);                  \
    else if ((P) == 1) bload_lds<1024>(m0w + (SLOT) * 32768, voffA, rsA, (unsigned)(U) * 16384u);          \
    else if (G3NT && (P) == 2) bload_lds_nt<0>(m0w + (SLOT) * 32768 + 16384, voffB0, rsB, (unsigned)(U) * 229376u);   \
    else if (G3NT) bload_lds_nt<0>(m0w + (SLOT) * 32768 + 16384 + 1024, voffB1, rsB, (unsigned)(U) * 229376u);          \
    else if ((P) == 2) bload_lds<0>(m0w + (SLOT) * 32768 + 16384, voffB0, rsB, (unsigned)(U) * 229376u);   \
    else bload_lds<0>(m0w + (SLOT) * 32768 + 16384 + 1024, voffB1, rsB, (unsigned)(U) * 229376u);          \
  } while (0)

#define G3ROT 1
DI int g3_line(int gx, int p) {
  const int t = p + (G3ROT ? (3 + gx) & 7 : 0);
  return 7 * gx + (t >= 7 ? t - 7 : t);
}
template <int FN>
DI void g3_body(char* smem, unsigned smem_base, int wm, int wn, int wid, int lane, int bm, int bn, rsrc_t rsA, rsrc_t rsB,
                rsrc_t rsP, unsigned voffA, unsigned voffB0, unsigned voffB1, bf16* __restrict__ act) {
  constexpr int NB = 2 * FN, NM = 4 * NB, RD = 5 + 2 * NB;
  const int fr = lane & 15, fq = lane >> 4;
  f32x4 acc[4][NB], accC[2];
  accC[0] = f32x4{0, 0, 0, 0}; accC[1] = f32x4{0, 0, 0, 0};
  asm volatile("" : "+v"(accC[0]), "+v"(accC[1]));
  const int rot = wm % FN;
  unsigned cad = smem_base + G3_CT + (fr < 4 ? fr * 64 : 256) + fq * 16;
#pragma unroll
  for (int i = 0; i < 4; ++i)
#pragma unroll
    for (int j = 0; j < NB; ++j) { acc[i][j] = f32x4{0, 0, 0, 0}; asm volatile("" : "+v"(acc[i][j])); }
  unsigned aad[2], bad[2][FN];
  aad[0] = smem_base + wm * 4096 + lane * 16; aad[1] = aad[0] + 65536;
  {
    const int q = (lane >> 2) & 3, p = lane & 3;
    const int r = 8 * fq + q;
    const int sw = (r & 3) | (((r >> 3) & 1) << 2);
#pragma unroll
    for (int j = 0; j < FN; ++j) {
      const int gi = wn * 4 + (j + rot) % FN + (bn & 1);
      bad[0][j] = smem_base + 16384 + r * 256 + ((gi ^ sw) * 32) + 8 * p;
      bad[1][j] = bad[0][j] + 65536;
    }
  }
  u32x4 aX[5], aY[5];
  u32x2 bXl[NB], bXh[NB], bYl[NB], bYh[NB];
  const unsigned m0w = smem_base + wid * 2048;
  const unsigned voffP = lane * 16;
#define G3_READ(AR, BL, BH, SLOT, R)                                                                       \
  do {                                                                                                     \
    if ((R) < 2 * NB) {                                                                                    \
      constexpr int jb = ((R) < 2 * NB ? (R) : 0) >> 1;                                                    \
      constexpr int off = ((SLOT) & 1) * 32768 + (jb / FN) * 8192 + ((R) & 1) * 1024;                      \
      if ((R) & 1) dsr_tr<off>(BH[jb], bad[(SLOT) >> 1][jb % FN]);                                         \
      else dsr_tr<off>(BL[jb], bad[(SLOT) >> 1][jb % FN]);                                                 \
    } else if ((R) < 2 * NB + 4) {                                                                         \
      constexpr int ia = ((R) >= 2 * NB && (R) < 2 * NB + 4) ? (R) - 2 * NB : 0;                           \
      dsr128<((SLOT) & 1) * 32768 + ia * 1024>(AR[ia], aad[(SLOT) >> 1]);                                  \
    } else {                                                                                               \
      if ((SLOT) == 0) cad += 1280;                                      \
      dsr128<(SLOT) * 320>(AR[4], cad);                                                                    \
    }                                                                                                      \
  } while (0)
  auto rd = [&](auto SL, auto RR, u32x4 (&AR)[5], u32x2 (&BL)[NB], u32x2 (&BH)[NB]) { G3_READ(AR, BL, BH, SL.value, RR.value); };
#define G3_UNIT(AR, BL, BH, NAR, NBL, NBH, SLOT, U)                                                        \
  do {                                                                                                     \
    WAIT_LGKM0();                                                                                          \
    u32x4 bb[NB];                                                                                          \
    _Pragma("unroll") for (int j = 0; j < NB; ++j) bb[j] = u32x4{BL[j][0], BL[j][1], BH[j][0], BH[j][1]};  \
    SCHED0();                                                                                              \
    asm volatile("s_nop 1");                                                                               \
    static_for<NM>([&](auto N) {                                                                           \
      constexpr int n = N.value;                                                                           \
      if constexpr (n == NM / 2) {                                                                         \
        wait_vm<NBR ? 10 : 8>(); __builtin_amdgcn_s_barrier();                                             \
      }                                                                                                    \
      if constexpr (n == NM / 2 + TPOS) {                                                                  \
                      \
        if (NBR) bload_lds<0>(smem_base + 131072 + 4096, voffP, rsP, (((U) & 7) == wid) ? (unsigned)(U) * 1024u : 0x40000000u); \
        else if (((U) & 7) == wid) bload_lds<0>(smem_base + 131072 + 4096, voffP, rsP, (unsigned)(U) * 1024u);  \
      }                                                                                                    \
      MFMA_AV(acc[n & 3][n >> 2], bb[n >> 2], AR[n & 3]);                                                  \
      if constexpr (n < NM / 2) {                                                                          \
        if constexpr (n % 3 == 1 && n / 3 < 4) G3_STAGE(((SLOT) + 3) & 3, n / 3, (U) + 3);                 \
      } else {                                                                                             \
        constexpr int r0 = (n - NM / 2) * 2;                                                               \
        if constexpr (r0 < RD) rd(ic<((SLOT) + 1) & 3>{}, ic<(r0 < RD ? r0 : 0)>{}, NAR, NBL, NBH);        \
        if constexpr (r0 + 1 < RD) rd(ic<((SLOT) + 1) & 3>{}, ic<(r0 + 1 < RD ? r0 + 1 : 0)>{}, NAR, NBL, NBH); \
      }                                                                                                    \
    });                                                                                                    \
    if (NBC || FN == 4 || wm < 3) {                                                                        \
      MFMA_AV(accC[0], bb[0], AR[4]);                                                                      \
      MFMA_AV(accC[1], bb[FN], AR[4]);                                                                     \
    }                                                                                                      \
    SCHED0();                                                                                              \
  } while (0)

  if (NBR) asm volatile("s_waitcnt vmcnt(10) lgkmcnt(0)" ::: "memory");
  else asm volatile("s_waitcnt vmcnt(8) lgkmcnt(0)" ::: "memory");
  __builtin_amdgcn_s_barrier();
  STAMPDECL;
  STAMPNW(5, 1);
  cad -= 1280;
  static_for<RD>([&](auto RR) { rd(ic<0>{}, RR, aX, bXl, bXh); });
  for (int u = 0; u < 32; u += 4) {
    G3_UNIT(aX, bXl, bXh, aY, bYl, bYh, 0, u);
    G3_UNIT(aY, bYl, bYh, aX, bXl, bXh, 1, u + 1);
    G3_UNIT(aX, bXl, bXh, aY, bYl, bYh, 2, u + 2);
    G3_UNIT(aY, bYl, bYh, aX, bXl, bXh, 3, u + 3);
  }
  asm volatile("s_waitcnt lgkmcnt(0)\n\ts_nop 15" ::: "memory");
#pragma unroll
  for (int i = 0; i < 5; ++i) asm volatile("" : "+v"(aX[i]));
#pragma unroll
  for (int j = 0; j < NB; ++j) asm volatile("" : "+v"(bXl[j]), "+v"(bXh[j]));
  SCHED0();
  STAMP(5, 2);
  STAMPFLUSH(5, 1);
  const f32x2* rstat = reinterpret_cast<const f32x2*>(smem + 131072);
  float* colc = reinterpret_cast<float*>(smem + 131072 + 2048);
#pragma unroll
  for (int m = 0; m < 2; ++m) {
    f32x4 v;
#pragma unroll
    for (int r = 0; r < 4; ++r) v[r] = accC[m][r] + __shfl_xor(accC[m][r], 1);
    if ((fr == 0 || fr == 2) && wm < FN) *reinterpret_cast<f32x4*>(colc + (m * 2 + (fr >> 1)) * 128 + (wn * 4 + rot) * 16 + fq * 4) = v;
  }
  asm volatile("s_waitcnt lgkmcnt(0)" ::: "memory");
  __builtin_amdgcn_s_barrier();
  asm volatile("" ::: "memory");
  f32x2 rs[4];
#pragma unroll
  for (int i = 0; i < 4; ++i) rs[i] = rstat[wm * 64 + i * 16 + fr];
#pragma unroll
  for (int j = 0; j < FN; ++j) {
    const int jg = (j + rot) % FN;
    const int cl = (wn * 4 + jg) * 16 + fq * 4;
    const f32x4 csg = *reinterpret_cast<const f32x4*>(colc + cl), bg = *reinterpret_cast<const f32x4*>(colc + 128 + cl);
    const f32x4 csu = *reinterpret_cast<const f32x4*>(colc + 256 + cl), bu = *reinterpret_cast<const f32x4*>(colc + 384 + cl);
    u32x2 pk[4];
#pragma unroll
    for (int i = 0; i < 4; ++i) {
#pragma unroll
      for (int hh = 0; hh < 2; ++hh) {
        const f32x2 ag = hh ? hi2(acc[i][j]) : lo2(acc[i][j]), au = hh ? hi2(acc[i][FN + j]) : lo2(acc[i][FN + j]);
        const f32x2 g = rs[i][0] * ag + (rs[i][1] * (hh ? hi2(csg) : lo2(csg)) + (hh ? hi2(bg) : lo2(bg)));
        const f32x2 uu = rs[i][0] * au + (rs[i][1] * (hh ? hi2(csu) : lo2(csu)) + (hh ? hi2(bu) : lo2(bu)));
        const f32x2 ea = g * -1.4426950408889634f;
        f32x2 e = {__builtin_amdgcn_exp2f(ea[0]), __builtin_amdgcn_exp2f(ea[1])};
        e = e + 1.0f;
        const f32x2 rc = {__builtin_amdgcn_rcpf(e[0]), __builtin_amdgcn_rcpf(e[1])};
        const f32x2 o = (g * uu) * rc;
        pk[i][hh] = pk_bf16(o[0], o[1]);
      }
    }
    const int Jv = (bn & 3) * 7 + wn * 4 + jg;
    const int J = g3_line(bn >> 2, Jv >> 2) * 4 + (Jv & 3);
    char* dst = (char*)act + ((size_t)((bm * 4 + wm) * (F_DIM / 32) + (J >> 1)) * 4 + (fq & 1)) * 1024 + (((J & 1) * 2 + (fq >> 1)) * 16 + fr) * 16;
#pragma unroll
    for (int i = 0; i < 4; i += 2) {
      const auto s0 = __builtin_amdgcn_permlane16_swap(pk[i][0], pk[i + 1][0], false, false);
      const auto s1 = __builtin_amdgcn_permlane16_swap(pk[i][1], pk[i + 1][1], false, false);
      gst128(dst + i * 1024, u32x4{s0[0], s1[0], s0[1], s1[1]});
    }
  }
}

__global__ __launch_bounds__(512, 2) void gemm3_kernel(const bf16* __restrict__ Y, const bf16* __restrict__ Wg,
                                                       const bf16* __restrict__ Wu, bf16* __restrict__ act,
                                                       const float* __restrict__ stat, const float* __restrict__ modp,
                                                       const bf16* __restrict__ Wd) {
  __shared__ __attribute__((aligned(16))) char smem[G3_CT + 33 * 320];
  STAMP(5, 0);
  const int tid = threadIdx.x, lane = tid & 63;
  const int wid = __builtin_amdgcn_readfirstlane(tid >> 6);
  const int wm = wid & 3, wn = wid >> 2;
  const int xcd = blockIdx.x & 7, idx = blockIdx.x >> 3;
  const int bm = idx & 7, bn = xcd * 4 + (idx >> 3);
  const unsigned smem_base = (unsigned)(uintptr_t)smem;
  const rsrc_t rsA = mk_rsrc((const char*)Y + (size_t)bm * 32 * 16384, 32 * 16384);
  const rsrc_t rsB = mk_rsrc(wn ? Wu : Wg, D_DIM * F_DIM * 2);
  const rsrc_t rsP = mk_rsrc((const char*)Wd + (size_t)blockIdx.x * 28672, 28672);
  const unsigned voffA = wid * 2048 + lane * 16;
  unsigned voffB[2];
#pragma unroll
  for (int i = 0; i < 2; ++i) {
    const int r = (wm * 2 + i) * 4 + (lane >> 4);
    const int sw = (r & 3) | (((r >> 3) & 1) << 2);
    const int c16 = lane & 15;
    const int vb = (((bn & 3) * 224) & ~63) + ((c16 >> 1) ^ sw) * 32 + (c16 & 1) * 16;
    voffB[i] = (unsigned)(r * (F_DIM * 2) + g3_line(bn >> 2, vb >> 7) * 128 + (vb & 127));
  }
  const unsigned m0w = smem_base + wid * 2048;
  const unsigned voffB0 = voffB[0], voffB1 = voffB[1];
  if (wn == 0) {
    u32x4 tv[8];
#pragma unroll
    for (int i = 0; i < 8; ++i) asm volatile("" : "=v"(tv[i]));
    const rsrc_t rsS = mk_rsrc(stat, S_LEN * 128);
    const unsigned vo = (unsigned)((bm * 256 + tid) * 16);
    static_for<8>([&](auto I) { bload_v<0>(tv[I.value], vo, rsS, I.value * 32768); });
    G3_STAGE(0, 0, 0); G3_STAGE(0, 1, 0); G3_STAGE(0, 2, 0); G3_STAGE(0, 3, 0);
    G3_STAGE(1, 0, 1); G3_STAGE(1, 1, 1); G3_STAGE(1, 2, 1); G3_STAGE(1, 3, 1);
    G3_DUMMY();
    G3_STAGE(2, 0, 2); G3_STAGE(2, 1, 2); G3_STAGE(2, 2, 2); G3_STAGE(2, 3, 2);
    G3_DUMMY();
    asm volatile("s_waitcnt vmcnt(" G3_TW ")" : "+v"(tv[0]), "+v"(tv[1]), "+v"(tv[2]), "+v"(tv[3]), "+v"(tv[4]), "+v"(tv[5]), "+v"(tv[6]), "+v"(tv[7])::"memory");
    float s = 0;
#pragma unroll
    for (int i = 0; i < 8; ++i) s += __uint_as_float(tv[i][0]) + __uint_as_float(tv[i][2]);
    const float mean = s * (1.0f / D_DIM);
    float m2 = 0;
#pragma unroll
    for (int i = 0; i < 8; ++i) {
      const float d0 = __uint_as_float(tv[i][0]) * (1.0f / 64.0f) - mean, d1 = __uint_as_float(tv[i][2]) * (1.0f / 64.0f) - mean;
      m2 += __uint_as_float(tv[i][1]) + __uint_as_float(tv[i][3]) + 64.0f * (d0 * d0 + d1 * d1);
    }
    const float rstd = rsqrtf(m2 * (1.0f / D_DIM) + 1e-5f);
    reinterpret_cast<f32x2*>(smem + 131072)[tid] = f32x2{rstd, -mean * rstd};
    g3_body<4>(smem, smem_base, wm, wn, wid, lane, bm, bn, rsA, rsB, rsP, voffA, voffB0, voffB1, act);
  } else {
    const rsrc_t rsC = mk_rsrc(modp, 4 * 6144 * 4);
    const int t = tid - 256;
    const unsigned vo = (unsigned)((3072 + t * 4) * 4);
    u32x4 tc[8];
#pragma unroll
    for (int i = 0; i < 8; ++i) { asm volatile("" : "=v"(tc[i])); bload_v<0>(tc[i], vo, rsC, (i >> 1) * 24576 + (i & 1) * 4096); }
    G3_STAGE(0, 0, 0); G3_STAGE(0, 1, 0); G3_STAGE(0, 2, 0); G3_STAGE(0, 3, 0);
    G3_STAGE(1, 0, 1); G3_STAGE(1, 1, 1); G3_STAGE(1, 2, 1); G3_STAGE(1, 3, 1);
    G3_DUMMY();
    G3_STAGE(2, 0, 2); G3_STAGE(2, 1, 2); G3_STAGE(2, 2, 2); G3_STAGE(2, 3, 2);
    G3_DUMMY();
    asm volatile("s_waitcnt vmcnt(" G3_TW ")" : "+v"(tc[0]), "+v"(tc[1]), "+v"(tc[2]), "+v"(tc[3]), "+v"(tc[4]), "+v"(tc[5]), "+v"(tc[6]), "+v"(tc[7])::"memory");
    f32x4 sv = (__builtin_bit_cast(f32x4, tc[0]) + __builtin_bit_cast(f32x4, tc[2])) + (__builtin_bit_cast(f32x4, tc[4]) + __builtin_bit_cast(f32x4, tc[6]));
    f32x4 gw = (__builtin_bit_cast(f32x4, tc[1]) + __builtin_bit_cast(f32x4, tc[3])) + (__builtin_bit_cast(f32x4, tc[5]) + __builtin_bit_cast(f32x4, tc[7]));
#pragma unroll
    for (int e = 0; e < 4; ++e) { gw[e] = gain_nz(gw[e]); sv[e] = rbf(sv[e]); }
    u32x2 gh, gl, sh, sl;
    split4(gw, gh, gl); split4(sv, sh, sl);
    char* d = smem + G3_CT + (t >> 3) * 320 + ((t >> 1) & 3) * 16 + (t & 1) * 8;
    *reinterpret_cast<u32x2*>(d) = gh; *reinterpret_cast<u32x2*>(d + 64) = gl;
    *reinterpret_cast<u32x2*>(d + 128) = sh; *reinterpret_cast<u32x2*>(d + 192) = sl;
    *reinterpret_cast<u32x2*>(d + 256) = u32x2{0, 0};
    g3_body<3>(smem, smem_base, wm, wn, wid, lane, bm, bn, rsA, rsB, rsP, voffA, voffB0, voffB1, act);
  }
  STAMP(5, 3);
  asm volatile("s_waitcnt vmcnt(0)");
  STAMP(5, 4);
}

#define XST 0
#define G4NF 11
#define HFS 3
#define HFL 4
#define HCK 5
#define XMAP 1
template <int OFF>
DI void bstore_sc1(u32x4 d, unsigned voff, rsrc_t rs, unsigned soff) {
  asm volatile("buffer_store_dwordx4 %0, %1, %2, %3 offen offset:%4 sc1\n\ts_nop 1" ::"v"(d), "v"(voff), "s"(rs), "s"(soff), "i"(OFF) : "memory");
}
#define DPERM(TB, Q, SL, PP)                                                                       \
  do {                                                                                             \
    if ((Q) & 1) PERM_HI(T[TB][1][(Q) >> 1], braw[SL][((Q) >> 1) * 2][PP], braw[SL][((Q) >> 1) * 2 + 1][PP]); \
    else PERM_LO(T[TB][0][(Q) >> 1], braw[SL][((Q) >> 1) * 2][PP], braw[SL][((Q) >> 1) * 2 + 1][PP]);         \
  } while (0)
namespace g4 {
constexpr int NF = G4NF, NH = 28 - 2 * NF, NS = NF + NH, HEP = NH - 3;
constexpr int nslot(int g) { return g < NF ? 64 : 32; }
constexpr int wslot(int g) { return g < NF - 1 ? 48 : (g == NF - 1 ? 56 : 24); }
constexpr int ring(int g, int n) {
  if (g < NF - 1) {
    if (n >= 48 || n % 3 != 1) return -1;
    const int l = n / 3;
    if (g + 2 < NF) return (g + 2) * 16 + l;
    return l < 12 ? (g + 2) * 16 + (l < 8 ? l : l + 4) : -1;
  }
  if (g == NF - 1) { if (n >= 48 || n % 4 != 1) return -1; const int l = n / 4; return (g + 2) * 16 + (l < 8 ? l : l + 4); }
  if (g == NF) { if (n >= 24) return -1; const int l = n % 12; return (g + 2 + n / 12) * 16 + (l < 8 ? l : l + 4); }
  if (g + 3 >= NS || n >= 24 || (n & 1)) return -1;
  const int l = n >> 1;
  return (g + 3) * 16 + (l < 8 ? l : l + 4);
}
constexpr int pwrite(int g, int n) {
  const int m = g == NF - 1 ? n : 64 + n;
  for (int e = 0; e < 24; ++e) if (12 + (13 * e) / 4 == m) return e;
  return -1;
}
constexpr int red0(int n) { return n < 8 ? 0 : (64 * (n - 8)) / 24; }
#define SPS 4
constexpr int sslot(int f) {
  const int ready = (f >> 2) * 32 + 8 + (((f & 3) + 1) * 16 * 24 + 63) / 64;
  const int paced = (f / SPS) * 32 + 15 + (f % SPS) * (32 / SPS);
  int m = ready > paced ? ready : paced;
  if ((m & 1) == 0) ++m;
  return m;
}
constexpr int dstore_f(int g, int n) {
  for (int f = 0; f < 8; ++f) if (sslot(f) == (g - NF) * 32 + n) return f;
  return -1;
}
constexpr bool dstore(int g, int n) { return g >= NF && dstore_f(g, n) >= 0; }
constexpr int vmops(int g, int n) {
  int c = (ring(g, n) >= 0 ? 1 : 0) + (dstore(g, n) ? 1 : 0);
  const int h = g - NF;
  if (h == HFS && n == 24) c += 1;
  if (h == HFL && n == 25) c += 1;
  if (h == HCK && n >= 24) c += 1;
  if (h == HEP && (n == 1 || n == 3)) c += 4;
  return c;
}
constexpr int between(int g0, int n0, int g1, int n1) {
  int c = 0;
  for (int g = g0; g <= g1; ++g)
    for (int n = (g == g0 ? n0 + 1 : 0); n < (g == g1 ? n1 : nslot(g)); ++n) c += vmops(g, n);
  return c;
}
constexpr int imin(int a, int b) { return a < b ? a : b; }
constexpr int waitcnt(int g) {
  int c = 63;
  if (g + 1 < NS && g + 1 >= 2) {
    for (int s = 0; s <= g; ++s)
      for (int n = 0; n < nslot(s); ++n)
        if (ring(s, n) == (g + 1) * 16 + 15) c = imin(c, between(s, n, g, wslot(g)) + (dstore(s, n) ? 1 : 0));
  }
  if (g == 0) c = 16;
  if (g - NF == HFS) c = imin(c, between(NF + sslot(7) / 32, sslot(7) % 32, g, 24));
  if (g - NF == HCK) c = imin(c, between(NF + HFL, 25, g, 24));
  return c;
}
constexpr int bslot(int b) { return b < NF + 3 ? b % 3 : ((b - NF) % 4 == 3 ? 3 : (NF + (b - NF) % 4) % 3); }
}
__global__ __launch_bounds__(256, 1) void gemm4_kernel(const bf16* __restrict__ At, const bf16* __restrict__ W,
                                                       const bf16* __restrict__ Yt, const float* __restrict__ modp,
                                                       bf16* __restrict__ C, float* xbuf, unsigned* flags) {
  using namespace g4;
  constexpr int KB = F_DIM / 32;
  constexpr int GT = 96 * 1024;
  __shared__ __attribute__((aligned(16))) char smem[GT + 1024];
  STAMPDECL;
  STAMP(6, 0);
  const int tid = threadIdx.x, lane = tid & 63;
  const int w = __builtin_amdgcn_readfirstlane(tid >> 6);
  const int xcd = blockIdx.x & 7, idx = blockIdx.x >> 3;
  const int kh = xcd & 1, bm = ((xcd >> 1) & 1) * 8 + (idx & 7), bn = (xcd >> 2) * 4 + (idx >> 3);
  const unsigned partner = blockIdx.x ^ 1;
  const int n0 = bn * 128, bm64 = bm * 2 + kh;
  const int fr = lane & 15, fq = lane >> 4;
  const rsrc_t rsA0 = mk_rsrc((const char*)At + (size_t)(bm64 ^ 1) * KB * 4096, KB * 4096);
  const rsrc_t rsA1 = mk_rsrc((const char*)At + (size_t)bm64 * KB * 4096, KB * 4096);
  const rsrc_t rsB = mk_rsrc((const char*)W + n0 * 2, KB * 65536 - n0 * 2);
  const unsigned voffL = lane * 16;
  unsigned voffA[4];
#pragma unroll
  for (int i = 0; i < 4; ++i) voffA[i] = voffL + ((i + w) & 3) * 1024;
  const unsigned voffB = fq * 8 * 2048 + fr * 16;
  u32x4 braw[4][8], afr[3][8], afa[4][4];
  u32x4 T[2][2];
  u32x4 xv[4], pv[8];
  unsigned gv[4], flg;
  f32x4 acc[8][8];
#pragma unroll
  for (int r = 0; r < 3; ++r)
#pragma unroll
    for (int j = 0; j < 8; ++j) asm volatile("" : "=v"(braw[r][j]), "=v"(afr[r][j]));
  const int kbF = (kh ? KB - 4 * NF : 0) + w, kbH = w;
#define ELOAD(B, L)                                                                                \
  do {                                                                                             \
    const unsigned kbv = (unsigned)(((B) < NF ? kbF : kbH) + (B) * 4);                             \
    if constexpr ((L) < 8) {                                                                       \
      const unsigned so = kbv * 65536u + ((L) >> 1) * 4096u;                                       \
      if constexpr (bslot(B) == 3 || (B) >= NF + 3) asm volatile("" : "=v"(braw[bslot(B)][(L) & 7]));        \
      if constexpr ((L) & 1) bload_v<2048>(braw[bslot(B)][(L) & 7], voffB, rsB, so);               \
      else bload_v<0>(braw[bslot(B)][(L) & 7], voffB, rsB, so);                                    \
    } else if constexpr ((B) >= NF + 2) {                                                          \
      asm volatile("" : "=a"(afa[((B) - NF) & 3][(L) & 3]));                                       \
      bload_a<0>(afa[((B) - NF) & 3][(L) & 3], voffA[(L) & 3], rsA1, kbv * 4096u);                 \
    } else {                                                                                       \
      bload_v<0>(afr[(B) % 3][(L) & 7], voffA[(L) & 3], ((L) & 4) ? rsA1 : rsA0, kbv * 4096u);     \
    }                                                                                              \
  } while (0)
#define MFMA_E(C, B, A) asm volatile("v_mfma_f32_16x16x32_bf16 %0, %1, %2, %0" : "+a"(C) : "v"(B), "v"(A))
  static_for<16>([&](auto L) { ELOAD(0, L.value); });
  static_for<16>([&](auto L) { ELOAD(1, L.value); });
#pragma unroll
  for (int i = 0; i < 8; ++i)
#pragma unroll
    for (int f = 0; f < 8; ++f) { acc[i][f] = f32x4{0, 0, 0, 0}; asm volatile("" : "+a"(acc[i][f])); }
  const unsigned sbase = (unsigned)(uintptr_t)smem + voffL;
  unsigned wlo[3];
#pragma unroll
  for (int i = 1; i < 4; ++i) wlo[i - 1] = sbase + (((w + i) & 3) * 3 + (i - 1)) * 8192;
  const unsigned rlo = sbase + w * 24576;
  const rsrc_t rsEx = mk_rsrc(xbuf, 256 * 32768);
  const unsigned exo = (unsigned)(blockIdx.x * 32768 + w * 8192);
  const rsrc_t rsFo = mk_rsrc(flags + blockIdx.x * 4 + w, 4), rsFi = mk_rsrc(flags + partner * 4 + w, 4);
  wait_vm<16>();
  STAMPNW(6, 1);
#pragma unroll
  for (int q = 0; q < 8; ++q) DPERM(0, q, 0, 0);
  asm volatile("s_nop 1");
  u32x4 pr[3][4];
  unsigned long long _ts[7];
  f32x4 tt[8], uu;
  static_for<NS>([&](auto G) {
    constexpr int g = G.value, h = g - NF, sl = bslot(g), ns = bslot(g + 1);
    constexpr bool full = g < NF - 1, lastf = g == NF - 1;
    constexpr bool red = (h == 0 || h == 1);
    constexpr int fb = h == 1 ? 4 : 0;
    if constexpr (h == 0 || h == 1) { WAIT_LGKM0(); __builtin_amdgcn_s_barrier(); }
    if constexpr (h == 3) __builtin_amdgcn_s_barrier();
    static_for<nslot(g)>([&](auto N) {
      constexpr int n = N.value;
      if constexpr (n == wslot(g)) {
        if constexpr (waitcnt(g) < 63) {
          if constexpr (h == HCK) asm volatile("s_waitcnt vmcnt(%1)" : "+v"(flg) : "n"(waitcnt(g)) : "memory");
          else wait_vm<waitcnt(g)>();
        }
        if constexpr (h == HFS) asm volatile("buffer_store_dword %0, %1, %2, 0 offen sc1" ::"v"(1u), "v"(voffL >> 2), "s"(rsFo) : "memory");
        if constexpr (h == HCK) {
          unsigned spins = 0;
          while (__builtin_amdgcn_readfirstlane(flg) == 0u && spins < (1u << 20)) {
            __builtin_amdgcn_s_sleep(1);
            bload1_sc1(flg, 0u, rsFi);
            asm volatile("s_waitcnt vmcnt(0)" : "+v"(flg)::"memory");
            ++spins;
          }
        }
      }
      if constexpr (red && n == 8) WAIT_LGKM0();
      if constexpr (full) {
        constexpr int p = n >> 4, q = n & 15;
        MFMA_E(acc[q & 7][2 * p + (q >> 3)], T[p & 1][q >> 3], afr[g % 3][q & 7]);
        if constexpr ((q & 1) == 0) {
          if constexpr (p < 3) DPERM((p + 1) & 1, q >> 1, sl, p + 1);
          else DPERM(0, q >> 1, ns, 0);
        }
      } else {
        constexpr int p = (n >> 3) & 3, q = n & 7, ib = (lastf && n < 32) ? 0 : 4;
        if constexpr (h >= 2) MFMA_A(acc[ib + (q & 3)][2 * p + (q >> 2)], T[p & 1][q >> 2], afa[h & 3][q & 3]);
        else MFMA_E(acc[ib + (q & 3)][2 * p + (q >> 2)], T[p & 1][q >> 2], afr[g % 3][ib + (q & 3)]);
        if constexpr (p < 3) DPERM((p + 1) & 1, q, sl, p + 1);
        else if constexpr (ib == 0) DPERM(0, q, sl, 0);
        else if constexpr (g + 1 < NS) DPERM(0, q, ns, 0);
      }
      if constexpr (ring(g, n) >= 0) ELOAD((ring(g, n) >> 4), (ring(g, n) & 15));
      if constexpr ((lastf || h == 0) && pwrite(g, n) >= 0) {
        constexpr int e = pwrite(g, n), i = 1 + e % 3, f = e / 3;
        dsw128a<f * 1024>(wlo[i - 1], acc[i][f]);
      }
      if constexpr (red) {
        if constexpr (n < 6) dsr128<(n % 3) * 8192 + (fb + n / 3) * 1024>(pr[n % 3][n / 3], rlo);
        if constexpr (n == 6 || n == 7) {
          static_for<3>([&](auto E) { dsr128<E.value * 8192 + (fb + n - 4) * 1024>(pr[E.value][n - 4], rlo); });
        }
        if constexpr (n >= 8) {
          constexpr int o0 = red0(n), o1 = red0(n + 1);
          static_for<o1 - o0>([&](auto O) {
            constexpr int o = o0 + O.value, fl = o / 16, k = o % 16, r = k & 3;
            (void)&uu; (void)&tt; (void)&pr;
            if constexpr (k < 4) asm volatile("v_accvgpr_read_b32 %0, %1" : "=v"(tt[fb + fl][r]) : "a"(acc[0][fb + fl][r]));
            else if constexpr (k < 8) asm volatile("v_add_f32 %0, %0, %1" : "+v"(tt[fb + fl][r]) : "v"(pr[0][fl][r]));
            else if constexpr (k < 12) asm volatile("v_add_f32 %0, %1, %2" : "=v"(uu[r]) : "v"(pr[1][fl][r]), "v"(pr[2][fl][r]));
            else asm volatile("v_add_f32 %0, %0, %1" : "+v"(tt[fb + fl][r]) : "v"(uu[r]));
          });
        }
      }
      if constexpr (h >= 0 && dstore(g, n)) {
        constexpr int f = dstore_f(g, n);
        bstore_sc1<(f & 3) * 1024>(__builtin_bit_cast(u32x4, tt[f]), voffL, rsEx, exo + (f >> 2) * 4096);
      }
      if constexpr (h == HFL && n == 25) {
        asm volatile("" : "=v"(flg));
        bload1_sc1(flg, 0u, rsFi);
      }
      if constexpr (h == HCK && n >= 24) {
        constexpr int f = n - 24;
        asm volatile("" : "=v"(pv[f]));
        bload_sc1<(f & 3) * 1024>(pv[f], voffL, rsEx, partner * 32768u + w * 8192u + (f >> 2) * 4096u);
      }
      if constexpr (h == HEP && n == 1) {
        const rsrc_t rsG = mk_rsrc(modp + (w >= 2 ? 4096 : 5120), 4 * 6144 * 4);
        const unsigned vo = (unsigned)((n0 + (tid & 127)) * 4);
#pragma unroll
        for (int r = 0; r < 4; ++r) { asm volatile("" : "=v"(gv[r])); bload1_v<0>(gv[r], vo, rsG, r * 24576); }
      }
      if constexpr (h == HEP && n == 3) {
        const rsrc_t rsX = mk_rsrc(Yt, S_LEN * D_DIM * 2);
        const unsigned vx = (unsigned)(((bm64 >> 2) * 32 + bn * 4 + fq) * 16384 + ((bm64 & 3) * 4 + w) * 1024 + fr * 16);
#pragma unroll
        for (int r = 0; r < 4; ++r) asm volatile("" : "=v"(xv[r]));
        bload_v<0>(xv[0], vx, rsX, 0); bload_v<256>(xv[1], vx, rsX, 0); bload_v<512>(xv[2], vx, rsX, 0); bload_v<768>(xv[3], vx, rsX, 0);
      }
      if constexpr (g == NS - 1 && n >= 8 && (n & 7) >= 2) {
        constexpr int e = (n & 7) - 2, i = 1 + e % 3, f = 2 * ((n >> 3) - 1) + e / 3;
        dsw128a<f * 1024>(wlo[i - 1], acc[4 + i][f]);
      }
    });
  });
  asm volatile("s_nop 15" ::: "memory");
#pragma unroll
  for (int f = 0; f < 8; ++f) asm volatile("" : "+a"(acc[4][f]));
  static_for<6>([&](auto E) { constexpr int i = 1 + E.value % 3, f = 6 + E.value / 3; dsw128a<f * 1024>(wlo[i - 1], acc[4 + i][f]); });
  asm volatile("s_waitcnt vmcnt(%8)" : "+v"(xv[0]), "+v"(xv[1]), "+v"(xv[2]), "+v"(xv[3]), "+v"(gv[0]), "+v"(gv[1]), "+v"(gv[2]), "+v"(gv[3]) : "n"(between(NF + HEP, 3, NS - 1, 32)) : "memory");
  STAMP(6, 2); STAMPFLUSH(6, 1);
  float* gtab = reinterpret_cast<float*>(smem + GT);
  {
    float sum = (__uint_as_float(gv[0]) + __uint_as_float(gv[1])) + (__uint_as_float(gv[2]) + __uint_as_float(gv[3]));
    sum = (w >= 2) ? 1.0f / gain_nz(sum) : rbf(sum);
    const int col = tid & 127;
    gtab[(tid & 128) + (col & 96) + (col & 7) * 4 + ((col >> 3) & 3)] = sum;
  }
  asm volatile("s_waitcnt lgkmcnt(0)" ::: "memory");
  __builtin_amdgcn_s_barrier();
  u32x4 pq[3][8], gt[8], ct[8];
  f32x4 fin[8];
  {
    const unsigned gsrc = (unsigned)(uintptr_t)smem + GT + fq * 128;
    static_for<24>([&](auto E) { dsr128<E.value * 1024>(pq[E.value >> 3][E.value & 7], rlo); });
    static_for<8>([&](auto F) { dsr128<F.value * 16>(gt[F.value], gsrc); });
    static_for<8>([&](auto F) { dsr128<512 + F.value * 16>(ct[F.value], gsrc); });
#pragma unroll
    for (int f = 0; f < 8; ++f) { fin[f] = acc[4][f]; asm volatile("" : "+v"(fin[f])); }
    asm volatile("s_waitcnt lgkmcnt(0)" ::: "memory");
#pragma unroll
    for (int f = 0; f < 8; ++f) asm volatile("" : "+v"(pq[0][f]), "+v"(pq[1][f]), "+v"(pq[2][f]), "+v"(gt[f]), "+v"(ct[f]));
  }
#pragma unroll
  for (int f = 0; f < 8; ++f) {
    fin[f] = (fin[f] + __builtin_bit_cast(f32x4, pq[0][f])) + (__builtin_bit_cast(f32x4, pq[1][f]) + __builtin_bit_cast(f32x4, pq[2][f]));
    asm volatile("" : "+v"(fin[f]));
  }
  asm volatile("s_waitcnt vmcnt(0)" : "+v"(pv[0]), "+v"(pv[1]), "+v"(pv[2]), "+v"(pv[3]), "+v"(pv[4]), "+v"(pv[5]), "+v"(pv[6]), "+v"(pv[7])::"memory");
  STAMP(6, 6);
#pragma unroll
  for (int f = 0; f < 8; ++f) {
    fin[f] += __builtin_bit_cast(f32x4, pv[f]);
    f32x4 xf;
#pragma unroll
    for (int r = 0; r < 4; ++r) xf[r] = (f & 1) ? bf_hi(xv[r][f >> 1]) : bf_lo(xv[r][f >> 1]);
    xf *= __builtin_bit_cast(f32x4, ct[f]);
    fin[f] = xf + __builtin_bit_cast(f32x4, gt[f]) * fin[f];
  }
  {
    char* tb = smem + w * 24576;
#pragma unroll
    for (int r = 0; r < 4; ++r) {
      const u32x4 ov = {pk_bf16(fin[0][r], fin[1][r]), pk_bf16(fin[2][r], fin[3][r]), pk_bf16(fin[4][r], fin[5][r]), pk_bf16(fin[6][r], fin[7][r])};
      *reinterpret_cast<u32x4*>(tb + fr * 256 + (((fq * 4 + r) ^ (fr & 7)) * 16)) = ov;
    }
#pragma unroll
    for (int j = 0; j < 4; ++j) {
      const int row = j * 4 + fq;
      const u32x4 ov = *reinterpret_cast<const u32x4*>(tb + row * 256 + ((fr ^ (row & 7)) * 16));
      gst128(C + (size_t)(bm64 * 64 + w * 16 + row) * D_DIM + n0 + fr * 8, ov);
    }
  }
  STAMP(6, 3);
  asm volatile("s_waitcnt vmcnt(0)");
  STAMP(6, 4);
}

__global__ void diag_kernel() {
  __shared__ unsigned long long r0[256], r1[256];
  const int t = threadIdx.x;
  unsigned long long a = ~0ull, b = (DMODE == 0) ? ~0ull : 0ull; unsigned long long cnt = 0;
  for (int i = t; i < PROF_NB; i += 256) {
    unsigned long long x = g_stamps[(0 * PROF_NB + i) * PROF_NS + 0];
    if (x && x < a) a = x;
    unsigned long long y = g_stamps[(DKB * PROF_NB + i) * PROF_NS + DSB];
    if (y) { if (DMODE == 0) { if (y < b) b = y; } else if (DMODE == 1) { if (y > b) b = y; } else { b += y; cnt++; } }
  }
  __shared__ unsigned long long r2[256];
  r0[t] = a; r1[t] = b; r2[t] = cnt;
  __syncthreads();
  if (t == 0) {
    for (int i = 1; i < 256; ++i) {
      if (r0[i] < a) a = r0[i];
      if (DMODE == 0) { if (r1[i] < b) b = r1[i]; } else if (DMODE == 1) { if (r1[i] > b) b = r1[i]; } else { b += r1[i]; cnt += r2[i]; }
    }
    if (DMODE == 2) b = cnt ? b / cnt : a;
    unsigned long long d = (DSB == 7) ? b / 100 : ((b > a) ? (b - a) : 0);
    if (d > 20000) d = 20000;
    const unsigned long long t0 = __builtin_amdgcn_s_memrealtime();
    while (__builtin_amdgcn_s_memrealtime() - t0 < d * DMULT) __builtin_amdgcn_s_sleep(1);
  }
}
extern "C" void kernel_launch(void* const* d_in, const int* in_sizes, int n_in, void* d_out, int out_size,
                              void* d_ws, size_t ws_size, hipStream_t stream) {
  const bf16* x = (const bf16*)d_in[0];
  const bf16* wqkv = (const bf16*)d_in[1];
  const bf16* wo = (const bf16*)d_in[2];
  const bf16* wg = (const bf16*)d_in[4];
  const bf16* wu = (const bf16*)d_in[5];
  const bf16* wd = (const bf16*)d_in[6];
  const bf16* c = (const bf16*)d_in[7];
  const bf16* wmod = (const bf16*)d_in[8];
  bf16* out = (bf16*)d_out;

  char* ws = (char*)d_ws;
  float* modp = (float*)ws;
  float* rstat1 = (float*)(ws + 114688);
  float* stat = (float*)(ws + 524288);
  bf16* ot = (bf16*)(ws + (1 << 20));
  bf16* yt = (bf16*)(ws + (9 << 20));
  bf16* ht = (bf16*)(ws + (13 << 20));
  bf16* act = (bf16*)(ws + (17 << 20));
  float* xbuf = (float*)(ws + (32 << 20));
  unsigned* flags = (unsigned*)(ws + 262144);

  pre_kernel<<<320, 512, 0, stream>>>(x, c, wmod, modp, rstat1, wqkv);
  ln_apply_kernel<<<256, 256, 0, stream>>>(x, modp, rstat1, ht, wqkv, flags, wo);
  qkv_attn_kernel<<<256, 512, 0, stream>>>(ht, wqkv, ot, wo, x, modp, yt, stat, flags + 1024, wg, wu);
  gemm3_kernel<<<256, 512, 0, stream>>>(yt, wg, wu, act, stat, modp, wd);
  gemm4_kernel<<<256, 256, 0, stream>>>(act, wd, yt, modp, out, xbuf, flags);
  diag_kernel<<<1, 256, 0, stream>>>();
}
```

```cpp
#define PROF
#define DKB 0
#define DSB 0
#define DMODE 0
#define DMULT 1
#include <hip/hip_runtime.h>
#include <hip/hip_bf16.h>
#include <stdint.h>

typedef __hip_bfloat16 bf16;
typedef __attribute__((ext_vector_type(8))) short bf16x8;
typedef __attribute__((ext_vector_type(4))) float f32x4;
typedef __attribute__((ext_vector_type(2))) float f32x2;
typedef __attribute__((ext_vector_type(4))) unsigned u32x4;
typedef __attribute__((ext_vector_type(2))) unsigned u32x2;
typedef __attribute__((ext_vector_type(2))) __bf16 bf16x2_t;
typedef __amdgpu_buffer_rsrc_t rsrc_t;

#define DI __device__ __forceinline__
#define QBAR 2
#define QRN 17
#define TQ 12
#define TG 4
#define K1MAP 1
#define QTM 1
#define WOT 1
#define CODEPF 53

#define PROF_NK 8
#define PROF_NB 1024
#define PROF_NS 8
__device__ unsigned long long g_stamps[PROF_NK * PROF_NB * PROF_NS];
#define STAMPDECL unsigned long long _tnw = 0, _cnw = 0
#define STAMPNW(k, s) do { _tnw = __builtin_amdgcn_s_memrealtime(); _cnw = __builtin_amdgcn_s_memtime(); __builtin_amdgcn_s_waitcnt(0xC07F); } while (0)
#define STAMPFLUSH(k, s) do { unsigned long long _c2 = __builtin_amdgcn_s_memtime(); if (threadIdx.x == 0 && blockIdx.x < PROF_NB) { g_stamps[((k) * PROF_NB + blockIdx.x) * PROF_NS + (s)] = _tnw; g_stamps[((k) * PROF_NB + blockIdx.x) * PROF_NS + 7] = _c2 - _cnw; } } while (0)
#define STAMP(k, s) do { if (threadIdx.x == 0 && blockIdx.x < PROF_NB) g_stamps[((k) * PROF_NB + blockIdx.x) * PROF_NS + (s)] = __builtin_amdgcn_s_memrealtime(); } while (0)
#define STAMPT(k, s, T) do { if (threadIdx.x == (T) && blockIdx.x < PROF_NB) g_stamps[((k) * PROF_NB + blockIdx.x) * PROF_NS + (s)] = __builtin_amdgcn_s_memrealtime(); } while (0)

constexpr int S_LEN = 2048;
constexpr int D_DIM = 1024;
constexpr int F_DIM = 3584;

template <int V> struct ic { static constexpr int value = V; };
template <int N, int I = 0, class F>
DI void static_for(F&& f) {
  if constexpr (I < N) { f(ic<I>{}); static_for<N, I + 1>(f); }
}

DI float bf_lo(unsigned u) { return __uint_as_float(u << 16); }
DI float bf_hi(unsigned u) { return __uint_as_float(u & 0xffff0000u); }
DI unsigned pk_bf16(float lo, float hi) {
  const f32x2 v = {lo, hi};
  return __builtin_bit_cast(unsigned, __builtin_convertvector(v, bf16x2_t));
}
DI rsrc_t mk_rsrc(const void* p, unsigned bytes) { return __builtin_amdgcn_make_buffer_rsrc((void*)p, (short)0, (int)bytes, 0x00020000); }

template <int N>
DI void wait_vm() { asm volatile("s_waitcnt vmcnt(%0)" ::"n"(N > 63 ? 63 : N) : "memory"); }
#define WAIT_LGKM0() do { asm volatile("s_waitcnt lgkmcnt(0)" ::: "memory"); __builtin_amdgcn_sched_barrier(0); } while (0)
#define SCHED0() __builtin_amdgcn_sched_barrier(0)

#define MFMA_A(C, B, A) asm volatile("v_mfma_f32_16x16x32_bf16 %0, %1, %2, %0" : "+a"(C) : "v"(B), "a"(A))
#define MFMA_AV(C, B, A) asm volatile("v_mfma_f32_16x16x32_bf16 %0, %1, %2, %0" : "+v"(C) : "v"(B), "v"(A))
#define PERM_LO(D, X, Y) asm volatile("v_perm_b32 %0, %1, %2, %3" : "=v"(D) : "v"(Y), "v"(X), "s"(0x05040100u))
#define PERM_HI(D, X, Y) asm volatile("v_perm_b32 %0, %1, %2, %3" : "=v"(D) : "v"(Y), "v"(X), "s"(0x07060302u))
template <int OFF>
DI void bload_v(u32x4& d, unsigned voff, rsrc_t rs, unsigned soff) {
  asm volatile("buffer_load_dwordx4 %0, %1, %2, %3 offen offset:%4" : "+v"(d) : "v"(voff), "s"(rs), "s"(soff), "i"(OFF) : "memory");
}
template <int OFF>
DI void bload_a(u32x4& d, unsigned voff, rsrc_t rs, unsigned soff) {
  asm volatile("buffer_load_dwordx4 %0, %1, %2, %3 offen offset:%4" : "+a"(d) : "v"(voff), "s"(rs), "s"(soff), "i"(OFF) : "memory");
}
template <int OFF>
DI void bload2_v(u32x2& d, unsigned voff, rsrc_t rs, unsigned soff) {
  asm volatile("buffer_load_dwordx2 %0, %1, %2, %3 offen offset:%4" : "+v"(d) : "v"(voff), "s"(rs), "s"(soff), "i"(OFF) : "memory");
}
template <int OFF>
DI void bload1_v(unsigned& d, unsigned voff, rsrc_t rs, unsigned soff) {
  asm volatile("buffer_load_dword %0, %1, %2, %3 offen offset:%4" : "+v"(d) : "v"(voff), "s"(rs), "s"(soff), "i"(OFF) : "memory");
}
template <int OFF>
DI void bload_lds(unsigned m0v, unsigned voff, rsrc_t rs, unsigned soff) {
  asm volatile("s_mov_b32 m0, %0\n\ts_nop 0\n\tbuffer_load_dwordx4 %1, %2, %3 offen offset:%4 lds" ::"s"(m0v), "v"(voff), "s"(rs), "s"(soff), "i"(OFF) : "memory");
}
DI void touch_lds(unsigned m0v, unsigned voff, rsrc_t rs, unsigned soff) {
  asm volatile("s_mov_b32 m0, %0\n\ts_nop 0\n\tbuffer_load_dword %1, %2, %3 offen lds" ::"s"(m0v), "v"(voff), "s"(rs), "s"(soff) : "memory");
}
DI void touch_nt(unsigned m0v, unsigned voff, rsrc_t rs, unsigned soff) {
  asm volatile("s_mov_b32 m0, %0\n\ts_nop 0\n\tbuffer_load_dword %1, %2, %3 offen nt lds\n\tbuffer_load_dword %1, %2, %3 offen offset:64 sc1 lds" ::"s"(m0v), "v"(voff), "s"(rs), "s"(soff) : "memory");
}
template <int OFF>
DI void bload_lds_nt(unsigned m0v, unsigned voff, rsrc_t rs, unsigned soff) {
  asm volatile("s_mov_b32 m0, %0\n\ts_nop 0\n\tbuffer_load_dwordx4 %1, %2, %3 offen offset:%4 nt lds" ::"s"(m0v), "v"(voff), "s"(rs), "s"(soff), "i"(OFF) : "memory");
}
template <int OFF>
DI void dsr128(u32x4& d, unsigned addr) { asm volatile("ds_read_b128 %0, %1 offset:%2" : "=v"(d) : "v"(addr), "i"(OFF) : "memory"); }
template <int OFF>
DI void dsr64(u32x2& d, unsigned addr) { asm volatile("ds_read_b64 %0, %1 offset:%2" : "=v"(d) : "v"(addr), "i"(OFF) : "memory"); }
template <int OFF>
DI void dsr_tr(u32x2& d, unsigned addr) { asm volatile("ds_read_b64_tr_b16 %0, %1 offset:%2" : "=v"(d) : "v"(addr), "i"(OFF) : "memory"); }
template <int OFF>
DI void dsw128(unsigned addr, u32x4 d) { asm volatile("ds_write_b128 %0, %1 offset:%2" ::"v"(addr), "v"(d), "i"(OFF) : "memory"); }
template <int OFF>
DI void dsw128a(unsigned addr, const f32x4& d) { asm volatile("ds_write_b128 %0, %1 offset:%2" ::"v"(addr), "a"(d), "i"(OFF) : "memory"); }
template <int OFF>
DI void dsw32(unsigned addr, unsigned d) { asm volatile("ds_write_b32 %0, %1 offset:%2" ::"v"(addr), "v"(d), "i"(OFF) : "memory"); }

DI void gst128(void* p, u32x4 v) {
  asm volatile("global_store_dwordx4 %0, %1, off sc1\n\ts_nop 1" ::"v"(p), "v"(v) : "memory");
}
DI f32x2 lo2(f32x4 v) { return __builtin_shufflevector(v, v, 0, 1); }
DI f32x2 hi2(f32x4 v) { return __builtin_shufflevector(v, v, 2, 3); }
DI float rowsum4(float v) {
  const unsigned u = __float_as_uint(v);
  const auto a = __builtin_amdgcn_permlane16_swap(u, u, false, false);
  const unsigned t = __float_as_uint(__uint_as_float(a[0]) + __uint_as_float(a[1]));
  const auto b = __builtin_amdgcn_permlane32_swap(t, t, false, false);
  return __uint_as_float(b[0]) + __uint_as_float(b[1]);
}
DI void split4(const f32x4 v, u32x2& hi, u32x2& lo) {
  hi[0] = pk_bf16(v[0], v[1]); hi[1] = pk_bf16(v[2], v[3]);
  lo[0] = pk_bf16(v[0] - bf_lo(hi[0]), v[1] - bf_hi(hi[0]));
  lo[1] = pk_bf16(v[2] - bf_lo(hi[1]), v[3] - bf_hi(hi[1]));
}
DI float silu_f(float v) { return v / (1.0f + __expf(-v)); }
DI float rbf(float v) { return bf_lo(pk_bf16(v, v)); }
DI float rowmax4(float v) {
  const unsigned u = __float_as_uint(v);
  const auto a = __builtin_amdgcn_permlane16_swap(u, u, false, false);
  const unsigned t = __float_as_uint(fmaxf(__uint_as_float(a[0]), __uint_as_float(a[1])));
  const auto b = __builtin_amdgcn_permlane32_swap(t, t, false, false);
  return fmaxf(__uint_as_float(b[0]), __uint_as_float(b[1]));
}
DI float gain_nz(float m) {
  const float g = rbf(1.0f + rbf(m));
  return __builtin_fabsf(g) < 8.67361738e-19f ? 8.67361738e-19f : g;
}
template <int OFF>
DI void bload_sc1(u32x4& d, unsigned voff, rsrc_t rs, unsigned soff) {
  asm volatile("buffer_load_dwordx4 %0, %1, %2, %3 offen offset:%4 sc1" : "+v"(d) : "v"(voff), "s"(rs), "s"(soff), "i"(OFF) : "memory");
}
DI void bload1_sc1(unsigned& d, unsigned voff, rsrc_t rs) {
  asm volatile("buffer_load_dword %0, %1, %2, 0 offen sc1" : "+v"(d) : "v"(voff), "s"(rs) : "memory");
}

__global__ __launch_bounds__(512) void pre_kernel(const bf16* __restrict__ x, const bf16* __restrict__ c,
                                                  const bf16* __restrict__ wmod, float* __restrict__ modp,
                                                  float* __restrict__ rstat1, const bf16* __restrict__ Wq) {
  __shared__ __attribute__((aligned(16))) float red[8 * 2 * 128 + 256];
  STAMP(0, 0);
  const int tid = threadIdx.x, lane = tid & 63, w = tid >> 6;
  if (blockIdx.x < 192) {
    const int fr = lane & 15, fq = lane >> 4;
    const int cg = blockIdx.x >> 2, kq = blockIdx.x & 3;
    const int col0 = cg * 128;
    const int k0 = kq * 256 + w * 32 + fq * 8;
    u32x4 raw[8];
#pragma unroll
    for (int j = 0; j < 8; ++j) {
      const u32x4* p_ = reinterpret_cast<const u32x4*>(wmod + (size_t)(k0 + j) * 6144 + col0 + fr * 8);
      raw[j] = __builtin_nontemporal_load(p_);
    }
    const u32x4 cv = *reinterpret_cast<const u32x4*>(c + k0);
    u32x4 cf;
#pragma unroll
    for (int e = 0; e < 4; ++e) {
      const float a = silu_f(bf_lo(cv[e])), b = silu_f(bf_hi(cv[e]));
      cf[e] = (fr == 0) ? pk_bf16(a, b) : 0u;
    }
    f32x4 acc[8];
#pragma unroll
    for (int p = 0; p < 4; ++p) {
      u32x4 lo, hi;
#pragma unroll
      for (int t = 0; t < 4; ++t) {
        lo[t] = __builtin_amdgcn_perm(raw[2 * t + 1][p], raw[2 * t][p], 0x05040100u);
        hi[t] = __builtin_amdgcn_perm(raw[2 * t + 1][p], raw[2 * t][p], 0x07060302u);
      }
      acc[2 * p] = __builtin_amdgcn_mfma_f32_16x16x32_bf16(__builtin_bit_cast(bf16x8, lo), __builtin_bit_cast(bf16x8, cf), f32x4{0, 0, 0, 0}, 0, 0, 0);
      acc[2 * p + 1] = __builtin_amdgcn_mfma_f32_16x16x32_bf16(__builtin_bit_cast(bf16x8, hi), __builtin_bit_cast(bf16x8, cf), f32x4{0, 0, 0, 0}, 0, 0, 0);
    }
    if (fr == 0) {
#pragma unroll
      for (int r = 0; r < 4; ++r) {
        float* d = red + w * 128 + fq * 32 + r * 8;
        *reinterpret_cast<f32x4*>(d) = f32x4{acc[0][r], acc[1][r], acc[2][r], acc[3][r]};
        *reinterpret_cast<f32x4*>(d + 4) = f32x4{acc[4][r], acc[5][r], acc[6][r], acc[7][r]};
      }
    }
    __syncthreads();
    if (tid < 128) {
      float s = 0;
#pragma unroll
      for (int g = 0; g < 8; ++g) s += red[g * 128 + tid];
      modp[kq * 6144 + col0 + tid] = s;
    }
    STAMP(0, 1);
    return;
  }
  const int row0 = (blockIdx.x - 192) * 16 + w * 2;
  u32x4 a[2][2];
#pragma unroll
  for (int i = 0; i < 2; ++i) {
    a[i][0] = *reinterpret_cast<const u32x4*>(x + (size_t)(row0 + i) * D_DIM + lane * 8);
    a[i][1] = *reinterpret_cast<const u32x4*>(x + (size_t)(row0 + i) * D_DIM + 512 + lane * 8);
  }
  if (blockIdx.x < 192 + 8) {
    unsigned long long pc;
    asm volatile("s_getpc_b64 %0" : "=s"(pc));
    const rsrc_t rsC = mk_rsrc((const void*)(uintptr_t)(pc & ~255ull), CODEPF * 1024);
#pragma unroll
    for (int i = 0; i < (CODEPF + 7) / 8; ++i) bload_lds<0>((unsigned)(uintptr_t)(red + 2048), tid * 16, rsC, i * 8192);
  }
#pragma unroll
  for (int i = 0; i < 2; ++i) {
    float v[16];
#pragma unroll
    for (int j = 0; j < 8; ++j) { v[2 * j] = bf_lo(a[i][j >> 2][j & 3]); v[2 * j + 1] = bf_hi(a[i][j >> 2][j & 3]); }
    float s = 0;
#pragma unroll
    for (int j = 0; j < 16; ++j) s += v[j];
#pragma unroll
    for (int o = 32; o > 0; o >>= 1) s += __shfl_xor(s, o);
    const float mean = s * (1.0f / D_DIM);
    float q = 0;
#pragma unroll
    for (int j = 0; j < 16; ++j) { const float d = v[j] - mean; q += d * d; }
#pragma unroll
    for (int o = 32; o > 0; o >>= 1) q += __shfl_xor(q, o);
    const float rstd = rsqrtf(q * (1.0f / D_DIM) + 1e-5f);
    if (lane == 0) *reinterpret_cast<f32x2*>(rstat1 + (row0 + i) * 2) = f32x2{rstd, -mean * rstd};
  }
  STAMP(0, 2);
}

#define QNT 0
#define K1POS 2
DI void k1_touch(char* sink, const bf16* Wq, const bf16* Wo, int part) {
  const int lane = threadIdx.x & 63, w = threadIdx.x >> 6;
  const rsrc_t rsP = mk_rsrc(Wq, D_DIM * 3 * D_DIM * 2);
  const unsigned vo = (unsigned)(((blockIdx.x >> 3) * 32 + (threadIdx.x >> 3)) * 6144 + (blockIdx.x & 3) * 512 + (threadIdx.x & 3) * 128);
  const unsigned v2 = (unsigned)(((blockIdx.x >> 3) * 32 + (threadIdx.x >> 2)) * 6144 + (blockIdx.x & 3) * 512 + (threadIdx.x & 3) * 128);
  if (part & 1) touch_lds((unsigned)(uintptr_t)sink, vo + ((threadIdx.x >> 2) & 1) * 2048, rsP, 0);
  if (part & 2) {
    if (w < 2) touch_lds((unsigned)(uintptr_t)sink, v2, rsP, 4096);
    else touch_lds((unsigned)(uintptr_t)sink, (w == 2 && WOT) ? blockIdx.x * 8192 + lane * 128 : 0x40000000u, mk_rsrc(Wo, D_DIM * D_DIM * 2), 0);
  }
}
__global__ __launch_bounds__(256) void ln_apply_kernel(const bf16* __restrict__ x, const float* __restrict__ modp,
                                                       const float* __restrict__ rstat1, bf16* __restrict__ H,
                                                       const bf16* __restrict__ Wq, unsigned* __restrict__ flags,
                                                       const bf16* __restrict__ Wo) {
  __shared__ __attribute__((aligned(16))) float tab[4][64];
  __shared__ __attribute__((aligned(16))) char sink[1024];
  STAMP(1, 0);
  const int lane = threadIdx.x & 63, w = threadIdx.x >> 6;
  const int fr = lane & 15, fq = lane >> 4;
  const int kb = (blockIdx.x >> 5) * 4 + w, mb0 = (blockIdx.x & 7) + ((blockIdx.x >> 3) & 3) * 32;
  constexpr int MBS = 8;
  u32x4 mr, xv[4];
  u32x2 rr[4];
  asm volatile("" : "=v"(mr));
  {
    bload_v<0>(mr, (unsigned)((fq * 6144 + ((lane >> 3) & 1) * 1024 + kb * 32 + (lane & 7) * 4) * 4), mk_rsrc(modp, 4 * 6144 * 4), 0);
    const rsrc_t rsX = mk_rsrc(x, S_LEN * D_DIM * 2), rsS = mk_rsrc(rstat1, S_LEN * 8);
    const unsigned vx = (unsigned)(((mb0 * 16 + fr) * D_DIM + kb * 32 + fq * 8) * 2), vs = (unsigned)((mb0 * 16 + fr) * 8);
#pragma unroll
    for (int i = 0; i < 4; ++i) {
      asm volatile("" : "=v"(xv[i]), "=v"(rr[i]));
      bload_v<0>(xv[i], vx, rsX, i * 32768 * MBS);
      bload2_v<0>(rr[i], vs, rsS, i * 128 * MBS);
    }
    if (K1POS == 0) k1_touch(sink, Wq, Wo, 3);
    if (K1POS == 2) k1_touch(sink, Wq, Wo, 2);
  }
  asm volatile("s_waitcnt vmcnt(%9)" : "+v"(mr), "+v"(xv[0]), "+v"(xv[1]), "+v"(xv[2]), "+v"(xv[3]), "+v"(rr[0]), "+v"(rr[1]), "+v"(rr[2]), "+v"(rr[3]) : "n"((K1POS == 0 ? 2 : (K1POS == 2 ? 1 : 0)) * (QNT ? 2 : 1)) : "memory");
  f32x4 mv = __builtin_bit_cast(f32x4, mr);
  f32x2 rs[4];
#pragma unroll
  for (int i = 0; i < 4; ++i) rs[i] = __builtin_bit_cast(f32x2, rr[i]);
#pragma unroll
  for (int e = 0; e < 4; ++e) {
    mv[e] = rbf(rowsum4(mv[e]));
    if (lane & 8) mv[e] = rbf(1.0f + mv[e]);
  }
  if (lane < 16) *reinterpret_cast<f32x4*>(&tab[w][lane * 4]) = mv;
  __builtin_amdgcn_wave_barrier();
  asm volatile("s_waitcnt lgkmcnt(0)" ::: "memory");
  const f32x4 s0 = *reinterpret_cast<const f32x4*>(&tab[w][fq * 8]), s1 = *reinterpret_cast<const f32x4*>(&tab[w][fq * 8 + 4]);
  const f32x4 g0 = *reinterpret_cast<const f32x4*>(&tab[w][32 + fq * 8]), g1 = *reinterpret_cast<const f32x4*>(&tab[w][32 + fq * 8 + 4]);
#pragma unroll
  for (int i = 0; i < 4; ++i) {
    u32x4 ho;
#pragma unroll
    for (int d = 0; d < 4; ++d) {
      const f32x4 gg = d < 2 ? g0 : g1, ss = d < 2 ? s0 : s1;
      const float a = (bf_lo(xv[i][d]) * rs[i][0] + rs[i][1]) * gg[(d & 1) * 2] + ss[(d & 1) * 2];
      const float b = (bf_hi(xv[i][d]) * rs[i][0] + rs[i][1]) * gg[(d & 1) * 2 + 1] + ss[(d & 1) * 2 + 1];
      ho[d] = pk_bf16(a, b);
    }
    gst128((char*)H + ((size_t)(mb0 + i * MBS) * 32 + kb) * 1024 + lane * 16, ho);
  }
  if (threadIdx.x < 4) flags[blockIdx.x * 4 + threadIdx.x] = 0u;
  if (threadIdx.x == 4) flags[1024 + blockIdx.x * 32] = 0u;
  STAMP(1, 1);
  if (K1POS == 1) k1_touch(sink, Wq, Wo, 3);
  if (K1POS == 2) k1_touch(sink, Wq, Wo, 1);
}

#define GMAP 0
#define T1 0
#define T2 0
#define GNT 0
#define TW 0
#define TP 8
#define TS 2
#define TN 2
#define GDEP 12
constexpr int Q_SLOT = 21504, Q_NSL = 7;
constexpr int QX_WH = 4 * Q_SLOT;
constexpr int QX_HX = 65536;
constexpr int QX_GT = QX_WH + 65536;
constexpr int QX_CNT = QX_GT + 512;
constexpr int QX_SINK = QX_CNT + 512;
constexpr int Q_LDS = QX_SINK + 1024;

DI unsigned lds_add(unsigned addr, unsigned v0) {
  unsigned o;
  asm volatile("ds_add_rtn_u32 %0, %1, %2\n\ts_waitcnt lgkmcnt(0)" : "=v"(o) : "v"(addr), "v"(v0) : "memory");
  return __builtin_amdgcn_readfirstlane(o);
}
template <int SLP>
DI void lds_wait(unsigned addr, unsigned n) {
  for (;;) {
    unsigned v;
    asm volatile("ds_read_b32 %0, %1\n\ts_waitcnt lgkmcnt(0)" : "=v"(v) : "v"(addr) : "memory");
    if (__builtin_amdgcn_readfirstlane(v) >= n) break;
    __builtin_amdgcn_s_sleep(SLP);
  }
}
DI void wo_piece(unsigned smem_base, unsigned p, unsigned rot, unsigned voff, rsrc_t rs) {
  const unsigned pb = p >> 2, kb = (pb & 24u) | ((pb + rot) & 7u);
  bload_lds<0>(smem_base + (p < 64u ? QX_WH + p * 1024u : (p - 64u) * 1024u), voff, rs, kb * 65536u + (p & 3u) * 16384u);
}

DI void gu_touch(unsigned smem_base, int e, unsigned vgu, const bf16* wg, const bf16* wu, bool nt) {
  if (nt) touch_nt(smem_base + QX_SINK, vgu, mk_rsrc((e & 1) ? wu : wg, D_DIM * F_DIM * 2), (unsigned)(e >> 1) * 256u * (F_DIM * 2));
  else touch_lds(smem_base + QX_SINK, vgu, mk_rsrc((e & 1) ? wu : wg, D_DIM * F_DIM * 2), (unsigned)(e >> 1) * 256u * (F_DIM * 2));
}
DI void qkv_loader(unsigned smem_base, int lane, int sw, int m0, int n0, const bf16* __restrict__ H, const bf16* __restrict__ W,
                   unsigned rot, unsigned voffO, rsrc_t rsO) {
  const rsrc_t rsH = mk_rsrc(H, S_LEN * D_DIM * 2);
  const rsrc_t rsW = mk_rsrc(W, D_DIM * 3 * D_DIM * 2);
  int fb0 = (m0 >> 4) - 1 + 3 * sw; fb0 = fb0 < 0 ? 0 : fb0;
  const unsigned voffH0 = (unsigned)(fb0 * 32768 + lane * 16);
  const unsigned voffH1 = (unsigned)(((m0 >> 4) + 3 * sw) * 32768 + lane * 16);
  unsigned voffW[2];
#pragma unroll
  for (int i = 0; i < 2; ++i) {
    const int r = i * 8 + (lane >> 3), ch = lane & 7;
    const int g = ((r >> 1) & 1) | (((r >> 3) & 1) << 1);
    voffW[i] = (unsigned)(r * 6144 + sw * 2048 + (n0 + (((ch >> 1) ^ g) * 16) + (ch & 1) * 8) * 2);
  }
  const unsigned m0a = smem_base + sw * 3072, m0w = smem_base + 9216 + sw * 4096;
#define Q_LOAD(SOFF, U)                                                                                          \
  do {                                                                                                           \
    const unsigned so_ = (unsigned)(U) * 1024u;                                                                  \
    bload_lds<0>(m0a + (SOFF), voffH0, rsH, so_);                                                                \
    bload_lds<0>(m0a + (SOFF) + 1024, voffH1, rsH, so_);                                                         \
    bload_lds<0>(m0a + (SOFF) + 2048, voffH1, rsH, so_ + 32768u);                                                \
    _Pragma("unroll") for (int i = 0; i < 4; ++i)                                                                \
      bload_lds<0>(m0w + (SOFF) + i * 1024, voffW[i & 1], rsW, (unsigned)(U) * 196608u + (i >> 1) * 98304u);     \
  } while (0)
#pragma unroll
  for (int u = 0; u < Q_NSL - 1; ++u) Q_LOAD(u * Q_SLOT, u);
  wait_vm<(Q_NSL - 2) * 7>();
  __builtin_amdgcn_s_barrier();
  unsigned sb = (Q_NSL - 1) * Q_SLOT;
  for (int u = 0; u < 32 - (Q_NSL - 1); ++u) {
    Q_LOAD(sb, u + Q_NSL - 1);
    sb = (sb == (Q_NSL - 1) * Q_SLOT) ? 0u : sb + Q_SLOT;
    wait_vm<(Q_NSL - 2) * 7>();
    __builtin_amdgcn_s_barrier();
  }
  for (int t = 0; t < 6; ++t) {
    const unsigned p0 = (unsigned)(t * 21 + (t >= 3 ? 1 : 0) + sw * 7);
#pragma unroll
    for (int i = 0; i < 7; ++i) wo_piece(smem_base, p0 + i, rot, voffO, rsO);
    if (t < 5) wait_vm<(Q_NSL - 2) * 7>();
    __builtin_amdgcn_s_barrier();
  }
}

DI f32x4 mfma0(u32x4 a, u32x4 b) {
  return __builtin_amdgcn_mfma_f32_16x16x32_bf16(__builtin_bit_cast(bf16x8, a), __builtin_bit_cast(bf16x8, b), f32x4{0, 0, 0, 0}, 0, 0, 0);
}
DI u32x4 pack8(const f32x4 a, const f32x4 b) { return u32x4{pk_bf16(a[0], a[1]), pk_bf16(a[2], a[3]), pk_bf16(b[0], b[1]), pk_bf16(b[2], b[3])}; }
template <class OP>
DI float rows4(const float (&v)[4], OP op) {
  const auto a = __builtin_amdgcn_permlane16_swap(__float_as_uint(v[0]), __float_as_uint(v[1]), false, false);
  const auto b = __builtin_amdgcn_permlane16_swap(__float_as_uint(v[2]), __float_as_uint(v[3]), false, false);
  const float t0 = op(__uint_as_float(a[0]), __uint_as_float(a[1])), t1 = op(__uint_as_float(b[0]), __uint_as_float(b[1]));
  const auto c = __builtin_amdgcn_permlane32_swap(__float_as_uint(t0), __float_as_uint(t1), false, false);
  return op(__uint_as_float(c[0]), __uint_as_float(c[1]));
}
DI void bcast4(float t, float (&v)[4]) {
  const auto c = __builtin_amdgcn_permlane32_swap(__float_as_uint(t), __float_as_uint(t), false, false);
  const auto a = __builtin_amdgcn_permlane16_swap(c[0], c[0], false, false);
  const auto b = __builtin_amdgcn_permlane16_swap(c[1], c[1], false, false);
  v[0] = __uint_as_float(a[0]); v[1] = __uint_as_float(a[1]); v[2] = __uint_as_float(b[0]); v[3] = __uint_as_float(b[1]);
}
DI float fmax2(float a, float b) { return __builtin_amdgcn_fmed3f(a, b, __builtin_inff()); }

template <int WM>
DI void qkv_mfma(unsigned smem_base, int lane, int wn, int bm, int bn, bf16* __restrict__ O) {
  const int fr = lane & 15, fq = lane >> 4;
  unsigned bb0, bb1;
  const unsigned ab = smem_base + lane * 16;
  {
    const int q = (lane >> 2) & 3, p = lane & 3;
    const int r = 8 * fq + q;
    const int g = ((r >> 1) & 1) | (((r >> 3) & 1) << 1);
    bb0 = smem_base + 9216 + r * 128 + (((wn * 2) ^ g) * 32) + 8 * p;
    bb1 = bb0 ^ 32u;
  }
  const bool par = fr & 1, c0 = 4 * fq + (fr & 1) <= fr, c1 = 4 * fq + 2 + (fr & 1) <= fr;
  const unsigned s0 = par ? 0x01000c0cu : 0x0c0c0100u, s1 = par ? 0x03020c0cu : 0x0c0c0302u;
  u32x4 sel = {c0 ? s0 : 0x0c0c0c0cu, c1 ? s1 : 0x0c0c0c0cu, c0 ? 0x0c0c0c0cu : s0, c1 ? 0x0c0c0c0cu : s1};
  unsigned hx = ab + QX_HX + wn * 3072;
  asm volatile("" : "+v"(sel), "+v"(hx));
  const rsrc_t rsO = mk_rsrc((char*)O + (size_t)((bm * 2 + WM) * 32 + bn * 2 + wn) * 4096, 4096);
  f32x4 acc[4][6], acch[2];
#pragma unroll
  for (int i = 0; i < 4; ++i)
#pragma unroll
    for (int j = 0; j < 6; ++j) { acc[i][j] = f32x4{0, 0, 0, 0}; asm volatile("" : "+v"(acc[i][j])); }
#pragma unroll
  for (int j = 0; j < 2; ++j) { acch[j] = f32x4{0, 0, 0, 0}; asm volatile("" : "+v"(acch[j])); }
  __builtin_amdgcn_s_setprio(2);
  __builtin_amdgcn_s_barrier();
  STAMPDECL;
  STAMPNW(2, 1);
  u32x4 aX[5], aY[5];
  u32x2 bXl[6], bXh[6], bYl[6], bYh[6];
  unsigned ra = ab, rb0 = bb0, rb1 = bb1, so = 0;
#define Q_READ(AR, BL, BH, R)                                                                                    \
  do {                                                                                                           \
    if ((R) < 12) {                                                                                              \
      constexpr int jb = ((R) < 12 ? (R) : 0) >> 1;                                                              \
      if ((R) & 1) dsr_tr<(jb >> 1) * 4096 + 512>(BH[jb], (jb & 1) ? rb1 : rb0);                                 \
      else dsr_tr<(jb >> 1) * 4096>(BL[jb], (jb & 1) ? rb1 : rb0);                                               \
    } else if ((R) < 16) {                                                                                       \
      constexpr int ia = ((R) >= 12 && (R) < 16) ? (R) - 12 : 0;                                                 \
      dsr128<1024 + WM * 4096 + ia * 1024>(AR[ia], ra);                                                          \
    } else {                                                                                                     \
      dsr128<0>(AR[4], ra);                                                                                      \
    }                                                                                                            \
  } while (0)
#define Q_MFMA(AR, BB, N)                                                                                        \
  do {                                                                                                           \
    if ((N) < 16) MFMA_AV(acc[(N) & 3][((N) < 24 ? (N) : 0) >> 2], BB[((N) < 24 ? (N) : 0) >> 2], AR[(N) & 3]);  \
    else if ((N) < 24) MFMA_AV(acc[(N) & 3][((N) < 24 ? (N) : 0) >> 2], AR[(N) & 3], BB[((N) < 24 ? (N) : 0) >> 2]); \
    else if (WM == 0) MFMA_AV(acch[(N) & 1], BB[2 + ((N) & 1)], AR[4]);                                          \
    else MFMA_AV(acch[(N) & 1], AR[4], BB[4 + ((N) & 1)]);                                                       \
  } while (0)
#define Q_ADV(K)                                                                                                 \
  do {                                                                                                           \
    if ((K) == 0) asm volatile("s_add_u32 %0, %0, %1\n\ts_cmp_eq_u32 %0, %2\n\ts_cselect_b32 %0, 0, %0" : "+s"(so) : "n"(Q_SLOT), "n"(Q_NSL * Q_SLOT) : "scc"); \
    else if ((K) == 1) asm volatile("v_add_u32 %0, %1, %2" : "=v"(rb0) : "s"(so), "v"(bb0));                     \
    else if ((K) == 2) asm volatile("v_add_u32 %0, %1, %2" : "=v"(rb1) : "s"(so), "v"(bb1));                     \
    else asm volatile("v_add_u32 %0, %1, %2" : "=v"(ra) : "s"(so), "v"(ab));                                     \
  } while (0)
#define Q_UNIT(AR, BL, BH, NAR, NBL, NBH)                                                                        \
  do {                                                                                                           \
    WAIT_LGKM0();                                                                                                \
    u32x4 bb[6];                                                                                                 \
    _Pragma("unroll") for (int j = 0; j < 6; ++j) bb[j] = u32x4{BL[j][0], BL[j][1], BH[j][0], BH[j][1]};         \
    SCHED0();                                                                                                    \
    static_for<26>([&](auto N) {                                                                                 \
      constexpr int n = N.value;                                                                                 \
      if constexpr (n == QBAR) __builtin_amdgcn_s_barrier();                                                     \
      Q_MFMA(AR, bb, n);                                                                                         \
      if constexpr (n >= QBAR && n < QBAR + 17) Q_READ(NAR, NBL, NBH, n - QBAR);                                 \
      if constexpr (n >= QBAR + 18 && n < QBAR + 22) Q_ADV(n - QBAR - 18);                                       \
    });                                                                                                          \
    SCHED0();                                                                                                    \
  } while (0)
  static_for<17>([&](auto R) { Q_READ(aX, bXl, bXh, R.value); });
  static_for<4>([&](auto K) { Q_ADV(K.value); });
  for (int u = 0; u < 32; u += 2) {
    Q_UNIT(aX, bXl, bXh, aY, bYl, bYh);
    Q_UNIT(aY, bYl, bYh, aX, bXl, bXh);
  }
  asm volatile("s_waitcnt lgkmcnt(0)\n\ts_nop 7" ::: "memory");
#pragma unroll
  for (int i = 0; i < 5; ++i) asm volatile("" : "+v"(aX[i]));
#pragma unroll
  for (int j = 0; j < 6; ++j) asm volatile("" : "+v"(bXl[j]), "+v"(bXh[j]));
#pragma unroll
  for (int i = 0; i < 4; ++i)
#pragma unroll
    for (int j = 0; j < 6; ++j) asm volatile("" : "+v"(acc[i][j]));
  asm volatile("" : "+v"(acch[0]), "+v"(acch[1]));
  SCHED0();
  STAMP(2, 2); STAMPFLUSH(2, 1);
  u32x4 kk[5], vv[5];
  if (WM == 0) {
    kk[4] = pack8(acc[3][2], acc[3][3]); vv[4] = pack8(acc[3][4], acc[3][5]);
    dsw128<0>(hx, kk[4]); dsw128<1024>(hx, vv[4]);
  } else {
    dsw128<2048>(hx, pack8(acch[0], acch[1]));
  }
  asm volatile("s_waitcnt lgkmcnt(0)" ::: "memory");
  __builtin_amdgcn_s_barrier();
  if (WM == 0) dsr128<2048>(vv[0], hx);
  else { dsr128<0>(kk[0], hx); dsr128<1024>(vv[0], hx); }
  SCHED0();
  if (WM == 0) kk[0] = pack8(acch[0], acch[1]);
#pragma unroll
  for (int i = 0; i < (WM == 0 ? 3 : 4); ++i) kk[i + 1] = pack8(acc[i][2], acc[i][3]);
  const float cs = 0.17677669529663687f * 1.4426950408889634f;
  float l0[4], l1[4], tm[4];
#pragma unroll
  for (int ii = 1; ii < 5; ++ii) {
    const int i = ii & 3;
    const u32x4 q = pack8(acc[i][0], acc[i][1]);
    if (WM == 1 && i == 0) asm volatile("s_waitcnt lgkmcnt(0)" : "+v"(kk[0]), "+v"(vv[0])::"memory");
    const f32x4 sc = mfma0(kk[i + 1], q), sp = mfma0(kk[i], q);
    float p0 = par ? sp[1] : sp[0], p1 = par ? sp[3] : sp[2];
    if (WM == 0 && i == 0 && bm == 0) { p0 = -5.656854249e9f; p1 = -5.656854249e9f; }
    l0[i] = c0 ? (par ? sc[1] : sc[0]) : p0;
    l1[i] = c1 ? (par ? sc[3] : sc[2]) : p1;
    tm[i] = fmax2(l0[i], l1[i]);
  }
#pragma unroll
  for (int i = 0; i < (WM == 0 ? 3 : 4); ++i) vv[i + 1] = pack8(acc[i][4], acc[i][5]);
  bcast4(rows4(tm, [](float a, float b) { return fmax2(a, b); }), tm);
#pragma unroll
  for (int i = 0; i < 4; ++i) {
    l0[i] = __builtin_amdgcn_exp2f((l0[i] - tm[i]) * cs);
    l1[i] = __builtin_amdgcn_exp2f((l1[i] - tm[i]) * cs);
    tm[i] = l0[i] + l1[i];
  }
  bcast4(__builtin_amdgcn_rcpf(rows4(tm, [](float a, float b) { return a + b; })), tm);
  if (WM == 0) asm volatile("s_waitcnt lgkmcnt(0)" : "+v"(vv[0])::"memory");
#pragma unroll
  for (int i = 0; i < 4; ++i) {
    const unsigned pp = pk_bf16(l0[i] * tm[i], l1[i] * tm[i]);
    const u32x4 pr = {__builtin_amdgcn_perm(pp, pp, sel[0]), __builtin_amdgcn_perm(pp, pp, sel[1]),
                      __builtin_amdgcn_perm(pp, pp, sel[2]), __builtin_amdgcn_perm(pp, pp, sel[3])};
    const u32x4 ov = pack8(mfma0(u32x4{vv[i + 1][0], vv[i + 1][1], vv[i][0], vv[i][1]}, pr), mfma0(u32x4{vv[i + 1][2], vv[i + 1][3], vv[i][2], vv[i][3]}, pr));
    asm volatile("buffer_store_dwordx4 %0, %1, %2, 0 offen offset:%3 sc1\n\ts_nop 1" ::"v"(ov), "v"(lane * 16), "s"(rsO), "i"(i * 1024) : "memory");
  }
  STAMP(2, 3);
}

__global__ __launch_bounds__(512, 2) void qkv_attn_kernel(const bf16* __restrict__ H, const bf16* __restrict__ W,
                                                          bf16* O, const bf16* __restrict__ wo, const bf16* __restrict__ X,
                                                          const float* __restrict__ modp, bf16* __restrict__ Y,
                                                          float* __restrict__ stat, unsigned* flags,
                                                          const bf16* __restrict__ wg, const bf16* __restrict__ wu) {
  __shared__ __attribute__((aligned(1024))) char smem[Q_LDS];
  STAMP(2, 0);
  const int tid = threadIdx.x, lane = tid & 63;
  const int wid = __builtin_amdgcn_readfirstlane(tid >> 6);
  const unsigned smem_base = (unsigned)(uintptr_t)smem;
  const int xcd = blockIdx.x & 7, idx = blockIdx.x >> 3;
  const int bm = (xcd >> 2) * 8 + (idx & 7), bn = (xcd & 3) * 4 + (idx >> 3);
  const int rb = xcd * 2 + (idx & 1), cg = idx >> 1;
  const unsigned rot = cg & 7;
  const unsigned vgu = (unsigned)((idx * 8 + (lane >> 3)) * (F_DIM * 2) + xcd * 896 + ((lane & 7) == 7 ? 6 : (lane & 7)) * 128);
  const unsigned cnt = smem_base + QX_CNT, cntl = cnt + lane * 4;
  if (wid < 4) {
    if (wid & 1) qkv_mfma<1>(smem_base, lane, wid >> 1, bm, bn, O);
    else qkv_mfma<0>(smem_base, lane, wid >> 1, bm, bn, O);
  } else {
    const rsrc_t rsO = mk_rsrc(wo, D_DIM * D_DIM * 2);
    const unsigned voffO = (unsigned)((lane >> 3) * 2048 + cg * 128 + ((((lane & 7) >> 1) ^ (lane >> 4)) * 32) + (lane & 1) * 16);
    if (wid < 7) qkv_loader(smem_base, lane, wid - 4, bm * 128, bn * 64, H, W, rot, voffO, rsO);
    else {
      unsigned gv[8];
      const rsrc_t rsG = mk_rsrc(modp, 4 * 6144 * 4);
#pragma unroll
      for (int i = 0; i < 8; ++i) { asm volatile("" : "=v"(gv[i])); bload1_v<0>(gv[i], (unsigned)((cg * 64 + lane) * 4), rsG, (i >> 1) * 24576 + 8192 + (i & 1) * 8192); }
      dsw32<0>(cntl, 0u); dsw32<256>(cntl, 0u);
      asm volatile("s_waitcnt lgkmcnt(0)" ::: "memory");
      for (int i = 0; i < 3; ++i) __builtin_amdgcn_s_barrier();
      asm volatile("s_waitcnt vmcnt(0)" : "+v"(gv[0]), "+v"(gv[1]), "+v"(gv[2]), "+v"(gv[3]), "+v"(gv[4]), "+v"(gv[5]), "+v"(gv[6]), "+v"(gv[7])::"memory");
      const float ga = (__uint_as_float(gv[0]) + __uint_as_float(gv[2])) + (__uint_as_float(gv[4]) + __uint_as_float(gv[6]));
      const float cb = (__uint_as_float(gv[1]) + __uint_as_float(gv[3])) + (__uint_as_float(gv[5]) + __uint_as_float(gv[7]));
      dsw32<0>(smem_base + QX_GT + lane * 4, __float_as_uint(rbf(ga)));
      dsw32<256>(smem_base + QX_GT + lane * 4, __float_as_uint(gain_nz(cb)));
      asm volatile("s_waitcnt lgkmcnt(0)" ::: "memory");
      {
        int nxt = TS + idx % TP, ev = 0;
        for (int i = 2; i < 27; ++i) {
          if (TW > 0 && i == nxt && ev < TN) {
#pragma unroll
            for (int j = 0; j < TW; ++j) gu_touch(smem_base, 7 - (ev * TW + j), vgu, wg, wu, GNT & 1);
            nxt += TP; ++ev;
          }
          __builtin_amdgcn_s_barrier();
        }
      }
      wo_piece(smem_base, 63u, rot, voffO, rsO);
      for (int i = 28; i < 33; ++i) __builtin_amdgcn_s_barrier();
      wo_piece(smem_base, 127u, rot, voffO, rsO);
    }
    __builtin_amdgcn_s_barrier();
    if (wid - 4 < T1) { gu_touch(smem_base, T2 + wid - 4, vgu, wg, wu, GNT & 2); wait_vm<(GNT & 2) ? 2 : 1>(); }
    else wait_vm<0>();
    lds_add(cntl, 1u);
  }
  const int fr = lane & 15, fq = lane >> 4;
  const int kq = wid & 3, rg = rb * 2 + (wid >> 2);
  lds_wait<2>(cnt, 4);
  u32x2 wl[8][4], wh[8][4];
  {
    const int q = (lane >> 2) & 3, p = lane & 3, r = 4 * fq + q;
    const unsigned wb = smem_base + (kq < 2 ? QX_WH + kq * 32768 : (kq - 2) * 32768) + r * 128 + (((r >> 1) & 3) * 32) + 8 * p;
    unsigned wa[4];
#pragma unroll
    for (int f = 0; f < 4; ++f) wa[f] = wb ^ (f * 32);
    static_for<32>([&](auto E) {
      constexpr int s = E.value >> 2, f = E.value & 3;
      dsr_tr<s * 4096>(wl[s][f], wa[f]);
      dsr_tr<s * 4096 + 2048>(wh[s][f], wa[f]);
    });
  }
  const rsrc_t rsA = mk_rsrc((const char*)O + (size_t)rg * 131072, 131072);
  const rsrc_t rsF = mk_rsrc(flags + (rb * 16 + kq * 4) * 32, 4 * 128);
  unsigned voffA[4], hs[8];
#pragma unroll
  for (int i = 0; i < 4; ++i) { voffA[i] = lane * 16 + ((kq + 1 + i) & 3) * 1024; asm volatile("" : "+v"(voffA[i])); }
#pragma unroll
  for (int i = 0; i < 8; ++i) { hs[i] = (kq * 8 + ((i + rot) & 7)) * 4096; asm volatile("" : "+s"(hs[i])); }
  asm volatile("" ::"s"(rsA), "s"(rsF));
  if (wid < 4) {
    const rsrc_t rsFo = mk_rsrc(flags + (bm * 16 + bn) * 32, 4);
    asm volatile("s_waitcnt vmcnt(0)" ::"s"(rsFo) : "memory");
    if ((lds_add(cntl + 256, 1u) & 15u) == 3u)
      asm volatile("buffer_store_dword %0, %1, %2, 0 offen sc1" ::"v"(1u), "v"(lane * 4), "s"(rsFo) : "memory");
    STAMP(2, 4);
  } else {
    lds_add(cntl + 256, 16u);
  }
  lds_wait<2>(cnt + 256, 68);
  u32x4 wf[8][4];
#pragma unroll
  for (int s = 0; s < 8; ++s)
#pragma unroll
    for (int f = 0; f < 4; ++f) {
      asm volatile("" : "+v"(wl[s][f]), "+v"(wh[s][f]));
      wf[s][f] = u32x4{wl[s][f][0], wl[s][f][1], wh[s][f][0], wh[s][f][1]};
    }
  {
    unsigned spins = 0;
    for (;;) {
      unsigned fl;
      asm volatile("buffer_load_dword %0, %1, %2, 0 offen sc1\n\ts_waitcnt vmcnt(0)" : "=v"(fl) : "v"((lane & 3) * 128), "s"(rsF) : "memory");
      if (__builtin_amdgcn_ballot_w64(fl == 0u) == 0ull || ++spins >= (1u << 20)) break;
      __builtin_amdgcn_s_sleep(1);
    }
  }
  STAMP(3, 0);
  const unsigned voffL = lane * 16;
  u32x4 af[32], xv[2], pr[3][4], gt[4], ct[4];
  f32x4 acc[4][4];
  auto aload = [&](auto N) {
    constexpr int n = N.value;
    asm volatile("" : "=v"(af[n]));
    bload_sc1<0>(af[n], voffA[n >> 3], rsA, hs[n & 7]);
  };
  static_for<GDEP>([&](auto N) { aload(N); });
  const unsigned sl = smem_base + voffL + (wid >> 2) * 49152;
  const int sw = ((fq & 1) << 1) | (fq >> 1);
  SCHED0();
  static_for<32>([&](auto N) {
    constexpr int n = N.value, ps = n >> 3, s = n & 7, XG = 32 - GDEP;
    constexpr int last = n - 1 + GDEP < 31 ? n - 1 + GDEP : 31;
    asm volatile("s_waitcnt vmcnt(%1)" : "+v"(af[n]) : "n"(last - n + (n > XG ? 2 : 0)) : "memory");
    if constexpr (n == 28) { WAIT_LGKM0(); __builtin_amdgcn_s_barrier(); }
    static_for<4>([&](auto F) {
      constexpr int f = F.value;
      if constexpr (s == 0) asm volatile("v_mfma_f32_16x16x32_bf16 %0, %1, %2, 0" : "=&v"(acc[ps][f]) : "v"(wf[s][f]), "v"(af[n]));
      else MFMA_AV(acc[ps][f], wf[s][f], af[n]);
      if constexpr (f == 0 && n + GDEP < 32) aload(ic<(n + GDEP < 32 ? n + GDEP : 0)>{});
      if constexpr (f == 0 && n == XG) {
        const rsrc_t rsX = mk_rsrc(X, S_LEN * D_DIM * 2);
        const unsigned vx = (unsigned)(((rg * 64 + kq * 16 + fr) * D_DIM + cg * 64 + sw * 8) * 2);
        asm volatile("" : "=v"(xv[0]), "=v"(xv[1]));
        bload_v<0>(xv[0], vx, rsX, 0); bload_v<64>(xv[1], vx, rsX, 0);
      }
      if constexpr (ps > 0 && (s == 2 || s == 3) && (f & 1)) {
        constexpr int e = (s - 2) * 2 + (f >> 1);
        dsw128<(ps - 1) * 4096 + e * 1024>(sl + ((kq + ps) & 3) * 12288, __builtin_bit_cast(u32x4, acc[ps - 1][e]));
      }
      if constexpr (n >= 28 && n < 31) dsr128<(n - 28) * 4096 + f * 1024>(pr[n - 28][f], sl + kq * 12288);
      if constexpr (n == 31) { dsr128<f * 64>(gt[f], smem_base + QX_GT + fq * 16); dsr128<256 + f * 64>(ct[f], smem_base + QX_GT + fq * 16); }
    });
  });
  asm volatile("s_waitcnt vmcnt(0)" : "+v"(xv[0]), "+v"(xv[1])::"memory");
  asm volatile("s_waitcnt lgkmcnt(0)\n\ts_nop 4" ::: "memory");
  STAMP(3, 2);
  f32x4 fin[4], yv[4];
#pragma unroll
  for (int f = 0; f < 4; ++f) asm volatile("" : "+v"(acc[3][f]), "+v"(pr[0][f]), "+v"(pr[1][f]), "+v"(pr[2][f]), "+v"(gt[f]), "+v"(ct[f]));
#pragma unroll
  for (int c = 0; c < 2; ++c) {
    const auto x0 = __builtin_amdgcn_permlane16_swap(xv[c][0], xv[c][2], false, false);
    const auto x1 = __builtin_amdgcn_permlane16_swap(xv[c][1], xv[c][3], false, false);
#pragma unroll
    for (int h = 0; h < 2; ++h) {
      const int f = 2 * c + h;
      const f32x4 a = (acc[3][f] + __builtin_bit_cast(f32x4, pr[0][f])) + (__builtin_bit_cast(f32x4, pr[1][f]) + __builtin_bit_cast(f32x4, pr[2][f]));
      const f32x4 xf = {bf_lo(x0[h]), bf_hi(x0[h]), bf_lo(x1[h]), bf_hi(x1[h])};
      fin[f] = xf + __builtin_bit_cast(f32x4, gt[f]) * a;
      yv[f] = fin[f] * __builtin_bit_cast(f32x4, ct[f]);
    }
    const auto y0 = __builtin_amdgcn_permlane16_swap(pk_bf16(yv[2 * c][0], yv[2 * c][1]), pk_bf16(yv[2 * c + 1][0], yv[2 * c + 1][1]), false, false);
    const auto y1 = __builtin_amdgcn_permlane16_swap(pk_bf16(yv[2 * c][2], yv[2 * c][3]), pk_bf16(yv[2 * c + 1][2], yv[2 * c + 1][3]), false, false);
    gst128((char*)Y + ((size_t)(rg >> 2) * 32 + cg * 2 + c) * 16384 + ((rg & 3) * 4 + kq) * 1024 + (sw * 16 + fr) * 16,
           u32x4{y0[0], y1[0], y0[1], y1[1]});
  }
  const f32x4 s4 = (fin[0] + fin[1]) + (fin[2] + fin[3]);
  const float s = rowsum4((s4[0] + s4[1]) + (s4[2] + s4[3]));
  const float mb = s * (1.0f / 64.0f);
  f32x4 q4 = {0, 0, 0, 0};
#pragma unroll
  for (int f = 0; f < 4; ++f) { const f32x4 d = fin[f] - mb; q4 += d * d; }
  const float m2 = rowsum4((q4[0] + q4[1]) + (q4[2] + q4[3]));
  if (fq == 0) *reinterpret_cast<f32x2*>(stat + (((size_t)(cg >> 1) * S_LEN + rg * 64 + kq * 16 + fr) * 2 + (cg & 1)) * 2) = f32x2{s, m2};
  STAMP(3, 3);
  asm volatile("s_waitcnt vmcnt(0)");
  STAMP(3, 4);
  if (wid < T2) gu_touch(smem_base, wid, vgu, wg, wu, GNT & 4);
}

#define G3NT 1
#define NBR 0
#define TPOS 6
#define NBC 1
#define G3_TW "12"
#define G3_DUMMY() do { } while (0)
constexpr int G3_CT = 131072 + 4096 + 1024;
#define G3_STAGE(SLOT, P, U)                                                                               \
  do {                                                                                                     \
    if ((P) == 0) bload_lds<0>(m0w + (SLOT) * 32768, voffA, rsA, (unsigned)(U) * 16384u);                  \
    else if ((P) == 1) bload_lds<1024>(m0w + (SLOT) * 32768, voffA, rsA, (unsigned)(U) * 16384u);          \
    else if (G3NT && (P) == 2) bload_lds_nt<0>(m0w + (SLOT) * 32768 + 16384, voffB0, rsB, (unsigned)(U) * 229376u);   \
    else if (G3NT) bload_lds_nt<0>(m0w + (SLOT) * 32768 + 16384 + 1024, voffB1, rsB, (unsigned)(U) * 229376u);          \
    else if ((P) == 2) bload_lds<0>(m0w + (SLOT) * 32768 + 16384, voffB0, rsB, (unsigned)(U) * 229376u);   \
    else bload_lds<0>(m0w + (SLOT) * 32768 + 16384 + 1024, voffB1, rsB, (unsigned)(U) * 229376u);          \
  } while (0)

#define G3ROT 1
DI int g3_line(int gx, int p) {
  const int t = p + (G3ROT ? (3 + gx) & 7 : 0);
  return 7 * gx + (t >= 7 ? t - 7 : t);
}
template <int FN>
DI void g3_body(char* smem, unsigned smem_base, int wm, int wn, int wid, int lane, int bm, int bn, rsrc_t rsA, rsrc_t rsB,
                rsrc_t rsP, unsigned voffA, unsigned voffB0, unsigned voffB1, bf16* __restrict__ act) {
  constexpr int NB = 2 * FN, NM = 4 * NB, RD = 5 + 2 * NB;
  const int fr = lane & 15, fq = lane >> 4;
  f32x4 acc[4][NB], accC[2];
  accC[0] = f32x4{0, 0, 0, 0}; accC[1] = f32x4{0, 0, 0, 0};
  asm volatile("" : "+v"(accC[0]), "+v"(accC[1]));
  const int rot = wm % FN;
  unsigned cad = smem_base + G3_CT + (fr < 4 ? fr * 64 : 256) + fq * 16;
#pragma unroll
  for (int i = 0; i < 4; ++i)
#pragma unroll
    for (int j = 0; j < NB; ++j) { acc[i][j] = f32x4{0, 0, 0, 0}; asm volatile("" : "+v"(acc[i][j])); }
  unsigned aad[2], bad[2][FN];
  aad[0] = smem_base + wm * 4096 + lane * 16; aad[1] = aad[0] + 65536;
  {
    const int q = (lane >> 2) & 3, p = lane & 3;
    const int r = 8 * fq + q;
    const int sw = (r & 3) | (((r >> 3) & 1) << 2);
#pragma unroll
    for (int j = 0; j < FN; ++j) {
      const int gi = wn * 4 + (j + rot) % FN + (bn & 1);
      bad[0][j] = smem_base + 16384 + r * 256 + ((gi ^ sw) * 32) + 8 * p;
      bad[1][j] = bad[0][j] + 65536;
    }
  }
  u32x4 aX[5], aY[5];
  u32x2 bXl[NB], bXh[NB], bYl[NB], bYh[NB];
  const unsigned m0w = smem_base + wid * 2048;
  const unsigned voffP = lane * 16;
#define G3_READ(AR, BL, BH, SLOT, R)                                                                       \
  do {                                                                                                     \
    if ((R) < 2 * NB) {                                                                                    \
      constexpr int jb = ((R) < 2 * NB ? (R) : 0) >> 1;                                                    \
      constexpr int off = ((SLOT) & 1) * 32768 + (jb / FN) * 8192 + ((R) & 1) * 1024;                      \
      if ((R) & 1) dsr_tr<off>(BH[jb], bad[(SLOT) >> 1][jb % FN]);                                         \
      else dsr_tr<off>(BL[jb], bad[(SLOT) >> 1][jb % FN]);                                                 \
    } else if ((R) < 2 * NB + 4) {                                                                         \
      constexpr int ia = ((R) >= 2 * NB && (R) < 2 * NB + 4) ? (R) - 2 * NB : 0;                           \
      dsr128<((SLOT) & 1) * 32768 + ia * 1024>(AR[ia], aad[(SLOT) >> 1]);                                  \
    } else {                                                                                               \
      if ((SLOT) == 0) cad += 1280;                                      \
      dsr128<(SLOT) * 320>(AR[4], cad);                                                                    \
    }                                                                                                      \
  } while (0)
  auto rd = [&](auto SL, auto RR, u32x4 (&AR)[5], u32x2 (&BL)[NB], u32x2 (&BH)[NB]) { G3_READ(AR, BL, BH, SL.value, RR.value); };
#define G3_UNIT(AR, BL, BH, NAR, NBL, NBH, SLOT, U)                                                        \
  do {                                                                                                     \
    WAIT_LGKM0();                                                                                          \
    u32x4 bb[NB];                                                                                          \
    _Pragma("unroll") for (int j = 0; j < NB; ++j) bb[j] = u32x4{BL[j][0], BL[j][1], BH[j][0], BH[j][1]};  \
    SCHED0();                                                                                              \
    asm volatile("s_nop 1");                                                                               \
    static_for<NM>([&](auto N) {                                                                           \
      constexpr int n = N.value;                                                                           \
      if constexpr (n == NM / 2) {                                                                         \
        wait_vm<NBR ? 10 : 8>(); __builtin_amdgcn_s_barrier();                                             \
      }                                                                                                    \
      if constexpr (n == NM / 2 + TPOS) {                                                                  \
                      \
        if (NBR) bload_lds<0>(smem_base + 131072 + 4096, voffP, rsP, (((U) & 7) == wid) ? (unsigned)(U) * 1024u : 0x40000000u); \
        else if (((U) & 7) == wid) bload_lds<0>(smem_base + 131072 + 4096, voffP, rsP, (unsigned)(U) * 1024u);  \
      }                                                                                                    \
      MFMA_AV(acc[n & 3][n >> 2], bb[n >> 2], AR[n & 3]);                                                  \
      if constexpr (n < NM / 2) {                                                                          \
        if constexpr (n % 3 == 1 && n / 3 < 4) G3_STAGE(((SLOT) + 3) & 3, n / 3, (U) + 3);                 \
      } else {                                                                                             \
        constexpr int r0 = (n - NM / 2) * 2;                                                               \
        if constexpr (r0 < RD) rd(ic<((SLOT) + 1) & 3>{}, ic<(r0 < RD ? r0 : 0)>{}, NAR, NBL, NBH);        \
        if constexpr (r0 + 1 < RD) rd(ic<((SLOT) + 1) & 3>{}, ic<(r0 + 1 < RD ? r0 + 1 : 0)>{}, NAR, NBL, NBH); \
      }                                                                                                    \
    });                                                                                                    \
    if (NBC || FN == 4 || wm < 3) {                                                                        \
      MFMA_AV(accC[0], bb[0], AR[4]);                                                                      \
      MFMA_AV(accC[1], bb[FN], AR[4]);                                                                     \
    }                                                                                                      \
    SCHED0();                                                                                              \
  } while (0)

  if (NBR) asm volatile("s_waitcnt vmcnt(10) lgkmcnt(0)" ::: "memory");
  else asm volatile("s_waitcnt vmcnt(8) lgkmcnt(0)" ::: "memory");
  __builtin_amdgcn_s_barrier();
  STAMPDECL;
  STAMPNW(5, 1);
  cad -= 1280;
  static_for<RD>([&](auto RR) { rd(ic<0>{}, RR, aX, bXl, bXh); });
  for (int u = 0; u < 32; u += 4) {
    G3_UNIT(aX, bXl, bXh, aY, bYl, bYh, 0, u);
    G3_UNIT(aY, bYl, bYh, aX, bXl, bXh, 1, u + 1);
    G3_UNIT(aX, bXl, bXh, aY, bYl, bYh, 2, u + 2);
    G3_UNIT(aY, bYl, bYh, aX, bXl, bXh, 3, u + 3);
  }
  asm volatile("s_waitcnt lgkmcnt(0)\n\ts_nop 15" ::: "memory");
#pragma unroll
  for (int i = 0; i < 5; ++i) asm volatile("" : "+v"(aX[i]));
#pragma unroll
  for (int j = 0; j < NB; ++j) asm volatile("" : "+v"(bXl[j]), "+v"(bXh[j]));
  SCHED0();
  STAMP(5, 2);
  STAMPFLUSH(5, 1);
  const f32x2* rstat = reinterpret_cast<const f32x2*>(smem + 131072);
  float* colc = reinterpret_cast<float*>(smem + 131072 + 2048);
#pragma unroll
  for (int m = 0; m < 2; ++m) {
    f32x4 v;
#pragma unroll
    for (int r = 0; r < 4; ++r) v[r] = accC[m][r] + __shfl_xor(accC[m][r], 1);
    if ((fr == 0 || fr == 2) && wm < FN) *reinterpret_cast<f32x4*>(colc + (m * 2 + (fr >> 1)) * 128 + (wn * 4 + rot) * 16 + fq * 4) = v;
  }
  asm volatile("s_waitcnt lgkmcnt(0)" ::: "memory");
  __builtin_amdgcn_s_barrier();
  asm volatile("" ::: "memory");
  f32x2 rs[4];
#pragma unroll
  for (int i = 0; i < 4; ++i) rs[i] = rstat[wm * 64 + i * 16 + fr];
#pragma unroll
  for (int j = 0; j < FN; ++j) {
    const int jg = (j + rot) % FN;
    const int cl = (wn * 4 + jg) * 16 + fq * 4;
    const f32x4 csg = *reinterpret_cast<const f32x4*>(colc + cl), bg = *reinterpret_cast<const f32x4*>(colc + 128 + cl);
    const f32x4 csu = *reinterpret_cast<const f32x4*>(colc + 256 + cl), bu = *reinterpret_cast<const f32x4*>(colc + 384 + cl);
    u32x2 pk[4];
#pragma unroll
    for (int i = 0; i < 4; ++i) {
#pragma unroll
      for (int hh = 0; hh < 2; ++hh) {
        const f32x2 ag = hh ? hi2(acc[i][j]) : lo2(acc[i][j]), au = hh ? hi2(acc[i][FN + j]) : lo2(acc[i][FN + j]);
        const f32x2 g = rs[i][0] * ag + (rs[i][1] * (hh ? hi2(csg) : lo2(csg)) + (hh ? hi2(bg) : lo2(bg)));
        const f32x2 uu = rs[i][0] * au + (rs[i][1] * (hh ? hi2(csu) : lo2(csu)) + (hh ? hi2(bu) : lo2(bu)));
        const f32x2 ea = g * -1.4426950408889634f;
        f32x2 e = {__builtin_amdgcn_exp2f(ea[0]), __builtin_amdgcn_exp2f(ea[1])};
        e = e + 1.0f;
        const f32x2 rc = {__builtin_amdgcn_rcpf(e[0]), __builtin_amdgcn_rcpf(e[1])};
        const f32x2 o = (g * uu) * rc;
        pk[i][hh] = pk_bf16(o[0], o[1]);
      }
    }
    const int Jv = (bn & 3) * 7 + wn * 4 + jg;
    const int J = g3_line(bn >> 2, Jv >> 2) * 4 + (Jv & 3);
    char* dst = (char*)act + ((size_t)((bm * 4 + wm) * (F_DIM / 32) + (J >> 1)) * 4 + (fq & 1)) * 1024 + (((J & 1) * 2 + (fq >> 1)) * 16 + fr) * 16;
#pragma unroll
    for (int i = 0; i < 4; i += 2) {
      const auto s0 = __builtin_amdgcn_permlane16_swap(pk[i][0], pk[i + 1][0], false, false);
      const auto s1 = __builtin_amdgcn_permlane16_swap(pk[i][1], pk[i + 1][1], false, false);
      gst128(dst + i * 1024, u32x4{s0[0], s1[0], s0[1], s1[1]});
    }
  }
}

__global__ __launch_bounds__(512, 2) void gemm3_kernel(const bf16* __restrict__ Y, const bf16* __restrict__ Wg,
                                                       const bf16* __restrict__ Wu, bf16* __restrict__ act,
                                                       const float* __restrict__ stat, const float* __restrict__ modp,
                                                       const bf16* __restrict__ Wd) {
  __shared__ __attribute__((aligned(16))) char smem[G3_CT + 33 * 320];
  STAMP(5, 0);
  const int tid = threadIdx.x, lane = tid & 63;
  const int wid = __builtin_amdgcn_readfirstlane(tid >> 6);
  const int wm = wid & 3, wn = wid >> 2;
  const int xcd = blockIdx.x & 7, idx = blockIdx.x >> 3;
  const int bm = idx & 7, bn = xcd * 4 + (idx >> 3);
  const unsigned smem_base = (unsigned)(uintptr_t)smem;
  const rsrc_t rsA = mk_rsrc((const char*)Y + (size_t)bm * 32 * 16384, 32 * 16384);
  const rsrc_t rsB = mk_rsrc(wn ? Wu : Wg, D_DIM * F_DIM * 2);
  const rsrc_t rsP = mk_rsrc((const char*)Wd + (size_t)blockIdx.x * 28672, 28672);
  const unsigned voffA = wid * 2048 + lane * 16;
  unsigned voffB[2];
#pragma unroll
  for (int i = 0; i < 2; ++i) {
    const int r = (wm * 2 + i) * 4 + (lane >> 4);
    const int sw = (r & 3) | (((r >> 3) & 1) << 2);
    const int c16 = lane & 15;
    const int vb = (((bn & 3) * 224) & ~63) + ((c16 >> 1) ^ sw) * 32 + (c16 & 1) * 16;
    voffB[i] = (unsigned)(r * (F_DIM * 2) + g3_line(bn >> 2, vb >> 7) * 128 + (vb & 127));
  }
  const unsigned m0w = smem_base + wid * 2048;
  const unsigned voffB0 = voffB[0], voffB1 = voffB[1];
  if (wn == 0) {
    u32x4 tv[8];
#pragma unroll
    for (int i = 0; i < 8; ++i) asm volatile("" : "=v"(tv[i]));
    const rsrc_t rsS = mk_rsrc(stat, S_LEN * 128);
    const unsigned vo = (unsigned)((bm * 256 + tid) * 16);
    static_for<8>([&](auto I) { bload_v<0>(tv[I.value], vo, rsS, I.value * 32768); });
    G3_STAGE(0, 0, 0); G3_STAGE(0, 1, 0); G3_STAGE(0, 2, 0); G3_STAGE(0, 3, 0);
    G3_STAGE(1, 0, 1); G3_STAGE(1, 1, 1); G3_STAGE(1, 2, 1); G3_STAGE(1, 3, 1);
    G3_DUMMY();
    G3_STAGE(2, 0, 2); G3_STAGE(2, 1, 2); G3_STAGE(2, 2, 2); G3_STAGE(2, 3, 2);
    G3_DUMMY();
    asm volatile("s_waitcnt vmcnt(" G3_TW ")" : "+v"(tv[0]), "+v"(tv[1]), "+v"(tv[2]), "+v"(tv[3]), "+v"(tv[4]), "+v"(tv[5]), "+v"(tv[6]), "+v"(tv[7])::"memory");
    float s = 0;
#pragma unroll
    for (int i = 0; i < 8; ++i) s += __uint_as_float(tv[i][0]) + __uint_as_float(tv[i][2]);
    const float mean = s * (1.0f / D_DIM);
    float m2 = 0;
#pragma unroll
    for (int i = 0; i < 8; ++i) {
      const float d0 = __uint_as_float(tv[i][0]) * (1.0f / 64.0f) - mean, d1 = __uint_as_float(tv[i][2]) * (1.0f / 64.0f) - mean;
      m2 += __uint_as_float(tv[i][1]) + __uint_as_float(tv[i][3]) + 64.0f * (d0 * d0 + d1 * d1);
    }
    const float rstd = rsqrtf(m2 * (1.0f / D_DIM) + 1e-5f);
    reinterpret_cast<f32x2*>(smem + 131072)[tid] = f32x2{rstd, -mean * rstd};
    g3_body<4>(smem, smem_base, wm, wn, wid, lane, bm, bn, rsA, rsB, rsP, voffA, voffB0, voffB1, act);
  } else {
    const rsrc_t rsC = mk_rsrc(modp, 4 * 6144 * 4);
    const int t = tid - 256;
    const unsigned vo = (unsigned)((3072 + t * 4) * 4);
    u32x4 tc[8];
#pragma unroll
    for (int i = 0; i < 8; ++i) { asm volatile("" : "=v"(tc[i])); bload_v<0>(tc[i], vo, rsC, (i >> 1) * 24576 + (i & 1) * 4096); }
    G3_STAGE(0, 0, 0); G3_STAGE(0, 1, 0); G3_STAGE(0, 2, 0); G3_STAGE(0, 3, 0);
    G3_STAGE(1, 0, 1); G3_STAGE(1, 1, 1); G3_STAGE(1, 2, 1); G3_STAGE(1, 3, 1);
    G3_DUMMY();
    G3_STAGE(2, 0, 2); G3_STAGE(2, 1, 2); G3_STAGE(2, 2, 2); G3_STAGE(2, 3, 2);
    G3_DUMMY();
    asm volatile("s_waitcnt vmcnt(" G3_TW ")" : "+v"(tc[0]), "+v"(tc[1]), "+v"(tc[2]), "+v"(tc[3]), "+v"(tc[4]), "+v"(tc[5]), "+v"(tc[6]), "+v"(tc[7])::"memory");
    f32x4 sv = (__builtin_bit_cast(f32x4, tc[0]) + __builtin_bit_cast(f32x4, tc[2])) + (__builtin_bit_cast(f32x4, tc[4]) + __builtin_bit_cast(f32x4, tc[6]));
    f32x4 gw = (__builtin_bit_cast(f32x4, tc[1]) + __builtin_bit_cast(f32x4, tc[3])) + (__builtin_bit_cast(f32x4, tc[5]) + __builtin_bit_cast(f32x4, tc[7]));
#pragma unroll
    for (int e = 0; e < 4; ++e) { gw[e] = gain_nz(gw[e]); sv[e] = rbf(sv[e]); }
    u32x2 gh, gl, sh, sl;
    split4(gw, gh, gl); split4(sv, sh, sl);
    char* d = smem + G3_CT + (t >> 3) * 320 + ((t >> 1) & 3) * 16 + (t & 1) * 8;
    *reinterpret_cast<u32x2*>(d) = gh; *reinterpret_cast<u32x2*>(d + 64) = gl;
    *reinterpret_cast<u32x2*>(d + 128) = sh; *reinterpret_cast<u32x2*>(d + 192) = sl;
    *reinterpret_cast<u32x2*>(d + 256) = u32x2{0, 0};
    g3_body<3>(smem, smem_base, wm, wn, wid, lane, bm, bn, rsA, rsB, rsP, voffA, voffB0, voffB1, act);
  }
  STAMP(5, 3);
  asm volatile("s_waitcnt vmcnt(0)");
  STAMP(5, 4);
}

#define XST 0
#define G4NF 11
#define HFS 3
#define HFL 4
#define HCK 5
#define XMAP 1
template <int OFF>
DI void bstore_sc1(u32x4 d, unsigned voff, rsrc_t rs, unsigned soff) {
  asm volatile("buffer_store_dwordx4 %0, %1, %2, %3 offen offset:%4 sc1\n\ts_nop 1" ::"v"(d), "v"(voff), "s"(rs), "s"(soff), "i"(OFF) : "memory");
}
#define DPERM(TB, Q, SL, PP)                                                                       \
  do {                                                                                             \
    if ((Q) & 1) PERM_HI(T[TB][1][(Q) >> 1], braw[SL][((Q) >> 1) * 2][PP], braw[SL][((Q) >> 1) * 2 + 1][PP]); \
    else PERM_LO(T[TB][0][(Q) >> 1], braw[SL][((Q) >> 1) * 2][PP], braw[SL][((Q) >> 1) * 2 + 1][PP]);         \
  } while (0)
namespace g4 {
constexpr int NF = G4NF, NH = 28 - 2 * NF, NS = NF + NH, HEP = NH - 3;
constexpr int nslot(int g) { return g < NF ? 64 : 32; }
constexpr int wslot(int g) { return g < NF - 1 ? 48 : (g == NF - 1 ? 56 : 24); }
constexpr int ring(int g, int n) {
  if (g < NF - 1) {
    if (n >= 48 || n % 3 != 1) return -1;
    const int l = n / 3;
    if (g + 2 < NF) return (g + 2) * 16 + l;
    return l < 12 ? (g + 2) * 16 + (l < 8 ? l : l + 4) : -1;
  }
  if (g == NF - 1) { if (n >= 48 || n % 4 != 1) return -1; const int l = n / 4; return (g + 2) * 16 + (l < 8 ? l : l + 4); }
  if (g == NF) { if (n >= 24) return -1; const int l = n % 12; return (g + 2 + n / 12) * 16 + (l < 8 ? l : l + 4); }
  if (g + 3 >= NS || n >= 24 || (n & 1)) return -1;
  const int l = n >> 1;
  return (g + 3) * 16 + (l < 8 ? l : l + 4);
}
constexpr int pwrite(int g, int n) {
  const int m = g == NF - 1 ? n : 64 + n;
  for (int e = 0; e < 24; ++e) if (12 + (13 * e) / 4 == m) return e;
  return -1;
}
constexpr int red0(int n) { return n < 8 ? 0 : (64 * (n - 8)) / 24; }
#define SPS 4
constexpr int sslot(int f) {
  const int ready = (f >> 2) * 32 + 8 + (((f & 3) + 1) * 16 * 24 + 63) / 64;
  const int paced = (f / SPS) * 32 + 15 + (f % SPS) * (32 / SPS);
  int m = ready > paced ? ready : paced;
  if ((m & 1) == 0) ++m;
  return m;
}
constexpr int dstore_f(int g, int n) {
  for (int f = 0; f < 8; ++f) if (sslot(f) == (g - NF) * 32 + n) return f;
  return -1;
}
constexpr bool dstore(int g, int n) { return g >= NF && dstore_f(g, n) >= 0; }
constexpr int vmops(int g, int n) {
  int c = (ring(g, n) >= 0 ? 1 : 0) + (dstore(g, n) ? 1 : 0);
  const int h = g - NF;
  if (h == HFS && n == 24) c += 1;
  if (h == HFL && n == 25) c += 1;
  if (h == HCK && n >= 24) c += 1;
  if (h == HEP && (n == 1 || n == 3)) c += 4;
  return c;
}
constexpr int between(int g0, int n0, int g1, int n1) {
  int c = 0;
  for (int g = g0; g <= g1; ++g)
    for (int n = (g == g0 ? n0 + 1 : 0); n < (g == g1 ? n1 : nslot(g)); ++n) c += vmops(g, n);
  return c;
}
constexpr int imin(int a, int b) { return a < b ? a : b; }
constexpr int waitcnt(int g) {
  int c = 63;
  if (g + 1 < NS && g + 1 >= 2) {
    for (int s = 0; s <= g; ++s)
      for (int n = 0; n < nslot(s); ++n)
        if (ring(s, n) == (g + 1) * 16 + 15) c = imin(c, between(s, n, g, wslot(g)) + (dstore(s, n) ? 1 : 0));
  }
  if (g == 0) c = 16;
  if (g - NF == HFS) c = imin(c, between(NF + sslot(7) / 32, sslot(7) % 32, g, 24));
  if (g - NF == HCK) c = imin(c, between(NF + HFL, 25, g, 24));
  return c;
}
constexpr int bslot(int b) { return b < NF + 3 ? b % 3 : ((b - NF) % 4 == 3 ? 3 : (NF + (b - NF) % 4) % 3); }
}
__global__ __launch_bounds__(256, 1) void gemm4_kernel(const bf16* __restrict__ At, const bf16* __restrict__ W,
                                                       const bf16* __restrict__ Yt, const float* __restrict__ modp,
                                                       bf16* __restrict__ C, float* xbuf, unsigned* flags) {
  using namespace g4;
  constexpr int KB = F_DIM / 32;
  constexpr int GT = 96 * 1024;
  __shared__ __attribute__((aligned(16))) char smem[GT + 1024];
  STAMPDECL;
  STAMP(6, 0);
  const int tid = threadIdx.x, lane = tid & 63;
  const int w = __builtin_amdgcn_readfirstlane(tid >> 6);
  const int xcd = blockIdx.x & 7, idx = blockIdx.x >> 3;
  const int kh = xcd & 1, bm = ((xcd >> 1) & 1) * 8 + (idx & 7), bn = (xcd >> 2) * 4 + (idx >> 3);
  const unsigned partner = blockIdx.x ^ 1;
  const int n0 = bn * 128, bm64 = bm * 2 + kh;
  const int fr = lane & 15, fq = lane >> 4;
  const rsrc_t rsA0 = mk_rsrc((const char*)At + (size_t)(bm64 ^ 1) * KB * 4096, KB * 4096);
  const rsrc_t rsA1 = mk_rsrc((const char*)At + (size_t)bm64 * KB * 4096, KB * 4096);
  const rsrc_t rsB = mk_rsrc((const char*)W + n0 * 2, KB * 65536 - n0 * 2);
  const unsigned voffL = lane * 16;
  unsigned voffA[4];
#pragma unroll
  for (int i = 0; i < 4; ++i) voffA[i] = voffL + ((i + w) & 3) * 1024;
  const unsigned voffB = fq * 8 * 2048 + fr * 16;
  u32x4 braw[4][8], afr[3][8], afa[4][4];
  u32x4 T[2][2];
  u32x4 xv[4], pv[8];
  unsigned gv[4], flg;
  f32x4 acc[8][8];
#pragma unroll
  for (int r = 0; r < 3; ++r)
#pragma unroll
    for (int j = 0; j < 8; ++j) asm volatile("" : "=v"(braw[r][j]), "=v"(afr[r][j]));
  const int kbF = (kh ? KB - 4 * NF : 0) + w, kbH = w;
#define ELOAD(B, L)                                                                                \
  do {                                                                                             \
    const unsigned kbv = (unsigned)(((B) < NF ? kbF : kbH) + (B) * 4);                             \
    if constexpr ((L) < 8) {                                                                       \
      const unsigned so = kbv * 65536u + ((L) >> 1) * 4096u;                                       \
      if constexpr (bslot(B) == 3 || (B) >= NF + 3) asm volatile("" : "=v"(braw[bslot(B)][(L) & 7]));        \
      if constexpr ((L) & 1) bload_v<2048>(braw[bslot(B)][(L) & 7], voffB, rsB, so);               \
      else bload_v<0>(braw[bslot(B)][(L) & 7], voffB, rsB, so);                                    \
    } else if constexpr ((B) >= NF + 2) {                                                          \
      asm volatile("" : "=a"(afa[((B) - NF) & 3][(L) & 3]));                                       \
      bload_a<0>(afa[((B) - NF) & 3][(L) & 3], voffA[(L) & 3], rsA1, kbv * 4096u);                 \
    } else {                                                                                       \
      bload_v<0>(afr[(B) % 3][(L) & 7], voffA[(L) & 3], ((L) & 4) ? rsA1 : rsA0, kbv * 4096u);     \
    }                                                                                              \
  } while (0)
#define MFMA_E(C, B, A) asm volatile("v_mfma_f32_16x16x32_bf16 %0, %1, %2, %0" : "+a"(C) : "v"(B), "v"(A))
  static_for<16>([&](auto L) { ELOAD(0, L.value); });
  static_for<16>([&](auto L) { ELOAD(1, L.value); });
#pragma unroll
  for (int i = 0; i < 8; ++i)
#pragma unroll
    for (int f = 0; f < 8; ++f) { acc[i][f] = f32x4{0, 0, 0, 0}; asm volatile("" : "+a"(acc[i][f])); }
  const unsigned sbase = (unsigned)(uintptr_t)smem + voffL;
  unsigned wlo[3];
#pragma unroll
  for (int i = 1; i < 4; ++i) wlo[i - 1] = sbase + (((w + i) & 3) * 3 + (i - 1)) * 8192;
  const unsigned rlo = sbase + w * 24576;
  const rsrc_t rsEx = mk_rsrc(xbuf, 256 * 32768);
  const unsigned exo = (unsigned)(blockIdx.x * 32768 + w * 8192);
  const rsrc_t rsFo = mk_rsrc(flags + blockIdx.x * 4 + w, 4), rsFi = mk_rsrc(flags + partner * 4 + w, 4);
  wait_vm<16>();
  STAMPNW(6, 1);
#pragma unroll
  for (int q = 0; q < 8; ++q) DPERM(0, q, 0, 0);
  asm volatile("s_nop 1");
  u32x4 pr[3][4];
  unsigned long long _ts[7];
  f32x4 tt[8], uu;
  static_for<NS>([&](auto G) {
    constexpr int g = G.value, h = g - NF, sl = bslot(g), ns = bslot(g + 1);
    constexpr bool full = g < NF - 1, lastf = g == NF - 1;
    constexpr bool red = (h == 0 || h == 1);
    constexpr int fb = h == 1 ? 4 : 0;
    if constexpr (h == 0 || h == 1) { WAIT_LGKM0(); __builtin_amdgcn_s_barrier(); }
    if constexpr (h == 3) __builtin_amdgcn_s_barrier();
    static_for<nslot(g)>([&](auto N) {
      constexpr int n = N.value;
      if constexpr (n == wslot(g)) {
        if constexpr (waitcnt(g) < 63) {
          if constexpr (h == HCK) asm volatile("s_waitcnt vmcnt(%1)" : "+v"(flg) : "n"(waitcnt(g)) : "memory");
          else wait_vm<waitcnt(g)>();
        }
        if constexpr (h == HFS) asm volatile("buffer_store_dword %0, %1, %2, 0 offen sc1" ::"v"(1u), "v"(voffL >> 2), "s"(rsFo) : "memory");
        if constexpr (h == HCK) {
          unsigned spins = 0;
          while (__builtin_amdgcn_readfirstlane(flg) == 0u && spins < (1u << 20)) {
            __builtin_amdgcn_s_sleep(1);
            bload1_sc1(flg, 0u, rsFi);
            asm volatile("s_waitcnt vmcnt(0)" : "+v"(flg)::"memory");
            ++spins;
          }
        }
      }
      if constexpr (red && n == 8) WAIT_LGKM0();
      if constexpr (full) {
        constexpr int p = n >> 4, q = n & 15;
        MFMA_E(acc[q & 7][2 * p + (q >> 3)], T[p & 1][q >> 3], afr[g % 3][q & 7]);
        if constexpr ((q & 1) == 0) {
          if constexpr (p < 3) DPERM((p + 1) & 1, q >> 1, sl, p + 1);
          else DPERM(0, q >> 1, ns, 0);
        }
      } else {
        constexpr int p = (n >> 3) & 3, q = n & 7, ib = (lastf && n < 32) ? 0 : 4;
        if constexpr (h >= 2) MFMA_A(acc[ib + (q & 3)][2 * p + (q >> 2)], T[p & 1][q >> 2], afa[h & 3][q & 3]);
        else MFMA_E(acc[ib + (q & 3)][2 * p + (q >> 2)], T[p & 1][q >> 2], afr[g % 3][ib + (q & 3)]);
        if constexpr (p < 3) DPERM((p + 1) & 1, q, sl, p + 1);
        else if constexpr (ib == 0) DPERM(0, q, sl, 0);
        else if constexpr (g + 1 < NS) DPERM(0, q, ns, 0);
      }
      if constexpr (ring(g, n) >= 0) ELOAD((ring(g, n) >> 4), (ring(g, n) & 15));
      if constexpr ((lastf || h == 0) && pwrite(g, n) >= 0) {
        constexpr int e = pwrite(g, n), i = 1 + e % 3, f = e / 3;
        dsw128a<f * 1024>(wlo[i - 1], acc[i][f]);
      }
      if constexpr (red) {
        if constexpr (n < 6) dsr128<(n % 3) * 8192 + (fb + n / 3) * 1024>(pr[n % 3][n / 3], rlo);
        if constexpr (n == 6 || n == 7) {
          static_for<3>([&](auto E) { dsr128<E.value * 8192 + (fb + n - 4) * 1024>(pr[E.value][n - 4], rlo); });
        }
        if constexpr (n >= 8) {
          constexpr int o0 = red0(n), o1 = red0(n + 1);
          static_for<o1 - o0>([&](auto O) {
            constexpr int o = o0 + O.value, fl = o / 16, k = o % 16, r = k & 3;
            (void)&uu; (void)&tt; (void)&pr;
            if constexpr (k < 4) asm volatile("v_accvgpr_read_b32 %0, %1" : "=v"(tt[fb + fl][r]) : "a"(acc[0][fb + fl][r]));
            else if constexpr (k < 8) asm volatile("v_add_f32 %0, %0, %1" : "+v"(tt[fb + fl][r]) : "v"(pr[0][fl][r]));
            else if constexpr (k < 12) asm volatile("v_add_f32 %0, %1, %2" : "=v"(uu[r]) : "v"(pr[1][fl][r]), "v"(pr[2][fl][r]));
            else asm volatile("v_add_f32 %0, %0, %1" : "+v"(tt[fb + fl][r]) : "v"(uu[r]));
          });
        }
      }
      if constexpr (h >= 0 && dstore(g, n)) {
        constexpr int f = dstore_f(g, n);
        bstore_sc1<(f & 3) * 1024>(__builtin_bit_cast(u32x4, tt[f]), voffL, rsEx, exo + (f >> 2) * 4096);
      }
      if constexpr (h == HFL && n == 25) {
        asm volatile("" : "=v"(flg));
        bload1_sc1(flg, 0u, rsFi);
      }
      if constexpr (h == HCK && n >= 24) {
        constexpr int f = n - 24;
        asm volatile("" : "=v"(pv[f]));
        bload_sc1<(f & 3) * 1024>(pv[f], voffL, rsEx, partner * 32768u + w * 8192u + (f >> 2) * 4096u);
      }
      if constexpr (h == HEP && n == 1) {
        const rsrc_t rsG = mk_rsrc(modp + (w >= 2 ? 4096 : 5120), 4 * 6144 * 4);
        const unsigned vo = (unsigned)((n0 + (tid & 127)) * 4);
#pragma unroll
        for (int r = 0; r < 4; ++r) { asm volatile("" : "=v"(gv[r])); bload1_v<0>(gv[r], vo, rsG, r * 24576); }
      }
      if constexpr (h == HEP && n == 3) {
        const rsrc_t rsX = mk_rsrc(Yt, S_LEN * D_DIM * 2);
        const unsigned vx = (unsigned)(((bm64 >> 2) * 32 + bn * 4 + fq) * 16384 + ((bm64 & 3) * 4 + w) * 1024 + fr * 16);
#pragma unroll
        for (int r = 0; r < 4; ++r) asm volatile("" : "=v"(xv[r]));
        bload_v<0>(xv[0], vx, rsX, 0); bload_v<256>(xv[1], vx, rsX, 0); bload_v<512>(xv[2], vx, rsX, 0); bload_v<768>(xv[3], vx, rsX, 0);
      }
      if constexpr (g == NS - 1 && n >= 8 && (n & 7) >= 2) {
        constexpr int e = (n & 7) - 2, i = 1 + e % 3, f = 2 * ((n >> 3) - 1) + e / 3;
        dsw128a<f * 1024>(wlo[i - 1], acc[4 + i][f]);
      }
    });
  });
  asm volatile("s_nop 15" ::: "memory");
#pragma unroll
  for (int f = 0; f < 8; ++f) asm volatile("" : "+a"(acc[4][f]));
  static_for<6>([&](auto E) { constexpr int i = 1 + E.value % 3, f = 6 + E.value / 3; dsw128a<f * 1024>(wlo[i - 1], acc[4 + i][f]); });
  asm volatile("s_waitcnt vmcnt(%8)" : "+v"(xv[0]), "+v"(xv[1]), "+v"(xv[2]), "+v"(xv[3]), "+v"(gv[0]), "+v"(gv[1]), "+v"(gv[2]), "+v"(gv[3]) : "n"(between(NF + HEP, 3, NS - 1, 32)) : "memory");
  STAMP(6, 2); STAMPFLUSH(6, 1);
  float* gtab = reinterpret_cast<float*>(smem + GT);
  {
    float sum = (__uint_as_float(gv[0]) + __uint_as_float(gv[1])) + (__uint_as_float(gv[2]) + __uint_as_float(gv[3]));
    sum = (w >= 2) ? 1.0f / gain_nz(sum) : rbf(sum);
    const int col = tid & 127;
    gtab[(tid & 128) + (col & 96) + (col & 7) * 4 + ((col >> 3) & 3)] = sum;
  }
  asm volatile("s_waitcnt lgkmcnt(0)" ::: "memory");
  __builtin_amdgcn_s_barrier();
  u32x4 pq[3][8], gt[8], ct[8];
  f32x4 fin[8];
  {
    const unsigned gsrc = (unsigned)(uintptr_t)smem + GT + fq * 128;
    static_for<24>([&](auto E) { dsr128<E.value * 1024>(pq[E.value >> 3][E.value & 7], rlo); });
    static_for<8>([&](auto F) { dsr128<F.value * 16>(gt[F.value], gsrc); });
    static_for<8>([&](auto F) { dsr128<512 + F.value * 16>(ct[F.value], gsrc); });
#pragma unroll
    for (int f = 0; f < 8; ++f) { fin[f] = acc[4][f]; asm volatile("" : "+v"(fin[f])); }
    asm volatile("s_waitcnt lgkmcnt(0)" ::: "memory");
#pragma unroll
    for (int f = 0; f < 8; ++f) asm volatile("" : "+v"(pq[0][f]), "+v"(pq[1][f]), "+v"(pq[2][f]), "+v"(gt[f]), "+v"(ct[f]));
  }
#pragma unroll
  for (int f = 0; f < 8; ++f) {
    fin[f] = (fin[f] + __builtin_bit_cast(f32x4, pq[0][f])) + (__builtin_bit_cast(f32x4, pq[1][f]) + __builtin_bit_cast(f32x4, pq[2][f]));
    asm volatile("" : "+v"(fin[f]));
  }
  asm volatile("s_waitcnt vmcnt(0)" : "+v"(pv[0]), "+v"(pv[1]), "+v"(pv[2]), "+v"(pv[3]), "+v"(pv[4]), "+v"(pv[5]), "+v"(pv[6]), "+v"(pv[7])::"memory");
  STAMP(6, 6);
#pragma unroll
  for (int f = 0; f < 8; ++f) {
    fin[f] += __builtin_bit_cast(f32x4, pv[f]);
    f32x4 xf;
#pragma unroll
    for (int r = 0; r < 4; ++r) xf[r] = (f & 1) ? bf_hi(xv[r][f >> 1]) : bf_lo(xv[r][f >> 1]);
    xf *= __builtin_bit_cast(f32x4, ct[f]);
    fin[f] = xf + __builtin_bit_cast(f32x4, gt[f]) * fin[f];
  }
  {
    char* tb = smem + w * 24576;
#pragma unroll
    for (int r = 0; r < 4; ++r) {
      const u32x4 ov = {pk_bf16(fin[0][r], fin[1][r]), pk_bf16(fin[2][r], fin[3][r]), pk_bf16(fin[4][r], fin[5][r]), pk_bf16(fin[6][r], fin[7][r])};
      *reinterpret_cast<u32x4*>(tb + fr * 256 + (((fq * 4 + r) ^ (fr & 7)) * 16)) = ov;
    }
#pragma unroll
    for (int j = 0; j < 4; ++j) {
      const int row = j * 4 + fq;
      const u32x4 ov = *reinterpret_cast<const u32x4*>(tb + row * 256 + ((fr ^ (row & 7)) * 16));
      gst128(C + (size_t)(bm64 * 64 + w * 16 + row) * D_DIM + n0 + fr * 8, ov);
    }
  }
  STAMP(6, 3);
  asm volatile("s_waitcnt vmcnt(0)");
  STAMP(6, 4);
}

__global__ void diag_kernel() {
  __shared__ unsigned long long r0[256], r1[256];
  const int t = threadIdx.x;
  unsigned long long a = ~0ull, b = (DMODE == 0) ? ~0ull : 0ull; unsigned long long cnt = 0;
  for (int i = t; i < PROF_NB; i += 256) {
    unsigned long long x = g_stamps[(0 * PROF_NB + i) * PROF_NS + 0];
    if (x && x < a) a = x;
    unsigned long long y = g_stamps[(DKB * PROF_NB + i) * PROF_NS + DSB];
    if (y) { if (DMODE == 0) { if (y < b) b = y; } else if (DMODE == 1) { if (y > b) b = y; } else { b += y; cnt++; } }
  }
  __shared__ unsigned long long r2[256];
  r0[t] = a; r1[t] = b; r2[t] = cnt;
  __syncthreads();
  if (t == 0) {
    for (int i = 1; i < 256; ++i) {
      if (r0[i] < a) a = r0[i];
      if (DMODE == 0) { if (r1[i] < b) b = r1[i]; } else if (DMODE == 1) { if (r1[i] > b) b = r1[i]; } else { b += r1[i]; cnt += r2[i]; }
    }
    if (DMODE == 2) b = cnt ? b / cnt : a;
    unsigned long long d = (DSB == 7) ? b / 100 : ((b > a) ? (b - a) : 0);
    if (d > 20000) d = 20000;
    const unsigned long long t0 = __builtin_amdgcn_s_memrealtime();
    while (__builtin_amdgcn_s_memrealtime() - t0 < d * DMULT) __builtin_amdgcn_s_sleep(1);
  }
}
extern "C" void kernel_launch(void* const* d_in, const int* in_sizes, int n_in, void* d_out, int out_size,
                              void* d_ws, size_t ws_size, hipStream_t stream) {
  const bf16* x = (const bf16*)d_in[0];
  const bf16* wqkv = (const bf16*)d_in[1];
  const bf16* wo = (const bf16*)d_in[2];
  const bf16* wg = (const bf16*)d_in[4];
  const bf16* wu = (const bf16*)d_in[5];
  const bf16* wd = (const bf16*)d_in[6];
  const bf16* c = (const bf16*)d_in[7];
  const bf16* wmod = (const bf16*)d_in[8];
  bf16* out = (bf16*)d_out;

  char* ws = (char*)d_ws;
  float* modp = (float*)ws;
  float* rstat1 = (float*)(ws + 114688);
  float* stat = (float*)(ws + 524288);
  bf16* ot = (bf16*)(ws + (1 << 20));
  bf16* yt = (bf16*)(ws + (9 << 20));
  bf16* ht = (bf16*)(ws + (13 << 20));
  bf16* act = (bf16*)(ws + (17 << 20));
  float* xbuf = (float*)(ws + (32 << 20));
  unsigned* flags = (unsigned*)(ws + 262144);

  pre_kernel<<<320, 512, 0, stream>>>(x, c, wmod, modp, rstat1, wqkv);
  ln_apply_kernel<<<256, 256, 0, stream>>>(x, modp, rstat1, ht, wqkv, flags, wo);
  qkv_attn_kernel<<<256, 512, 0, stream>>>(ht, wqkv, ot, wo, x, modp, yt, stat, flags + 1024, wg, wu);
  gemm3_kernel<<<256, 512, 0, stream>>>(yt, wg, wu, act, stat, modp, wd);
  gemm4_kernel<<<256, 256, 0, stream>>>(act, wd, yt, modp, out, xbuf, flags);
  diag_kernel<<<1, 256, 0, stream>>>();
}
```

```cpp
#include <hip/hip_runtime.h>
#include <hip/hip_bf16.h>
#include <stdint.h>

typedef __hip_bfloat16 bf16;
typedef __attribute__((ext_vector_type(8))) short bf16x8;
typedef __attribute__((ext_vector_type(4))) float f32x4;
typedef __attribute__((ext_vector_type(2))) float f32x2;
typedef __attribute__((ext_vector_type(4))) unsigned u32x4;
typedef __attribute__((ext_vector_type(2))) unsigned u32x2;
typedef __attribute__((ext_vector_type(2))) __bf16 bf16x2_t;
typedef __amdgpu_buffer_rsrc_t rsrc_t;

#define DI __device__ __forceinline__
#define QBAR 2
#define QRN 17
#define TQ 12
#define TG 4
#define K1MAP 1
#define QTM 1
#define WOT 1
#define G4S 0
#define SMOD "sc1"
#define YALL 0
#define SEAMS 2
#define CODEPF 61

#define STAMP(k, s) do { } while (0)
#define STAMPT(k, s, T) do { } while (0)
#define STAMPNW(k, s)
#define STAMPFLUSH(k, s)
#define STAMPDECL

constexpr int S_LEN = 2048;
constexpr int D_DIM = 1024;
constexpr int F_DIM = 3584;

template <int V> struct ic { static constexpr int value = V; };
template <int N, int I = 0, class F>
DI void static_for(F&& f) {
  if constexpr (I < N) { f(ic<I>{}); static_for<N, I + 1>(f); }
}

DI float bf_lo(unsigned u) { return __uint_as_float(u << 16); }
DI float bf_hi(unsigned u) { return __uint_as_float(u & 0xffff0000u); }
DI unsigned pk_bf16(float lo, float hi) {
  const f32x2 v = {lo, hi};
  return __builtin_bit_cast(unsigned, __builtin_convertvector(v, bf16x2_t));
}
DI rsrc_t mk_rsrc(const void* p, unsigned bytes) { return __builtin_amdgcn_make_buffer_rsrc((void*)p, (short)0, (int)bytes, 0x00020000); }

template <int N>
DI void wait_vm() { asm volatile("s_waitcnt vmcnt(%0)" ::"n"(N > 63 ? 63 : N) : "memory"); }
#define WAIT_LGKM0() do { asm volatile("s_waitcnt lgkmcnt(0)" ::: "memory"); __builtin_amdgcn_sched_barrier(0); } while (0)
#define SCHED0() __builtin_amdgcn_sched_barrier(0)

#define MFMA_A(C, B, A) asm volatile("v_mfma_f32_16x16x32_bf16 %0, %1, %2, %0" : "+a"(C) : "v"(B), "a"(A))
#define MFMA_AV(C, B, A) asm volatile("v_mfma_f32_16x16x32_bf16 %0, %1, %2, %0" : "+v"(C) : "v"(B), "v"(A))
#define PERM_LO(D, X, Y) asm volatile("v_perm_b32 %0, %1, %2, %3" : "=v"(D) : "v"(Y), "v"(X), "s"(0x05040100u))
#define PERM_HI(D, X, Y) asm volatile("v_perm_b32 %0, %1, %2, %3" : "=v"(D) : "v"(Y), "v"(X), "s"(0x07060302u))
template <int OFF>
DI void bload_v(u32x4& d, unsigned voff, rsrc_t rs, unsigned soff) {
  asm volatile("buffer_load_dwordx4 %0, %1, %2, %3 offen offset:%4" : "+v"(d) : "v"(voff), "s"(rs), "s"(soff), "i"(OFF) : "memory");
}
template <int OFF>
DI void bload_a(u32x4& d, unsigned voff, rsrc_t rs, unsigned soff) {
  asm volatile("buffer_load_dwordx4 %0, %1, %2, %3 offen offset:%4" : "+a"(d) : "v"(voff), "s"(rs), "s"(soff), "i"(OFF) : "memory");
}
template <int OFF>
DI void bload2_v(u32x2& d, unsigned voff, rsrc_t rs, unsigned soff) {
  asm volatile("buffer_load_dwordx2 %0, %1, %2, %3 offen offset:%4" : "+v"(d) : "v"(voff), "s"(rs), "s"(soff), "i"(OFF) : "memory");
}
template <int OFF>
DI void bload1_v(unsigned& d, unsigned voff, rsrc_t rs, unsigned soff) {
  asm volatile("buffer_load_dword %0, %1, %2, %3 offen offset:%4" : "+v"(d) : "v"(voff), "s"(rs), "s"(soff), "i"(OFF) : "memory");
}
template <int OFF>
DI void bload_lds(unsigned m0v, unsigned voff, rsrc_t rs, unsigned soff) {
  asm volatile("s_mov_b32 m0, %0\n\ts_nop 0\n\tbuffer_load_dwordx4 %1, %2, %3 offen offset:%4 lds" ::"s"(m0v), "v"(voff), "s"(rs), "s"(soff), "i"(OFF) : "memory");
}
DI void touch_lds(unsigned m0v, unsigned voff, rsrc_t rs, unsigned soff) {
  asm volatile("s_mov_b32 m0, %0\n\ts_nop 0\n\tbuffer_load_dword %1, %2, %3 offen lds" ::"s"(m0v), "v"(voff), "s"(rs), "s"(soff) : "memory");
}
DI void touch_nt(unsigned m0v, unsigned voff, rsrc_t rs, unsigned soff) {
  asm volatile("s_mov_b32 m0, %0\n\ts_nop 0\n\tbuffer_load_dword %1, %2, %3 offen nt lds\n\tbuffer_load_dword %1, %2, %3 offen offset:64 sc1 lds" ::"s"(m0v), "v"(voff), "s"(rs), "s"(soff) : "memory");
}
template <int OFF>
DI void bload_lds_nt(unsigned m0v, unsigned voff, rsrc_t rs, unsigned soff) {
  asm volatile("s_mov_b32 m0, %0\n\ts_nop 0\n\tbuffer_load_dwordx4 %1, %2, %3 offen offset:%4 nt lds" ::"s"(m0v), "v"(voff), "s"(rs), "s"(soff), "i"(OFF) : "memory");
}
template <int OFF>
DI void bload_lds_sc1(unsigned m0v, unsigned voff, rsrc_t rs, unsigned soff) {
  asm volatile("s_mov_b32 m0, %0\n\ts_nop 0\n\tbuffer_load_dwordx4 %1, %2, %3 offen offset:%4 sc1 lds" ::"s"(m0v), "v"(voff), "s"(rs), "s"(soff), "i"(OFF) : "memory");
}
template <int OFF>
DI void dsr128(u32x4& d, unsigned addr) { asm volatile("ds_read_b128 %0, %1 offset:%2" : "=v"(d) : "v"(addr), "i"(OFF) : "memory"); }
template <int OFF>
DI void dsr64(u32x2& d, unsigned addr) { asm volatile("ds_read_b64 %0, %1 offset:%2" : "=v"(d) : "v"(addr), "i"(OFF) : "memory"); }
template <int OFF>
DI void dsr_tr(u32x2& d, unsigned addr) { asm volatile("ds_read_b64_tr_b16 %0, %1 offset:%2" : "=v"(d) : "v"(addr), "i"(OFF) : "memory"); }
template <int OFF>
DI void dsw128(unsigned addr, u32x4 d) { asm volatile("ds_write_b128 %0, %1 offset:%2" ::"v"(addr), "v"(d), "i"(OFF) : "memory"); }
template <int OFF>
DI void dsw128a(unsigned addr, const f32x4& d) { asm volatile("ds_write_b128 %0, %1 offset:%2" ::"v"(addr), "a"(d), "i"(OFF) : "memory"); }
template <int OFF>
DI void dsw32(unsigned addr, unsigned d) { asm volatile("ds_write_b32 %0, %1 offset:%2" ::"v"(addr), "v"(d), "i"(OFF) : "memory"); }

DI void gst128(void* p, u32x4 v) {
  asm volatile("global_store_dwordx4 %0, %1, off sc1\n\ts_nop 1" ::"v"(p), "v"(v) : "memory");
}
DI int fresh_lane() {
  int l;
  asm volatile("v_mbcnt_lo_u32_b32 %0, -1, 0\n\tv_mbcnt_hi_u32_b32 %0, -1, %0" : "=v"(l));
  return l;
}
DI f32x2 lo2(f32x4 v) { return __builtin_shufflevector(v, v, 0, 1); }
DI f32x2 hi2(f32x4 v) { return __builtin_shufflevector(v, v, 2, 3); }
DI float rowsum4(float v) {
  const unsigned u = __float_as_uint(v);
  const auto a = __builtin_amdgcn_permlane16_swap(u, u, false, false);
  const unsigned t = __float_as_uint(__uint_as_float(a[0]) + __uint_as_float(a[1]));
  const auto b = __builtin_amdgcn_permlane32_swap(t, t, false, false);
  return __uint_as_float(b[0]) + __uint_as_float(b[1]);
}
DI void split4(const f32x4 v, u32x2& hi, u32x2& lo) {
  hi[0] = pk_bf16(v[0], v[1]); hi[1] = pk_bf16(v[2], v[3]);
  lo[0] = pk_bf16(v[0] - bf_lo(hi[0]), v[1] - bf_hi(hi[0]));
  lo[1] = pk_bf16(v[2] - bf_lo(hi[1]), v[3] - bf_hi(hi[1]));
}
DI float silu_f(float v) { return v / (1.0f + __expf(-v)); }
DI float rbf(float v) { return bf_lo(pk_bf16(v, v)); }
DI float rowmax4(float v) {
  const unsigned u = __float_as_uint(v);
  const auto a = __builtin_amdgcn_permlane16_swap(u, u, false, false);
  const unsigned t = __float_as_uint(fmaxf(__uint_as_float(a[0]), __uint_as_float(a[1])));
  const auto b = __builtin_amdgcn_permlane32_swap(t, t, false, false);
  return fmaxf(__uint_as_float(b[0]), __uint_as_float(b[1]));
}
DI float gain_nz(float m) {
  const float g = rbf(1.0f + rbf(m));
  return __builtin_fabsf(g) < 8.67361738e-19f ? 8.67361738e-19f : g;
}
template <int OFF>
DI void bload_sc1(u32x4& d, unsigned voff, rsrc_t rs, unsigned soff) {
  asm volatile("buffer_load_dwordx4 %0, %1, %2, %3 offen offset:%4 sc1" : "+v"(d) : "v"(voff), "s"(rs), "s"(soff), "i"(OFF) : "memory");
}
template <int OFF>
DI void bstore_sc1(u32x4 d, unsigned voff, rsrc_t rs, unsigned soff) {
  asm volatile("buffer_store_dwordx4 %0, %1, %2, %3 offen offset:%4 sc1\n\ts_nop 1" ::"v"(d), "v"(voff), "s"(rs), "s"(soff), "i"(OFF) : "memory");
}
DI void bstore_mark(u32x4 d, unsigned voff, rsrc_t rs, unsigned soff) {
  asm volatile("buffer_store_dwordx4 %0, %1, %2, %3 offen " SMOD "\n\ts_nop 1" ::"v"(d), "v"(voff), "s"(rs), "s"(soff) : "memory");
}
DI void bload1_sc1(unsigned& d, unsigned voff, rsrc_t rs) {
  asm volatile("buffer_load_dword %0, %1, %2, 0 offen sc1" : "+v"(d) : "v"(voff), "s"(rs) : "memory");
}

__global__ __launch_bounds__(512) void pre_kernel(const bf16* __restrict__ x, const bf16* __restrict__ c,
                                                  const bf16* __restrict__ wmod, float* __restrict__ modp,
                                                  float* __restrict__ rstat1, const bf16* __restrict__ Wq) {
  __shared__ __attribute__((aligned(16))) float red[8 * 2 * 128 + 256];
  STAMP(0, 0);
  const int tid = threadIdx.x, lane = tid & 63, w = tid >> 6;
  if (blockIdx.x < 192) {
    const int fr = lane & 15, fq = lane >> 4;
    const int cg = blockIdx.x >> 2, kq = blockIdx.x & 3;
    const int col0 = cg * 128;
    const int k0 = kq * 256 + w * 32 + fq * 8;
    u32x4 raw[8];
#pragma unroll
    for (int j = 0; j < 8; ++j) {
      const u32x4* p_ = reinterpret_cast<const u32x4*>(wmod + (size_t)(k0 + j) * 6144 + col0 + fr * 8);
      raw[j] = __builtin_nontemporal_load(p_);
    }
    const u32x4 cv = *reinterpret_cast<const u32x4*>(c + k0);
    u32x4 cf;
#pragma unroll
    for (int e = 0; e < 4; ++e) {
      const float a = silu_f(bf_lo(cv[e])), b = silu_f(bf_hi(cv[e]));
      cf[e] = (fr == 0) ? pk_bf16(a, b) : 0u;
    }
    f32x4 acc[8];
#pragma unroll
    for (int p = 0; p < 4; ++p) {
      u32x4 lo, hi;
#pragma unroll
      for (int t = 0; t < 4; ++t) {
        lo[t] = __builtin_amdgcn_perm(raw[2 * t + 1][p], raw[2 * t][p], 0x05040100u);
        hi[t] = __builtin_amdgcn_perm(raw[2 * t + 1][p], raw[2 * t][p], 0x07060302u);
      }
      acc[2 * p] = __builtin_amdgcn_mfma_f32_16x16x32_bf16(__builtin_bit_cast(bf16x8, lo), __builtin_bit_cast(bf16x8, cf), f32x4{0, 0, 0, 0}, 0, 0, 0);
      acc[2 * p + 1] = __builtin_amdgcn_mfma_f32_16x16x32_bf16(__builtin_bit_cast(bf16x8, hi), __builtin_bit_cast(bf16x8, cf), f32x4{0, 0, 0, 0}, 0, 0, 0);
    }
    if (fr == 0) {
#pragma unroll
      for (int r = 0; r < 4; ++r) {
        float* d = red + w * 128 + fq * 32 + r * 8;
        *reinterpret_cast<f32x4*>(d) = f32x4{acc[0][r], acc[1][r], acc[2][r], acc[3][r]};
        *reinterpret_cast<f32x4*>(d + 4) = f32x4{acc[4][r], acc[5][r], acc[6][r], acc[7][r]};
      }
    }
    __syncthreads();
    if (tid < 128) {
      float s = 0;
#pragma unroll
      for (int g = 0; g < 8; ++g) s += red[g * 128 + tid];
      modp[kq * 6144 + col0 + tid] = s;
    }
    STAMP(0, 1);
    return;
  }
  const int row0 = (blockIdx.x - 192) * 16 + w * 2;
  u32x4 a[2][2];
#pragma unroll
  for (int i = 0; i < 2; ++i) {
    a[i][0] = *reinterpret_cast<const u32x4*>(x + (size_t)(row0 + i) * D_DIM + lane * 8);
    a[i][1] = *reinterpret_cast<const u32x4*>(x + (size_t)(row0 + i) * D_DIM + 512 + lane * 8);
  }
  if (blockIdx.x < 192 + 8) {
    unsigned long long pc;
    asm volatile("s_getpc_b64 %0" : "=s"(pc));
    const rsrc_t rsC = mk_rsrc((const void*)(uintptr_t)(pc & ~255ull), CODEPF * 1024);
#pragma unroll
    for (int i = 0; i < (CODEPF + 7) / 8; ++i) bload_lds<0>((unsigned)(uintptr_t)(red + 2048), tid * 16, rsC, i * 8192);
  }
#pragma unroll
  for (int i = 0; i < 2; ++i) {
    float v[16];
#pragma unroll
    for (int j = 0; j < 8; ++j) { v[2 * j] = bf_lo(a[i][j >> 2][j & 3]); v[2 * j + 1] = bf_hi(a[i][j >> 2][j & 3]); }
    float s = 0;
#pragma unroll
    for (int j = 0; j < 16; ++j) s += v[j];
#pragma unroll
    for (int o = 32; o > 0; o >>= 1) s += __shfl_xor(s, o);
    const float mean = s * (1.0f / D_DIM);
    float q = 0;
#pragma unroll
    for (int j = 0; j < 16; ++j) { const float d = v[j] - mean; q += d * d; }
#pragma unroll
    for (int o = 32; o > 0; o >>= 1) q += __shfl_xor(q, o);
    const float rstd = rsqrtf(q * (1.0f / D_DIM) + 1e-5f);
    if (lane == 0) *reinterpret_cast<f32x2*>(rstat1 + (row0 + i) * 2) = f32x2{rstd, -mean * rstd};
  }
  STAMP(0, 2);
}

#define QNT 0
#define K1POS 2
DI void k1_touch(char* sink, const bf16* Wq, const bf16* Wo, int part) {
  const int lane = threadIdx.x & 63, w = threadIdx.x >> 6;
  const rsrc_t rsP = mk_rsrc(Wq, D_DIM * 3 * D_DIM * 2);
  const unsigned vo = (unsigned)(((blockIdx.x >> 3) * 32 + (threadIdx.x >> 3)) * 6144 + (blockIdx.x & 3) * 512 + (threadIdx.x & 3) * 128);
  const unsigned v2 = (unsigned)(((blockIdx.x >> 3) * 32 + (threadIdx.x >> 2)) * 6144 + (blockIdx.x & 3) * 512 + (threadIdx.x & 3) * 128);
  if (part & 1) touch_lds((unsigned)(uintptr_t)sink, vo + ((threadIdx.x >> 2) & 1) * 2048, rsP, 0);
  if (part & 2) {
    if (w < 2) touch_lds((unsigned)(uintptr_t)sink, v2, rsP, 4096);
    else touch_lds((unsigned)(uintptr_t)sink, (w == 2 && WOT) ? blockIdx.x * 8192 + lane * 128 : 0x40000000u, mk_rsrc(Wo, D_DIM * D_DIM * 2), 0);
  }
}
__global__ __launch_bounds__(256) void ln_apply_kernel(const bf16* __restrict__ x, const float* __restrict__ modp,
                                                       const float* __restrict__ rstat1, bf16* __restrict__ H,
                                                       const bf16* __restrict__ Wq, unsigned* __restrict__ flags,
                                                       const bf16* __restrict__ Wo, unsigned* __restrict__ ctab) {
  __shared__ __attribute__((aligned(16))) float tab[4][64];
  __shared__ __attribute__((aligned(16))) char sink[1024];
  STAMP(1, 0);
  const int lane = threadIdx.x & 63, w = threadIdx.x >> 6;
  const int fr = lane & 15, fq = lane >> 4;
  const int kb = (blockIdx.x >> 5) * 4 + w, mb0 = (blockIdx.x & 7) + ((blockIdx.x >> 3) & 3) * 32;
  constexpr int MBS = 8;
  u32x4 mr, m2, xv[4];
  u32x2 rr[4];
  asm volatile("" : "=v"(mr), "=v"(m2));
  {
    const unsigned vm = (unsigned)((fq * 6144 + ((lane >> 3) & 1) * 1024 + kb * 32 + (lane & 7) * 4) * 4);
    bload_v<0>(m2, vm, mk_rsrc(modp, 4 * 6144 * 4), (blockIdx.x & 31) ? 0x40000000u : 12288u);
    bload_v<0>(mr, vm, mk_rsrc(modp, 4 * 6144 * 4), 0);
    const rsrc_t rsX = mk_rsrc(x, S_LEN * D_DIM * 2), rsS = mk_rsrc(rstat1, S_LEN * 8);
    const unsigned vx = (unsigned)(((mb0 * 16 + fr) * D_DIM + kb * 32 + fq * 8) * 2), vs = (unsigned)((mb0 * 16 + fr) * 8);
#pragma unroll
    for (int i = 0; i < 4; ++i) {
      asm volatile("" : "=v"(xv[i]), "=v"(rr[i]));
      bload_v<0>(xv[i], vx, rsX, i * 32768 * MBS);
      bload2_v<0>(rr[i], vs, rsS, i * 128 * MBS);
    }
    if (K1POS == 0) k1_touch(sink, Wq, Wo, 3);
    if (K1POS == 2) k1_touch(sink, Wq, Wo, 2);
  }
  asm volatile("s_waitcnt vmcnt(%10)" : "+v"(m2), "+v"(mr), "+v"(xv[0]), "+v"(xv[1]), "+v"(xv[2]), "+v"(xv[3]), "+v"(rr[0]), "+v"(rr[1]), "+v"(rr[2]), "+v"(rr[3]) : "n"((K1POS == 0 ? 2 : (K1POS == 2 ? 1 : 0)) * (QNT ? 2 : 1)) : "memory");
  f32x4 mv = __builtin_bit_cast(f32x4, mr);
  f32x2 rs[4];
#pragma unroll
  for (int i = 0; i < 4; ++i) rs[i] = __builtin_bit_cast(f32x2, rr[i]);
#pragma unroll
  for (int e = 0; e < 4; ++e) {
    mv[e] = rbf(rowsum4(mv[e]));
    if (lane & 8) mv[e] = rbf(1.0f + mv[e]);
  }
  if (lane < 16) *reinterpret_cast<f32x4*>(&tab[w][lane * 4]) = mv;
  __builtin_amdgcn_wave_barrier();
  asm volatile("s_waitcnt lgkmcnt(0)" ::: "memory");
  const f32x4 s0 = *reinterpret_cast<const f32x4*>(&tab[w][fq * 8]), s1 = *reinterpret_cast<const f32x4*>(&tab[w][fq * 8 + 4]);
  const f32x4 g0 = *reinterpret_cast<const f32x4*>(&tab[w][32 + fq * 8]), g1 = *reinterpret_cast<const f32x4*>(&tab[w][32 + fq * 8 + 4]);
#pragma unroll
  for (int i = 0; i < 4; ++i) {
    u32x4 ho;
#pragma unroll
    for (int d = 0; d < 4; ++d) {
      const f32x4 gg = d < 2 ? g0 : g1, ss = d < 2 ? s0 : s1;
      const float a = (bf_lo(xv[i][d]) * rs[i][0] + rs[i][1]) * gg[(d & 1) * 2] + ss[(d & 1) * 2];
      const float b = (bf_hi(xv[i][d]) * rs[i][0] + rs[i][1]) * gg[(d & 1) * 2 + 1] + ss[(d & 1) * 2 + 1];
      ho[d] = pk_bf16(a, b);
    }
    gst128((char*)H + ((size_t)(mb0 + i * MBS) * 32 + kb) * 1024 + lane * 16, ho);
  }
  if ((blockIdx.x & 31) == 0) {
    f32x4 cv = __builtin_bit_cast(f32x4, m2);
#pragma unroll
    for (int e = 0; e < 4; ++e) {
      cv[e] = rowsum4(cv[e]);
      if (lane & 8) cv[e] = gain_nz(cv[e]);
    }
    if (lane < 16) *reinterpret_cast<u32x2*>(ctab + kb * 32 + ((lane & 15) ^ 8) * 2) = u32x2{pk_bf16(cv[0], cv[1]), pk_bf16(cv[2], cv[3])};
  }
  if (threadIdx.x < 4) flags[blockIdx.x * 4 + threadIdx.x] = 0u;
  if (threadIdx.x == 4) flags[1024 + blockIdx.x * 32] = 0u;
  if (threadIdx.x == 5) flags[9216 + blockIdx.x * 32] = 0u;
  if (threadIdx.x == 6) flags[17408 + blockIdx.x * 32] = 0u;
  STAMP(1, 1);
  if (K1POS == 1) k1_touch(sink, Wq, Wo, 3);
  if (K1POS == 2) k1_touch(sink, Wq, Wo, 1);
}

#define GMAP 0
#define T1 0
#define T2 0
#define GNT 0
#define TW 1
#define TP 8
#define TS 4
#define TN 2
#define SM0 4
#define SMN 2
#define GS0 28
#define GSNT 0
#define GROT 1
#define GDEP 12
constexpr int Q_SLOT = 21504, Q_NSL = 7;
constexpr int QX_WH = 4 * Q_SLOT;
constexpr int QX_HX = 65536;
constexpr int QX_GT = QX_WH + 65536;
constexpr int QX_CNT = QX_GT + 512;
constexpr int QX_SINK = QX_CNT + 512;
constexpr int G3_CT = QX_SINK + 1024;
constexpr int X_CN = G3_CT + 4096;
constexpr int Q_LDS = X_CN + 1024;

DI unsigned lds_add(unsigned addr, unsigned v0) {
  unsigned o;
  asm volatile("ds_add_rtn_u32 %0, %1, %2\n\ts_waitcnt lgkmcnt(0)" : "=v"(o) : "v"(addr), "v"(v0) : "memory");
  return __builtin_amdgcn_readfirstlane(o);
}
template <int SLP>
DI void lds_wait(unsigned addr, unsigned n) {
  for (;;) {
    unsigned v;
    asm volatile("ds_read_b32 %0, %1\n\ts_waitcnt lgkmcnt(0)" : "=v"(v) : "v"(addr) : "memory");
    if (__builtin_amdgcn_readfirstlane(v) >= n) break;
    __builtin_amdgcn_s_sleep(SLP);
  }
}
template <int K, int NO, int NI>
DI void seam(unsigned lcnt, int lane, unsigned* out, unsigned* in, unsigned n) {
  const rsrc_t rsCo = mk_rsrc(out, (NO - 1) * 1024 + 4), rsCi = mk_rsrc(in, NI * 128);
  asm volatile("s_waitcnt vmcnt(0)" ::"s"(rsCo), "s"(rsCi) : "memory");
  STAMP(K, 4);
  if (lds_add(lcnt + lane * 4, 1u) == 7u) {
    asm volatile("buffer_atomic_add %0, %1, %2, 0 offen sc1" ::"v"(1u), "v"(lane * 1024), "s"(rsCo) : "memory");
    for (unsigned spins = 0;;) {
      unsigned fl;
      asm volatile("buffer_load_dword %0, %1, %2, 0 offen sc1\n\ts_waitcnt vmcnt(0)" : "=v"(fl) : "v"((lane & (NI - 1)) * 128), "s"(rsCi) : "memory");
      if (__builtin_amdgcn_ballot_w64(fl < n) == 0ull || ++spins >= (1u << 20)) break;
      __builtin_amdgcn_s_sleep(SEAMS);
    }
  }
  __builtin_amdgcn_s_barrier();
}
DI void wo_piece(unsigned smem_base, unsigned p, unsigned rot, unsigned voff, rsrc_t rs) {
  const unsigned pb = p >> 2, kb = (pb & 24u) | ((pb + rot) & 7u);
  bload_lds<0>(smem_base + (p < 64u ? QX_WH + p * 1024u : (p - 64u) * 1024u), voff, rs, kb * 65536u + (p & 3u) * 16384u);
}

DI void gu_touch(unsigned smem_base, int e, unsigned vgu, const bf16* wg, const bf16* wu, bool nt) {
  if (nt) touch_nt(smem_base + QX_SINK, vgu, mk_rsrc((e & 1) ? wu : wg, D_DIM * F_DIM * 2), (unsigned)(e >> 1) * 256u * (F_DIM * 2));
  else touch_lds(smem_base + QX_SINK, vgu, mk_rsrc((e & 1) ? wu : wg, D_DIM * F_DIM * 2), (unsigned)(e >> 1) * 256u * (F_DIM * 2));
}
DI void qkv_loader(unsigned smem_base, int lane, int sw, int m0, int n0, const bf16* __restrict__ H, const bf16* __restrict__ W,
                   unsigned rot, unsigned voffO, rsrc_t rsO) {
  const rsrc_t rsH = mk_rsrc(H, S_LEN * D_DIM * 2);
  const rsrc_t rsW = mk_rsrc(W, D_DIM * 3 * D_DIM * 2);
  int fb0 = (m0 >> 4) - 1 + 3 * sw; fb0 = fb0 < 0 ? 0 : fb0;
  const unsigned voffH0 = (unsigned)(fb0 * 32768 + lane * 16);
  const unsigned voffH1 = (unsigned)(((m0 >> 4) + 3 * sw) * 32768 + lane * 16);
  unsigned voffW[2];
#pragma unroll
  for (int i = 0; i < 2; ++i) {
    const int r = i * 8 + (lane >> 3), ch = lane & 7;
    const int g = ((r >> 1) & 1) | (((r >> 3) & 1) << 1);
    voffW[i] = (unsigned)(r * 6144 + sw * 2048 + (n0 + (((ch >> 1) ^ g) * 16) + (ch & 1) * 8) * 2);
  }
  const unsigned m0a = smem_base + sw * 3072, m0w = smem_base + 9216 + sw * 4096;
#define Q_LOAD(SOFF, U)                                                                                          \
  do {                                                                                                           \
    const unsigned so_ = (unsigned)(U) * 1024u;                                                                  \
    bload_lds<0>(m0a + (SOFF), voffH0, rsH, so_);                                                                \
    bload_lds<0>(m0a + (SOFF) + 1024, voffH1, rsH, so_);                                                         \
    bload_lds<0>(m0a + (SOFF) + 2048, voffH1, rsH, so_ + 32768u);                                                \
    _Pragma("unroll") for (int i = 0; i < 4; ++i)                                                                \
      bload_lds<0>(m0w + (SOFF) + i * 1024, voffW[i & 1], rsW, (unsigned)(U) * 196608u + (i >> 1) * 98304u);     \
  } while (0)
#pragma unroll
  for (int u = 0; u < Q_NSL - 1; ++u) Q_LOAD(u * Q_SLOT, u);
  wait_vm<(Q_NSL - 2) * 7>();
  __builtin_amdgcn_s_barrier();
  unsigned sb = (Q_NSL - 1) * Q_SLOT;
  for (int u = 0; u < 32 - (Q_NSL - 1); ++u) {
    Q_LOAD(sb, u + Q_NSL - 1);
    sb = (sb == (Q_NSL - 1) * Q_SLOT) ? 0u : sb + Q_SLOT;
    wait_vm<(Q_NSL - 2) * 7>();
    __builtin_amdgcn_s_barrier();
  }
  for (int t = 0; t < 6; ++t) {
    const unsigned p0 = (unsigned)(t * 21 + (t >= 3 ? 1 : 0) + sw * 7);
#pragma unroll
    for (int i = 0; i < 7; ++i) wo_piece(smem_base, p0 + i, rot, voffO, rsO);
    if (t < 5) wait_vm<(Q_NSL - 2) * 7>();
    __builtin_amdgcn_s_barrier();
  }
}

DI f32x4 mfma0(u32x4 a, u32x4 b) {
  return __builtin_amdgcn_mfma_f32_16x16x32_bf16(__builtin_bit_cast(bf16x8, a), __builtin_bit_cast(bf16x8, b), f32x4{0, 0, 0, 0}, 0, 0, 0);
}
DI u32x4 pack8(const f32x4 a, const f32x4 b) { return u32x4{pk_bf16(a[0], a[1]), pk_bf16(a[2], a[3]), pk_bf16(b[0], b[1]), pk_bf16(b[2], b[3])}; }
template <class OP>
DI float rows4(const float (&v)[4], OP op) {
  const auto a = __builtin_amdgcn_permlane16_swap(__float_as_uint(v[0]), __float_as_uint(v[1]), false, false);
  const auto b = __builtin_amdgcn_permlane16_swap(__float_as_uint(v[2]), __float_as_uint(v[3]), false, false);
  const float t0 = op(__uint_as_float(a[0]), __uint_as_float(a[1])), t1 = op(__uint_as_float(b[0]), __uint_as_float(b[1]));
  const auto c = __builtin_amdgcn_permlane32_swap(__float_as_uint(t0), __float_as_uint(t1), false, false);
  return op(__uint_as_float(c[0]), __uint_as_float(c[1]));
}
DI void bcast4(float t, float (&v)[4]) {
  const auto c = __builtin_amdgcn_permlane32_swap(__float_as_uint(t), __float_as_uint(t), false, false);
  const auto a = __builtin_amdgcn_permlane16_swap(c[0], c[0], false, false);
  const auto b = __builtin_amdgcn_permlane16_swap(c[1], c[1], false, false);
  v[0] = __uint_as_float(a[0]); v[1] = __uint_as_float(a[1]); v[2] = __uint_as_float(b[0]); v[3] = __uint_as_float(b[1]);
}
DI float fmax2(float a, float b) { return __builtin_amdgcn_fmed3f(a, b, __builtin_inff()); }

template <int WM>
DI void qkv_mfma(unsigned smem_base, int lane, int wn, int bm, int bn, bf16* __restrict__ O) {
  const int fr = lane & 15, fq = lane >> 4;
  unsigned bb0, bb1;
  const unsigned ab = smem_base + lane * 16;
  {
    const int q = (lane >> 2) & 3, p = lane & 3;
    const int r = 8 * fq + q;
    const int g = ((r >> 1) & 1) | (((r >> 3) & 1) << 1);
    bb0 = smem_base + 9216 + r * 128 + (((wn * 2) ^ g) * 32) + 8 * p;
    bb1 = bb0 ^ 32u;
  }
  const bool par = fr & 1, c0 = 4 * fq + (fr & 1) <= fr, c1 = 4 * fq + 2 + (fr & 1) <= fr;
  const unsigned s0 = par ? 0x01000c0cu : 0x0c0c0100u, s1 = par ? 0x03020c0cu : 0x0c0c0302u;
  u32x4 sel = {c0 ? s0 : 0x0c0c0c0cu, c1 ? s1 : 0x0c0c0c0cu, c0 ? 0x0c0c0c0cu : s0, c1 ? 0x0c0c0c0cu : s1};
  unsigned hx = ab + QX_HX + wn * 3072;
  asm volatile("" : "+v"(sel), "+v"(hx));
  const rsrc_t rsO = mk_rsrc((char*)O + (size_t)((bm * 2 + WM) * 32 + bn * 2 + wn) * 4096, 4096);
  f32x4 acc[4][6], acch[2];
#pragma unroll
  for (int i = 0; i < 4; ++i)
#pragma unroll
    for (int j = 0; j < 6; ++j) { acc[i][j] = f32x4{0, 0, 0, 0}; asm volatile("" : "+v"(acc[i][j])); }
#pragma unroll
  for (int j = 0; j < 2; ++j) { acch[j] = f32x4{0, 0, 0, 0}; asm volatile("" : "+v"(acch[j])); }
  __builtin_amdgcn_s_setprio(2);
  __builtin_amdgcn_s_barrier();
  STAMPDECL;
  STAMPNW(2, 1);
  u32x4 aX[5], aY[5];
  u32x2 bXl[6], bXh[6], bYl[6], bYh[6];
  unsigned ra = ab, rb0 = bb0, rb1 = bb1, so = 0;
#define Q_READ(AR, BL, BH, R)                                                                                    \
  do {                                                                                                           \
    if ((R) < 12) {                                                                                              \
      constexpr int jb = ((R) < 12 ? (R) : 0) >> 1;                                                              \
      if ((R) & 1) dsr_tr<(jb >> 1) * 4096 + 512>(BH[jb], (jb & 1) ? rb1 : rb0);                                 \
      else dsr_tr<(jb >> 1) * 4096>(BL[jb], (jb & 1) ? rb1 : rb0);                                               \
    } else if ((R) < 16) {                                                                                       \
      constexpr int ia = ((R) >= 12 && (R) < 16) ? (R) - 12 : 0;                                                 \
      dsr128<1024 + WM * 4096 + ia * 1024>(AR[ia], ra);                                                          \
    } else {                                                                                                     \
      dsr128<0>(AR[4], ra);                                                                                      \
    }                                                                                                            \
  } while (0)
#define Q_MFMA(AR, BB, N)                                                                                        \
  do {                                                                                                           \
    if ((N) < 16) MFMA_AV(acc[(N) & 3][((N) < 24 ? (N) : 0) >> 2], BB[((N) < 24 ? (N) : 0) >> 2], AR[(N) & 3]);  \
    else if ((N) < 24) MFMA_AV(acc[(N) & 3][((N) < 24 ? (N) : 0) >> 2], AR[(N) & 3], BB[((N) < 24 ? (N) : 0) >> 2]); \
    else if (WM == 0) MFMA_AV(acch[(N) & 1], BB[2 + ((N) & 1)], AR[4]);                                          \
    else MFMA_AV(acch[(N) & 1], AR[4], BB[4 + ((N) & 1)]);                                                       \
  } while (0)
#define Q_ADV(K)                                                                                                 \
  do {                                                                                                           \
    if ((K) == 0) asm volatile("s_add_u32 %0, %0, %1\n\ts_cmp_eq_u32 %0, %2\n\ts_cselect_b32 %0, 0, %0" : "+s"(so) : "n"(Q_SLOT), "n"(Q_NSL * Q_SLOT) : "scc"); \
    else if ((K) == 1) asm volatile("v_add_u32 %0, %1, %2" : "=v"(rb0) : "s"(so), "v"(bb0));                     \
    else if ((K) == 2) asm volatile("v_add_u32 %0, %1, %2" : "=v"(rb1) : "s"(so), "v"(bb1));                     \
    else asm volatile("v_add_u32 %0, %1, %2" : "=v"(ra) : "s"(so), "v"(ab));                                     \
  } while (0)
#define Q_UNIT(AR, BL, BH, NAR, NBL, NBH)                                                                        \
  do {                                                                                                           \
    WAIT_LGKM0();                                                                                                \
    u32x4 bb[6];                                                                                                 \
    _Pragma("unroll") for (int j = 0; j < 6; ++j) bb[j] = u32x4{BL[j][0], BL[j][1], BH[j][0], BH[j][1]};         \
    SCHED0();                                                                                                    \
    static_for<26>([&](auto N) {                                                                                 \
      constexpr int n = N.value;                                                                                 \
      if constexpr (n == QBAR) __builtin_amdgcn_s_barrier();                                                     \
      Q_MFMA(AR, bb, n);                                                                                         \
      if constexpr (n >= QBAR && n < QBAR + 17) Q_READ(NAR, NBL, NBH, n - QBAR);                                 \
      if constexpr (n >= QBAR + 18 && n < QBAR + 22) Q_ADV(n - QBAR - 18);                                       \
    });                                                                                                          \
    SCHED0();                                                                                                    \
  } while (0)
  static_for<17>([&](auto R) { Q_READ(aX, bXl, bXh, R.value); });
  static_for<4>([&](auto K) { Q_ADV(K.value); });
  for (int u = 0; u < 32; u += 2) {
    Q_UNIT(aX, bXl, bXh, aY, bYl, bYh);
    Q_UNIT(aY, bYl, bYh, aX, bXl, bXh);
  }
  asm volatile("s_waitcnt lgkmcnt(0)\n\ts_nop 7" ::: "memory");
#pragma unroll
  for (int i = 0; i < 5; ++i) asm volatile("" : "+v"(aX[i]));
#pragma unroll
  for (int j = 0; j < 6; ++j) asm volatile("" : "+v"(bXl[j]), "+v"(bXh[j]));
#pragma unroll
  for (int i = 0; i < 4; ++i)
#pragma unroll
    for (int j = 0; j < 6; ++j) asm volatile("" : "+v"(acc[i][j]));
  asm volatile("" : "+v"(acch[0]), "+v"(acch[1]));
  SCHED0();
  STAMP(2, 2); STAMPFLUSH(2, 1);
  u32x4 kk[5], vv[5];
  if (WM == 0) {
    kk[4] = pack8(acc[3][2], acc[3][3]); vv[4] = pack8(acc[3][4], acc[3][5]);
    dsw128<0>(hx, kk[4]); dsw128<1024>(hx, vv[4]);
  } else {
    dsw128<2048>(hx, pack8(acch[0], acch[1]));
  }
  asm volatile("s_waitcnt lgkmcnt(0)" ::: "memory");
  __builtin_amdgcn_s_barrier();
  if (WM == 0) dsr128<2048>(vv[0], hx);
  else { dsr128<0>(kk[0], hx); dsr128<1024>(vv[0], hx); }
  SCHED0();
  if (WM == 0) kk[0] = pack8(acch[0], acch[1]);
#pragma unroll
  for (int i = 0; i < (WM == 0 ? 3 : 4); ++i) kk[i + 1] = pack8(acc[i][2], acc[i][3]);
  const float cs = 0.17677669529663687f * 1.4426950408889634f;
  float l0[4], l1[4], tm[4];
#pragma unroll
  for (int ii = 1; ii < 5; ++ii) {
    const int i = ii & 3;
    const u32x4 q = pack8(acc[i][0], acc[i][1]);
    if (WM == 1 && i == 0) asm volatile("s_waitcnt lgkmcnt(0)" : "+v"(kk[0]), "+v"(vv[0])::"memory");
    const f32x4 sc = mfma0(kk[i + 1], q), sp = mfma0(kk[i], q);
    float p0 = par ? sp[1] : sp[0], p1 = par ? sp[3] : sp[2];
    if (WM == 0 && i == 0 && bm == 0) { p0 = -5.656854249e9f; p1 = -5.656854249e9f; }
    l0[i] = c0 ? (par ? sc[1] : sc[0]) : p0;
    l1[i] = c1 ? (par ? sc[3] : sc[2]) : p1;
    tm[i] = fmax2(l0[i], l1[i]);
  }
#pragma unroll
  for (int i = 0; i < (WM == 0 ? 3 : 4); ++i) vv[i + 1] = pack8(acc[i][4], acc[i][5]);
  bcast4(rows4(tm, [](float a, float b) { return fmax2(a, b); }), tm);
#pragma unroll
  for (int i = 0; i < 4; ++i) {
    l0[i] = __builtin_amdgcn_exp2f((l0[i] - tm[i]) * cs);
    l1[i] = __builtin_amdgcn_exp2f((l1[i] - tm[i]) * cs);
    tm[i] = l0[i] + l1[i];
  }
  bcast4(__builtin_amdgcn_rcpf(rows4(tm, [](float a, float b) { return a + b; })), tm);
  if (WM == 0) asm volatile("s_waitcnt lgkmcnt(0)" : "+v"(vv[0])::"memory");
#pragma unroll
  for (int i = 0; i < 4; ++i) {
    const unsigned pp = pk_bf16(l0[i] * tm[i], l1[i] * tm[i]);
    const u32x4 pr = {__builtin_amdgcn_perm(pp, pp, sel[0]), __builtin_amdgcn_perm(pp, pp, sel[1]),
                      __builtin_amdgcn_perm(pp, pp, sel[2]), __builtin_amdgcn_perm(pp, pp, sel[3])};
    const u32x4 ov = pack8(mfma0(u32x4{vv[i + 1][0], vv[i + 1][1], vv[i][0], vv[i][1]}, pr), mfma0(u32x4{vv[i + 1][2], vv[i + 1][3], vv[i][2], vv[i][3]}, pr));
    asm volatile("buffer_store_dwordx4 %0, %1, %2, 0 offen offset:%3 sc1\n\ts_nop 1" ::"v"(ov), "v"(lane * 16), "s"(rsO), "i"(i * 1024) : "memory");
  }
  STAMP(2, 3);
}

template <class GST>
DI void attn_phase(unsigned smem_base, int wid, const bf16* __restrict__ H, const bf16* __restrict__ W, bf16* O,
                   const bf16* __restrict__ wo, const bf16* __restrict__ X, const float* __restrict__ modp, bf16* Y, float* stat,
                   unsigned* flags, const bf16* __restrict__ wg, const bf16* __restrict__ wu, float* xbuf,
                   const unsigned* __restrict__ ctab, GST gst) {
  const int tid = threadIdx.x, lane = tid & 63;
  const int xcd = blockIdx.x & 7, idx = blockIdx.x >> 3;
  const int bm = (xcd >> 2) * 8 + (idx & 7), bn = (xcd & 3) * 4 + (idx >> 3);
  const int rb = xcd * 2 + (idx & 1), cg = idx >> 1;
  const unsigned rot = __builtin_amdgcn_readfirstlane(GMAP ? (cg & 4) : (cg >> 2) * 2);
  const unsigned vgu = (unsigned)((idx * 8 + (lane >> 3)) * (F_DIM * 2) + xcd * 896 + ((lane & 7) == 7 ? 6 : (lane & 7)) * 128);
  const unsigned cnt = smem_base + QX_CNT, cntl = cnt + lane * 4;
  if (wid < 4) {
    if (wid & 1) qkv_mfma<1>(smem_base, lane, wid >> 1, bm, bn, O);
    else qkv_mfma<0>(smem_base, lane, wid >> 1, bm, bn, O);
  } else {
    const rsrc_t rsO = mk_rsrc(wo, D_DIM * D_DIM * 2);
    const unsigned voffO = (unsigned)((lane >> 3) * 2048 + cg * 128 + ((((lane & 7) >> 1) ^ (lane >> 4)) * 32) + (lane & 1) * 16);
    if (wid < 7) qkv_loader(smem_base, lane, wid - 4, bm * 128, bn * 64, H, W, rot, voffO, rsO);
    else {
      unsigned gv[8];
      const rsrc_t rsG = mk_rsrc(modp, 4 * 6144 * 4);
#pragma unroll
      for (int i = 0; i < 8; ++i) { asm volatile("" : "=v"(gv[i])); bload1_v<0>(gv[i], (unsigned)((cg * 64 + lane) * 4), rsG, (i >> 1) * 24576 + 8192 + (i & 1) * 8192); }
      dsw32<0>(cntl, 0u); dsw32<256>(cntl, 0u);
      dsw128<0>(smem_base + X_CN + lane * 16, u32x4{0u, 0u, 0u, 0u});
      asm volatile("s_waitcnt lgkmcnt(0)" ::: "memory");
      for (int i = 0; i < 3; ++i) __builtin_amdgcn_s_barrier();
      asm volatile("s_waitcnt vmcnt(0)" : "+v"(gv[0]), "+v"(gv[1]), "+v"(gv[2]), "+v"(gv[3]), "+v"(gv[4]), "+v"(gv[5]), "+v"(gv[6]), "+v"(gv[7])::"memory");
      const float ga = (__uint_as_float(gv[0]) + __uint_as_float(gv[2])) + (__uint_as_float(gv[4]) + __uint_as_float(gv[6]));
      const float cb = (__uint_as_float(gv[1]) + __uint_as_float(gv[3])) + (__uint_as_float(gv[5]) + __uint_as_float(gv[7]));
      dsw32<0>(smem_base + QX_GT + lane * 4, __float_as_uint(rbf(ga)));
      dsw32<256>(smem_base + QX_GT + lane * 4, __float_as_uint(gain_nz(cb)));
      asm volatile("s_waitcnt lgkmcnt(0)" ::: "memory");
      static_for<4>([&](auto I) { bload_lds<I.value * 1024>(smem_base + G3_CT, lane * 16, mk_rsrc(ctab, 4096), 0); });
      {
        int nxt = TS + idx % TP, ev = 0;
        const rsrc_t rsE = mk_rsrc((char*)xbuf + (size_t)blockIdx.x * 32768, 32768);
        for (int i = 2; i < 27; ++i) {
          if (G4S == 2 && i >= SM0 && i < SM0 + 32 / SMN) {
#pragma unroll
            for (int j = 0; j < SMN; ++j) bstore_mark(u32x4{~0u, ~0u, ~0u, ~0u}, lane * 16, rsE, (unsigned)((i - SM0) * SMN + j) * 1024u);
          }
          if (TW > 0 && i == nxt && ev < TN) {
#pragma unroll
            for (int j = 0; j < TW; ++j) gu_touch(smem_base, ev * TW + j, vgu, wg, wu, GNT & 1);
            nxt += TP; ++ev;
          }
          __builtin_amdgcn_s_barrier();
        }
      }
      wo_piece(smem_base, 63u, rot, voffO, rsO);
      for (int i = 28; i < 33; ++i) __builtin_amdgcn_s_barrier();
      wo_piece(smem_base, 127u, rot, voffO, rsO);
    }
    __builtin_amdgcn_s_barrier();
    if (wid - 4 < T1) { gu_touch(smem_base, T2 + wid - 4, vgu, wg, wu, GNT & 2); wait_vm<(GNT & 2) ? 2 : 1>(); }
    else wait_vm<0>();
    lds_add(cntl, 1u);
  }
  const int fr = lane & 15, fq = lane >> 4;
  const int kq = wid & 3, rg = rb * 2 + (wid >> 2);
  const int ko = GROT ? (kq + cg) & 3 : kq;
  lds_wait<2>(cnt, 4);
  u32x2 wl[8][4], wh[8][4];
  {
    const int q = (lane >> 2) & 3, p = lane & 3, r = 4 * fq + q;
    const unsigned wb = smem_base + (kq < 2 ? QX_WH + kq * 32768 : (kq - 2) * 32768) + r * 128 + (((r >> 1) & 3) * 32) + 8 * p;
    unsigned wa[4];
#pragma unroll
    for (int f = 0; f < 4; ++f) wa[f] = wb ^ (f * 32);
    static_for<32>([&](auto E) {
      constexpr int s = E.value >> 2, f = E.value & 3;
      dsr_tr<s * 4096>(wl[s][f], wa[f]);
      dsr_tr<s * 4096 + 2048>(wh[s][f], wa[f]);
    });
  }
  const rsrc_t rsA = mk_rsrc((const char*)O + (size_t)rg * 131072, 131072);
  const rsrc_t rsF = mk_rsrc(flags + (rb * 16 + kq * 4) * 32, 4 * 128);
  unsigned voffA[4], hs[8];
#pragma unroll
  for (int i = 0; i < 4; ++i) { voffA[i] = lane * 16 + ((ko + 1 + i) & 3) * 1024; asm volatile("" : "+v"(voffA[i])); }
#pragma unroll
  for (int i = 0; i < 8; ++i) { hs[i] = __builtin_amdgcn_readfirstlane((kq * 8 + ((i + rot) & 7)) * 4096); asm volatile("" : "+s"(hs[i])); }
  asm volatile("" ::"s"(rsA), "s"(rsF));
  if (wid < 4) {
    const rsrc_t rsFo = mk_rsrc(flags + (bm * 16 + bn) * 32, 4);
    asm volatile("s_waitcnt vmcnt(0)" ::"s"(rsFo) : "memory");
    if ((lds_add(cntl + 256, 1u) & 15u) == 3u)
      asm volatile("buffer_store_dword %0, %1, %2, 0 offen sc1" ::"v"(1u), "v"(lane * 4), "s"(rsFo) : "memory");
    STAMP(2, 4);
  } else {
    lds_add(cntl + 256, 16u);
  }
  lds_wait<2>(cnt + 256, 68);
  u32x4 wf[8][4];
#pragma unroll
  for (int s = 0; s < 8; ++s)
#pragma unroll
    for (int f = 0; f < 4; ++f) {
      asm volatile("" : "+v"(wl[s][f]), "+v"(wh[s][f]));
      wf[s][f] = u32x4{wl[s][f][0], wl[s][f][1], wh[s][f][0], wh[s][f][1]};
    }
  {
    unsigned spins = 0;
    for (;;) {
      unsigned fl;
      asm volatile("buffer_load_dword %0, %1, %2, 0 offen sc1\n\ts_waitcnt vmcnt(0)" : "=v"(fl) : "v"((lane & 3) * 128), "s"(rsF) : "memory");
      if (__builtin_amdgcn_ballot_w64(fl == 0u) == 0ull || ++spins >= (1u << 20)) break;
      __builtin_amdgcn_s_sleep(1);
    }
  }
  STAMP(3, 0);
  const unsigned voffL = lane * 16;
  u32x4 af[32], xv[2], pr[3][4], gt[4], ct[4];
  f32x4 acc[4][4];
  auto aload = [&](auto N) {
    constexpr int n = N.value;
    asm volatile("" : "=v"(af[n]));
    bload_sc1<0>(af[n], voffA[n >> 3], rsA, hs[n & 7]);
  };
  static_for<GDEP>([&](auto N) { aload(N); });
  const unsigned sl = smem_base + 49152 + voffL + (wid >> 2) * 49152;
  const int sw = ((fq & 1) << 1) | (fq >> 1);
  SCHED0();
  static_for<32>([&](auto N) {
    constexpr int n = N.value, ps = n >> 3, s = n & 7, XG = 32 - GDEP;
    constexpr int last = n - 1 + GDEP < 31 ? n - 1 + GDEP : 31;
    asm volatile("s_waitcnt vmcnt(%1)" : "+v"(af[n]) : "n"(last - n + (n > XG ? 2 : 0) + (n > GS0 + 3 ? 6 : (n > GS0 ? 2 * (n - GS0) : 0))) : "memory");
    if constexpr (n == 28) { WAIT_LGKM0(); __builtin_amdgcn_s_barrier(); }
    static_for<4>([&](auto F) {
      constexpr int f = F.value;
      if constexpr (s == 0) asm volatile("v_mfma_f32_16x16x32_bf16 %0, %1, %2, 0" : "=&v"(acc[ps][f]) : "v"(wf[s][f]), "v"(af[n]));
      else MFMA_AV(acc[ps][f], wf[s][f], af[n]);
      if constexpr (f == 0 && n + GDEP < 32) aload(ic<(n + GDEP < 32 ? n + GDEP : 0)>{});
      if constexpr (f == 0 && n == XG) {
        const rsrc_t rsX = mk_rsrc(X, S_LEN * D_DIM * 2);
        const unsigned vx = (unsigned)(((rg * 64 + ko * 16 + fr) * D_DIM + cg * 64 + sw * 8) * 2);
        asm volatile("" : "=v"(xv[0]), "=v"(xv[1]));
        bload_v<0>(xv[0], vx, rsX, 0); bload_v<64>(xv[1], vx, rsX, 0);
      }
      if constexpr ((f & 1) && n >= GS0 && n < GS0 + 3) gst(ic<(n >= GS0 ? n - GS0 : 0)>{}, ic<(f >> 1)>{});
      if constexpr (ps > 0 && (s == 2 || s == 3) && (f & 1)) {
        constexpr int e = (s - 2) * 2 + (f >> 1);
        dsw128<(ps - 1) * 4096 + e * 1024>(sl + ((kq + ps) & 3) * 12288, __builtin_bit_cast(u32x4, acc[ps - 1][e]));
      }
      if constexpr (n >= 28 && n < 31) dsr128<(n - 28) * 4096 + f * 1024>(pr[n - 28][f], sl + kq * 12288);
      if constexpr (n == 31) { dsr128<f * 64>(gt[f], smem_base + QX_GT + fq * 16); dsr128<256 + f * 64>(ct[f], smem_base + QX_GT + fq * 16); }
    });
  });
  asm volatile("s_waitcnt vmcnt(6)" : "+v"(xv[0]), "+v"(xv[1])::"memory");
  asm volatile("s_waitcnt lgkmcnt(0)\n\ts_nop 4" ::: "memory");
  STAMP(3, 2);
  f32x4 fin[4], yv[4];
#pragma unroll
  for (int f = 0; f < 4; ++f) asm volatile("" : "+v"(acc[3][f]), "+v"(pr[0][f]), "+v"(pr[1][f]), "+v"(pr[2][f]), "+v"(gt[f]), "+v"(ct[f]));
#pragma unroll
  for (int c = 0; c < 2; ++c) {
    const auto x0 = __builtin_amdgcn_permlane16_swap(xv[c][0], xv[c][2], false, false);
    const auto x1 = __builtin_amdgcn_permlane16_swap(xv[c][1], xv[c][3], false, false);
#pragma unroll
    for (int h = 0; h < 2; ++h) {
      const int f = 2 * c + h;
      const f32x4 a = (acc[3][f] + __builtin_bit_cast(f32x4, pr[0][f])) + (__builtin_bit_cast(f32x4, pr[1][f]) + __builtin_bit_cast(f32x4, pr[2][f]));
      const f32x4 xf = {bf_lo(x0[h]), bf_hi(x0[h]), bf_lo(x1[h]), bf_hi(x1[h])};
      fin[f] = xf + __builtin_bit_cast(f32x4, gt[f]) * a;
      yv[f] = fin[f] * __builtin_bit_cast(f32x4, ct[f]);
    }
    const auto y0 = __builtin_amdgcn_permlane16_swap(pk_bf16(yv[2 * c][0], yv[2 * c][1]), pk_bf16(yv[2 * c + 1][0], yv[2 * c + 1][1]), false, false);
    const auto y1 = __builtin_amdgcn_permlane16_swap(pk_bf16(yv[2 * c][2], yv[2 * c][3]), pk_bf16(yv[2 * c + 1][2], yv[2 * c + 1][3]), false, false);
    gst128((char*)Y + ((size_t)(rg >> 2) * 32 + cg * 2 + c) * 16384 + ((rg & 3) * 4 + ko) * 1024 + (sw * 16 + fr) * 16,
           u32x4{y0[0], y1[0], y0[1], y1[1]});
  }
  const f32x4 s4 = (fin[0] + fin[1]) + (fin[2] + fin[3]);
  const float s = rowsum4((s4[0] + s4[1]) + (s4[2] + s4[3]));
  const float mb = s * (1.0f / 64.0f);
  f32x4 q4 = {0, 0, 0, 0};
#pragma unroll
  for (int f = 0; f < 4; ++f) { const f32x4 d = fin[f] - mb; q4 += d * d; }
  const float m2 = rowsum4((q4[0] + q4[1]) + (q4[2] + q4[3]));
  asm volatile("buffer_store_dwordx2 %0, %1, %2, 0 offen sc1" ::"v"(f32x2{s, m2}), "v"(lane * 8),
               "s"(mk_rsrc(stat + ((size_t)cg * S_LEN + rg * 64 + ko * 16) * 2, 128)) : "memory");
  STAMP(3, 3);
}

#define G3NT 1
#define TPOS 6
#define SPOS 9
#define NBC 1
#define G3_STAGE(SLOT, P, U)                                                                               \
  do {                                                                                                     \
    if ((P) == 0) bload_lds_sc1<0>(m0w + 65536 + (SLOT) * 16384, voffA, rsA, (unsigned)(U) * 16384u);      \
    else if ((P) == 1) bload_lds_sc1<1024>(m0w + 65536 + (SLOT) * 16384, voffA, rsA, (unsigned)(U) * 16384u);  \
    else if ((P) == 2) bload_lds_nt<0>(m0w + (SLOT) * 16384, voffB0, rsB, (unsigned)(U) * 229376u);        \
    else bload_lds_nt<0>(m0w + (SLOT) * 16384 + 1024, voffB1, rsB, (unsigned)(U) * 229376u);               \
  } while (0)

#define G3ROT 1
DI int g3_line(int gx, int p) {
  const int t = p + (G3ROT ? (3 + gx) & 7 : 0);
  return 7 * gx + (t >= 7 ? t - 7 : t);
}
template <int FN, class WST, class HOOK>
DI void g3_body(char* smem, unsigned smem_base, int wm, int wn, int wid, int lane, int bm, int bn, rsrc_t rsA, rsrc_t rsB,
                rsrc_t rsP, rsrc_t rsE, unsigned voffA, unsigned voffB0, unsigned voffB1, bf16* act, WST wst, HOOK hook) {
  constexpr int NB = 2 * FN, NM = 4 * NB, RD = 5 + 2 * NB;
  int fr = lane & 15, fq = lane >> 4;
  f32x4 acc[4][NB], accC[2];
  accC[0] = f32x4{0, 0, 0, 0}; accC[1] = f32x4{0, 0, 0, 0};
  asm volatile("" : "+v"(accC[0]), "+v"(accC[1]));
  const int rot = wm % FN;
  unsigned cad = smem_base + G3_CT + (fr & 1) * 64 + fq * 16;
#pragma unroll
  for (int i = 0; i < 4; ++i)
#pragma unroll
    for (int j = 0; j < NB; ++j) { acc[i][j] = f32x4{0, 0, 0, 0}; asm volatile("" : "+v"(acc[i][j])); }
  const unsigned aad = smem_base + 65536 + wm * 4096 + lane * 16;
  unsigned bad[FN];
  {
    const int q = (lane >> 2) & 3, p = lane & 3;
    const int r = 8 * fq + q;
    const int sw = (r & 3) | (((r >> 3) & 1) << 2);
#pragma unroll
    for (int j = 0; j < FN; ++j) {
      const int gi = wn * 4 + (j + rot) % FN + (bn & 1);
      bad[j] = smem_base + r * 256 + ((gi ^ sw) * 32) + 8 * p;
    }
  }
  u32x4 aX[5], aY[5];
  u32x2 bXl[NB], bXh[NB], bYl[NB], bYh[NB];
  const unsigned m0w = smem_base + wid * 2048;
  const unsigned voffP = lane * 16;
  u32x4 ones = {~0u, ~0u, ~0u, ~0u};
  if (G4S == 1 && FN == 3) asm volatile("" : "+v"(ones));
#define G3_READ(AR, BL, BH, SLOT, R)                                                                       \
  do {                                                                                                     \
    if ((R) < 2 * NB) {                                                                                    \
      constexpr int jb = ((R) < 2 * NB ? (R) : 0) >> 1;                                                    \
      constexpr int off = (SLOT) * 16384 + (jb / FN) * 8192 + ((R) & 1) * 1024;                            \
      if ((R) & 1) dsr_tr<off>(BH[jb], bad[jb % FN]);                                                       \
      else dsr_tr<off>(BL[jb], bad[jb % FN]);                                                               \
    } else if ((R) < 2 * NB + 4) {                                                                         \
      constexpr int ia = ((R) >= 2 * NB && (R) < 2 * NB + 4) ? (R) - 2 * NB : 0;                           \
      dsr128<(SLOT) * 16384 + ia * 1024>(AR[ia], aad);                                                      \
    } else {                                                                                               \
      if ((SLOT) == 0) cad += 512;                                       \
      dsr128<(SLOT) * 128>(AR[4], cad);                                                                    \
    }                                                                                                      \
  } while (0)
  auto rd = [&](auto SL, auto RR, u32x4 (&AR)[5], u32x2 (&BL)[NB], u32x2 (&BH)[NB]) { G3_READ(AR, BL, BH, SL.value, RR.value); };
#define G3_UNIT(AR, BL, BH, NAR, NBL, NBH, SLOT, U, LI)                                                    \
  do {                                                                                                     \
    WAIT_LGKM0();                                                                                          \
    u32x4 bb[NB];                                                                                          \
    _Pragma("unroll") for (int j = 0; j < NB; ++j) bb[j] = u32x4{BL[j][0], BL[j][1], BH[j][0], BH[j][1]};  \
    SCHED0();                                                                                              \
    asm volatile("s_nop 1");                                                                               \
    static_for<NM>([&](auto N) {                                                                           \
      constexpr int n = N.value;                                                                           \
      if constexpr (n == NM / 2 && !(LI && (SLOT) == 3)) {                                                 \
        wait_vm<(LI && (SLOT) == 0 && FN == 3) ? 28 : 8>(); __builtin_amdgcn_s_barrier();                  \
      }                                                                                                    \
      if constexpr (n == NM / 2 + TPOS && !LI) {                                                           \
                      \
        if (((U) & 7) == wid) bload_lds<0>(smem_base + 131072 + 4096, voffP, rsP, (unsigned)(U) * 1024u);  \
      }                                                                                                    \
      if constexpr (G4S == 1 && FN == 3 && n == NM / 2 + SPOS) {                                               \
                                          \
        if ((((SLOT) + 2) & 3) == wm) bstore_mark(ones, voffP, rsE, (unsigned)(U) * 1024u);                \
      }                                                                                                    \
      MFMA_AV(acc[n & 3][n >> 2], bb[n >> 2], AR[n & 3]);                                                  \
      if constexpr (n < NM / 2) {                                                                          \
        if constexpr (LI && (SLOT) == 0 && n == 0) hook(ic<0>{});                                          \
        if constexpr (n % 3 == 1 && n / 3 < 4) {                                                           \
          if constexpr (LI && (SLOT) > 0) wst(ic<((SLOT) + 3) & 3>{}, ic<(n / 3) & 3>{});                  \
          else G3_STAGE(((SLOT) + 3) & 3, n / 3, (U) + 3);                                                 \
        }                                                                                                  \
      } else if constexpr (LI && (SLOT) == 3) {                                                            \
        if constexpr (n == NM / 2) hook(ic<1>{});                                                          \
      } else {                                                                                             \
        constexpr int r0 = (n - NM / 2) * 2;                                                               \
        if constexpr (r0 < RD) rd(ic<((SLOT) + 1) & 3>{}, ic<(r0 < RD ? r0 : 0)>{}, NAR, NBL, NBH);        \
        if constexpr (r0 + 1 < RD) rd(ic<((SLOT) + 1) & 3>{}, ic<(r0 + 1 < RD ? r0 + 1 : 0)>{}, NAR, NBL, NBH); \
      }                                                                                                    \
    });                                                                                                    \
    if (NBC || FN == 4 || wm < 3) {                                                                        \
      MFMA_AV(accC[0], bb[0], AR[4]);                                                                      \
      MFMA_AV(accC[1], bb[FN], AR[4]);                                                                     \
    }                                                                                                      \
    SCHED0();                                                                                              \
  } while (0)

  asm volatile("s_waitcnt vmcnt(6)" ::: "memory");
  __builtin_amdgcn_s_barrier();
  STAMPDECL;
  STAMPNW(5, 1);
  cad -= 512;
  static_for<RD>([&](auto RR) { rd(ic<0>{}, RR, aX, bXl, bXh); });
  for (int u = 0; u < 28; u += 4) {
    G3_UNIT(aX, bXl, bXh, aY, bYl, bYh, 0, u, false);
    G3_UNIT(aY, bYl, bYh, aX, bXl, bXh, 1, u + 1, false);
    G3_UNIT(aX, bXl, bXh, aY, bYl, bYh, 2, u + 2, false);
    G3_UNIT(aY, bYl, bYh, aX, bXl, bXh, 3, u + 3, false);
  }
  G3_UNIT(aX, bXl, bXh, aY, bYl, bYh, 0, 28, true);
  G3_UNIT(aY, bYl, bYh, aX, bXl, bXh, 1, 29, true);
  G3_UNIT(aX, bXl, bXh, aY, bYl, bYh, 2, 30, true);
  G3_UNIT(aY, bYl, bYh, aX, bXl, bXh, 3, 31, true);
  asm volatile("s_nop 15" ::: "memory");
  SCHED0();
  STAMP(5, 2);
  STAMPFLUSH(5, 1);
  lane = fresh_lane(); fr = lane & 15; fq = lane >> 4;
  const f32x2* rstat = reinterpret_cast<const f32x2*>(smem + 131072);
  float* colc = reinterpret_cast<float*>(smem + 131072 + 2048);
  asm volatile("" : "+v"(accC[0]), "+v"(accC[1]));
  if (fr < 2 && wm < FN) {
#pragma unroll
    for (int m = 0; m < 2; ++m) *reinterpret_cast<f32x4*>(colc + (m * 2 + fr) * 128 + (wn * 4 + rot) * 16 + fq * 4) = accC[m];
  }
  asm volatile("s_waitcnt lgkmcnt(0)" ::: "memory");
  __builtin_amdgcn_s_barrier();
  asm volatile("" ::: "memory");
  static_for<4>([&](auto J) { wst(ic<3>{}, J); });
  f32x2 rs[4];
#pragma unroll
  for (int i = 0; i < 4; ++i) rs[i] = rstat[wm * 64 + i * 16 + fr];
#pragma unroll
  for (int j = 0; j < FN; ++j) {
    const int jg = (j + rot) % FN;
    const int cl = (wn * 4 + jg) * 16 + fq * 4;
    const f32x4 csg = *reinterpret_cast<const f32x4*>(colc + cl), bg = *reinterpret_cast<const f32x4*>(colc + 128 + cl);
    const f32x4 csu = *reinterpret_cast<const f32x4*>(colc + 256 + cl), bu = *reinterpret_cast<const f32x4*>(colc + 384 + cl);
    u32x2 pk[4];
#pragma unroll
    for (int i = 0; i < 4; ++i) {
#pragma unroll
      for (int hh = 0; hh < 2; ++hh) {
        const f32x2 ag = hh ? hi2(acc[i][j]) : lo2(acc[i][j]), au = hh ? hi2(acc[i][FN + j]) : lo2(acc[i][FN + j]);
        const f32x2 g = rs[i][0] * ag + (rs[i][1] * (hh ? hi2(csg) : lo2(csg)) + (hh ? hi2(bg) : lo2(bg)));
        const f32x2 uu = rs[i][0] * au + (rs[i][1] * (hh ? hi2(csu) : lo2(csu)) + (hh ? hi2(bu) : lo2(bu)));
        const f32x2 ea = g * -1.4426950408889634f;
        f32x2 e = {__builtin_amdgcn_exp2f(ea[0]), __builtin_amdgcn_exp2f(ea[1])};
        e = e + 1.0f;
        const f32x2 rc = {__builtin_amdgcn_rcpf(e[0]), __builtin_amdgcn_rcpf(e[1])};
        const f32x2 o = (g * uu) * rc;
        pk[i][hh] = pk_bf16(o[0], o[1]);
      }
    }
    const int Jv = (bn & 3) * 7 + wn * 4 + jg;
    const int J = g3_line(bn >> 2, Jv >> 2) * 4 + (Jv & 3);
    char* dst = (char*)act + ((size_t)((bm * 4 + wm) * (F_DIM / 32) + (J >> 1)) * 4 + (fq & 1)) * 1024 + (((J & 1) * 2 + (fq >> 1)) * 16 + fr) * 16;
#pragma unroll
    for (int i = 0; i < 4; i += 2) {
      const auto s0 = __builtin_amdgcn_permlane16_swap(pk[i][0], pk[i + 1][0], false, false);
      const auto s1 = __builtin_amdgcn_permlane16_swap(pk[i][1], pk[i + 1][1], false, false);
      gst128(dst + i * 1024, u32x4{s0[0], s1[0], s0[1], s1[1]});
    }
  }
}

#define G4PRI 2
#define XM 2
#define XB 1
#define G4AD 3
namespace g4 {
constexpr int NP = XB ? 4 : 8;
constexpr int NU = 14, SL = 4, AD = G4AD;
constexpr int CN = X_CN;
constexpr int GT = 131072 + 5120;
constexpr int na(int t) { return t + AD - 1 < NU ? 4 : 0; }
constexpr int nb(int mh, int t) { return t < 0 ? 0 : (t + SL < NU ? 4 : (t == NU - 3 && mh ? 4 : 0)); }
constexpr int behind(int mh, int t0, int u, int b) {
  int s = b ? na(u) : 0;
  for (int t = t0 + 1; t < u; ++t) s += na(t) + nb(mh, t);
  return s;
}
constexpr int vm(int mh, int u, int b) {
  const int wa = nb(mh, u - AD + 1) + behind(mh, u - AD + 1, u, b), wb = behind(mh, u + 1 - SL, u, b);
  return b == 0 ? wa + 2 : (u + 1 < NU && u + 1 >= SL && wb < wa ? wb : wa);
}
}

template <int MH, class WST, class PRE, class EXTRA>
DI void g4_loop(f32x4 (&acc)[2][8], unsigned smem_base, int lane, int wid, int kb0, rsrc_t rsA, WST ldb, PRE pre, EXTRA extra) {
  using namespace g4;
  const int fq = lane >> 4, k2 = wid & 1;
  unsigned voffA[2], bad[8];
#pragma unroll
  for (int i = 0; i < 2; ++i) voffA[i] = lane * 16 + ((wid & 3) ^ i) * 1024;
  {
    const int q = (lane >> 2) & 3, r = 8 * fq + q, sw = q | ((fq & 1) << 2);
#pragma unroll
    for (int f = 0; f < 8; ++f) bad[f] = smem_base + k2 * 65536 + r * 256 + ((f ^ sw) * 32) + 8 * (lane & 3);
  }
  const unsigned sA = (unsigned)(kb0 + 2 * k2) * 4096u;
  u32x4 af[AD][4];
  u32x2 bl[2][8], bh[2][8];
  auto lda = [&](auto V, auto J) {
    constexpr int v = V.value, j = J.value;
    asm volatile("" : "=v"(af[v % AD][j]));
    bload_sc1<0>(af[v % AD][j], voffA[j & 1], rsA, sA + (4 * v + (j >> 1)) * 4096u);
  };
  auto rdb = [&](auto V, auto B, auto E) {
    constexpr int o = (V.value & 3) * 16384 + B.value * 8192, f = E.value >> 1;
    if constexpr (E.value & 1) dsr_tr<o + 1024>(bh[B.value][f], bad[f]);
    else dsr_tr<o>(bl[B.value][f], bad[f]);
  };
  unsigned wp = smem_base + k2 * 65536 + 32768 + (wid >> 1) * 8192 + lane * 16;
#pragma unroll
  for (int f = 0; f < 8; ++f) asm volatile("" : "+v"(bad[f]));
  asm volatile("" : "+v"(voffA[0]), "+v"(voffA[1]), "+v"(wp) : "s"(rsA), "s"(sA));
  pre();
  static_for<4 * (AD - 1)>([&](auto J) { lda(ic<J.value / 4>{}, ic<J.value % 4>{}); });
  STAMPDECL;
  STAMPNW(6, 1);
  static_for<16>([&](auto E) { rdb(ic<0>{}, ic<0>{}, E); });
  static_for<NU>([&](auto U) {
    constexpr int u = U.value;
    constexpr bool last = u == NU - 1;
    static_for<32>([&](auto N) {
      constexpr int n = N.value, b = n >> 4, m = n & 15;
      constexpr int f = (last && b) ? (m & 7) : (m >> 1), i = (last && b) ? 1 - (m >> 3) : (m & 1);
      if constexpr (m == 0) {
        asm volatile("s_waitcnt vmcnt(%2) lgkmcnt(0)" : "+v"(af[u % AD][2 * b]), "+v"(af[u % AD][2 * b + 1])
                     : "n"(vm(MH, u, b)) : "memory");
#pragma unroll
        for (int g = 0; g < 8; ++g) asm volatile("" : "+v"(bl[b][g]), "+v"(bh[b][g]));
        if constexpr (b == 1 && !last) __builtin_amdgcn_s_barrier();
        if constexpr (b == 1 && u == NU - 2 && MH == 0) __builtin_amdgcn_s_setprio(G4PRI);
      }
      const u32x4 bb = {bl[b][f][0], bl[b][f][1], bh[b][f][0], bh[b][f][1]};
      if constexpr (u == 0 && b == 0) asm volatile("v_mfma_f32_16x16x32_bf16 %0, %1, %2, 0" : "=&v"(acc[i][f]) : "v"(bb), "v"(af[u % AD][i]));
      else MFMA_AV(acc[i][f], bb, af[u % AD][2 * b + i]);
      if constexpr (m < 8 && !(last && b)) {
        rdb(ic<u + b>{}, ic<1 - b>{}, ic<2 * m>{});
        rdb(ic<u + b>{}, ic<1 - b>{}, ic<2 * m + 1>{});
      }
      if constexpr ((m & 3) == 2) {
        if constexpr (b == 0 && na(u)) lda(ic<u + AD - 1>{}, ic<(m >> 2)>{});
        if constexpr (b == 1 && u + SL < NU) ldb(ic<u + SL>{}, ic<(m >> 2)>{});
        if constexpr (b == 1 && u + 3 == NU) extra(ic<(m >> 2)>{});
      }
      if constexpr (last && n >= 24) dsw128<(m & 7) * 1024>(wp, __builtin_bit_cast(u32x4, acc[1][m & 7]));
    });
  });
  dsw32<0>(smem_base + CN + wid * 4, 1u);
  STAMPFLUSH(6, 1);
  const unsigned rp = wp + 65536 - k2 * 131072;
  u32x4 pq[8];
  for (;;) {
    unsigned fl;
    asm volatile("ds_read_b32 %0, %1" : "=v"(fl) : "v"(smem_base + CN + (wid ^ 1) * 4) : "memory");
    static_for<8>([&](auto F) { dsr128<F.value * 1024>(pq[F.value], rp); });
    asm volatile("s_waitcnt lgkmcnt(0)" : "+v"(fl), "+v"(pq[0]), "+v"(pq[1]), "+v"(pq[2]), "+v"(pq[3]), "+v"(pq[4]), "+v"(pq[5]), "+v"(pq[6]), "+v"(pq[7])::"memory");
    if (__builtin_amdgcn_readfirstlane(fl) != 0u) break;
  }
#pragma unroll
  for (int f = 0; f < 8; ++f) {
    asm volatile("" : "+v"(acc[0][f]));
    acc[0][f] += __builtin_bit_cast(f32x4, pq[f]);
  }
}

#define STAMP2(s)
#define STAMP2F(k, a, b)
DI bool unset8(const u32x4 (&v)[8]) {
  unsigned m = 0;
#pragma unroll
  for (int f = 0; f < g4::NP; ++f) m = max(max(m, max(v[f][0], v[f][1])), max(v[f][2], v[f][3]));
  return __builtin_amdgcn_ballot_w64(m == ~0u) != 0ull;
}
template <int MH, class WST>
DI void g4_phase(unsigned smem_base, int wid, const bf16* At, WST wst, const bf16* Yt,
                 bf16* __restrict__ C, float* xbuf, unsigned* flags) {
  using namespace g4;
  constexpr int KB = F_DIM / 32;
  STAMP(6, 0);
  const int lane = fresh_lane();
  constexpr int mh = MH;
  const int kq = wid & 3;
  const int xcd = blockIdx.x & 7, idx = blockIdx.x >> 3;
  const int kh = xcd & 1, bm = ((xcd >> 1) & 1) * 8 + (idx & 7), bn = (xcd >> 2) * 4 + (idx >> 3);
  const unsigned partner = blockIdx.x ^ 1;
  const int n0 = bn * 128, bm64 = bm * 2 + kh;
  const int fr = lane & 15, fq = lane >> 4;
  const unsigned voffL = lane * 16;
  const rsrc_t rsA = mk_rsrc((const char*)At + (size_t)(bm64 ^ mh ^ 1) * KB * 4096, KB * 4096);
  const rsrc_t rsEx = mk_rsrc(xbuf, 256 * 32768);
  const int kb0 = kh * (KB / 2);
  f32x4 acc[2][8];
  auto pre = [&] { seam<5, 1, 1>(smem_base + CN + 512, lane, flags + ((idx & 7) * 2 + (xcd >> 2)) * 32, flags + ((bm >> 1) * 2 + kh) * 32, 16u); };
  if constexpr (mh == 0) {
    g4_loop<0>(acc, smem_base, lane, wid, kb0, rsA, wst, pre, [](auto) {});
    STAMP2(1);
    static_for<NP>([&](auto F) {
      constexpr int f = F.value;
      u32x4 t;
      if constexpr (XB) t = pack8(acc[0][2 * f], acc[0][2 * f + 1]);
      else t = __builtin_bit_cast(u32x4, acc[0][f]);
      if (XM != 2) {
#pragma unroll
        for (int e = 0; e < 4; ++e) t[e] = min(t[e], 0xfffffffeu);
      }
      bstore_sc1<(f & 3) * 1024>(t, voffL, rsEx, blockIdx.x * 32768u + kq * 8192u + (f >> 2) * 4096u);
    });
    if (XM == 2) {
      STAMP2(2);
      const rsrc_t rsFo = mk_rsrc(flags + 9216 + blockIdx.x * 32, 4);
      asm volatile("s_waitcnt vmcnt(0)" ::"s"(rsFo) : "memory");
      if (lds_add(smem_base + CN + 256 + lane * 4, 1u) == 3u) asm volatile("buffer_store_dword %0, %1, %2, 0 offen sc1" ::"v"(1u), "v"(lane * 4), "s"(rsFo) : "memory");
      STAMP2(3);
      STAMP2F(7, 1, 4);
      return;
    }
    STAMP2(2);
    STAMP2(3);
    STAMP2F(7, 1, 4);
    return;
  } else {
  u32x4 yv[4];
  const rsrc_t rsX = mk_rsrc(Yt, S_LEN * D_DIM * 2);
  const unsigned vx = (unsigned)(((bm64 >> 2) * 32 + bn * 4) * 16384 + ((bm64 & 3) * 4 + kq) * 1024 + ((((fq & 1) << 1) | (fq >> 1)) * 16 + fr) * 16);
  g4_loop<1>(acc, smem_base, lane, wid, kb0, rsA, wst, pre,
             [&](auto J) { asm volatile("" : "=v"(yv[J.value])); bload_sc1<0>(yv[J.value], vx, rsX, J.value * 16384); });
  STAMP2(2);
  u32x4 pv[8], gt[8], ct[8];
  auto fetch = [&] {
    static_for<NP>([&](auto F) {
      constexpr int f = F.value;
      bload_sc1<(f & 3) * 1024>(pv[f], voffL, rsEx, partner * 32768u + kq * 8192u + (f >> 2) * 4096u);
    });
  };
#pragma unroll
  for (int f = 0; f < 8; ++f) asm volatile("" : "=v"(pv[f]));
  if (XM == 0) fetch();
  static_for<8>([&](auto F) { dsr128<F.value * 64>(gt[F.value], smem_base + GT + fq * 16); dsr128<512 + F.value * 64>(ct[F.value], smem_base + GT + fq * 16); });
  asm volatile("s_waitcnt vmcnt(%4) lgkmcnt(0)" : "+v"(yv[0]), "+v"(yv[1]), "+v"(yv[2]), "+v"(yv[3]) : "n"(XM == 0 ? NP : 0) : "memory");
#pragma unroll
  for (int c = 0; c < 4; ++c) {
    const auto x0 = __builtin_amdgcn_permlane16_swap(yv[c][0], yv[c][2], false, false);
    const auto x1 = __builtin_amdgcn_permlane16_swap(yv[c][1], yv[c][3], false, false);
#pragma unroll
    for (int h = 0; h < 2; ++h) {
      const int f = 2 * c + h;
      asm volatile("" : "+v"(gt[f]), "+v"(ct[f]));
      acc[0][f] = f32x4{bf_lo(x0[h]), bf_hi(x0[h]), bf_lo(x1[h]), bf_hi(x1[h])} * __builtin_bit_cast(f32x4, ct[f]) + __builtin_bit_cast(f32x4, gt[f]) * acc[0][f];
      asm volatile("" : "+v"(acc[0][f]));
    }
  }
  char* cp = (char*)C + (size_t)(bm64 * 64 + kq * 16 + (fr & 7)) * (D_DIM * 2) + n0 * 2 + (((fq & 1) << 1) | (fq >> 1)) * 16 + (fr >> 3) * 64;
  asm volatile("" : "+v"(cp));
  STAMP2(5);
  unsigned spins = 0;
  auto landed = [&] { asm volatile("s_waitcnt vmcnt(0)" : "+v"(pv[0]), "+v"(pv[1]), "+v"(pv[2]), "+v"(pv[3]), "+v"(pv[4]), "+v"(pv[5]), "+v"(pv[6]), "+v"(pv[7])::"memory"); };
  if (XM >= 2) {
    const rsrc_t rsFi = XM == 2 ? mk_rsrc(flags + 9216 + partner * 32, 4) : mk_rsrc((char*)xbuf + partner * 32768u + kq * 8192u, 4);
    for (;;) {
      unsigned fl;
      asm volatile("buffer_load_dword %0, %1, %2, 0 offen sc1\n\ts_waitcnt vmcnt(0)" : "=v"(fl) : "v"(0u), "s"(rsFi) : "memory");
      if (__builtin_amdgcn_readfirstlane(fl) != (XM == 2 ? 0u : ~0u) || ++spins >= (1u << 20)) break;
      __builtin_amdgcn_s_sleep(1);
    }
    STAMP2(7);
  }
  if (XM != 0) fetch();
  landed();
  while (XM != 2 && unset8(pv) && ++spins < (1u << 20)) {
    fetch();
    landed();
  }
  STAMP2(6);
  static_for<2>([&](auto L) {
    constexpr int l = L.value;
    u32x4 ov[2];
#pragma unroll
    for (int c = 0; c < 2; ++c) {
      const int f = 4 * l + 2 * c;
      const u32x4 pb = pv[f >> 1];
      const f32x4 p0 = XB ? f32x4{bf_lo(pb[0]), bf_hi(pb[0]), bf_lo(pb[1]), bf_hi(pb[1])} : __builtin_bit_cast(f32x4, pv[f]);
      const f32x4 p1 = XB ? f32x4{bf_lo(pb[2]), bf_hi(pb[2]), bf_lo(pb[3]), bf_hi(pb[3])} : __builtin_bit_cast(f32x4, pv[f + 1]);
      const f32x4 o0 = acc[0][f] + __builtin_bit_cast(f32x4, gt[f]) * p0;
      const f32x4 o1 = acc[0][f + 1] + __builtin_bit_cast(f32x4, gt[f + 1]) * p1;
      const auto s0 = __builtin_amdgcn_permlane16_swap(pk_bf16(o0[0], o0[1]), pk_bf16(o1[0], o1[1]), false, false);
      const auto s1 = __builtin_amdgcn_permlane16_swap(pk_bf16(o0[2], o0[3]), pk_bf16(o1[2], o1[3]), false, false);
      ov[c] = u32x4{s0[0], s1[0], s0[1], s1[1]};
    }
    u32x4 lo, hi;
#pragma unroll
    for (int e = 0; e < 4; ++e) {
      lo[e] = __builtin_amdgcn_update_dpp(ov[0][e], ov[1][e], 0x128, 0xf, 0xc, false);
      hi[e] = __builtin_amdgcn_update_dpp(ov[1][e], ov[0][e], 0x128, 0xf, 0x3, false);
    }
    gst128(cp + l * 128, lo);
    gst128(cp + 8 * (D_DIM * 2) + l * 128, hi);
  });
  STAMP2(3);
  STAMP2(4);
  STAMP2F(6, 2, 7);
}
}

__global__ __launch_bounds__(512, 2) void block_kernel(const bf16* __restrict__ H, const bf16* __restrict__ Wq, bf16* O,
                                                       const bf16* __restrict__ Wo, const bf16* __restrict__ X, bf16* Y,
                                                       const bf16* __restrict__ Wg, const bf16* __restrict__ Wu, bf16* act,
                                                       float* stat, const float* __restrict__ modp, const unsigned* __restrict__ ctab,
                                                       const bf16* __restrict__ Wd, float* xbuf, bf16* __restrict__ C, unsigned* flags) {
  __shared__ __attribute__((aligned(1024))) char smem[Q_LDS];
  STAMP(2, 0);
  const int wid = __builtin_amdgcn_readfirstlane(threadIdx.x >> 6);
  const int wm = wid & 3, wn = wid >> 2;
  const int xcd = blockIdx.x & 7, idx = blockIdx.x >> 3;
  const int bm = idx & 7, bn = xcd * 4 + (idx >> 3);
  const unsigned smem_base = (unsigned)(uintptr_t)smem;
  const rsrc_t rsA = mk_rsrc((const char*)Y + (size_t)bm * 32 * 16384, 32 * 16384);
  const rsrc_t rsB = mk_rsrc(wn ? Wu : Wg, D_DIM * F_DIM * 2);
  const rsrc_t rsP = mk_rsrc((const char*)Wd + (size_t)blockIdx.x * 28672, 28672);
  const rsrc_t rsE = mk_rsrc((char*)xbuf + (size_t)blockIdx.x * 32768, 32768);
  const unsigned m0w = smem_base + wid * 2048;
  unsigned voffB0 = 0, voffB1 = 0;
  attn_phase(smem_base, wid, H, Wq, O, Wo, X, modp, Y, stat, flags + 1024, Wg, Wu, xbuf, ctab, [&](auto S, auto I) {
    if constexpr (S.value == 0) {
      const int l2 = fresh_lane();
      const int r = (wm * 2 + I.value) * 4 + (l2 >> 4);
      const int sw = (r & 3) | (((r >> 3) & 1) << 2);
      const int c16 = l2 & 15;
      const int vb = (((bn & 3) * 224) & ~63) + ((c16 >> 1) ^ sw) * 32 + (c16 & 1) * 16;
      (I.value ? voffB1 : voffB0) = (unsigned)(r * (F_DIM * 2) + g3_line(bn >> 2, vb >> 7) * 128 + (vb & 127));
    }
    if (GSNT) bload_lds_nt<0>(m0w + S.value * 16384 + I.value * 1024, I.value ? voffB1 : voffB0, rsB, S.value * 229376u);
    else bload_lds<0>(m0w + S.value * 16384 + I.value * 1024, I.value ? voffB1 : voffB0, rsB, S.value * 229376u);
  });
  __builtin_amdgcn_s_setprio(0);
  const int lane = fresh_lane();
  const unsigned voffA = wid * 2048 + lane * 16;
  seam<3, 8, 1>(smem_base + X_CN + 768, lane, flags + 17408 + xcd * 32, flags + 17408 + xcd * 256 + bm * 32, 32u);
  STAMP(5, 0);
  G3_STAGE(0, 0, 0); G3_STAGE(0, 1, 0); G3_STAGE(1, 0, 1); G3_STAGE(1, 1, 1); G3_STAGE(2, 0, 2); G3_STAGE(2, 1, 2);
#pragma unroll
  for (int i = 0; i < 2; ++i) bload_lds<0>(smem_base + 131072 + 4096, voffA, rsP, 0x40000000u);
  const int n0 = ((xcd >> 2) * 4 + (idx >> 3)) * 128;
  unsigned vo = 0;
  auto wst = [&](auto V, auto J) {
    constexpr int v = V.value, j = J.value;
    if constexpr (v == 0 && j == 0) {
      const int l2 = fresh_lane();
      vo = (l2 >> 4) * 2048 + ((((l2 & 15) >> 1) ^ ((l2 >> 4) | ((wid & 1) << 2))) * 32) + (l2 & 1) * 16;
    }
    bload_lds<0>(m0w + (j >> 1) * 65536 + (v & 3) * 16384 + (j & 1) * 1024, vo, mk_rsrc((const char*)Wd + n0 * 2, F_DIM * D_DIM * 2 - n0 * 2),
                 (unsigned)(((xcd & 1) * (F_DIM / 64) + wn) * 65536 + wm * 16384) + (4 * v + 2 * (j >> 1)) * 65536u + (j & 1) * 8192u);
  };
  if (wn == 0) {
    g3_body<4>(smem, smem_base, wm, wn, wid, lane, bm, bn, rsA, rsB, rsP, rsE, voffA, voffB0, voffB1, act, wst, [](auto) {});
    STAMP(5, 3);
    g4_phase<0>(smem_base, wid, act, wst, Y, C, xbuf, flags);
  } else {
    u32x2 tv[16];
    unsigned gv[4];
    g3_body<3>(smem, smem_base, wm, wn, wid, lane, bm, bn, rsA, rsB, rsP, rsE, voffA, voffB0, voffB1, act, wst, [&](auto K) {
      const int tid = wm * 64 + fresh_lane();
      if constexpr (K.value == 0) {
        const rsrc_t rsG = mk_rsrc(modp + (wm >= 2 ? 4096 : 5120), 4 * 6144 * 4);
#pragma unroll
        for (int r = 0; r < 4; ++r) { asm volatile("" : "=v"(gv[r])); bload1_v<0>(gv[r], (unsigned)((n0 + (tid & 127)) * 4), rsG, r * 24576); }
        const rsrc_t rsS = mk_rsrc(stat, S_LEN * 128);
#pragma unroll
        for (int i = 0; i < 16; ++i)
          asm volatile("buffer_load_dwordx2 %0, %1, %2, %3 offen sc1" : "=v"(tv[i]) : "v"((bm * 256 + tid) * 8), "s"(rsS), "s"(i * 16384) : "memory");
      } else {
#pragma unroll
        for (int i = 0; i < 16; ++i) asm volatile("" : "+v"(tv[i]));
        float s = 0;
#pragma unroll
        for (int i = 0; i < 16; i += 2) s += __uint_as_float(tv[i][0]) + __uint_as_float(tv[i + 1][0]);
        const float mean = s * (1.0f / D_DIM);
        float m2 = 0;
#pragma unroll
        for (int i = 0; i < 16; i += 2) {
          const float d0 = __uint_as_float(tv[i][0]) * (1.0f / 64.0f) - mean, d1 = __uint_as_float(tv[i + 1][0]) * (1.0f / 64.0f) - mean;
          m2 += __uint_as_float(tv[i][1]) + __uint_as_float(tv[i + 1][1]) + 64.0f * (d0 * d0 + d1 * d1);
        }
        const float rstd = rsqrtf(m2 * (1.0f / D_DIM) + 1e-5f);
        reinterpret_cast<f32x2*>(smem + 131072)[tid & 255] = f32x2{rstd, -mean * rstd};
        asm volatile("" : "+v"(gv[0]), "+v"(gv[1]), "+v"(gv[2]), "+v"(gv[3]));
        float sum = (__uint_as_float(gv[0]) + __uint_as_float(gv[1])) + (__uint_as_float(gv[2]) + __uint_as_float(gv[3]));
        sum = (wm >= 2) ? 1.0f / gain_nz(sum) : rbf(sum);
        dsw32<0>(smem_base + g4::GT + (tid & 255) * 4, __float_as_uint(sum));
      }
    });
    g4_phase<1>(smem_base, wid, act, wst, Y, C, xbuf, flags);
  }
}

extern "C" void kernel_launch(void* const* d_in, const int* in_sizes, int n_in, void* d_out, int out_size,
                              void* d_ws, size_t ws_size, hipStream_t stream) {
  const bf16* x = (const bf16*)d_in[0];
  const bf16* wqkv = (const bf16*)d_in[1];
  const bf16* wo = (const bf16*)d_in[2];
  const bf16* wg = (const bf16*)d_in[4];
  const bf16* wu = (const bf16*)d_in[5];
  const bf16* wd = (const bf16*)d_in[6];
  const bf16* c = (const bf16*)d_in[7];
  const bf16* wmod = (const bf16*)d_in[8];
  bf16* out = (bf16*)d_out;

  char* ws = (char*)d_ws;
  float* modp = (float*)ws;
  float* rstat1 = (float*)(ws + 114688);
  unsigned* ctab = (unsigned*)(ws + 131072);
  float* stat = (float*)(ws + 524288);
  bf16* ot = (bf16*)(ws + (1 << 20));
  bf16* yt = (bf16*)(ws + (9 << 20));
#define XAL 0
  bf16* ht = (bf16*)(ws + ((XAL ? 5 : 13) << 20));
  bf16* act = (bf16*)(ws + (17 << 20));
  float* xbuf = (float*)(ws + ((XAL ? 1 : 32) << 20));
  unsigned* flags = (unsigned*)(ws + 262144);

  pre_kernel<<<320, 512, 0, stream>>>(x, c, wmod, modp, rstat1, wqkv);
  ln_apply_kernel<<<256, 256, 0, stream>>>(x, modp, rstat1, ht, wqkv, flags, wo, ctab);
  block_kernel<<<256, 512, 0, stream>>>(ht, wqkv, ot, wo, x, yt, wg, wu, act, stat, modp, ctab, wd, xbuf, out, flags);
}
```

```cpp
#include <hip/hip_runtime.h>
#include <hip/hip_bf16.h>
#include <stdint.h>

typedef __hip_bfloat16 bf16;
typedef __attribute__((ext_vector_type(8))) short bf16x8;
typedef __attribute__((ext_vector_type(4))) float f32x4;
typedef __attribute__((ext_vector_type(2))) float f32x2;
typedef __attribute__((ext_vector_type(4))) unsigned u32x4;
typedef __attribute__((ext_vector_type(2))) unsigned u32x2;
typedef __attribute__((ext_vector_type(2))) __bf16 bf16x2_t;
typedef __amdgpu_buffer_rsrc_t rsrc_t;

#define DI __device__ __forceinline__
#define QBAR 2
#define QRN 17
#define TQ 12
#define TG 4
#define K1MAP 1
#define QTM 1
#define WOT 1
#define G4S 0
#define SMOD "sc1"
#define SEAMS 2
#define SBAR 1
#define TWAIT 0
#define WPRE 6
#define QN 2
#define QK0 0
#define PSLP 4
#define PFIRST 1
#define CODEPF 62

#define STAMP(k, s) do { } while (0)
#define STAMPT(k, s, T) do { } while (0)
#define STAMPNW(k, s)
#define STAMPFLUSH(k, s)
#define STAMPDECL

#define STAMP2(s)
#define STAMP2F(k, a, b)

constexpr int S_LEN = 2048;
constexpr int D_DIM = 1024;
constexpr int F_DIM = 3584;

template <int V> struct ic { static constexpr int value = V; };
template <int N, int I = 0, class F>
DI void static_for(F&& f) {
  if constexpr (I < N) { f(ic<I>{}); static_for<N, I + 1>(f); }
}

DI float bf_lo(unsigned u) { return __uint_as_float(u << 16); }
DI float bf_hi(unsigned u) { return __uint_as_float(u & 0xffff0000u); }
DI unsigned pk_bf16(float lo, float hi) {
  const f32x2 v = {lo, hi};
  return __builtin_bit_cast(unsigned, __builtin_convertvector(v, bf16x2_t));
}
DI rsrc_t mk_rsrc(const void* p, unsigned bytes) { return __builtin_amdgcn_make_buffer_rsrc((void*)p, (short)0, (int)bytes, 0x00020000); }

template <int N>
DI void wait_vm() { asm volatile("s_waitcnt vmcnt(%0)" ::"n"(N > 63 ? 63 : N) : "memory"); }
#define WAIT_LGKM0() do { asm volatile("s_waitcnt lgkmcnt(0)" ::: "memory"); __builtin_amdgcn_sched_barrier(0); } while (0)
#define SCHED0() __builtin_amdgcn_sched_barrier(0)

#define MFMA_A(C, B, A) asm volatile("v_mfma_f32_16x16x32_bf16 %0, %1, %2, %0" : "+a"(C) : "v"(B), "a"(A))
#define MFMA_AV(C, B, A) asm volatile("v_mfma_f32_16x16x32_bf16 %0, %1, %2, %0" : "+v"(C) : "v"(B), "v"(A))
#define PERM_LO(D, X, Y) asm volatile("v_perm_b32 %0, %1, %2, %3" : "=v"(D) : "v"(Y), "v"(X), "s"(0x05040100u))
#define PERM_HI(D, X, Y) asm volatile("v_perm_b32 %0, %1, %2, %3" : "=v"(D) : "v"(Y), "v"(X), "s"(0x07060302u))
template <int OFF>
DI void bload_v(u32x4& d, unsigned voff, rsrc_t rs, unsigned soff) {
  asm volatile("buffer_load_dwordx4 %0, %1, %2, %3 offen offset:%4" : "+v"(d) : "v"(voff), "s"(rs), "s"(soff), "i"(OFF) : "memory");
}
template <int OFF>
DI void bload_a(u32x4& d, unsigned voff, rsrc_t rs, unsigned soff) {
  asm volatile("buffer_load_dwordx4 %0, %1, %2, %3 offen offset:%4" : "+a"(d) : "v"(voff), "s"(rs), "s"(soff), "i"(OFF) : "memory");
}
template <int OFF>
DI void bload2_v(u32x2& d, unsigned voff, rsrc_t rs, unsigned soff) {
  asm volatile("buffer_load_dwordx2 %0, %1, %2, %3 offen offset:%4" : "+v"(d) : "v"(voff), "s"(rs), "s"(soff), "i"(OFF) : "memory");
}
template <int OFF>
DI void bload1_v(unsigned& d, unsigned voff, rsrc_t rs, unsigned soff) {
  asm volatile("buffer_load_dword %0, %1, %2, %3 offen offset:%4" : "+v"(d) : "v"(voff), "s"(rs), "s"(soff), "i"(OFF) : "memory");
}
template <int OFF>
DI void bload_lds(unsigned m0v, unsigned voff, rsrc_t rs, unsigned soff) {
  asm volatile("s_mov_b32 m0, %0\n\ts_nop 0\n\tbuffer_load_dwordx4 %1, %2, %3 offen offset:%4 lds" ::"s"(m0v), "v"(voff), "s"(rs), "s"(soff), "i"(OFF) : "memory");
}
DI void touch_lds(unsigned m0v, unsigned voff, rsrc_t rs, unsigned soff) {
  asm volatile("s_mov_b32 m0, %0\n\ts_nop 0\n\tbuffer_load_dword %1, %2, %3 offen lds" ::"s"(m0v), "v"(voff), "s"(rs), "s"(soff) : "memory");
}
DI void touch_nt(unsigned m0v, unsigned voff, rsrc_t rs, unsigned soff) {
  asm volatile("s_mov_b32 m0, %0\n\ts_nop 0\n\tbuffer_load_dword %1, %2, %3 offen nt lds\n\tbuffer_load_dword %1, %2, %3 offen offset:64 sc1 lds" ::"s"(m0v), "v"(voff), "s"(rs), "s"(soff) : "memory");
}
template <int OFF>
DI void bload_lds_nt(unsigned m0v, unsigned voff, rsrc_t rs, unsigned soff) {
  asm volatile("s_mov_b32 m0, %0\n\ts_nop 0\n\tbuffer_load_dwordx4 %1, %2, %3 offen offset:%4 nt lds" ::"s"(m0v), "v"(voff), "s"(rs), "s"(soff), "i"(OFF) : "memory");
}
template <int OFF>
DI void bload_lds_sc1(unsigned m0v, unsigned voff, rsrc_t rs, unsigned soff) {
  asm volatile("s_mov_b32 m0, %0\n\ts_nop 0\n\tbuffer_load_dwordx4 %1, %2, %3 offen offset:%4 sc1 lds" ::"s"(m0v), "v"(voff), "s"(rs), "s"(soff), "i"(OFF) : "memory");
}
template <int OFF>
DI void dsr128(u32x4& d, unsigned addr) { asm volatile("ds_read_b128 %0, %1 offset:%2" : "=v"(d) : "v"(addr), "i"(OFF) : "memory"); }
template <int OFF>
DI void dsr64(u32x2& d, unsigned addr) { asm volatile("ds_read_b64 %0, %1 offset:%2" : "=v"(d) : "v"(addr), "i"(OFF) : "memory"); }
template <int OFF>
DI void dsr_tr(u32x2& d, unsigned addr) { asm volatile("ds_read_b64_tr_b16 %0, %1 offset:%2" : "=v"(d) : "v"(addr), "i"(OFF) : "memory"); }
template <int OFF>
DI void dsw128(unsigned addr, u32x4 d) { asm volatile("ds_write_b128 %0, %1 offset:%2" ::"v"(addr), "v"(d), "i"(OFF) : "memory"); }
template <int OFF>
DI void dsw128a(unsigned addr, const f32x4& d) { asm volatile("ds_write_b128 %0, %1 offset:%2" ::"v"(addr), "a"(d), "i"(OFF) : "memory"); }
template <int OFF>
DI void dsw32(unsigned addr, unsigned d) { asm volatile("ds_write_b32 %0, %1 offset:%2" ::"v"(addr), "v"(d), "i"(OFF) : "memory"); }

DI void gst128(void* p, u32x4 v) {
  asm volatile("global_store_dwordx4 %0, %1, off sc1\n\ts_nop 1" ::"v"(p), "v"(v) : "memory");
}
DI int fresh_lane() {
  int l;
  asm volatile("v_mbcnt_lo_u32_b32 %0, -1, 0\n\tv_mbcnt_hi_u32_b32 %0, -1, %0" : "=v"(l));
  return l;
}
DI f32x2 lo2(f32x4 v) { return __builtin_shufflevector(v, v, 0, 1); }
DI f32x2 hi2(f32x4 v) { return __builtin_shufflevector(v, v, 2, 3); }
DI float rowsum4(float v) {
  const unsigned u = __float_as_uint(v);
  const auto a = __builtin_amdgcn_permlane16_swap(u, u, false, false);
  const unsigned t = __float_as_uint(__uint_as_float(a[0]) + __uint_as_float(a[1]));
  const auto b = __builtin_amdgcn_permlane32_swap(t, t, false, false);
  return __uint_as_float(b[0]) + __uint_as_float(b[1]);
}
DI void split4(const f32x4 v, u32x2& hi, u32x2& lo) {
  hi[0] = pk_bf16(v[0], v[1]); hi[1] = pk_bf16(v[2], v[3]);
  lo[0] = pk_bf16(v[0] - bf_lo(hi[0]), v[1] - bf_hi(hi[0]));
  lo[1] = pk_bf16(v[2] - bf_lo(hi[1]), v[3] - bf_hi(hi[1]));
}
DI float silu_f(float v) { return v / (1.0f + __expf(-v)); }
DI float rbf(float v) { return bf_lo(pk_bf16(v, v)); }
DI float rowmax4(float v) {
  const unsigned u = __float_as_uint(v);
  const auto a = __builtin_amdgcn_permlane16_swap(u, u, false, false);
  const unsigned t = __float_as_uint(fmaxf(__uint_as_float(a[0]), __uint_as_float(a[1])));
  const auto b = __builtin_amdgcn_permlane32_swap(t, t, false, false);
  return fmaxf(__uint_as_float(b[0]), __uint_as_float(b[1]));
}
DI float gain_nz(float m) {
  const float g = rbf(1.0f + rbf(m));
  return __builtin_fabsf(g) < 8.67361738e-19f ? 8.67361738e-19f : g;
}
template <int OFF>
DI void bload_sc1(u32x4& d, unsigned voff, rsrc_t rs, unsigned soff) {
  asm volatile("buffer_load_dwordx4 %0, %1, %2, %3 offen offset:%4 sc1" : "+v"(d) : "v"(voff), "s"(rs), "s"(soff), "i"(OFF) : "memory");
}
template <int OFF>
DI void bstore_sc1(u32x4 d, unsigned voff, rsrc_t rs, unsigned soff) {
  asm volatile("buffer_store_dwordx4 %0, %1, %2, %3 offen offset:%4 sc1\n\ts_nop 1" ::"v"(d), "v"(voff), "s"(rs), "s"(soff), "i"(OFF) : "memory");
}
DI void bstore_mark(u32x4 d, unsigned voff, rsrc_t rs, unsigned soff) {
  asm volatile("buffer_store_dwordx4 %0, %1, %2, %3 offen " SMOD "\n\ts_nop 1" ::"v"(d), "v"(voff), "s"(rs), "s"(soff) : "memory");
}
DI void bload1_sc1(unsigned& d, unsigned voff, rsrc_t rs) {
  asm volatile("buffer_load_dword %0, %1, %2, 0 offen sc1" : "+v"(d) : "v"(voff), "s"(rs) : "memory");
}

__global__ __launch_bounds__(512) void pre_kernel(const bf16* __restrict__ x, const bf16* __restrict__ c,
                                                  const bf16* __restrict__ wmod, float* __restrict__ modp,
                                                  float* __restrict__ rstat1, unsigned* __restrict__ flags, const bf16* __restrict__ wq) {
  __shared__ __attribute__((aligned(16))) float red[8 * 2 * 128 + 256];
  STAMP(0, 0);
  const int tid = threadIdx.x, lane = tid & 63, w = tid >> 6;
  if (blockIdx.x < 192) {
    const int fr = lane & 15, fq = lane >> 4;
    const int cg = blockIdx.x >> 2, kq = blockIdx.x & 3;
    const int col0 = cg * 128;
    const int k0 = kq * 256 + w * 32 + fq * 8;
    u32x4 raw[8];
#pragma unroll
    for (int j = 0; j < 8; ++j) {
      const u32x4* p_ = reinterpret_cast<const u32x4*>(wmod + (size_t)(k0 + j) * 6144 + col0 + fr * 8);
      raw[j] = __builtin_nontemporal_load(p_);
    }
    const u32x4 cv = *reinterpret_cast<const u32x4*>(c + k0);
    u32x4 cf;
#pragma unroll
    for (int e = 0; e < 4; ++e) {
      const float a = silu_f(bf_lo(cv[e])), b = silu_f(bf_hi(cv[e]));
      cf[e] = (fr == 0) ? pk_bf16(a, b) : 0u;
    }
    f32x4 acc[8];
#pragma unroll
    for (int p = 0; p < 4; ++p) {
      u32x4 lo, hi;
#pragma unroll
      for (int t = 0; t < 4; ++t) {
        lo[t] = __builtin_amdgcn_perm(raw[2 * t + 1][p], raw[2 * t][p], 0x05040100u);
        hi[t] = __builtin_amdgcn_perm(raw[2 * t + 1][p], raw[2 * t][p], 0x07060302u);
      }
      acc[2 * p] = __builtin_amdgcn_mfma_f32_16x16x32_bf16(__builtin_bit_cast(bf16x8, lo), __builtin_bit_cast(bf16x8, cf), f32x4{0, 0, 0, 0}, 0, 0, 0);
      acc[2 * p + 1] = __builtin_amdgcn_mfma_f32_16x16x32_bf16(__builtin_bit_cast(bf16x8, hi), __builtin_bit_cast(bf16x8, cf), f32x4{0, 0, 0, 0}, 0, 0, 0);
    }
    if (fr == 0) {
#pragma unroll
      for (int r = 0; r < 4; ++r) {
        float* d = red + w * 128 + fq * 32 + r * 8;
        *reinterpret_cast<f32x4*>(d) = f32x4{acc[0][r], acc[1][r], acc[2][r], acc[3][r]};
        *reinterpret_cast<f32x4*>(d + 4) = f32x4{acc[4][r], acc[5][r], acc[6][r], acc[7][r]};
      }
    }
    __syncthreads();
    if (tid < 128) {
      float s = 0;
#pragma unroll
      for (int g = 0; g < 8; ++g) s += red[g * 128 + tid];
      modp[kq * 6144 + col0 + tid] = s;
    }
    STAMP(0, 1);
    return;
  }
  const int row0 = (blockIdx.x & 7) * 256 + ((blockIdx.x - 192) >> 3) * 16 + w * 2;
  if (tid < 64) reinterpret_cast<u32x4*>(flags)[(blockIdx.x - 192) * 64 + tid] = u32x4{0u, 0u, 0u, 0u};
  u32x4 a[2][2];
#pragma unroll
  for (int i = 0; i < 2; ++i) {
    a[i][0] = *reinterpret_cast<const u32x4*>(x + (size_t)(row0 + i) * D_DIM + lane * 8);
    a[i][1] = *reinterpret_cast<const u32x4*>(x + (size_t)(row0 + i) * D_DIM + 512 + lane * 8);
  }
  if ((w >> 1) < QK0) {
    const int vi = ((blockIdx.x - 192) >> 3) * 2 + (w & 1);
    const unsigned vq = lane >= 48 ? 0x40000000u : (unsigned)(((w >> 1) * 128 + vi * 4 + lane / 12) * 6144 + ((lane % 12) >> 2) * 2048 + (blockIdx.x & 3) * 512 + (lane & 3) * 128);
    touch_lds((unsigned)(uintptr_t)(red + 2048), vq, mk_rsrc(wq, D_DIM * 3 * D_DIM * 2), 0);
  }
  if (blockIdx.x < 192 + 8) {
    unsigned long long pc;
    asm volatile("s_getpc_b64 %0" : "=s"(pc));
    const rsrc_t rsC = mk_rsrc((const void*)(uintptr_t)(pc & ~255ull), CODEPF * 1024);
#pragma unroll
    for (int i = 0; i < (CODEPF + 7) / 8; ++i) bload_lds<0>((unsigned)(uintptr_t)(red + 2048), tid * 16, rsC, i * 8192);
  }
#pragma unroll
  for (int i = 0; i < 2; ++i) {
    float v[16];
#pragma unroll
    for (int j = 0; j < 8; ++j) { v[2 * j] = bf_lo(a[i][j >> 2][j & 3]); v[2 * j + 1] = bf_hi(a[i][j >> 2][j & 3]); }
    float s = 0;
#pragma unroll
    for (int j = 0; j < 16; ++j) s += v[j];
#pragma unroll
    for (int o = 32; o > 0; o >>= 1) s += __shfl_xor(s, o);
    const float mean = s * (1.0f / D_DIM);
    float q = 0;
#pragma unroll
    for (int j = 0; j < 16; ++j) { const float d = v[j] - mean; q += d * d; }
#pragma unroll
    for (int o = 32; o > 0; o >>= 1) q += __shfl_xor(q, o);
    const float rstd = rsqrtf(q * (1.0f / D_DIM) + 1e-5f);
    if (lane == 0) *reinterpret_cast<f32x2*>(rstat1 + (row0 + i) * 2) = f32x2{rstd, -mean * rstd};
  }
  STAMP(0, 2);
}

DI void ln_phase(unsigned tab, int lane, int w, int rgp, int cgp, const bf16* __restrict__ x, const float* __restrict__ modp,
                 const float* __restrict__ rstat1, bf16* H, unsigned* ctab) {
  const int fr = lane & 15, fq = lane >> 4;
  const int kb = cgp * 4 + w, mb0 = rgp * 4;
  u32x4 mr, m2, xv[4];
  u32x2 rr[4];
  asm volatile("" : "=v"(mr), "=v"(m2));
  {
    const unsigned vm = (unsigned)((fq * 6144 + ((lane >> 3) & 1) * 1024 + kb * 32 + (lane & 7) * 4) * 4);
    bload_v<0>(mr, vm, mk_rsrc(modp, 4 * 6144 * 4), 0);
    const rsrc_t rsX = mk_rsrc(x, S_LEN * D_DIM * 2), rsS = mk_rsrc(rstat1, S_LEN * 8);
    const unsigned vx = (unsigned)(((mb0 * 16 + fr) * D_DIM + kb * 32 + fq * 8) * 2), vs = (unsigned)((mb0 * 16 + fr) * 8);
#pragma unroll
    for (int i = 0; i < 4; ++i) {
      asm volatile("" : "=v"(xv[i]), "=v"(rr[i]));
      bload_v<0>(xv[i], vx, rsX, i * 32768);
      bload2_v<0>(rr[i], vs, rsS, i * 128);
    }
    bload_v<0>(m2, vm, mk_rsrc(modp, 4 * 6144 * 4), (rgp & 1) ? 0x40000000u : 12288u);
  }
  STAMP2(0);
  __builtin_amdgcn_s_barrier();
  asm volatile("s_waitcnt vmcnt(9)" : "+v"(mr)::"memory");
  STAMP2(2);
  f32x4 mv = __builtin_bit_cast(f32x4, mr);
#pragma unroll
  for (int e = 0; e < 4; ++e) {
    mv[e] = rbf(rowsum4(mv[e]));
    if (lane & 8) mv[e] = rbf(1.0f + mv[e]);
  }
  dsw128<0>(tab + w * 256 + fr * 16, __builtin_bit_cast(u32x4, mv));
  u32x4 tb[4];
  dsr128<0>(tb[0], tab + w * 256 + fq * 32); dsr128<16>(tb[1], tab + w * 256 + fq * 32);
  dsr128<128>(tb[2], tab + w * 256 + fq * 32); dsr128<144>(tb[3], tab + w * 256 + fq * 32);
  asm volatile("s_waitcnt vmcnt(0) lgkmcnt(0)" : "+v"(m2), "+v"(xv[0]), "+v"(xv[1]), "+v"(xv[2]), "+v"(xv[3]), "+v"(rr[0]), "+v"(rr[1]), "+v"(rr[2]), "+v"(rr[3]),
               "+v"(tb[0]), "+v"(tb[1]), "+v"(tb[2]), "+v"(tb[3])::"memory");
  STAMP2(3);
#pragma unroll
  for (int i = 0; i < 4; ++i) {
    const f32x2 rs = __builtin_bit_cast(f32x2, rr[i]);
    u32x4 ho;
#pragma unroll
    for (int d = 0; d < 4; ++d) {
      const f32x4 gg = __builtin_bit_cast(f32x4, tb[2 + (d >> 1)]), ss = __builtin_bit_cast(f32x4, tb[d >> 1]);
      const float a = (bf_lo(xv[i][d]) * rs[0] + rs[1]) * gg[(d & 1) * 2] + ss[(d & 1) * 2];
      const float b = (bf_hi(xv[i][d]) * rs[0] + rs[1]) * gg[(d & 1) * 2 + 1] + ss[(d & 1) * 2 + 1];
      ho[d] = pk_bf16(a, b);
    }
    gst128((char*)H + ((size_t)(mb0 + i) * 32 + kb) * 1024 + lane * 16, ho);
  }
  if (!(rgp & 1)) {
    f32x4 cv = __builtin_bit_cast(f32x4, m2);
#pragma unroll
    for (int e = 0; e < 4; ++e) {
      cv[e] = rowsum4(cv[e]);
      if (lane & 8) cv[e] = gain_nz(cv[e]);
    }
    asm volatile("buffer_store_dwordx2 %0, %1, %2, 0 offen sc1" ::"v"(u32x2{pk_bf16(cv[0], cv[1]), pk_bf16(cv[2], cv[3])}), "v"((lane ^ 8) * 8),
                 "s"(mk_rsrc(ctab + (rgp >> 1) * 1024 + kb * 32, 128)) : "memory");
  }
  STAMP2(1);
  if (SBAR) __builtin_amdgcn_s_barrier();
  STAMP2F(1, 0, 4);
}

#define GMAP 0
#define T1 0
#define T2 0
#define GNT 0
#define TW 1
#define TP 8
#define TS 4
#define TN 2
#define SM0 4
#define SMN 2
#define GS0 28
#define GSNT 0
#define QT0 2
#define QS0 0
#define QP 3
#define WOS 15
#define GROT 1
#define GDEP 12
constexpr int Q_SLOT = 21504, Q_NSL = 7;
constexpr int QX_WH = 4 * Q_SLOT;
constexpr int QX_HX = 65536;
constexpr int QX_GT = QX_WH + 65536;
constexpr int QX_CNT = QX_GT + 512;
constexpr int QX_SINK = QX_CNT + 512;
constexpr int G3_CT = QX_SINK + 1024;
constexpr int X_CN = G3_CT + 4096;
constexpr int X_TAB = X_CN + 1280;
constexpr int Q_LDS = X_TAB + 1024;

DI unsigned lds_add(unsigned addr, unsigned v0) {
  unsigned o;
  asm volatile("ds_add_rtn_u32 %0, %1, %2\n\ts_waitcnt lgkmcnt(0)" : "=v"(o) : "v"(addr), "v"(v0) : "memory");
  return __builtin_amdgcn_readfirstlane(o);
}
template <int SLP>
DI void lds_wait(unsigned addr, unsigned n) {
  for (;;) {
    unsigned v;
    asm volatile("ds_read_b32 %0, %1\n\ts_waitcnt lgkmcnt(0)" : "=v"(v) : "v"(addr) : "memory");
    if (__builtin_amdgcn_readfirstlane(v) >= n) break;
    __builtin_amdgcn_s_sleep(SLP);
  }
}
struct poll_t { unsigned a, b; };
struct sig_t { rsrc_t rs; unsigned vo; };
DI sig_t drain(unsigned* out, int lane, int no, int so) {
  sig_t g = {mk_rsrc(out, (no - 1) * so + 4), (unsigned)(lane * so)};
  asm volatile("s_waitcnt vmcnt(0)" : "+v"(g.vo) : "s"(g.rs) : "memory");
  return g;
}
DI void signal(const sig_t& g) { asm volatile("buffer_atomic_add %0, %1, %2, 0 offen sc1" ::"v"(1u), "v"(g.vo), "s"(g.rs) : "memory"); }
#define POLL_RD(r) "v_mov_b32 " r ", 0\n\tbuffer_atomic_add " r ", %3, %4, 0 offen sc0 sc1\n\t"
#define POLL_CK(r) "s_waitcnt vmcnt(1)\n\tv_cmp_lt_u32 vcc, " r ", %5\n\ts_cbranch_vccz 2f\n\t"
DI poll_t poll(unsigned skip, unsigned* in, int lane, int nl, unsigned n) {
  poll_t r;
  asm volatile("s_cmp_lg_u32 %2, 0\n\ts_cbranch_scc1 2f\n\t" POLL_RD("%0") "s_sleep %6\n1:\n\t" POLL_RD("%1") POLL_CK("%0") POLL_RD("%0") POLL_CK("%1")
               "s_add_u32 %2, %2, 1\n\ts_cmp_lt_u32 %2, 0x80000\n\ts_cbranch_scc1 1b\n2:"
               : "=&v"(r.a), "=&v"(r.b), "+s"(skip) : "v"(lane * 128), "s"(mk_rsrc(in, nl * 128)), "v"(lane < nl ? n : 0u), "n"(PSLP) : "memory", "vcc", "scc");
  return r;
}
DI void poll_end(const poll_t& r) { asm volatile("" ::"v"(r.a), "v"(r.b)); }
template <int K, int NW, int NO, int SO>
DI poll_t seam(unsigned lcnt, int lane, unsigned* out, unsigned* in, int nl, unsigned n) {
  asm volatile("" ::"s"(mk_rsrc(in, nl * 128)), "v"(lane * 128), "v"(lane < nl ? n : 0u), "v"(lcnt + lane * 4));
  const sig_t g = drain(out, lane, NO, SO);
  STAMP(K, 4);
  const unsigned o = lds_add(lcnt + lane * 4, 1u);
  if (o == NW - 1) {
    signal(g);
  }
  const poll_t r = poll(PFIRST ? o : NW - 1 - o, in, lane, nl, n);
  __builtin_amdgcn_s_barrier();
  return r;
}
constexpr int FL_H = 0, FL_O = 128, FL_P = 256, FL_Y = 512, FL_A = 768;
DI void wo_piece(unsigned smem_base, unsigned p, unsigned rot, unsigned voff, rsrc_t rs) {
  const unsigned pb = p >> 2, kb = (pb & 24u) | ((pb + rot) & 7u);
  bload_lds<0>(smem_base + (p < 64u ? QX_WH + p * 1024u : (p - 64u) * 1024u), voff, rs, kb * 65536u + (p & 3u) * 16384u);
}

DI void gu_touch(unsigned smem_base, int e, unsigned vgu, const bf16* wg, const bf16* wu, bool nt) {
  if (nt) touch_nt(smem_base + QX_SINK, vgu, mk_rsrc((e & 1) ? wu : wg, D_DIM * F_DIM * 2), (unsigned)(e >> 1) * 256u * (F_DIM * 2));
  else touch_lds(smem_base + QX_SINK, vgu, mk_rsrc((e & 1) ? wu : wg, D_DIM * F_DIM * 2), (unsigned)(e >> 1) * 256u * (F_DIM * 2));
}
DI void qkv_loader(unsigned smem_base, int lane, int sw, int m0, int n0, const bf16* H, const bf16* __restrict__ W,
                   unsigned rot, unsigned voffO, rsrc_t rsO) {
  const rsrc_t rsH = mk_rsrc(H, S_LEN * D_DIM * 2);
  const rsrc_t rsW = mk_rsrc(W, D_DIM * 3 * D_DIM * 2);
  int fb0 = (m0 >> 4) - 1 + 3 * sw; fb0 = fb0 < 0 ? 0 : fb0;
  const unsigned voffH0 = (unsigned)(fb0 * 32768 + lane * 16);
  const unsigned voffH1 = (unsigned)(((m0 >> 4) + 3 * sw) * 32768 + lane * 16);
  unsigned voffW[2];
#pragma unroll
  for (int i = 0; i < 2; ++i) {
    const int r = i * 8 + (lane >> 3), ch = lane & 7;
    const int g = ((r >> 1) & 1) | (((r >> 3) & 1) << 1);
    voffW[i] = (unsigned)(r * 6144 + sw * 2048 + (n0 + (((ch >> 1) ^ g) * 16) + (ch & 1) * 8) * 2);
  }
  const unsigned m0a = smem_base + sw * 3072, m0w = smem_base + 9216 + sw * 4096;
  auto ldh = [&](unsigned so, unsigned u) {
    bload_lds_sc1<0>(m0a + so, voffH0, rsH, u * 1024u);
    bload_lds_sc1<0>(m0a + so + 1024, voffH1, rsH, u * 1024u);
    bload_lds_sc1<0>(m0a + so + 2048, voffH1, rsH, u * 1024u + 32768u);
  };
  auto ldw = [&](unsigned so, unsigned u) {
#pragma unroll
    for (int i = 0; i < 4; ++i) bload_lds<0>(m0w + so + i * 1024, voffW[i & 1], rsW, u * 196608u + (i >> 1) * 98304u);
  };
  if (SBAR) __builtin_amdgcn_s_barrier();
  wait_vm<TWAIT>();
#pragma unroll
  for (int u = 0; u < WPRE; ++u) ldw(u * Q_SLOT, u);
  __builtin_amdgcn_s_barrier();
#pragma unroll
  for (int u = 0; u < Q_NSL - 1; ++u) { ldh(u * Q_SLOT, u); if (u >= WPRE) ldw(u * Q_SLOT, u); }
  wait_vm<(Q_NSL - 2) * 3 + 4 * (Q_NSL - 1 - WPRE)>();
  __builtin_amdgcn_s_barrier();
  static_for<Q_NSL - 3>([&](auto U) {
    constexpr int u = U.value, so = ((u + Q_NSL - 1) % Q_NSL) * Q_SLOT;
    ldh(so, u + Q_NSL - 1); ldw(so, u + Q_NSL - 1);
    wait_vm<3 * (Q_NSL - 3 - u) + 4 * (Q_NSL - 1 - (WPRE > u + 2 ? WPRE : u + 2)) + 7 * (u + 1)>();
    __builtin_amdgcn_s_barrier();
  });
  unsigned sb = (Q_NSL - 4) * Q_SLOT;
  for (int u = Q_NSL - 3; u < 32 - (Q_NSL - 1); ++u) {
    ldh(sb, u + Q_NSL - 1); ldw(sb, u + Q_NSL - 1);
    sb = (sb == (Q_NSL - 1) * Q_SLOT) ? 0u : sb + Q_SLOT;
    wait_vm<(Q_NSL - 2) * 7>();
    __builtin_amdgcn_s_barrier();
  }
  for (int t = 0; t < 6; ++t) {
    const unsigned p0 = (unsigned)(t * 21 + (t >= 3 ? 1 : 0) + sw * 7);
#pragma unroll
    for (int i = 0; i < 7; ++i) wo_piece(smem_base, p0 + i, rot, voffO, rsO);
    if (t < 5) wait_vm<(Q_NSL - 2) * 7>();
    __builtin_amdgcn_s_barrier();
  }
}

DI f32x4 mfma0(u32x4 a, u32x4 b) {
  return __builtin_amdgcn_mfma_f32_16x16x32_bf16(__builtin_bit_cast(bf16x8, a), __builtin_bit_cast(bf16x8, b), f32x4{0, 0, 0, 0}, 0, 0, 0);
}
DI u32x4 pack8(const f32x4 a, const f32x4 b) { return u32x4{pk_bf16(a[0], a[1]), pk_bf16(a[2], a[3]), pk_bf16(b[0], b[1]), pk_bf16(b[2], b[3])}; }
template <class OP>
DI float rows4(const float (&v)[4], OP op) {
  const auto a = __builtin_amdgcn_permlane16_swap(__float_as_uint(v[0]), __float_as_uint(v[1]), false, false);
  const auto b = __builtin_amdgcn_permlane16_swap(__float_as_uint(v[2]), __float_as_uint(v[3]), false, false);
  const float t0 = op(__uint_as_float(a[0]), __uint_as_float(a[1])), t1 = op(__uint_as_float(b[0]), __uint_as_float(b[1]));
  const auto c = __builtin_amdgcn_permlane32_swap(__float_as_uint(t0), __float_as_uint(t1), false, false);
  return op(__uint_as_float(c[0]), __uint_as_float(c[1]));
}
DI void bcast4(float t, float (&v)[4]) {
  const auto c = __builtin_amdgcn_permlane32_swap(__float_as_uint(t), __float_as_uint(t), false, false);
  const auto a = __builtin_amdgcn_permlane16_swap(c[0], c[0], false, false);
  const auto b = __builtin_amdgcn_permlane16_swap(c[1], c[1], false, false);
  v[0] = __uint_as_float(a[0]); v[1] = __uint_as_float(a[1]); v[2] = __uint_as_float(b[0]); v[3] = __uint_as_float(b[1]);
}
DI float fmax2(float a, float b) { return __builtin_amdgcn_fmed3f(a, b, __builtin_inff()); }

template <int WM>
DI void qkv_mfma(unsigned smem_base, int lane, int wn, int bm, int bn, bf16* __restrict__ O) {
  const int fr = lane & 15, fq = lane >> 4;
  unsigned bb0, bb1;
  const unsigned ab = smem_base + lane * 16;
  {
    const int q = (lane >> 2) & 3, p = lane & 3;
    const int r = 8 * fq + q;
    const int g = ((r >> 1) & 1) | (((r >> 3) & 1) << 1);
    bb0 = smem_base + 9216 + r * 128 + (((wn * 2) ^ g) * 32) + 8 * p;
    bb1 = bb0 ^ 32u;
  }
  const bool par = fr & 1, c0 = 4 * fq + (fr & 1) <= fr, c1 = 4 * fq + 2 + (fr & 1) <= fr;
  const unsigned s0 = par ? 0x01000c0cu : 0x0c0c0100u, s1 = par ? 0x03020c0cu : 0x0c0c0302u;
  u32x4 sel = {c0 ? s0 : 0x0c0c0c0cu, c1 ? s1 : 0x0c0c0c0cu, c0 ? 0x0c0c0c0cu : s0, c1 ? 0x0c0c0c0cu : s1};
  unsigned hx = ab + QX_HX + wn * 3072;
  asm volatile("" : "+v"(sel), "+v"(hx));
  const rsrc_t rsO = mk_rsrc((char*)O + (size_t)((bm * 2 + WM) * 32 + bn * 2 + wn) * 4096, 4096);
  f32x4 acc[4][6], acch[2];
#pragma unroll
  for (int i = 0; i < 4; ++i)
#pragma unroll
    for (int j = 0; j < 6; ++j) { acc[i][j] = f32x4{0, 0, 0, 0}; asm volatile("" : "+v"(acc[i][j])); }
#pragma unroll
  for (int j = 0; j < 2; ++j) { acch[j] = f32x4{0, 0, 0, 0}; asm volatile("" : "+v"(acch[j])); }
  __builtin_amdgcn_s_setprio(2);
  __builtin_amdgcn_s_barrier();
  STAMPDECL;
  STAMPNW(2, 1);
  u32x4 aX[5], aY[5];
  u32x2 bXl[6], bXh[6], bYl[6], bYh[6];
  unsigned ra = ab, rb0 = bb0, rb1 = bb1, so = 0;
#define Q_READ(AR, BL, BH, R)                                                                                    \
  do {                                                                                                           \
    if ((R) < 12) {                                                                                              \
      constexpr int jb = ((R) < 12 ? (R) : 0) >> 1;                                                              \
      if ((R) & 1) dsr_tr<(jb >> 1) * 4096 + 512>(BH[jb], (jb & 1) ? rb1 : rb0);                                 \
      else dsr_tr<(jb >> 1) * 4096>(BL[jb], (jb & 1) ? rb1 : rb0);                                               \
    } else if ((R) < 16) {                                                                                       \
      constexpr int ia = ((R) >= 12 && (R) < 16) ? (R) - 12 : 0;                                                 \
      dsr128<1024 + WM * 4096 + ia * 1024>(AR[ia], ra);                                                          \
    } else {                                                                                                     \
      dsr128<0>(AR[4], ra);                                                                                      \
    }                                                                                                            \
  } while (0)
#define Q_MFMA(AR, BB, N)                                                                                        \
  do {                                                                                                           \
    if ((N) < 16) MFMA_AV(acc[(N) & 3][((N) < 24 ? (N) : 0) >> 2], BB[((N) < 24 ? (N) : 0) >> 2], AR[(N) & 3]);  \
    else if ((N) < 24) MFMA_AV(acc[(N) & 3][((N) < 24 ? (N) : 0) >> 2], AR[(N) & 3], BB[((N) < 24 ? (N) : 0) >> 2]); \
    else if (WM == 0) MFMA_AV(acch[(N) & 1], BB[2 + ((N) & 1)], AR[4]);                                          \
    else MFMA_AV(acch[(N) & 1], AR[4], BB[4 + ((N) & 1)]);                                                       \
  } while (0)
#define Q_ADV(K)                                                                                                 \
  do {                                                                                                           \
    if ((K) == 0) asm volatile("s_add_u32 %0, %0, %1\n\ts_cmp_eq_u32 %0, %2\n\ts_cselect_b32 %0, 0, %0" : "+s"(so) : "n"(Q_SLOT), "n"(Q_NSL * Q_SLOT) : "scc"); \
    else if ((K) == 1) asm volatile("v_add_u32 %0, %1, %2" : "=v"(rb0) : "s"(so), "v"(bb0));                     \
    else if ((K) == 2) asm volatile("v_add_u32 %0, %1, %2" : "=v"(rb1) : "s"(so), "v"(bb1));                     \
    else asm volatile("v_add_u32 %0, %1, %2" : "=v"(ra) : "s"(so), "v"(ab));                                     \
  } while (0)
#define Q_UNIT(AR, BL, BH, NAR, NBL, NBH)                                                                        \
  do {                                                                                                           \
    WAIT_LGKM0();                                                                                                \
    u32x4 bb[6];                                                                                                 \
    _Pragma("unroll") for (int j = 0; j < 6; ++j) bb[j] = u32x4{BL[j][0], BL[j][1], BH[j][0], BH[j][1]};         \
    SCHED0();                                                                                                    \
    static_for<26>([&](auto N) {                                                                                 \
      constexpr int n = N.value;                                                                                 \
      if constexpr (n == QBAR) __builtin_amdgcn_s_barrier();                                                     \
      Q_MFMA(AR, bb, n);                                                                                         \
      if constexpr (n >= QBAR && n < QBAR + 17) Q_READ(NAR, NBL, NBH, n - QBAR);                                 \
      if constexpr (n >= QBAR + 18 && n < QBAR + 22) Q_ADV(n - QBAR - 18);                                       \
    });                                                                                                          \
    SCHED0();                                                                                                    \
  } while (0)
  static_for<17>([&](auto R) { Q_READ(aX, bXl, bXh, R.value); });
  static_for<4>([&](auto K) { Q_ADV(K.value); });
  for (int u = 0; u < 32; u += 2) {
    Q_UNIT(aX, bXl, bXh, aY, bYl, bYh);
    Q_UNIT(aY, bYl, bYh, aX, bXl, bXh);
  }
  asm volatile("s_waitcnt lgkmcnt(0)\n\ts_nop 7" ::: "memory");
#pragma unroll
  for (int i = 0; i < 5; ++i) asm volatile("" : "+v"(aX[i]));
#pragma unroll
  for (int j = 0; j < 6; ++j) asm volatile("" : "+v"(bXl[j]), "+v"(bXh[j]));
#pragma unroll
  for (int i = 0; i < 4; ++i)
#pragma unroll
    for (int j = 0; j < 6; ++j) asm volatile("" : "+v"(acc[i][j]));
  asm volatile("" : "+v"(acch[0]), "+v"(acch[1]));
  SCHED0();
  STAMP(2, 2); STAMPFLUSH(2, 1);
  u32x4 kk[5], vv[5];
  if (WM == 0) {
    kk[4] = pack8(acc[3][2], acc[3][3]); vv[4] = pack8(acc[3][4], acc[3][5]);
    dsw128<0>(hx, kk[4]); dsw128<1024>(hx, vv[4]);
  } else {
    dsw128<2048>(hx, pack8(acch[0], acch[1]));
  }
  asm volatile("s_waitcnt lgkmcnt(0)" ::: "memory");
  __builtin_amdgcn_s_barrier();
  if (WM == 0) dsr128<2048>(vv[0], hx);
  else { dsr128<0>(kk[0], hx); dsr128<1024>(vv[0], hx); }
  SCHED0();
  if (WM == 0) kk[0] = pack8(acch[0], acch[1]);
#pragma unroll
  for (int i = 0; i < (WM == 0 ? 3 : 4); ++i) kk[i + 1] = pack8(acc[i][2], acc[i][3]);
  const float cs = 0.17677669529663687f * 1.4426950408889634f;
  float l0[4], l1[4], tm[4];
#pragma unroll
  for (int ii = 1; ii < 5; ++ii) {
    const int i = ii & 3;
    const u32x4 q = pack8(acc[i][0], acc[i][1]);
    if (WM == 1 && i == 0) asm volatile("s_waitcnt lgkmcnt(0)" : "+v"(kk[0]), "+v"(vv[0])::"memory");
    const f32x4 sc = mfma0(kk[i + 1], q), sp = mfma0(kk[i], q);
    float p0 = par ? sp[1] : sp[0], p1 = par ? sp[3] : sp[2];
    if (WM == 0 && i == 0 && bm == 0) { p0 = -5.656854249e9f; p1 = -5.656854249e9f; }
    l0[i] = c0 ? (par ? sc[1] : sc[0]) : p0;
    l1[i] = c1 ? (par ? sc[3] : sc[2]) : p1;
    tm[i] = fmax2(l0[i], l1[i]);
  }
#pragma unroll
  for (int i = 0; i < (WM == 0 ? 3 : 4); ++i) vv[i + 1] = pack8(acc[i][4], acc[i][5]);
  bcast4(rows4(tm, [](float a, float b) { return fmax2(a, b); }), tm);
#pragma unroll
  for (int i = 0; i < 4; ++i) {
    l0[i] = __builtin_amdgcn_exp2f((l0[i] - tm[i]) * cs);
    l1[i] = __builtin_amdgcn_exp2f((l1[i] - tm[i]) * cs);
    tm[i] = l0[i] + l1[i];
  }
  bcast4(__builtin_amdgcn_rcpf(rows4(tm, [](float a, float b) { return a + b; })), tm);
  if (WM == 0) asm volatile("s_waitcnt lgkmcnt(0)" : "+v"(vv[0])::"memory");
#pragma unroll
  for (int i = 0; i < 4; ++i) {
    const unsigned pp = pk_bf16(l0[i] * tm[i], l1[i] * tm[i]);
    const u32x4 pr = {__builtin_amdgcn_perm(pp, pp, sel[0]), __builtin_amdgcn_perm(pp, pp, sel[1]),
                      __builtin_amdgcn_perm(pp, pp, sel[2]), __builtin_amdgcn_perm(pp, pp, sel[3])};
    const u32x4 ov = pack8(mfma0(u32x4{vv[i + 1][0], vv[i + 1][1], vv[i][0], vv[i][1]}, pr), mfma0(u32x4{vv[i + 1][2], vv[i + 1][3], vv[i][2], vv[i][3]}, pr));
    asm volatile("buffer_store_dwordx4 %0, %1, %2, 0 offen offset:%3 sc1\n\ts_nop 1" ::"v"(ov), "v"(lane * 16), "s"(rsO), "i"(i * 1024) : "memory");
  }
  STAMP(2, 3);
}

template <class GST>
DI void attn_phase(unsigned smem_base, int wid, bf16* H, const bf16* __restrict__ W, bf16* O,
                   const bf16* __restrict__ wo, const bf16* __restrict__ X, const float* __restrict__ modp, bf16* Y, float* stat,
                   unsigned* flags, const bf16* __restrict__ wg, const bf16* __restrict__ wu, float* xbuf,
                   unsigned* ctab, const float* __restrict__ rstat1, GST gst) {
  const int tid = threadIdx.x, lane = tid & 63;
  const int xcd = blockIdx.x & 7, idx = blockIdx.x >> 3;
  const int bm = (xcd >> 2) * 8 + (idx & 7), bn = (xcd & 3) * 4 + (idx >> 3);
  const int rb = xcd * 2 + (idx & 1), cg = idx >> 1;
  const unsigned rot = __builtin_amdgcn_readfirstlane(GMAP ? (cg & 4) : (cg >> 2) * 2);
  const unsigned vgu = (unsigned)((idx * 8 + (lane >> 3)) * (F_DIM * 2) + xcd * 896 + ((lane & 7) == 7 ? 6 : (lane & 7)) * 128);
  const unsigned cnt = smem_base + QX_CNT, cntl = cnt + lane * 4;
  const unsigned vq = lane >= 48 ? 0x40000000u : (unsigned)((idx * 4 + lane / 12) * 6144 + ((lane % 12) >> 2) * 2048 + (xcd & 3) * 512 + (lane & 3) * 128);
  auto q_touch = [&](unsigned j) { touch_lds(smem_base + QX_SINK, vq, mk_rsrc(W, D_DIM * 3 * D_DIM * 2), j * 786432u); };
  if (wid < 4) {
    const int rgp = xcd * 4 + (idx & 3);
    ln_phase(smem_base + X_TAB, lane, wid, rgp, idx >> 2, X, modp, rstat1, H, ctab);
    const poll_t pl = seam<1, 4, 4, 4096>(smem_base + X_CN + 1024, lane, flags + (FL_H + rgp) * 32,
                                          flags + (FL_H + (xcd & 3) * 32 + (bm ? 2 * bm - 1 : 0)) * 32, bm ? 3 : 2, 8u);
    asm volatile("s_waitcnt vmcnt(0)" ::: "memory");
    poll_end(pl);
    if (wid & 1) qkv_mfma<1>(smem_base, lane, wid >> 1, bm, bn, O);
    else qkv_mfma<0>(smem_base, lane, wid >> 1, bm, bn, O);
  } else {
    const rsrc_t rsO = mk_rsrc(wo, D_DIM * D_DIM * 2);
    const unsigned voffO = (unsigned)((lane >> 3) * 2048 + cg * 128 + ((((lane & 7) >> 1) ^ (lane >> 4)) * 32) + (lane & 1) * 16);
    if (wid == 7) {
      dsw32<0>(cntl, 0u); dsw32<256>(cntl, 0u);
      dsw128<0>(smem_base + X_CN + lane * 16, u32x4{0u, 0u, 0u, 0u}); dsw32<1024>(smem_base + X_CN + lane * 4, 0u);
      asm volatile("s_waitcnt lgkmcnt(0)" ::: "memory");
    }
    __builtin_amdgcn_s_barrier();
#pragma unroll
    for (int j = 0; j < 2; ++j)
      if (QK0 + j * 4 + wid - 4 < QT0) q_touch(QK0 + j * 4 + wid - 4);
    if (wid < 7) qkv_loader(smem_base, lane, wid - 4, bm * 128, bn * 64, H, W, rot, voffO, rsO);
    else {
      unsigned gv[8];
      const rsrc_t rsG = mk_rsrc(modp, 4 * 6144 * 4);
#pragma unroll
      for (int i = 0; i < 8; ++i) { asm volatile("" : "=v"(gv[i])); bload1_v<0>(gv[i], (unsigned)((cg * 64 + lane) * 4), rsG, (i >> 1) * 24576 + 8192 + (i & 1) * 8192); }
      if (WOS < 0) touch_lds(smem_base + QX_SINK, blockIdx.x * 8192 + lane * 128, rsO, 0);
      for (int i = 0; i < SBAR; ++i) __builtin_amdgcn_s_barrier();
      asm volatile("s_waitcnt vmcnt(0)" : "+v"(gv[0]), "+v"(gv[1]), "+v"(gv[2]), "+v"(gv[3]), "+v"(gv[4]), "+v"(gv[5]), "+v"(gv[6]), "+v"(gv[7])::"memory");
      const float ga = (__uint_as_float(gv[0]) + __uint_as_float(gv[2])) + (__uint_as_float(gv[4]) + __uint_as_float(gv[6]));
      const float cb = (__uint_as_float(gv[1]) + __uint_as_float(gv[3])) + (__uint_as_float(gv[5]) + __uint_as_float(gv[7]));
      dsw32<0>(smem_base + QX_GT + lane * 4, __float_as_uint(rbf(ga)));
      dsw32<256>(smem_base + QX_GT + lane * 4, __float_as_uint(gain_nz(cb)));
      asm volatile("s_waitcnt lgkmcnt(0)" ::: "memory");
      __builtin_amdgcn_s_barrier();
      __builtin_amdgcn_s_barrier();
      {
        int nxt = TS + idx % TP, ev = 0;
        const rsrc_t rsE = mk_rsrc((char*)xbuf + (size_t)blockIdx.x * 32768, 32768);
        int qj = QT0;
        for (int i = 0; i < 27; ++i) {
          if (G4S == 2 && i >= SM0 && i < SM0 + 32 / SMN) {
#pragma unroll
            for (int j = 0; j < SMN; ++j) bstore_mark(u32x4{~0u, ~0u, ~0u, ~0u}, lane * 16, rsE, (unsigned)((i - SM0) * SMN + j) * 1024u);
          }
          if (QT0 < 8 && i >= QS0 && (i - QS0) % QP == 0) {
#pragma unroll
            for (int j = 0; j < QN; ++j) { if (qj < 8) q_touch(qj); ++qj; }
          }
          if (i == WOS) touch_lds(smem_base + QX_SINK, blockIdx.x * 8192 + lane * 128, rsO, 0);
          if (TW > 0 && i == nxt && ev < TN) {
#pragma unroll
            for (int j = 0; j < TW; ++j) gu_touch(smem_base, ev * TW + j, vgu, wg, wu, GNT & 1);
            nxt += TP; ++ev;
          }
          __builtin_amdgcn_s_barrier();
        }
      }
      static_for<4>([&](auto I) { bload_lds_sc1<I.value * 1024>(smem_base + G3_CT, lane * 16, mk_rsrc(ctab + bm * 1024, 4096), 0); });
      wo_piece(smem_base, 63u, rot, voffO, rsO);
      for (int i = 28; i < 33; ++i) __builtin_amdgcn_s_barrier();
      wo_piece(smem_base, 127u, rot, voffO, rsO);
    }
    __builtin_amdgcn_s_barrier();
    if (wid - 4 < T1) { gu_touch(smem_base, T2 + wid - 4, vgu, wg, wu, GNT & 2); wait_vm<(GNT & 2) ? 2 : 1>(); }
    else wait_vm<0>();
    lds_add(cntl, 1u);
  }
  const int fr = lane & 15, fq = lane >> 4;
  const int kq = wid & 3, rg = rb * 2 + (wid >> 2);
  const int ko = GROT ? (kq + cg) & 3 : kq;
  lds_wait<2>(cnt, 4);
  u32x2 wl[8][4], wh[8][4];
  {
    const int q = (lane >> 2) & 3, p = lane & 3, r = 4 * fq + q;
    const unsigned wb = smem_base + (kq < 2 ? QX_WH + kq * 32768 : (kq - 2) * 32768) + r * 128 + (((r >> 1) & 3) * 32) + 8 * p;
    unsigned wa[4];
#pragma unroll
    for (int f = 0; f < 4; ++f) wa[f] = wb ^ (f * 32);
    static_for<32>([&](auto E) {
      constexpr int s = E.value >> 2, f = E.value & 3;
      dsr_tr<s * 4096>(wl[s][f], wa[f]);
      dsr_tr<s * 4096 + 2048>(wh[s][f], wa[f]);
    });
  }
  const rsrc_t rsA = mk_rsrc((const char*)O + (size_t)rg * 131072, 131072);
  unsigned voffA[4], hs[8];
#pragma unroll
  for (int i = 0; i < 4; ++i) { voffA[i] = lane * 16 + ((ko + 1 + i) & 3) * 1024; asm volatile("" : "+v"(voffA[i])); }
#pragma unroll
  for (int i = 0; i < 8; ++i) { hs[i] = __builtin_amdgcn_readfirstlane((kq * 8 + ((i + rot) & 7)) * 4096); asm volatile("" : "+s"(hs[i])); }
  asm volatile("" ::"s"(rsA));
  if (wid < 4) {
    const sig_t g = drain(flags + (FL_O + bm * 8 + (bn & 1)) * 32, lane, 4, 256);
    if (lds_add(cntl + 256, 1u) == 3u) signal(g);
    STAMP(2, 4);
  }
  const poll_t pl = poll(wid - 7, flags + (FL_O + rb * 8 + (cg & 3) * 2) * 32, lane, 2, 8u);
  asm volatile("s_waitcnt lgkmcnt(0)" ::: "memory");
  __builtin_amdgcn_s_barrier();
  u32x4 wf[8][4];
#pragma unroll
  for (int s = 0; s < 8; ++s)
#pragma unroll
    for (int f = 0; f < 4; ++f) {
      asm volatile("" : "+v"(wl[s][f]), "+v"(wh[s][f]));
      wf[s][f] = u32x4{wl[s][f][0], wl[s][f][1], wh[s][f][0], wh[s][f][1]};
    }
  STAMP(3, 0);
  const unsigned voffL = lane * 16;
  u32x4 af[32], xv[2], pr[3][4], gt[4], ct[4];
  f32x4 acc[4][4];
  auto aload = [&](auto N) {
    constexpr int n = N.value;
    asm volatile("" : "=v"(af[n]));
    bload_sc1<0>(af[n], voffA[n >> 3], rsA, hs[n & 7]);
  };
  static_for<GDEP>([&](auto N) { aload(N); });
  const unsigned sl = smem_base + 49152 + voffL + (wid >> 2) * 49152;
  const int sw = ((fq & 1) << 1) | (fq >> 1);
  SCHED0();
  static_for<32>([&](auto N) {
    constexpr int n = N.value, ps = n >> 3, s = n & 7, XG = 32 - GDEP;
    constexpr int last = n - 1 + GDEP < 31 ? n - 1 + GDEP : 31;
    asm volatile("s_waitcnt vmcnt(%1)" : "+v"(af[n]) : "n"(last - n + (n > XG ? 2 : 0) + (n > GS0 + 3 ? 6 : (n > GS0 ? 2 * (n - GS0) : 0))) : "memory");
    if constexpr (n == 0) poll_end(pl);
    if constexpr (n == 28) { WAIT_LGKM0(); __builtin_amdgcn_s_barrier(); }
    static_for<4>([&](auto F) {
      constexpr int f = F.value;
      if constexpr (s == 0) asm volatile("v_mfma_f32_16x16x32_bf16 %0, %1, %2, 0" : "=&v"(acc[ps][f]) : "v"(wf[s][f]), "v"(af[n]));
      else MFMA_AV(acc[ps][f], wf[s][f], af[n]);
      if constexpr (f == 0 && n + GDEP < 32) aload(ic<(n + GDEP < 32 ? n + GDEP : 0)>{});
      if constexpr (f == 0 && n == XG) {
        const rsrc_t rsX = mk_rsrc(X, S_LEN * D_DIM * 2);
        const unsigned vx = (unsigned)(((rg * 64 + ko * 16 + fr) * D_DIM + cg * 64 + sw * 8) * 2);
        asm volatile("" : "=v"(xv[0]), "=v"(xv[1]));
        bload_v<0>(xv[0], vx, rsX, 0); bload_v<64>(xv[1], vx, rsX, 0);
      }
      if constexpr ((f & 1) && n >= GS0 && n < GS0 + 3) gst(ic<(n >= GS0 ? n - GS0 : 0)>{}, ic<(f >> 1)>{});
      if constexpr (ps > 0 && (s == 2 || s == 3) && (f & 1)) {
        constexpr int e = (s - 2) * 2 + (f >> 1);
        dsw128<(ps - 1) * 4096 + e * 1024>(sl + ((kq + ps) & 3) * 12288, __builtin_bit_cast(u32x4, acc[ps - 1][e]));
      }
      if constexpr (n >= 28 && n < 31) dsr128<(n - 28) * 4096 + f * 1024>(pr[n - 28][f], sl + kq * 12288);
      if constexpr (n == 31) { dsr128<f * 64>(gt[f], smem_base + QX_GT + fq * 16); dsr128<256 + f * 64>(ct[f], smem_base + QX_GT + fq * 16); }
    });
  });
  asm volatile("s_waitcnt vmcnt(6)" : "+v"(xv[0]), "+v"(xv[1])::"memory");
  asm volatile("s_waitcnt lgkmcnt(0)\n\ts_nop 4" ::: "memory");
  STAMP(3, 2);
  f32x4 fin[4], yv[4];
#pragma unroll
  for (int f = 0; f < 4; ++f) asm volatile("" : "+v"(acc[3][f]), "+v"(pr[0][f]), "+v"(pr[1][f]), "+v"(pr[2][f]), "+v"(gt[f]), "+v"(ct[f]));
#pragma unroll
  for (int c = 0; c < 2; ++c) {
    const auto x0 = __builtin_amdgcn_permlane16_swap(xv[c][0], xv[c][2], false, false);
    const auto x1 = __builtin_amdgcn_permlane16_swap(xv[c][1], xv[c][3], false, false);
#pragma unroll
    for (int h = 0; h < 2; ++h) {
      const int f = 2 * c + h;
      const f32x4 a = (acc[3][f] + __builtin_bit_cast(f32x4, pr[0][f])) + (__builtin_bit_cast(f32x4, pr[1][f]) + __builtin_bit_cast(f32x4, pr[2][f]));
      const f32x4 xf = {bf_lo(x0[h]), bf_hi(x0[h]), bf_lo(x1[h]), bf_hi(x1[h])};
      fin[f] = xf + __builtin_bit_cast(f32x4, gt[f]) * a;
      yv[f] = fin[f] * __builtin_bit_cast(f32x4, ct[f]);
    }
    const auto y0 = __builtin_amdgcn_permlane16_swap(pk_bf16(yv[2 * c][0], yv[2 * c][1]), pk_bf16(yv[2 * c + 1][0], yv[2 * c + 1][1]), false, false);
    const auto y1 = __builtin_amdgcn_permlane16_swap(pk_bf16(yv[2 * c][2], yv[2 * c][3]), pk_bf16(yv[2 * c + 1][2], yv[2 * c + 1][3]), false, false);
    gst128((char*)Y + ((size_t)(rg >> 2) * 32 + cg * 2 + c) * 16384 + ((rg & 3) * 4 + ko) * 1024 + (sw * 16 + fr) * 16,
           u32x4{y0[0], y1[0], y0[1], y1[1]});
  }
  const f32x4 s4 = (fin[0] + fin[1]) + (fin[2] + fin[3]);
  const float s = rowsum4((s4[0] + s4[1]) + (s4[2] + s4[3]));
  const float mb = s * (1.0f / 64.0f);
  f32x4 q4 = {0, 0, 0, 0};
#pragma unroll
  for (int f = 0; f < 4; ++f) { const f32x4 d = fin[f] - mb; q4 += d * d; }
  const float m2 = rowsum4((q4[0] + q4[1]) + (q4[2] + q4[3]));
  asm volatile("buffer_store_dwordx2 %0, %1, %2, 0 offen sc1" ::"v"(f32x2{s, m2}), "v"(lane * 8),
               "s"(mk_rsrc(stat + ((size_t)cg * S_LEN + rg * 64 + ko * 16) * 2, 128)) : "memory");
  STAMP(3, 3);
}

#define G3NT 1
#define TPOS 6
#define SPOS 9
#define NBC 1
#define G3_STAGE(SLOT, P, U)                                                                               \
  do {                                                                                                     \
    if ((P) == 0) bload_lds_sc1<0>(m0w + 65536 + (SLOT) * 16384, voffA, rsA, (unsigned)(U) * 16384u);      \
    else if ((P) == 1) bload_lds_sc1<1024>(m0w + 65536 + (SLOT) * 16384, voffA, rsA, (unsigned)(U) * 16384u);  \
    else if ((P) == 2) bload_lds_nt<0>(m0w + (SLOT) * 16384, voffB0, rsB, (unsigned)(U) * 229376u);        \
    else bload_lds_nt<0>(m0w + (SLOT) * 16384 + 1024, voffB1, rsB, (unsigned)(U) * 229376u);               \
  } while (0)

#define G3ROT 1
DI int g3_line(int gx, int p) {
  const int t = p + (G3ROT ? (3 + gx) & 7 : 0);
  return 7 * gx + (t >= 7 ? t - 7 : t);
}
template <int FN, class WST, class HOOK>
DI void g3_body(char* smem, unsigned smem_base, int wm, int wn, int wid, int lane, int bm, int bn, rsrc_t rsA, rsrc_t rsB,
                rsrc_t rsP, rsrc_t rsE, unsigned voffA, unsigned voffB0, unsigned voffB1, bf16* act, WST wst, HOOK hook, const poll_t& pl) {
  constexpr int NB = 2 * FN, NM = 4 * NB, RD = 5 + 2 * NB;
  int fr = lane & 15, fq = lane >> 4;
  f32x4 acc[4][NB], accC[2];
  accC[0] = f32x4{0, 0, 0, 0}; accC[1] = f32x4{0, 0, 0, 0};
  asm volatile("" : "+v"(accC[0]), "+v"(accC[1]));
  const int rot = wm % FN;
  unsigned cad = smem_base + G3_CT + (fr & 1) * 64 + fq * 16;
#pragma unroll
  for (int i = 0; i < 4; ++i)
#pragma unroll
    for (int j = 0; j < NB; ++j) { acc[i][j] = f32x4{0, 0, 0, 0}; asm volatile("" : "+v"(acc[i][j])); }
  const unsigned aad = smem_base + 65536 + wm * 4096 + lane * 16;
  unsigned bad[FN];
  {
    const int q = (lane >> 2) & 3, p = lane & 3;
    const int r = 8 * fq + q;
    const int sw = (r & 3) | (((r >> 3) & 1) << 2);
#pragma unroll
    for (int j = 0; j < FN; ++j) {
      const int gi = wn * 4 + (j + rot) % FN + (bn & 1);
      bad[j] = smem_base + r * 256 + ((gi ^ sw) * 32) + 8 * p;
    }
  }
  u32x4 aX[5], aY[5];
  u32x2 bXl[NB], bXh[NB], bYl[NB], bYh[NB];
  const unsigned m0w = smem_base + wid * 2048;
  const unsigned voffP = lane * 16;
  u32x4 ones = {~0u, ~0u, ~0u, ~0u};
  if (G4S == 1 && FN == 3) asm volatile("" : "+v"(ones));
#define G3_READ(AR, BL, BH, SLOT, R)                                                                       \
  do {                                                                                                     \
    if ((R) < 2 * NB) {                                                                                    \
      constexpr int jb = ((R) < 2 * NB ? (R) : 0) >> 1;                                                    \
      constexpr int off = (SLOT) * 16384 + (jb / FN) * 8192 + ((R) & 1) * 1024;                            \
      if ((R) & 1) dsr_tr<off>(BH[jb], bad[jb % FN]);                                                       \
      else dsr_tr<off>(BL[jb], bad[jb % FN]);                                                               \
    } else if ((R) < 2 * NB + 4) {                                                                         \
      constexpr int ia = ((R) >= 2 * NB && (R) < 2 * NB + 4) ? (R) - 2 * NB : 0;                           \
      dsr128<(SLOT) * 16384 + ia * 1024>(AR[ia], aad);                                                      \
    } else {                                                                                               \
      if ((SLOT) == 0) cad += 512;                                       \
      dsr128<(SLOT) * 128>(AR[4], cad);                                                                    \
    }                                                                                                      \
  } while (0)
  auto rd = [&](auto SL, auto RR, u32x4 (&AR)[5], u32x2 (&BL)[NB], u32x2 (&BH)[NB]) { G3_READ(AR, BL, BH, SL.value, RR.value); };
#define G3_UNIT(AR, BL, BH, NAR, NBL, NBH, SLOT, U, LI)                                                    \
  do {                                                                                                     \
    WAIT_LGKM0();                                                                                          \
    u32x4 bb[NB];                                                                                          \
    _Pragma("unroll") for (int j = 0; j < NB; ++j) bb[j] = u32x4{BL[j][0], BL[j][1], BH[j][0], BH[j][1]};  \
    SCHED0();                                                                                              \
    asm volatile("s_nop 1");                                                                               \
    static_for<NM>([&](auto N) {                                                                           \
      constexpr int n = N.value;                                                                           \
      if constexpr (n == NM / 2 && !(LI && (SLOT) == 3)) {                                                 \
        wait_vm<(LI && (SLOT) == 0 && FN == 3) ? 28 : 8>(); __builtin_amdgcn_s_barrier();                  \
      }                                                                                                    \
      if constexpr (n == NM / 2 + TPOS && !LI) {                                                           \
                      \
        if (((U) & 7) == wid) bload_lds<0>(smem_base + 131072 + 4096, voffP, rsP, (unsigned)(U) * 1024u);  \
      }                                                                                                    \
      if constexpr (G4S == 1 && FN == 3 && n == NM / 2 + SPOS) {                                               \
                                          \
        if ((((SLOT) + 2) & 3) == wm) bstore_mark(ones, voffP, rsE, (unsigned)(U) * 1024u);                \
      }                                                                                                    \
      MFMA_AV(acc[n & 3][n >> 2], bb[n >> 2], AR[n & 3]);                                                  \
      if constexpr (n < NM / 2) {                                                                          \
        if constexpr (LI && (SLOT) == 0 && n == 0) hook(ic<0>{});                                          \
        if constexpr (n % 3 == 1 && n / 3 < 4) {                                                           \
          if constexpr (LI && (SLOT) > 0) wst(ic<((SLOT) + 3) & 3>{}, ic<(n / 3) & 3>{});                  \
          else G3_STAGE(((SLOT) + 3) & 3, n / 3, (U) + 3);                                                 \
        }                                                                                                  \
      } else if constexpr (LI && (SLOT) == 3) {                                                            \
        if constexpr (n == NM / 2) hook(ic<1>{});                                                          \
      } else {                                                                                             \
        constexpr int r0 = (n - NM / 2) * 2;                                                               \
        if constexpr (r0 < RD) rd(ic<((SLOT) + 1) & 3>{}, ic<(r0 < RD ? r0 : 0)>{}, NAR, NBL, NBH);        \
        if constexpr (r0 + 1 < RD) rd(ic<((SLOT) + 1) & 3>{}, ic<(r0 + 1 < RD ? r0 + 1 : 0)>{}, NAR, NBL, NBH); \
      }                                                                                                    \
    });                                                                                                    \
    if (NBC || FN == 4 || wm < 3) {                                                                        \
      MFMA_AV(accC[0], bb[0], AR[4]);                                                                      \
      MFMA_AV(accC[1], bb[FN], AR[4]);                                                                     \
    }                                                                                                      \
    SCHED0();                                                                                              \
  } while (0)

  asm volatile("s_waitcnt vmcnt(6)" ::: "memory");
  poll_end(pl);
  __builtin_amdgcn_s_barrier();
  STAMPDECL;
  STAMPNW(5, 1);
  cad -= 512;
  static_for<RD>([&](auto RR) { rd(ic<0>{}, RR, aX, bXl, bXh); });
  for (int u = 0; u < 28; u += 4) {
    G3_UNIT(aX, bXl, bXh, aY, bYl, bYh, 0, u, false);
    G3_UNIT(aY, bYl, bYh, aX, bXl, bXh, 1, u + 1, false);
    G3_UNIT(aX, bXl, bXh, aY, bYl, bYh, 2, u + 2, false);
    G3_UNIT(aY, bYl, bYh, aX, bXl, bXh, 3, u + 3, false);
  }
  G3_UNIT(aX, bXl, bXh, aY, bYl, bYh, 0, 28, true);
  G3_UNIT(aY, bYl, bYh, aX, bXl, bXh, 1, 29, true);
  G3_UNIT(aX, bXl, bXh, aY, bYl, bYh, 2, 30, true);
  G3_UNIT(aY, bYl, bYh, aX, bXl, bXh, 3, 31, true);
  asm volatile("s_nop 15" ::: "memory");
  SCHED0();
  STAMP(5, 2);
  STAMPFLUSH(5, 1);
  lane = fresh_lane(); fr = lane & 15; fq = lane >> 4;
  const f32x2* rstat = reinterpret_cast<const f32x2*>(smem + 131072);
  float* colc = reinterpret_cast<float*>(smem + 131072 + 2048);
  asm volatile("" : "+v"(accC[0]), "+v"(accC[1]));
  if (fr < 2 && wm < FN) {
#pragma unroll
    for (int m = 0; m < 2; ++m) *reinterpret_cast<f32x4*>(colc + (m * 2 + fr) * 128 + (wn * 4 + rot) * 16 + fq * 4) = accC[m];
  }
  asm volatile("s_waitcnt lgkmcnt(0)" ::: "memory");
  __builtin_amdgcn_s_barrier();
  asm volatile("" ::: "memory");
  static_for<4>([&](auto J) { wst(ic<3>{}, J); });
  f32x2 rs[4];
#pragma unroll
  for (int i = 0; i < 4; ++i) rs[i] = rstat[wm * 64 + i * 16 + fr];
#pragma unroll
  for (int j = 0; j < FN; ++j) {
    const int jg = (j + rot) % FN;
    const int cl = (wn * 4 + jg) * 16 + fq * 4;
    const f32x4 csg = *reinterpret_cast<const f32x4*>(colc + cl), bg = *reinterpret_cast<const f32x4*>(colc + 128 + cl);
    const f32x4 csu = *reinterpret_cast<const f32x4*>(colc + 256 + cl), bu = *reinterpret_cast<const f32x4*>(colc + 384 + cl);
    u32x2 pk[4];
#pragma unroll
    for (int i = 0; i < 4; ++i) {
#pragma unroll
      for (int hh = 0; hh < 2; ++hh) {
        const f32x2 ag = hh ? hi2(acc[i][j]) : lo2(acc[i][j]), au = hh ? hi2(acc[i][FN + j]) : lo2(acc[i][FN + j]);
        const f32x2 g = rs[i][0] * ag + (rs[i][1] * (hh ? hi2(csg) : lo2(csg)) + (hh ? hi2(bg) : lo2(bg)));
        const f32x2 uu = rs[i][0] * au + (rs[i][1] * (hh ? hi2(csu) : lo2(csu)) + (hh ? hi2(bu) : lo2(bu)));
        const f32x2 ea = g * -1.4426950408889634f;
        f32x2 e = {__builtin_amdgcn_exp2f(ea[0]), __builtin_amdgcn_exp2f(ea[1])};
        e = e + 1.0f;
        const f32x2 rc = {__builtin_amdgcn_rcpf(e[0]), __builtin_amdgcn_rcpf(e[1])};
        const f32x2 o = (g * uu) * rc;
        pk[i][hh] = pk_bf16(o[0], o[1]);
      }
    }
    const int Jv = (bn & 3) * 7 + wn * 4 + jg;
    const int J = g3_line(bn >> 2, Jv >> 2) * 4 + (Jv & 3);
    char* dst = (char*)act + ((size_t)((bm * 4 + wm) * (F_DIM / 32) + (J >> 1)) * 4 + (fq & 1)) * 1024 + (((J & 1) * 2 + (fq >> 1)) * 16 + fr) * 16;
#pragma unroll
    for (int i = 0; i < 4; i += 2) {
      const auto s0 = __builtin_amdgcn_permlane16_swap(pk[i][0], pk[i + 1][0], false, false);
      const auto s1 = __builtin_amdgcn_permlane16_swap(pk[i][1], pk[i + 1][1], false, false);
      gst128(dst + i * 1024, u32x4{s0[0], s1[0], s0[1], s1[1]});
    }
  }
}

#define G4PRI 2
#define XM 2
#define XB 1
#define G4AD 3
namespace g4 {
constexpr int NP = XB ? 4 : 8;
constexpr int NU = 14, SL = 4, AD = G4AD;
constexpr int CN = X_CN;
constexpr int GT = 131072 + 5120;
constexpr int na(int t) { return t + AD - 1 < NU ? 4 : 0; }
constexpr int nb(int mh, int t) { return t < 0 ? 0 : (t + SL < NU ? 4 : (t == NU - 3 && mh ? 4 : 0)); }
constexpr int behind(int mh, int t0, int u, int b) {
  int s = b ? na(u) : 0;
  for (int t = t0 + 1; t < u; ++t) s += na(t) + nb(mh, t);
  return s;
}
constexpr int vm(int mh, int u, int b) {
  const int wa = nb(mh, u - AD + 1) + behind(mh, u - AD + 1, u, b), wb = behind(mh, u + 1 - SL, u, b);
  return b == 0 ? wa + 2 : (u + 1 < NU && u + 1 >= SL && wb < wa ? wb : wa);
}
}

template <int MH, class WST, class PRE, class EXTRA>
DI void g4_loop(f32x4 (&acc)[2][8], unsigned smem_base, int lane, int wid, int kb0, rsrc_t rsA, WST ldb, PRE pre, EXTRA extra) {
  using namespace g4;
  const int fq = lane >> 4, k2 = wid & 1;
  unsigned voffA[2], bad[8];
#pragma unroll
  for (int i = 0; i < 2; ++i) voffA[i] = lane * 16 + ((wid & 3) ^ i) * 1024;
  {
    const int q = (lane >> 2) & 3, r = 8 * fq + q, sw = q | ((fq & 1) << 2);
#pragma unroll
    for (int f = 0; f < 8; ++f) bad[f] = smem_base + k2 * 65536 + r * 256 + ((f ^ sw) * 32) + 8 * (lane & 3);
  }
  const unsigned sA = (unsigned)(kb0 + 2 * k2) * 4096u;
  u32x4 af[AD][4];
  u32x2 bl[2][8], bh[2][8];
  auto lda = [&](auto V, auto J) {
    constexpr int v = V.value, j = J.value;
    asm volatile("" : "=v"(af[v % AD][j]));
    bload_sc1<0>(af[v % AD][j], voffA[j & 1], rsA, sA + (4 * v + (j >> 1)) * 4096u);
  };
  auto rdb = [&](auto V, auto B, auto E) {
    constexpr int o = (V.value & 3) * 16384 + B.value * 8192, f = E.value >> 1;
    if constexpr (E.value & 1) dsr_tr<o + 1024>(bh[B.value][f], bad[f]);
    else dsr_tr<o>(bl[B.value][f], bad[f]);
  };
  unsigned wp = smem_base + k2 * 65536 + 32768 + (wid >> 1) * 8192 + lane * 16;
#pragma unroll
  for (int f = 0; f < 8; ++f) asm volatile("" : "+v"(bad[f]));
  asm volatile("" : "+v"(voffA[0]), "+v"(voffA[1]), "+v"(wp) : "s"(rsA), "s"(sA));
  const poll_t pl = pre();
  static_for<4 * (AD - 1)>([&](auto J) { lda(ic<J.value / 4>{}, ic<J.value % 4>{}); });
  STAMPDECL;
  STAMPNW(6, 1);
  static_for<16>([&](auto E) { rdb(ic<0>{}, ic<0>{}, E); });
  static_for<NU>([&](auto U) {
    constexpr int u = U.value;
    constexpr bool last = u == NU - 1;
    static_for<32>([&](auto N) {
      constexpr int n = N.value, b = n >> 4, m = n & 15;
      constexpr int f = (last && b) ? (m & 7) : (m >> 1), i = (last && b) ? 1 - (m >> 3) : (m & 1);
      if constexpr (m == 0) {
        asm volatile("s_waitcnt vmcnt(%2) lgkmcnt(0)" : "+v"(af[u % AD][2 * b]), "+v"(af[u % AD][2 * b + 1])
                     : "n"(vm(MH, u, b)) : "memory");
        if constexpr (u == 0 && b == 0) poll_end(pl);
#pragma unroll
        for (int g = 0; g < 8; ++g) asm volatile("" : "+v"(bl[b][g]), "+v"(bh[b][g]));
        if constexpr (b == 1 && !last) __builtin_amdgcn_s_barrier();
        if constexpr (b == 1 && u == NU - 2 && MH == 0) __builtin_amdgcn_s_setprio(G4PRI);
      }
      const u32x4 bb = {bl[b][f][0], bl[b][f][1], bh[b][f][0], bh[b][f][1]};
      if constexpr (u == 0 && b == 0) asm volatile("v_mfma_f32_16x16x32_bf16 %0, %1, %2, 0" : "=&v"(acc[i][f]) : "v"(bb), "v"(af[u % AD][i]));
      else MFMA_AV(acc[i][f], bb, af[u % AD][2 * b + i]);
      if constexpr (m < 8 && !(last && b)) {
        rdb(ic<u + b>{}, ic<1 - b>{}, ic<2 * m>{});
        rdb(ic<u + b>{}, ic<1 - b>{}, ic<2 * m + 1>{});
      }
      if constexpr ((m & 3) == 2) {
        if constexpr (b == 0 && na(u)) lda(ic<u + AD - 1>{}, ic<(m >> 2)>{});
        if constexpr (b == 1 && u + SL < NU) ldb(ic<u + SL>{}, ic<(m >> 2)>{});
        if constexpr (b == 1 && u + 3 == NU) extra(ic<(m >> 2)>{});
      }
      if constexpr (last && n >= 24) dsw128<(m & 7) * 1024>(wp, __builtin_bit_cast(u32x4, acc[1][m & 7]));
    });
  });
  dsw32<0>(smem_base + CN + wid * 4, 1u);
  STAMPFLUSH(6, 1);
  const unsigned rp = wp + 65536 - k2 * 131072;
  u32x4 pq[8];
  for (;;) {
    unsigned fl;
    asm volatile("ds_read_b32 %0, %1" : "=v"(fl) : "v"(smem_base + CN + (wid ^ 1) * 4) : "memory");
    static_for<8>([&](auto F) { dsr128<F.value * 1024>(pq[F.value], rp); });
    asm volatile("s_waitcnt lgkmcnt(0)" : "+v"(fl), "+v"(pq[0]), "+v"(pq[1]), "+v"(pq[2]), "+v"(pq[3]), "+v"(pq[4]), "+v"(pq[5]), "+v"(pq[6]), "+v"(pq[7])::"memory");
    if (__builtin_amdgcn_readfirstlane(fl) != 0u) break;
  }
#pragma unroll
  for (int f = 0; f < 8; ++f) {
    asm volatile("" : "+v"(acc[0][f]));
    acc[0][f] += __builtin_bit_cast(f32x4, pq[f]);
  }
}

DI bool unset8(const u32x4 (&v)[8]) {
  unsigned m = 0;
#pragma unroll
  for (int f = 0; f < g4::NP; ++f) m = max(max(m, max(v[f][0], v[f][1])), max(v[f][2], v[f][3]));
  return __builtin_amdgcn_ballot_w64(m == ~0u) != 0ull;
}
template <int MH, class WST>
DI void g4_phase(unsigned smem_base, int wid, const bf16* At, WST wst, const bf16* Yt,
                 bf16* __restrict__ C, float* xbuf, unsigned* flags) {
  using namespace g4;
  constexpr int KB = F_DIM / 32;
  STAMP(6, 0);
  const int lane = fresh_lane();
  constexpr int mh = MH;
  const int kq = wid & 3;
  const int xcd = blockIdx.x & 7, idx = blockIdx.x >> 3;
  const int kh = xcd & 1, bm = ((xcd >> 1) & 1) * 8 + (idx & 7), bn = (xcd >> 2) * 4 + (idx >> 3);
  const unsigned partner = blockIdx.x ^ 1;
  const int n0 = bn * 128, bm64 = bm * 2 + kh;
  const int fr = lane & 15, fq = lane >> 4;
  const unsigned voffL = lane * 16;
  const rsrc_t rsA = mk_rsrc((const char*)At + (size_t)(bm64 ^ mh ^ 1) * KB * 4096, KB * 4096);
  const rsrc_t rsEx = mk_rsrc(xbuf, 256 * 32768);
  const int kb0 = kh * (KB / 2);
  f32x4 acc[2][8];
  auto pre = [&] {
    return seam<5, 8, 4, 256>(smem_base + CN + 512, lane, flags + (FL_A + ((idx & 7) * 2 + (xcd >> 2)) * 8 + (xcd & 1)) * 32,
                              flags + (FL_A + ((bm >> 1) * 2 + kh) * 8 + (idx >> 3) * 2) * 32, 2, 8u);
  };
  if constexpr (mh == 0) {
    g4_loop<0>(acc, smem_base, lane, wid, kb0, rsA, wst, pre, [](auto) {});
    STAMP2(1);
    static_for<NP>([&](auto F) {
      constexpr int f = F.value;
      u32x4 t;
      if constexpr (XB) t = pack8(acc[0][2 * f], acc[0][2 * f + 1]);
      else t = __builtin_bit_cast(u32x4, acc[0][f]);
      if (XM != 2) {
#pragma unroll
        for (int e = 0; e < 4; ++e) t[e] = min(t[e], 0xfffffffeu);
      }
      bstore_sc1<(f & 3) * 1024>(t, voffL, rsEx, blockIdx.x * 32768u + kq * 8192u + (f >> 2) * 4096u);
    });
    if (XM == 2) {
      STAMP2(2);
      signal(drain(flags + (FL_P + blockIdx.x) * 32 + kq, lane, 1, 4));
      STAMP2(3);
      STAMP2F(7, 1, 4);
      return;
    }
    STAMP2(2);
    STAMP2(3);
    STAMP2F(7, 1, 4);
    return;
  } else {
  u32x4 yv[4];
  const rsrc_t rsX = mk_rsrc(Yt, S_LEN * D_DIM * 2);
  const unsigned vx = (unsigned)(((bm64 >> 2) * 32 + bn * 4) * 16384 + ((bm64 & 3) * 4 + kq) * 1024 + ((((fq & 1) << 1) | (fq >> 1)) * 16 + fr) * 16);
  g4_loop<1>(acc, smem_base, lane, wid, kb0, rsA, wst, pre,
             [&](auto J) { asm volatile("" : "=v"(yv[J.value])); bload_sc1<0>(yv[J.value], vx, rsX, J.value * 16384); });
  STAMP2(2);
  u32x4 pv[8], gt[8], ct[8];
  auto fetch = [&] {
    static_for<NP>([&](auto F) {
      constexpr int f = F.value;
      bload_sc1<(f & 3) * 1024>(pv[f], voffL, rsEx, partner * 32768u + kq * 8192u + (f >> 2) * 4096u);
    });
  };
#pragma unroll
  for (int f = 0; f < 8; ++f) asm volatile("" : "=v"(pv[f]));
  if (XM == 0) fetch();
  static_for<8>([&](auto F) { dsr128<F.value * 64>(gt[F.value], smem_base + GT + fq * 16); dsr128<512 + F.value * 64>(ct[F.value], smem_base + GT + fq * 16); });
  asm volatile("s_waitcnt vmcnt(%4) lgkmcnt(0)" : "+v"(yv[0]), "+v"(yv[1]), "+v"(yv[2]), "+v"(yv[3]) : "n"(XM == 0 ? NP : 0) : "memory");
#pragma unroll
  for (int c = 0; c < 4; ++c) {
    const auto x0 = __builtin_amdgcn_permlane16_swap(yv[c][0], yv[c][2], false, false);
    const auto x1 = __builtin_amdgcn_permlane16_swap(yv[c][1], yv[c][3], false, false);
#pragma unroll
    for (int h = 0; h < 2; ++h) {
      const int f = 2 * c + h;
      asm volatile("" : "+v"(gt[f]), "+v"(ct[f]));
      acc[0][f] = f32x4{bf_lo(x0[h]), bf_hi(x0[h]), bf_lo(x1[h]), bf_hi(x1[h])} * __builtin_bit_cast(f32x4, ct[f]) + __builtin_bit_cast(f32x4, gt[f]) * acc[0][f];
      asm volatile("" : "+v"(acc[0][f]));
    }
  }
  char* cp = (char*)C + (size_t)(bm64 * 64 + kq * 16 + (fr & 7)) * (D_DIM * 2) + n0 * 2 + (((fq & 1) << 1) | (fq >> 1)) * 16 + (fr >> 3) * 64;
  asm volatile("" : "+v"(cp));
  STAMP2(5);
  unsigned spins = 0;
  auto landed = [&] { asm volatile("s_waitcnt vmcnt(0)" : "+v"(pv[0]), "+v"(pv[1]), "+v"(pv[2]), "+v"(pv[3]), "+v"(pv[4]), "+v"(pv[5]), "+v"(pv[6]), "+v"(pv[7])::"memory"); };
  const poll_t pl = poll(0u, flags + (FL_P + partner) * 32 + kq, lane, 1, 1u);
  STAMP2(7);
  if (XM != 0) fetch();
  landed();
  poll_end(pl);
  while (XM != 2 && unset8(pv) && ++spins < (1u << 20)) {
    fetch();
    landed();
  }
  STAMP2(6);
  static_for<2>([&](auto L) {
    constexpr int l = L.value;
    u32x4 ov[2];
#pragma unroll
    for (int c = 0; c < 2; ++c) {
      const int f = 4 * l + 2 * c;
      const u32x4 pb = pv[f >> 1];
      const f32x4 p0 = XB ? f32x4{bf_lo(pb[0]), bf_hi(pb[0]), bf_lo(pb[1]), bf_hi(pb[1])} : __builtin_bit_cast(f32x4, pv[f]);
      const f32x4 p1 = XB ? f32x4{bf_lo(pb[2]), bf_hi(pb[2]), bf_lo(pb[3]), bf_hi(pb[3])} : __builtin_bit_cast(f32x4, pv[f + 1]);
      const f32x4 o0 = acc[0][f] + __builtin_bit_cast(f32x4, gt[f]) * p0;
      const f32x4 o1 = acc[0][f + 1] + __builtin_bit_cast(f32x4, gt[f + 1]) * p1;
      const auto s0 = __builtin_amdgcn_permlane16_swap(pk_bf16(o0[0], o0[1]), pk_bf16(o1[0], o1[1]), false, false);
      const auto s1 = __builtin_amdgcn_permlane16_swap(pk_bf16(o0[2], o0[3]), pk_bf16(o1[2], o1[3]), false, false);
      ov[c] = u32x4{s0[0], s1[0], s0[1], s1[1]};
    }
    u32x4 lo, hi;
#pragma unroll
    for (int e = 0; e < 4; ++e) {
      lo[e] = __builtin_amdgcn_update_dpp(ov[0][e], ov[1][e], 0x128, 0xf, 0xc, false);
      hi[e] = __builtin_amdgcn_update_dpp(ov[1][e], ov[0][e], 0x128, 0xf, 0x3, false);
    }
    gst128(cp + l * 128, lo);
    gst128(cp + 8 * (D_DIM * 2) + l * 128, hi);
  });
  STAMP2(3);
  STAMP2(4);
  STAMP2F(6, 2, 7);
}
}

__global__ __launch_bounds__(512, 2) void block_kernel(bf16* H, const bf16* __restrict__ Wq, bf16* O,
                                                       const bf16* __restrict__ Wo, const bf16* __restrict__ X, bf16* Y,
                                                       const bf16* __restrict__ Wg, const bf16* __restrict__ Wu, bf16* act,
                                                       float* stat, const float* __restrict__ modp, unsigned* ctab, const float* __restrict__ rstat1,
                                                       const bf16* __restrict__ Wd, float* xbuf, bf16* __restrict__ C, unsigned* flags) {
  __shared__ __attribute__((aligned(1024))) char smem[Q_LDS];
  STAMP(2, 0);
  const int wid = __builtin_amdgcn_readfirstlane(threadIdx.x >> 6);
  const int wm = wid & 3, wn = wid >> 2;
  const int xcd = blockIdx.x & 7, idx = blockIdx.x >> 3;
  const int bm = idx & 7, bn = xcd * 4 + (idx >> 3);
  const unsigned smem_base = (unsigned)(uintptr_t)smem;
  const rsrc_t rsA = mk_rsrc((const char*)Y + (size_t)bm * 32 * 16384, 32 * 16384);
  const rsrc_t rsB = mk_rsrc(wn ? Wu : Wg, D_DIM * F_DIM * 2);
  const rsrc_t rsP = mk_rsrc((const char*)Wd + (size_t)blockIdx.x * 28672, 28672);
  const rsrc_t rsE = mk_rsrc((char*)xbuf + (size_t)blockIdx.x * 32768, 32768);
  const unsigned m0w = smem_base + wid * 2048;
  unsigned voffB0 = 0, voffB1 = 0;
  attn_phase(smem_base, wid, H, Wq, O, Wo, X, modp, Y, stat, flags, Wg, Wu, xbuf, ctab, rstat1, [&](auto S, auto I) {
    if constexpr (S.value == 0) {
      const int l2 = fresh_lane();
      const int r = (wm * 2 + I.value) * 4 + (l2 >> 4);
      const int sw = (r & 3) | (((r >> 3) & 1) << 2);
      const int c16 = l2 & 15;
      const int vb = (((bn & 3) * 224) & ~63) + ((c16 >> 1) ^ sw) * 32 + (c16 & 1) * 16;
      (I.value ? voffB1 : voffB0) = (unsigned)(r * (F_DIM * 2) + g3_line(bn >> 2, vb >> 7) * 128 + (vb & 127));
    }
    if (GSNT) bload_lds_nt<0>(m0w + S.value * 16384 + I.value * 1024, I.value ? voffB1 : voffB0, rsB, S.value * 229376u);
    else bload_lds<0>(m0w + S.value * 16384 + I.value * 1024, I.value ? voffB1 : voffB0, rsB, S.value * 229376u);
  });
  __builtin_amdgcn_s_setprio(0);
  const int lane = fresh_lane();
  const unsigned voffA = wid * 2048 + lane * 16;
  const poll_t pl = seam<3, 8, 8, 512>(smem_base + X_CN + 768, lane, flags + (FL_Y + xcd * 32 + (idx & 3)) * 32, flags + (FL_Y + bm * 32 + xcd * 4) * 32, 4, 8u);
  STAMP(5, 0);
  G3_STAGE(0, 0, 0); G3_STAGE(0, 1, 0); G3_STAGE(1, 0, 1); G3_STAGE(1, 1, 1); G3_STAGE(2, 0, 2); G3_STAGE(2, 1, 2);
#pragma unroll
  for (int i = 0; i < 2; ++i) bload_lds<0>(smem_base + 131072 + 4096, voffA, rsP, 0x40000000u);
  const int n0 = ((xcd >> 2) * 4 + (idx >> 3)) * 128;
  unsigned vo = 0;
  auto wst = [&](auto V, auto J) {
    constexpr int v = V.value, j = J.value;
    if constexpr (v == 0 && j == 0) {
      const int l2 = fresh_lane();
      vo = (l2 >> 4) * 2048 + ((((l2 & 15) >> 1) ^ ((l2 >> 4) | ((wid & 1) << 2))) * 32) + (l2 & 1) * 16;
    }
    bload_lds<0>(m0w + (j >> 1) * 65536 + (v & 3) * 16384 + (j & 1) * 1024, vo, mk_rsrc((const char*)Wd + n0 * 2, F_DIM * D_DIM * 2 - n0 * 2),
                 (unsigned)(((xcd & 1) * (F_DIM / 64) + wn) * 65536 + wm * 16384) + (4 * v + 2 * (j >> 1)) * 65536u + (j & 1) * 8192u);
  };
  if (wn == 0) {
    g3_body<4>(smem, smem_base, wm, wn, wid, lane, bm, bn, rsA, rsB, rsP, rsE, voffA, voffB0, voffB1, act, wst, [](auto) {}, pl);
    STAMP(5, 3);
    g4_phase<0>(smem_base, wid, act, wst, Y, C, xbuf, flags);
  } else {
    u32x2 tv[16];
    unsigned gv[4];
    g3_body<3>(smem, smem_base, wm, wn, wid, lane, bm, bn, rsA, rsB, rsP, rsE, voffA, voffB0, voffB1, act, wst, [&](auto K) {
      const int tid = wm * 64 + fresh_lane();
      if constexpr (K.value == 0) {
        const rsrc_t rsG = mk_rsrc(modp + (wm >= 2 ? 4096 : 5120), 4 * 6144 * 4);
#pragma unroll
        for (int r = 0; r < 4; ++r) { asm volatile("" : "=v"(gv[r])); bload1_v<0>(gv[r], (unsigned)((n0 + (tid & 127)) * 4), rsG, r * 24576); }
        const rsrc_t rsS = mk_rsrc(stat, S_LEN * 128);
#pragma unroll
        for (int i = 0; i < 16; ++i)
          asm volatile("buffer_load_dwordx2 %0, %1, %2, %3 offen sc1" : "=v"(tv[i]) : "v"((bm * 256 + tid) * 8), "s"(rsS), "s"(i * 16384) : "memory");
      } else {
#pragma unroll
        for (int i = 0; i < 16; ++i) asm volatile("" : "+v"(tv[i]));
        float s = 0;
#pragma unroll
        for (int i = 0; i < 16; i += 2) s += __uint_as_float(tv[i][0]) + __uint_as_float(tv[i + 1][0]);
        const float mean = s * (1.0f / D_DIM);
        float m2 = 0;
#pragma unroll
        for (int i = 0; i < 16; i += 2) {
          const float d0 = __uint_as_float(tv[i][0]) * (1.0f / 64.0f) - mean, d1 = __uint_as_float(tv[i + 1][0]) * (1.0f / 64.0f) - mean;
          m2 += __uint_as_float(tv[i][1]) + __uint_as_float(tv[i + 1][1]) + 64.0f * (d0 * d0 + d1 * d1);
        }
        const float rstd = rsqrtf(m2 * (1.0f / D_DIM) + 1e-5f);
        reinterpret_cast<f32x2*>(smem + 131072)[tid & 255] = f32x2{rstd, -mean * rstd};
        asm volatile("" : "+v"(gv[0]), "+v"(gv[1]), "+v"(gv[2]), "+v"(gv[3]));
        float sum = (__uint_as_float(gv[0]) + __uint_as_float(gv[1])) + (__uint_as_float(gv[2]) + __uint_as_float(gv[3]));
        sum = (wm >= 2) ? 1.0f / gain_nz(sum) : rbf(sum);
        dsw32<0>(smem_base + g4::GT + (tid & 255) * 4, __float_as_uint(sum));
      }
    }, pl);
    g4_phase<1>(smem_base, wid, act, wst, Y, C, xbuf, flags);
  }
}

extern "C" void kernel_launch(void* const* d_in, const int* in_sizes, int n_in, void* d_out, int out_size,
                              void* d_ws, size_t ws_size, hipStream_t stream) {
  const bf16* x = (const bf16*)d_in[0];
  const bf16* wqkv = (const bf16*)d_in[1];
  const bf16* wo = (const bf16*)d_in[2];
  const bf16* wg = (const bf16*)d_in[4];
  const bf16* wu = (const bf16*)d_in[5];
  const bf16* wd = (const bf16*)d_in[6];
  const bf16* c = (const bf16*)d_in[7];
  const bf16* wmod = (const bf16*)d_in[8];
  bf16* out = (bf16*)d_out;

  char* ws = (char*)d_ws;
  float* modp = (float*)ws;
  float* rstat1 = (float*)(ws + 114688);
  unsigned* ctab = (unsigned*)(ws + 131072);
  float* stat = (float*)(ws + 524288);
  bf16* ot = (bf16*)(ws + (1 << 20));
  bf16* yt = (bf16*)(ws + (9 << 20));
#define XAL 0
  bf16* ht = (bf16*)(ws + ((XAL ? 5 : 13) << 20));
  bf16* act = (bf16*)(ws + (17 << 20));
  float* xbuf = (float*)(ws + ((XAL ? 1 : 32) << 20));
  unsigned* flags = (unsigned*)(ws + 262144);

  pre_kernel<<<320, 512, 0, stream>>>(x, c, wmod, modp, rstat1, flags, wqkv);
  block_kernel<<<256, 512, 0, stream>>>(ht, wqkv, ot, wo, x, yt, wg, wu, act, stat, modp, ctab, rstat1, wd, xbuf, out, flags);
}
```

```cpp
#include <hip/hip_runtime.h>
#include <hip/hip_bf16.h>
#include <stdint.h>

typedef __hip_bfloat16 bf16;
typedef __attribute__((ext_vector_type(8))) short bf16x8;
typedef __attribute__((ext_vector_type(4))) float f32x4;
typedef __attribute__((ext_vector_type(2))) float f32x2;
typedef __attribute__((ext_vector_type(4))) unsigned u32x4;
typedef __attribute__((ext_vector_type(2))) unsigned u32x2;
typedef __attribute__((ext_vector_type(2))) __bf16 bf16x2_t;
typedef __amdgpu_buffer_rsrc_t rsrc_t;

#define DI __device__ __forceinline__
#define QBAR 2
#define QRN 17
#define TQ 12
#define TG 4
#define K1MAP 1
#define QTM 1
#define WOT 1
#define G4S 0
#define SMOD "sc1"
#define SEAMS 2
#define SBAR 1
#define TWAIT 0
#define NP1 5
#define QN 2
#define QK0 0
#define PSLP 4
#define PFIRST 1
#define T24 4
#define G4SK 0
#define EFOLD 1
#define NDLY 20
#define TDLY 15
#define P8 1
#define FULL 3
#define XNT 0
#define TQ0 0
#define TWIDE 0
#define PFF 3
#define CT2 0
#define CT3 24
#define ZL 1
#define EP 0
#define EPRI 0
#define ENOST 0
#define XFIRST 0
#define NTQ 4
#define CODEPF 67

#define STAMP(k, s) do { } while (0)
#define STAMPT(k, s, T) do { } while (0)
#define STAMPNW(k, s)
#define STAMPFLUSH(k, s)
#define STAMPDECL

#define STAMP2(s)
#define STAMP2F(k, a, b)

constexpr int S_LEN = 2048;
constexpr int D_DIM = 1024;
constexpr int F_DIM = 3584;

template <int V> struct ic { static constexpr int value = V; };
template <int N, int I = 0, class F>
DI void static_for(F&& f) {
  if constexpr (I < N) { f(ic<I>{}); static_for<N, I + 1>(f); }
}

DI float bf_lo(unsigned u) { return __uint_as_float(u << 16); }
DI float bf_hi(unsigned u) { return __uint_as_float(u & 0xffff0000u); }
DI unsigned pk_bf16(float lo, float hi) {
  const f32x2 v = {lo, hi};
  return __builtin_bit_cast(unsigned, __builtin_convertvector(v, bf16x2_t));
}
DI rsrc_t mk_rsrc(const void* p, unsigned bytes) { return __builtin_amdgcn_make_buffer_rsrc((void*)p, (short)0, (int)bytes, 0x00020000); }

template <int N>
DI void wait_vm() { asm volatile("s_waitcnt vmcnt(%0)" ::"n"(N > 63 ? 63 : N) : "memory"); }
#define WAIT_LGKM0() do { asm volatile("s_waitcnt lgkmcnt(0)" ::: "memory"); __builtin_amdgcn_sched_barrier(0); } while (0)
#define SCHED0() __builtin_amdgcn_sched_barrier(0)

#define MFMA_A(C, B, A) asm volatile("v_mfma_f32_16x16x32_bf16 %0, %1, %2, %0" : "+a"(C) : "v"(B), "a"(A))
#define MFMA_AV(C, B, A) asm volatile("v_mfma_f32_16x16x32_bf16 %0, %1, %2, %0" : "+v"(C) : "v"(B), "v"(A))
#define PERM_LO(D, X, Y) asm volatile("v_perm_b32 %0, %1, %2, %3" : "=v"(D) : "v"(Y), "v"(X), "s"(0x05040100u))
#define PERM_HI(D, X, Y) asm volatile("v_perm_b32 %0, %1, %2, %3" : "=v"(D) : "v"(Y), "v"(X), "s"(0x07060302u))
template <int OFF>
DI void bload_v(u32x4& d, unsigned voff, rsrc_t rs, unsigned soff) {
  asm volatile("buffer_load_dwordx4 %0, %1, %2, %3 offen offset:%4" : "+v"(d) : "v"(voff), "s"(rs), "s"(soff), "i"(OFF) : "memory");
}
template <int OFF>
DI void bload_a(u32x4& d, unsigned voff, rsrc_t rs, unsigned soff) {
  asm volatile("buffer_load_dwordx4 %0, %1, %2, %3 offen offset:%4" : "+a"(d) : "v"(voff), "s"(rs), "s"(soff), "i"(OFF) : "memory");
}
template <int OFF>
DI void bload2_v(u32x2& d, unsigned voff, rsrc_t rs, unsigned soff) {
  asm volatile("buffer_load_dwordx2 %0, %1, %2, %3 offen offset:%4" : "+v"(d) : "v"(voff), "s"(rs), "s"(soff), "i"(OFF) : "memory");
}
template <int OFF>
DI void bload1_v(unsigned& d, unsigned voff, rsrc_t rs, unsigned soff) {
  asm volatile("buffer_load_dword %0, %1, %2, %3 offen offset:%4" : "+v"(d) : "v"(voff), "s"(rs), "s"(soff), "i"(OFF) : "memory");
}
template <int OFF>
DI void bload_lds(unsigned m0v, unsigned voff, rsrc_t rs, unsigned soff) {
  asm volatile("s_mov_b32 m0, %0\n\ts_nop 0\n\tbuffer_load_dwordx4 %1, %2, %3 offen offset:%4 lds" ::"s"(m0v), "v"(voff), "s"(rs), "s"(soff), "i"(OFF) : "memory");
}
DI void touch_lds(unsigned m0v, unsigned voff, rsrc_t rs, unsigned soff) {
  asm volatile("s_mov_b32 m0, %0\n\ts_nop 0\n\tbuffer_load_dword %1, %2, %3 offen lds" ::"s"(m0v), "v"(voff), "s"(rs), "s"(soff) : "memory");
}
DI void touch_nt(unsigned m0v, unsigned voff, rsrc_t rs, unsigned soff) {
  asm volatile("s_mov_b32 m0, %0\n\ts_nop 0\n\tbuffer_load_dword %1, %2, %3 offen nt lds\n\tbuffer_load_dword %1, %2, %3 offen offset:64 sc1 lds" ::"s"(m0v), "v"(voff), "s"(rs), "s"(soff) : "memory");
}
template <int OFF>
DI void bload_lds_nt(unsigned m0v, unsigned voff, rsrc_t rs, unsigned soff) {
  asm volatile("s_mov_b32 m0, %0\n\ts_nop 0\n\tbuffer_load_dwordx4 %1, %2, %3 offen offset:%4 nt lds" ::"s"(m0v), "v"(voff), "s"(rs), "s"(soff), "i"(OFF) : "memory");
}
template <int OFF>
DI void bload_lds_sc1(unsigned m0v, unsigned voff, rsrc_t rs, unsigned soff) {
  asm volatile("s_mov_b32 m0, %0\n\ts_nop 0\n\tbuffer_load_dwordx4 %1, %2, %3 offen offset:%4 sc1 lds" ::"s"(m0v), "v"(voff), "s"(rs), "s"(soff), "i"(OFF) : "memory");
}
template <int OFF>
DI void dsr128(u32x4& d, unsigned addr) { asm volatile("ds_read_b128 %0, %1 offset:%2" : "=v"(d) : "v"(addr), "i"(OFF) : "memory"); }
template <int OFF>
DI void dsr32(unsigned& d, unsigned addr) { asm volatile("ds_read_b32 %0, %1 offset:%2" : "=v"(d) : "v"(addr), "i"(OFF) : "memory"); }
DI void bload_nt(u32x4& d, unsigned voff, rsrc_t rs, unsigned soff) {
  asm volatile("buffer_load_dwordx4 %0, %1, %2, %3 offen nt" : "=v"(d) : "v"(voff), "s"(rs), "s"(soff) : "memory");
}
template <int OFF>
DI void dsr64(u32x2& d, unsigned addr) { asm volatile("ds_read_b64 %0, %1 offset:%2" : "=v"(d) : "v"(addr), "i"(OFF) : "memory"); }
template <int OFF>
DI void dsr_tr(u32x2& d, unsigned addr) { asm volatile("ds_read_b64_tr_b16 %0, %1 offset:%2" : "=v"(d) : "v"(addr), "i"(OFF) : "memory"); }
template <int OFF>
DI void dsw128(unsigned addr, u32x4 d) { asm volatile("ds_write_b128 %0, %1 offset:%2" ::"v"(addr), "v"(d), "i"(OFF) : "memory"); }
template <int OFF>
DI void dsw128a(unsigned addr, const f32x4& d) { asm volatile("ds_write_b128 %0, %1 offset:%2" ::"v"(addr), "a"(d), "i"(OFF) : "memory"); }
template <int OFF>
DI void dsw32(unsigned addr, unsigned d) { asm volatile("ds_write_b32 %0, %1 offset:%2" ::"v"(addr), "v"(d), "i"(OFF) : "memory"); }

DI void gst128(void* p, u32x4 v) {
  asm volatile("global_store_dwordx4 %0, %1, off sc1\n\ts_nop 1" ::"v"(p), "v"(v) : "memory");
}
DI int fresh_lane() {
  int l;
  asm volatile("v_mbcnt_lo_u32_b32 %0, -1, 0\n\tv_mbcnt_hi_u32_b32 %0, -1, %0" : "=v"(l));
  return l;
}
DI f32x2 lo2(f32x4 v) { return __builtin_shufflevector(v, v, 0, 1); }
DI f32x2 hi2(f32x4 v) { return __builtin_shufflevector(v, v, 2, 3); }
DI float rowsum4(float v) {
  const unsigned u = __float_as_uint(v);
  const auto a = __builtin_amdgcn_permlane16_swap(u, u, false, false);
  const unsigned t = __float_as_uint(__uint_as_float(a[0]) + __uint_as_float(a[1]));
  const auto b = __builtin_amdgcn_permlane32_swap(t, t, false, false);
  return __uint_as_float(b[0]) + __uint_as_float(b[1]);
}
DI void split4(const f32x4 v, u32x2& hi, u32x2& lo) {
  hi[0] = pk_bf16(v[0], v[1]); hi[1] = pk_bf16(v[2], v[3]);
  lo[0] = pk_bf16(v[0] - bf_lo(hi[0]), v[1] - bf_hi(hi[0]));
  lo[1] = pk_bf16(v[2] - bf_lo(hi[1]), v[3] - bf_hi(hi[1]));
}
DI float silu_f(float v) { return v * __builtin_amdgcn_rcpf(1.0f + __builtin_amdgcn_exp2f(v * -1.4426950408889634f)); }
DI float rbf(float v) { return bf_lo(pk_bf16(v, v)); }
DI float rowmax4(float v) {
  const unsigned u = __float_as_uint(v);
  const auto a = __builtin_amdgcn_permlane16_swap(u, u, false, false);
  const unsigned t = __float_as_uint(fmaxf(__uint_as_float(a[0]), __uint_as_float(a[1])));
  const auto b = __builtin_amdgcn_permlane32_swap(t, t, false, false);
  return fmaxf(__uint_as_float(b[0]), __uint_as_float(b[1]));
}
DI float gain_nz(float m) {
  const float g = rbf(1.0f + rbf(m));
  return __builtin_fabsf(g) < 8.67361738e-19f ? 8.67361738e-19f : g;
}
template <int OFF>
DI void bload_sc1(u32x4& d, unsigned voff, rsrc_t rs, unsigned soff) {
  asm volatile("buffer_load_dwordx4 %0, %1, %2, %3 offen offset:%4 sc1" : "+v"(d) : "v"(voff), "s"(rs), "s"(soff), "i"(OFF) : "memory");
}
template <int OFF>
DI void bstore_sc1(u32x4 d, unsigned voff, rsrc_t rs, unsigned soff) {
  asm volatile("buffer_store_dwordx4 %0, %1, %2, %3 offen offset:%4 sc1\n\ts_nop 1" ::"v"(d), "v"(voff), "s"(rs), "s"(soff), "i"(OFF) : "memory");
}
DI void bstore_mark(u32x4 d, unsigned voff, rsrc_t rs, unsigned soff) {
  asm volatile("buffer_store_dwordx4 %0, %1, %2, %3 offen " SMOD "\n\ts_nop 1" ::"v"(d), "v"(voff), "s"(rs), "s"(soff) : "memory");
}
DI void bload1_sc1(unsigned& d, unsigned voff, rsrc_t rs) {
  asm volatile("buffer_load_dword %0, %1, %2, 0 offen sc1" : "+v"(d) : "v"(voff), "s"(rs) : "memory");
}

#define GMAP 0
#define T1 0
#define T2 0
#define GNT 0
#define TW 1
#define TP 6
#define TS 12
#define TN 2
#define SM0 4
#define SMN 2
#define GS0 28
#define GSNT 0
#define QT0 8
#define QS0 0
#define QP 3
#define WOS 6
#define GROT 1
#define GDEP 12
constexpr int Q_SLOT = 21504, Q_NSL = 7;
constexpr int QX_WH = 4 * Q_SLOT;
constexpr int QX_HX = 65536;
constexpr int QX_GT = QX_WH + 65536;
constexpr int QX_CNT = QX_GT + 512;
constexpr int QX_SINK = QX_CNT + 512;
constexpr int G3_CT = QX_SINK + 1024;
constexpr int X_CN = G3_CT + 4096;
constexpr int Q_LDS = X_CN + 2048;
constexpr int P_TAB = 4 * Q_SLOT, P_RED = 5 * Q_SLOT, P_ST = P_RED + 4096;

DI unsigned lds_add(unsigned addr, unsigned v0) {
  unsigned o;
  asm volatile("ds_add_rtn_u32 %0, %1, %2\n\ts_waitcnt lgkmcnt(0)" : "=v"(o) : "v"(addr), "v"(v0) : "memory");
  return __builtin_amdgcn_readfirstlane(o);
}
template <int SLP>
DI void lds_wait(unsigned addr, unsigned n) {
  for (;;) {
    unsigned v;
    asm volatile("ds_read_b32 %0, %1\n\ts_waitcnt lgkmcnt(0)" : "=v"(v) : "v"(addr) : "memory");
    if (__builtin_amdgcn_readfirstlane(v) >= n) break;
    __builtin_amdgcn_s_sleep(SLP);
  }
}
struct poll_t { unsigned a, b; };
struct sig_t { rsrc_t rs; unsigned vo; };
DI sig_t drain(unsigned* out, int lane, int no, int so) {
  sig_t g = {mk_rsrc(out, (no - 1) * so + 4), (unsigned)(lane * so)};
  asm volatile("s_waitcnt vmcnt(0)" : "+v"(g.vo) : "s"(g.rs) : "memory");
  return g;
}
DI void signal(const sig_t& g) { asm volatile("buffer_atomic_add %0, %1, %2, 0 offen sc1" ::"v"(1u), "v"(g.vo), "s"(g.rs) : "memory"); }
#define POLL_RD(r) "v_mov_b32 " r ", 0\n\tbuffer_atomic_add " r ", %3, %4, 0 offen sc0 sc1\n\t"
#define POLL_CK(r) "s_waitcnt vmcnt(1)\n\tv_cmp_lt_u32 vcc, " r ", %5\n\ts_cbranch_vccz 2f\n\t"
DI poll_t poll(unsigned skip, unsigned* in, int lane, int nl, unsigned n) {
  poll_t r;
  asm volatile("s_cmp_lg_u32 %2, 0\n\ts_cbranch_scc1 2f\n\t" POLL_RD("%0") "s_sleep %6\n1:\n\t" POLL_RD("%1") POLL_CK("%0") POLL_RD("%0") POLL_CK("%1")
               "s_add_u32 %2, %2, 1\n\ts_cmp_lt_u32 %2, 0x80000\n\ts_cbranch_scc1 1b\n2:"
               : "=&v"(r.a), "=&v"(r.b), "+s"(skip) : "v"(lane * 128), "s"(mk_rsrc(in, nl * 128)), "v"(lane < nl ? n : 0u), "n"(PSLP) : "memory", "vcc", "scc");
  return r;
}
DI void poll_end(const poll_t& r) { asm volatile("" ::"v"(r.a), "v"(r.b)); }
template <int K, int NW, int NO, int SO>
DI unsigned arrive(unsigned lcnt, int lane, unsigned* out) {
  asm volatile("" ::"v"(lcnt + lane * 4));
  const sig_t g = drain(out, lane, NO, SO);
  STAMP(K, 4);
  const unsigned o = lds_add(lcnt + lane * 4, 1u);
  if (o == NW - 1) {
    signal(g);
  }
  return o;
}
template <int K>
DI poll_t await(unsigned o, int lane, unsigned* in, int nl, unsigned n) {
  const poll_t r = poll(o, in, lane, nl, n);
  __builtin_amdgcn_s_barrier();
  return r;
}
constexpr int FL_M = 0, FL_H = 544, FL_O = 608, FL_P = 736, FL_Y = 992, FL_A = 1248, FL_S = 1376;

DI f32x4 mfma0(u32x4 a, u32x4 b) {
  return __builtin_amdgcn_mfma_f32_16x16x32_bf16(__builtin_bit_cast(bf16x8, a), __builtin_bit_cast(bf16x8, b), f32x4{0, 0, 0, 0}, 0, 0, 0);
}
DI u32x4 pack8(const f32x4 a, const f32x4 b) { return u32x4{pk_bf16(a[0], a[1]), pk_bf16(a[2], a[3]), pk_bf16(b[0], b[1]), pk_bf16(b[2], b[3])}; }
template <class OP>
DI float rows4(const float (&v)[4], OP op) {
  const auto a = __builtin_amdgcn_permlane16_swap(__float_as_uint(v[0]), __float_as_uint(v[1]), false, false);
  const auto b = __builtin_amdgcn_permlane16_swap(__float_as_uint(v[2]), __float_as_uint(v[3]), false, false);
  const float t0 = op(__uint_as_float(a[0]), __uint_as_float(a[1])), t1 = op(__uint_as_float(b[0]), __uint_as_float(b[1]));
  const auto c = __builtin_amdgcn_permlane32_swap(__float_as_uint(t0), __float_as_uint(t1), false, false);
  return op(__uint_as_float(c[0]), __uint_as_float(c[1]));
}
DI void bcast4(float t, float (&v)[4]) {
  const auto c = __builtin_amdgcn_permlane32_swap(__float_as_uint(t), __float_as_uint(t), false, false);
  const auto a = __builtin_amdgcn_permlane16_swap(c[0], c[0], false, false);
  const auto b = __builtin_amdgcn_permlane16_swap(c[1], c[1], false, false);
  v[0] = __uint_as_float(a[0]); v[1] = __uint_as_float(a[1]); v[2] = __uint_as_float(b[0]); v[3] = __uint_as_float(b[1]);
}
DI float fmax2(float a, float b) { return __builtin_amdgcn_fmed3f(a, b, __builtin_inff()); }

DI float sum16(float v) {
  v += __builtin_bit_cast(float, __builtin_amdgcn_update_dpp(0, __builtin_bit_cast(int, v), 0x128, 0xf, 0xf, false));
  v += __builtin_bit_cast(float, __builtin_amdgcn_update_dpp(0, __builtin_bit_cast(int, v), 0x124, 0xf, 0xf, false));
  v += __builtin_bit_cast(float, __builtin_amdgcn_update_dpp(0, __builtin_bit_cast(int, v), 0x122, 0xf, 0xf, false));
  v += __builtin_bit_cast(float, __builtin_amdgcn_update_dpp(0, __builtin_bit_cast(int, v), 0x121, 0xf, 0xf, false));
  return v;
}
DI f32x4 sum4(const u32x4* p) {
  return (__builtin_bit_cast(f32x4, p[0]) + __builtin_bit_cast(f32x4, p[1])) + (__builtin_bit_cast(f32x4, p[2]) + __builtin_bit_cast(f32x4, p[3]));
}
template <int NB, int NMID, class MID>
DI void mod_gemv(unsigned red, int lane, int col0, int k0, const bf16* __restrict__ c, const bf16* __restrict__ wmod, MID mid) {
  const int fr = lane & 15, fq = lane >> 4;
  const rsrc_t rsW = mk_rsrc(wmod, 1024 * 6144 * 2);
  const unsigned vw = (unsigned)(((k0 + (fr >> 3) * 32 + fq * 8) * 6144 + col0 + (fr & 7) * 8) * 2);
  u32x4 raw[NB][8], cv[NB];
  static_for<NB>([&](auto B) {
    asm volatile("" : "=v"(cv[B.value]));
    bload_v<B.value * 128>(cv[B.value], (unsigned)((k0 + (fr & 1) * 32 + fq * 8) * 2), mk_rsrc(c, 2048), 0);
  });
  static_for<NB * 8>([&](auto N) {
    constexpr int b = N.value >> 3, j = N.value & 7;
    bload_nt(raw[b][j], vw, rsW, (b * 64 + j) * 12288);
  });
  __builtin_amdgcn_s_barrier();
  f32x4 acc[8];
#pragma unroll
  for (int n = 0; n < 8; ++n) acc[n] = f32x4{0, 0, 0, 0};
  u32x4 cf[NB];
  static_for<NB>([&](auto B) {
    constexpr int b = B.value;
    asm volatile("s_waitcnt vmcnt(%1)" : "+v"(cv[b]) : "n"(NB * 8) : "memory");
#pragma unroll
    for (int e = 0; e < 4; ++e) cf[b][e] = pk_bf16(silu_f(bf_lo(cv[b][e])), silu_f(bf_hi(cv[b][e])));
    asm volatile("" : "+v"(cf[b]));
  });
  static_for<NB>([&](auto B) {
    constexpr int b = B.value;
    if constexpr (b == 1) {
#pragma unroll
      for (int n = 0; n < 8; ++n) asm volatile("" : "+v"(acc[n]));
      mid();
    }
    asm volatile("s_waitcnt vmcnt(%8)" : "+v"(raw[b][0]), "+v"(raw[b][1]), "+v"(raw[b][2]), "+v"(raw[b][3]), "+v"(raw[b][4]), "+v"(raw[b][5]), "+v"(raw[b][6]), "+v"(raw[b][7])
                 : "n"(b ? NMID : (NB - 1) * 8) : "memory");
#pragma unroll
    for (int p = 0; p < 4; ++p) {
      u32x4 lo, hi;
#pragma unroll
      for (int t = 0; t < 4; ++t) {
        lo[t] = __builtin_amdgcn_perm(raw[b][2 * t + 1][p], raw[b][2 * t][p], 0x05040100u);
        hi[t] = __builtin_amdgcn_perm(raw[b][2 * t + 1][p], raw[b][2 * t][p], 0x07060302u);
      }
      acc[2 * p] = __builtin_amdgcn_mfma_f32_16x16x32_bf16(__builtin_bit_cast(bf16x8, lo), __builtin_bit_cast(bf16x8, cf[b]), acc[2 * p], 0, 0, 0);
      acc[2 * p + 1] = __builtin_amdgcn_mfma_f32_16x16x32_bf16(__builtin_bit_cast(bf16x8, hi), __builtin_bit_cast(bf16x8, cf[b]), acc[2 * p + 1], 0, 0, 0);
    }
  });
  if (fr == (fq >> 1)) {
    static_for<8>([&](auto R) {
      constexpr int r = R.value >> 1, h = (R.value & 1) * 4;
      dsw128<R.value * 16>(red + fq * 128, __builtin_bit_cast(u32x4, f32x4{acc[h][r], acc[h + 1][r], acc[h + 2][r], acc[h + 3][r]}));
    });
  }
}
template <int NR>
DI float red_sum(unsigned red, int lane) {
  unsigned v[NR];
  static_for<NR>([&](auto R) { dsr32<R.value * 256>(v[R.value], red + lane * 4); });
  asm volatile("s_waitcnt lgkmcnt(0)" ::: "memory");
  float s = 0;
#pragma unroll
  for (int r = 0; r < NR; r += 2) {
    asm volatile("" : "+v"(v[r]), "+v"(v[r + 1]));
    s += __uint_as_float(v[r]) + __uint_as_float(v[r + 1]);
  }
  return s;
}
DI unsigned cnt_read(unsigned* in, int lane, int nl) {
  unsigned t = 0;
  asm volatile("buffer_atomic_add %0, %1, %2, 0 offen sc0 sc1\n\ts_waitcnt vmcnt(0)" : "+v"(t) : "v"(lane * 128), "s"(mk_rsrc(in, nl * 128)) : "memory");
  return t;
}
DI bool words_set(unsigned* word, int lane) {
  unsigned t = 0;
  asm volatile("buffer_atomic_add %0, %1, %2, 0 offen sc0 sc1\n\ts_waitcnt vmcnt(0)" : "+v"(t) : "v"(lane * 8192), "s"(mk_rsrc(word, 7 * 8192 + 4)) : "memory");
  return (__builtin_amdgcn_ballot_w64(t == 0u) & 255u) == 0u;
}
DI void confirm(unsigned* word, int lane) {
#pragma unroll 1
  for (unsigned spins = 0; !words_set(word, lane) && ++spins < (1u << 20);) __builtin_amdgcn_s_sleep(2);
}
DI void set_words(unsigned* flags, int lane) {
  bstore_sc1<0>(u32x4{1u, 1u, 1u, 1u}, (unsigned)(lane * 128), mk_rsrc(flags + (FL_S + (blockIdx.x & 7) * 64) * 32, 64 * 128), 0);
}

template <class PRE>
DI void first_phase(unsigned smem_base, int wid, int lane, const bf16* __restrict__ x, const bf16* __restrict__ c, const bf16* __restrict__ wmod,
                    const bf16* __restrict__ W, float* modp, unsigned* ctab, bf16* H, unsigned* flags, PRE prefill) {
  const int xcd = blockIdx.x & 7, idx = blockIdx.x >> 3;
  const int fr = lane & 15, fq = lane >> 4;
  const int u = xcd * 16 + (idx & 15), v = xcd * 8 + idx;
  unsigned* word = flags + __builtin_amdgcn_readfirstlane((FL_S + (blockIdx.x >> 2)) * 32 + (blockIdx.x & 3));
  const bool master = idx == 15;
  if (wid == (idx >= 16 ? 0 : 7)) {
    if (FULL & 1) asm volatile("buffer_store_dword %0, %1, %2, 0 offen sc1" ::"v"(0u), "v"(lane * 8192), "s"(mk_rsrc(word, 7 * 8192 + 4)) : "memory");
    else asm volatile("buffer_atomic_swap %0, %1, %2, 0 offen sc1" ::"v"(0u), "v"(lane * 8192), "s"(mk_rsrc(word, 7 * 8192 + 4)) : "memory");
  }
  if (FULL & 2) {
    if (master) {
      bstore_sc1<0>(u32x4{0u, 0u, 0u, 0u}, (unsigned)(wid * 2048 + lane * 16), mk_rsrc(flags + xcd * (FL_O / 8) * 32, FL_O / 8 * 128), 0);
      bstore_sc1<1024>(u32x4{0u, 0u, 0u, 0u}, (unsigned)(wid * 2048 + lane * 16), mk_rsrc(flags + xcd * (FL_O / 8) * 32, FL_O / 8 * 128), 0);
    }
  } else if (master && wid < 2) bstore_sc1<0>(u32x4{0u, 0u, 0u, 0u}, (unsigned)((wid * 64 + lane) * 128), mk_rsrc(flags + xcd * (FL_O / 8) * 32, FL_O / 8 * 128), 0);
  if (wid == 7) {
    dsw32<0>(smem_base + QX_CNT + lane * 4, 0u); dsw32<256>(smem_base + QX_CNT + lane * 4, 0u);
    dsw128<0>(smem_base + X_CN + lane * 16, u32x4{0u, 0u, 0u, 0u}); dsw128<1024>(smem_base + X_CN + lane * 16, u32x4{0u, 0u, 0u, 0u});
    asm volatile("s_waitcnt lgkmcnt(0)" ::: "memory");
  }
  if (__builtin_expect(idx >= 16, 1)) {
    unsigned o = 0;
    if (wid < 4) {
      const int col0 = P8 ? (u >> 3) * 128 : (u >> 2) * 64, kp = P8 ? u & 7 : u & 3;
      if (XFIRST) __builtin_amdgcn_s_sleep(XFIRST);
      mod_gemv<1, 0>(smem_base + P_RED + wid * 512, lane, col0 + (P8 ? (wid & 1) * 64 : 0), P8 ? kp * 128 + (wid >> 1) * 64 : kp * 256 + wid * 64, c, wmod, [] {});
      const sig_t g = {mk_rsrc(flags + (FL_M + (u & 15)) * 32, 16 * 4096 + 4), (unsigned)(lane * 4096)};
      asm volatile("" ::"s"(g.rs), "v"(g.vo));
      o = lds_add(smem_base + X_CN + 1280 + lane * 4, 1u);
      if (o == 3) {
        STAMP2(0);
        u32x2 r[4];
        static_for<4>([&](auto R) { dsr64<(R.value >> 1) * 1024 + (R.value & 1) * 256>(r[R.value], smem_base + P_RED + (lane >> 5) * 512 + (lane & 31) * 8); });
        asm volatile("s_waitcnt lgkmcnt(0)" : "+v"(r[0]), "+v"(r[1]), "+v"(r[2]), "+v"(r[3])::"memory");
        const f32x2 sum = (__builtin_bit_cast(f32x2, r[0]) + __builtin_bit_cast(f32x2, r[1])) + (__builtin_bit_cast(f32x2, r[2]) + __builtin_bit_cast(f32x2, r[3]));
        asm volatile("buffer_store_dwordx2 %0, %1, %2, 0 offen sc1" ::"v"(sum), "v"(lane * 8), "s"(mk_rsrc(modp + kp * 2048 + col0, 512)) : "memory");
        confirm(word, lane);
        signal(g);
        STAMP2(1);
      }
    } else {
      const rsrc_t rsX = mk_rsrc(x, S_LEN * D_DIM * 2);
      const int r0 = (wid - 4) * 4;
      u32x4 a[8];
      static_for<8>([&](auto N) {
        asm volatile("" : "=v"(a[N.value]));
        if (XNT) bload_nt(a[N.value], (unsigned)((u * 16 + r0) * 2048 + lane * 16), rsX, N.value * 1024);
        else bload_v<(N.value & 1) * 1024>(a[N.value], (unsigned)((u * 16 + r0) * 2048 + lane * 16), rsX, (N.value >> 1) * 2048);
      });
      __builtin_amdgcn_s_barrier();
      asm volatile("s_waitcnt vmcnt(0)" : "+v"(a[0]), "+v"(a[1]), "+v"(a[2]), "+v"(a[3]), "+v"(a[4]), "+v"(a[5]), "+v"(a[6]), "+v"(a[7])::"memory");
      STAMPT(0, 4, 256);
      float sm[4], q[4];
      f32x2 xf[4][8];
#pragma unroll
      for (int i = 0; i < 4; ++i) {
        const unsigned ad = smem_base + (lane >> 5) * Q_SLOT + (lane & 31) * 256 + (((r0 + i) ^ (lane & 7)) * 16);
        dsw128<0>(ad, a[2 * i]); dsw128<2 * Q_SLOT>(ad, a[2 * i + 1]);
#pragma unroll
        for (int j = 0; j < 8; ++j) xf[i][j] = f32x2{bf_lo(a[2 * i + (j >> 2)][j & 3]), bf_hi(a[2 * i + (j >> 2)][j & 3])};
        const f32x2 t = ((xf[i][0] + xf[i][1]) + (xf[i][2] + xf[i][3])) + ((xf[i][4] + xf[i][5]) + (xf[i][6] + xf[i][7]));
        sm[i] = t[0] + t[1];
      }
      const float mrow = sum16(rows4(sm, [](float p, float r) { return p + r; })) * (1.0f / D_DIM);
      bcast4(mrow, sm);
#pragma unroll
      for (int i = 0; i < 4; ++i) {
        f32x2 t0 = xf[i][0] - sm[i], t1 = xf[i][1] - sm[i];
        t0 *= t0; t1 *= t1;
#pragma unroll
        for (int j = 2; j < 8; j += 2) {
          const f32x2 d0 = xf[i][j] - sm[i], d1 = xf[i][j + 1] - sm[i];
          t0 += d0 * d0; t1 += d1 * d1;
        }
        t0 += t1;
        q[i] = t0[0] + t0[1];
      }
      const float rstd = __builtin_amdgcn_rsqf(sum16(rows4(q, [](float p, float r) { return p + r; })) * (1.0f / D_DIM) + 1e-5f);
      asm volatile("ds_write_b64 %0, %1\n\ts_waitcnt lgkmcnt(0)" ::"v"(smem_base + P_ST + (r0 + fq) * 8), "v"(f32x2{rstd, -mrow * rstd}) : "memory");
      STAMPT(0, 3, 256);
      prefill(wid - 4); prefill(wid);
    }
    u32x4 mp[8], xv[4], tb[4][4];
    u32x2 rr;
    const rsrc_t rsM = mk_rsrc(modp, 8 * 2048 * 4);
    const unsigned vm = (unsigned)((lane >> 5) * 4096 + wid * 512 + (lane & 31) * 16);
    asm volatile("" ::"s"(rsM), "v"(vm));
    const poll_t pm = poll(3 - o, flags + (FL_M + (1 + (u >> 3)) * 32) * 32, lane, 16, 8u);
    __builtin_amdgcn_s_barrier();
#pragma unroll
    for (int i = 0; i < 8; ++i) { asm volatile("" : "=v"(mp[i])); if (P8 || i < 4) bload_sc1<0>(mp[i], vm, rsM, i * 8192); }
    const unsigned xa = smem_base + (wid >> 1) * Q_SLOT + (wid & 1) * 4096 + fq * 256 + ((fr ^ fq) * 16);
    dsr128<0>(xv[0], xa); dsr128<1024>(xv[1], xa ^ 64u); dsr128<2048>(xv[2], xa); dsr128<3072>(xv[3], xa ^ 64u);
    dsr64<0>(rr, smem_base + P_ST + fr * 8);
    asm volatile("s_waitcnt lgkmcnt(0)" : "+v"(xv[0]), "+v"(xv[1]), "+v"(xv[2]), "+v"(xv[3]), "+v"(rr)::"memory");
    f32x2 xn[4][4];
#pragma unroll
    for (int i = 0; i < 4; ++i)
#pragma unroll
      for (int d = 0; d < 4; ++d) {
        xn[i][d] = f32x2{bf_lo(xv[i][d]), bf_hi(xv[i][d])} * __uint_as_float(rr[0]) + __uint_as_float(rr[1]);
        asm volatile("" : "+v"(xn[i][d]));
      }
    char* hp = (char*)H + ((size_t)u * 32 + wid * 4) * 1024 + lane * 16;
    asm volatile("s_waitcnt vmcnt(0)" : "+v"(mp[0]), "+v"(mp[1]), "+v"(mp[2]), "+v"(mp[3]), "+v"(mp[4]), "+v"(mp[5]), "+v"(mp[6]), "+v"(mp[7]), "+v"(hp)::"memory");
    poll_end(pm);
    STAMP(1, 0);
    f32x4 mv = P8 ? sum4(mp) + sum4(mp + 4) : sum4(mp);
#pragma unroll
    for (int e = 0; e < 4; ++e) {
      mv[e] = rbf(mv[e]);
      if (lane & 32) mv[e] = rbf(1.0f + mv[e]);
    }
    dsw128<0>(smem_base + P_TAB + wid * 1024 + lane * 16, __builtin_bit_cast(u32x4, mv));
    static_for<16>([&](auto N) {
      constexpr int i = N.value >> 2, j = N.value & 3;
      dsr128<i * 128 + (j >> 1) * 512 + (j & 1) * 16>(tb[i][j], smem_base + P_TAB + wid * 1024 + fq * 32);
    });
    static_for<4>([&](auto I) {
      constexpr int i = I.value;
      asm volatile("s_waitcnt lgkmcnt(%4)" : "+v"(tb[i][0]), "+v"(tb[i][1]), "+v"(tb[i][2]), "+v"(tb[i][3]) : "n"(12 - 4 * i) : "memory");
      u32x4 ho;
#pragma unroll
      for (int d = 0; d < 4; ++d) {
        const f32x4 gg = __builtin_bit_cast(f32x4, tb[i][2 + (d >> 1)]), ss = __builtin_bit_cast(f32x4, tb[i][d >> 1]);
        const f32x2 hv = xn[i][d] * ((d & 1) ? hi2(gg) : lo2(gg)) + ((d & 1) ? hi2(ss) : lo2(ss));
        ho[d] = pk_bf16(hv[0], hv[1]);
      }
      gst128(hp + i * 1024, ho);
    });
    STAMP(1, 1);
    const sig_t g = drain(flags + (FL_H + (u >> 3) * 4) * 32, lane, ((u & 7) == 7 && u != 127) ? 8 : 4, 128);
    STAMP(1, 4);
    if (lds_add(smem_base + X_CN + 1024 + lane * 4, 1u) == 7) {
      signal(g);
    }
  } else {
    const rsrc_t rsS = mk_rsrc(word, wid == 7 ? 7 * 8192 + 4 : 0);
    unsigned seen = 0;
    auto mid = [&] {
      asm volatile("buffer_atomic_add %0, %1, %2, 0 offen sc0 sc1" : "+v"(seen) : "v"(lane * 8192), "s"(rsS) : "memory");
      if (!PFF) prefill(wid);
    };
    if (PFF && !master) {
      const int ql = (wid * 7 + idx - 8) * 64 + lane;
      if (idx >= 8 && wid < 3) touch_lds(smem_base + QX_SINK, (unsigned)((ql >> 1) * 2048 + (xcd & 3) * 512 + 256 + (ql & 1) * 128), mk_rsrc(W, D_DIM * 3 * D_DIM * 2), 0);
      prefill(wid);
    }
    if (idx < 8) {
      if (wid == 0)
        bstore_sc1<0>(u32x4{0u, 0u, 0u, 0u}, (unsigned)(lane * 128), mk_rsrc(flags + (FL_O + v * ((FL_S - FL_O) / 64)) * 32, (FL_S - FL_O) / 64 * 128), 0);
      if (NDLY) __builtin_amdgcn_s_sleep(NDLY);
      mod_gemv<2, PFF ? 1 : 13>(smem_base + P_RED + wid * 512, lane, 2048 + (xcd * 8 + wid) * 64, idx * 128, c, wmod, mid);
      const float sum = red_sum<2>(smem_base + P_RED + wid * 512, lane);
      asm volatile("buffer_store_dword %1, %2, %3, 0 offen sc1\n\ts_waitcnt vmcnt(1)" : "+v"(seen) : "v"(sum), "v"(lane * 4), "s"(mk_rsrc(modp + 16384 + ((xcd * 8 + wid) * 8 + idx) * 64, 256)) : "memory");
    } else if (!master) {
      if (TWIDE) {
        if (TDLY) __builtin_amdgcn_s_sleep(TDLY);
        const unsigned vt = (unsigned)(((wid * 7 + idx - 8) * 2 + (lane >> 5)) * 2048 + (xcd & 3) * 512 + (lane & 31) * 16);
#pragma unroll 4
        for (int q = 0; q < TWIDE; ++q) bload_lds<0>(smem_base + QX_SINK, vt, mk_rsrc(W, D_DIM * 3 * D_DIM * 2), q * 112 * 2048);
      }
#pragma unroll
      for (int q = 0; q < (TWIDE ? 0 : PFF ? PFF : NTQ); ++q) {
        if (TDLY && q == TQ0) __builtin_amdgcn_s_sleep(TDLY);
        const int ql = ((q * 8 + wid) * 7 + idx - 8) * 64 + lane + (PFF ? 224 * 12 : 0);
        touch_lds(smem_base + QX_SINK, (unsigned)((ql >> 2) * 2048 + (xcd & 3) * 512 + (ql & 3) * 128), mk_rsrc(W, D_DIM * 3 * D_DIM * 2), 0);
      }
      __builtin_amdgcn_s_barrier();
      wait_vm<TWIDE ? TWIDE : PFF ? PFF : NTQ>();
      mid();
      asm volatile("s_waitcnt vmcnt(%1)" : "+v"(seen) : "n"(PFF ? 0 : 12) : "memory");
    } else {
      STAMP(4, 0);
      wait_vm<0>();
      __builtin_amdgcn_s_barrier();
      if (FULL & 1) bstore_sc1<0>(u32x4{1u, 1u, 1u, 1u}, (unsigned)(wid * 1024 + lane * 16), mk_rsrc(flags + (FL_S + xcd * 64) * 32, 64 * 128), 0);
      unsigned spins = 0;
      if (wid == 7) {
        STAMPT(4, 1, 448);
#pragma unroll 1
        do set_words(flags, lane); while (!words_set(word, lane) && ++spins < (1u << 20));
        STAMPT(4, 4, 448);
      }
      __builtin_amdgcn_s_barrier();
      prefill(wid);
      if (wid == 7) {
#pragma unroll 1
        do set_words(flags, lane); while (__builtin_amdgcn_ballot_w64(cnt_read(flags + FL_M * 32, lane, 16) < (lane < 16 ? 8u : 0u)) != 0ull && ++spins < (1u << 20));
        STAMPT(4, 2, 448);
      }
      seen = 1;
    }
    STAMPT(1, 2, 448);
    if (wid != 7) wait_vm<0>();
    else if ((__builtin_amdgcn_ballot_w64(seen == 0u) & 255u) != 0u) confirm(word, lane);
    STAMP(4, 3);
  }
}
DI void wo_piece(unsigned smem_base, unsigned p, unsigned rot, unsigned voff, rsrc_t rs) {
  const unsigned pb = p >> 2, kb = (pb & 24u) | ((pb + rot) & 7u);
  bload_lds<0>(smem_base + (p < 64u ? QX_WH + p * 1024u : (p - 64u) * 1024u), voff, rs, kb * 65536u + (p & 3u) * 16384u);
}

DI void gu_touch(unsigned smem_base, int e, unsigned vgu, const bf16* wg, const bf16* wu, bool nt) {
  if (nt) touch_nt(smem_base + QX_SINK, vgu, mk_rsrc((e & 1) ? wu : wg, D_DIM * F_DIM * 2), (unsigned)(e >> 1) * (T24 == 4 ? 146u : 256u) * (F_DIM * 2));
  else touch_lds(smem_base + QX_SINK, vgu, mk_rsrc((e & 1) ? wu : wg, D_DIM * F_DIM * 2), (unsigned)(e >> 1) * (T24 == 4 ? 146u : 256u) * (F_DIM * 2));
}
DI void qkv_ldw(unsigned smem_base, int lane, int sw, int n0, rsrc_t rsW, unsigned so, unsigned u) {
#pragma unroll
  for (int i = 0; i < 4; ++i) {
    const int r = (i & 1) * 8 + (lane >> 3), ch = lane & 7;
    const int g = ((r >> 1) & 1) | (((r >> 3) & 1) << 1);
    bload_lds<0>(smem_base + 9216 + sw * 4096 + so + i * 1024, (unsigned)(r * 6144 + sw * 2048 + (n0 + (((ch >> 1) ^ g) * 16) + (ch & 1) * 8) * 2), rsW,
                 u * 196608u + (i >> 1) * 98304u);
  }
}
DI void qkv_loader(unsigned smem_base, int lane, int sw, int m0, int n0, const bf16* H, rsrc_t rsW,
                   unsigned rot, unsigned voffO, rsrc_t rsO) {
  const rsrc_t rsH = mk_rsrc(H, S_LEN * D_DIM * 2);
  int fb0 = (m0 >> 4) - 1 + 3 * sw; fb0 = fb0 < 0 ? 0 : fb0;
  const unsigned voffH0 = (unsigned)(fb0 * 32768 + lane * 16);
  const unsigned voffH1 = (unsigned)(((m0 >> 4) + 3 * sw) * 32768 + lane * 16);
  const unsigned m0a = smem_base + sw * 3072;
  auto ldh = [&](unsigned so, unsigned u) {
    bload_lds_sc1<0>(m0a + so, voffH0, rsH, u * 1024u);
    bload_lds_sc1<0>(m0a + so + 1024, voffH1, rsH, u * 1024u);
    bload_lds_sc1<0>(m0a + so + 2048, voffH1, rsH, u * 1024u + 32768u);
  };
  auto ldw = [&](unsigned so, unsigned u) { qkv_ldw(smem_base, lane, sw, n0, rsW, so, u); };
  asm volatile("" ::"v"(voffH0), "v"(voffH1), "s"(rsH), "s"(m0a));
  __builtin_amdgcn_s_barrier();
#pragma unroll
  for (int u = 1; u <= NP1; ++u) ldh(u * Q_SLOT, u);
  wait_vm<3 * (NP1 - 1)>();
  __builtin_amdgcn_s_barrier();
#pragma unroll
  for (int u = NP1 + 1; u < Q_NSL; ++u) ldh(u * Q_SLOT, u);
  static_for<Q_NSL - 2, 1>([&](auto U) {
    constexpr int u = U.value;
    ldh((u - 1) * Q_SLOT, u + Q_NSL - 1); ldw((u - 1) * Q_SLOT, u + Q_NSL - 1);
    wait_vm<3 * (Q_NSL - 2 - u) + 7 * u>();
    __builtin_amdgcn_s_barrier();
  });
  unsigned sb = (Q_NSL - 3) * Q_SLOT;
  for (int u = Q_NSL - 2; u < 32 - (Q_NSL - 1); ++u) {
    ldh(sb, u + Q_NSL - 1); ldw(sb, u + Q_NSL - 1);
    sb = (sb == (Q_NSL - 1) * Q_SLOT) ? 0u : sb + Q_SLOT;
    wait_vm<(Q_NSL - 2) * 7>();
    __builtin_amdgcn_s_barrier();
  }
  for (int t = 0; t < 6; ++t) {
    const unsigned p0 = (unsigned)(t * 21 + (t >= 3 ? 1 : 0) + sw * 7);
#pragma unroll
    for (int i = 0; i < 7; ++i) wo_piece(smem_base, p0 + i, rot, voffO, rsO);
    if (t < 5) wait_vm<(Q_NSL - 2) * 7>();
    __builtin_amdgcn_s_barrier();
  }
}

template <int WM>
DI void qkv_mfma(unsigned smem_base, int lane, int wn, int bm, int bn, const bf16* H, bf16* __restrict__ O) {
  const int fr = lane & 15, fq = lane >> 4;
  unsigned bb0, bb1;
  const unsigned ab = smem_base + lane * 16;
  {
    const int q = (lane >> 2) & 3, p = lane & 3;
    const int r = 8 * fq + q;
    const int g = ((r >> 1) & 1) | (((r >> 3) & 1) << 1);
    bb0 = smem_base + 9216 + r * 128 + (((wn * 2) ^ g) * 32) + 8 * p;
    bb1 = bb0 ^ 32u;
  }
  const bool par = fr & 1, c0 = 4 * fq + (fr & 1) <= fr, c1 = 4 * fq + 2 + (fr & 1) <= fr;
  const unsigned s0 = par ? 0x01000c0cu : 0x0c0c0100u, s1 = par ? 0x03020c0cu : 0x0c0c0302u;
  u32x4 sel = {c0 ? s0 : 0x0c0c0c0cu, c1 ? s1 : 0x0c0c0c0cu, c0 ? 0x0c0c0c0cu : s0, c1 ? 0x0c0c0c0cu : s1};
  unsigned hx = ab + QX_HX + wn * 3072;
  asm volatile("" : "+v"(sel), "+v"(hx));
  const rsrc_t rsO = mk_rsrc((char*)O + (size_t)((bm * 2 + WM) * 32 + bn * 2 + wn) * 4096, 4096);
  f32x4 acc[4][6], acch[2];
#pragma unroll
  for (int i = 0; i < 4; ++i)
#pragma unroll
    for (int j = 0; j < 6; ++j) { acc[i][j] = f32x4{0, 0, 0, 0}; asm volatile("" : "+v"(acc[i][j])); }
#pragma unroll
  for (int j = 0; j < 2; ++j) { acch[j] = f32x4{0, 0, 0, 0}; asm volatile("" : "+v"(acch[j])); }
  __builtin_amdgcn_s_setprio(2);
  u32x4 aX[5], aY[5];
  u32x2 bXl[6], bXh[6], bYl[6], bYh[6];
  unsigned ra = ab, rb0 = bb0, rb1 = bb1, so = 0;
#define Q_READ(AR, BL, BH, R)                                                                                    \
  do {                                                                                                           \
    if ((R) < 12) {                                                                                              \
      constexpr int jb = ((R) < 12 ? (R) : 0) >> 1;                                                              \
      if ((R) & 1) dsr_tr<(jb >> 1) * 4096 + 512>(BH[jb], (jb & 1) ? rb1 : rb0);                                 \
      else dsr_tr<(jb >> 1) * 4096>(BL[jb], (jb & 1) ? rb1 : rb0);                                               \
    } else if ((R) < 16) {                                                                                       \
      constexpr int ia = ((R) >= 12 && (R) < 16) ? (R) - 12 : 0;                                                 \
      dsr128<1024 + WM * 4096 + ia * 1024>(AR[ia], ra);                                                          \
    } else {                                                                                                     \
      dsr128<0>(AR[4], ra);                                                                                      \
    }                                                                                                            \
  } while (0)
#define Q_MFMA(AR, BB, N)                                                                                        \
  do {                                                                                                           \
    if ((N) < 16) MFMA_AV(acc[(N) & 3][((N) < 24 ? (N) : 0) >> 2], BB[((N) < 24 ? (N) : 0) >> 2], AR[(N) & 3]);  \
    else if ((N) < 24) MFMA_AV(acc[(N) & 3][((N) < 24 ? (N) : 0) >> 2], AR[(N) & 3], BB[((N) < 24 ? (N) : 0) >> 2]); \
    else if (WM == 0) MFMA_AV(acch[(N) & 1], BB[2 + ((N) & 1)], AR[4]);                                          \
    else MFMA_AV(acch[(N) & 1], AR[4], BB[4 + ((N) & 1)]);                                                       \
  } while (0)
#define Q_ADV(K)                                                                                                 \
  do {                                                                                                           \
    if ((K) == 0) asm volatile("s_add_u32 %0, %0, %1\n\ts_cmp_eq_u32 %0, %2\n\ts_cselect_b32 %0, 0, %0" : "+s"(so) : "n"(Q_SLOT), "n"(Q_NSL * Q_SLOT) : "scc"); \
    else if ((K) == 1) asm volatile("v_add_u32 %0, %1, %2" : "=v"(rb0) : "s"(so), "v"(bb0));                     \
    else if ((K) == 2) asm volatile("v_add_u32 %0, %1, %2" : "=v"(rb1) : "s"(so), "v"(bb1));                     \
    else asm volatile("v_add_u32 %0, %1, %2" : "=v"(ra) : "s"(so), "v"(ab));                                     \
  } while (0)
#define Q_UNIT(AR, BL, BH, NAR, NBL, NBH)                                                                        \
  do {                                                                                                           \
    WAIT_LGKM0();                                                                                                \
    u32x4 bb[6];                                                                                                 \
    _Pragma("unroll") for (int j = 0; j < 6; ++j) bb[j] = u32x4{BL[j][0], BL[j][1], BH[j][0], BH[j][1]};         \
    SCHED0();                                                                                                    \
    static_for<26>([&](auto N) {                                                                                 \
      constexpr int n = N.value;                                                                                 \
      if constexpr (n == QBAR) __builtin_amdgcn_s_barrier();                                                     \
      Q_MFMA(AR, bb, n);                                                                                         \
      if constexpr (n >= QBAR && n < QBAR + 17) Q_READ(NAR, NBL, NBH, n - QBAR);                                 \
      if constexpr (n >= QBAR + 18 && n < QBAR + 22) Q_ADV(n - QBAR - 18);                                       \
    });                                                                                                          \
    SCHED0();                                                                                                    \
  } while (0)
  {
    const rsrc_t rsH = mk_rsrc(H, S_LEN * D_DIM * 2);
    unsigned vh = lane * 16 + (bm * 8 + WM * 4) * 32768, vl = lane * 16 + (bm ? bm * 8 - 1 : 0) * 32768;
    asm volatile("" : "+v"(vh), "+v"(vl), "+v"(rb0), "+v"(rb1) : "s"(rsH));
    __builtin_amdgcn_s_barrier();
    static_for<5>([&](auto I) { asm volatile("" : "=v"(aX[I.value])); bload_sc1<0>(aX[I.value], I.value < 4 ? vh : vl, rsH, (I.value & 3) * 32768); });
    static_for<12>([&](auto R) { Q_READ(aX, bXl, bXh, R.value); });
    static_for<4>([&](auto K) { Q_ADV(K.value); });
    asm volatile("s_waitcnt vmcnt(0)" : "+v"(aX[0]), "+v"(aX[1]), "+v"(aX[2]), "+v"(aX[3]), "+v"(aX[4])::"memory");
  }
  STAMPDECL;
  STAMPNW(2, 1);
  for (int u = 0; u < 32; u += 2) {
    Q_UNIT(aX, bXl, bXh, aY, bYl, bYh);
    Q_UNIT(aY, bYl, bYh, aX, bXl, bXh);
  }
  asm volatile("s_waitcnt lgkmcnt(0)\n\ts_nop 7" ::: "memory");
#pragma unroll
  for (int i = 0; i < 5; ++i) asm volatile("" : "+v"(aX[i]));
#pragma unroll
  for (int j = 0; j < 6; ++j) asm volatile("" : "+v"(bXl[j]), "+v"(bXh[j]));
#pragma unroll
  for (int i = 0; i < 4; ++i)
#pragma unroll
    for (int j = 0; j < 6; ++j) asm volatile("" : "+v"(acc[i][j]));
  asm volatile("" : "+v"(acch[0]), "+v"(acch[1]));
  SCHED0();
  STAMP(2, 2); STAMPFLUSH(2, 1);
  u32x4 kk[5], vv[5];
  if (WM == 0) {
    kk[4] = pack8(acc[3][2], acc[3][3]); vv[4] = pack8(acc[3][4], acc[3][5]);
    dsw128<0>(hx, kk[4]); dsw128<1024>(hx, vv[4]);
  } else {
    dsw128<2048>(hx, pack8(acch[0], acch[1]));
  }
  asm volatile("s_waitcnt lgkmcnt(0)" ::: "memory");
  __builtin_amdgcn_s_barrier();
  if (WM == 0) dsr128<2048>(vv[0], hx);
  else { dsr128<0>(kk[0], hx); dsr128<1024>(vv[0], hx); }
  SCHED0();
  if (WM == 0) kk[0] = pack8(acch[0], acch[1]);
#pragma unroll
  for (int i = 0; i < (WM == 0 ? 3 : 4); ++i) kk[i + 1] = pack8(acc[i][2], acc[i][3]);
  const float cs = 0.17677669529663687f * 1.4426950408889634f;
  float l0[4], l1[4], tm[4];
#pragma unroll
  for (int ii = 1; ii < 5; ++ii) {
    const int i = ii & 3;
    const u32x4 q = pack8(acc[i][0], acc[i][1]);
    if (WM == 1 && i == 0) asm volatile("s_waitcnt lgkmcnt(0)" : "+v"(kk[0]), "+v"(vv[0])::"memory");
    const f32x4 sc = mfma0(kk[i + 1], q), sp = mfma0(kk[i], q);
    float p0 = par ? sp[1] : sp[0], p1 = par ? sp[3] : sp[2];
    if (WM == 0 && i == 0 && bm == 0) { p0 = -5.656854249e9f; p1 = -5.656854249e9f; }
    l0[i] = c0 ? (par ? sc[1] : sc[0]) : p0;
    l1[i] = c1 ? (par ? sc[3] : sc[2]) : p1;
    tm[i] = fmax2(l0[i], l1[i]);
  }
#pragma unroll
  for (int i = 0; i < (WM == 0 ? 3 : 4); ++i) vv[i + 1] = pack8(acc[i][4], acc[i][5]);
  bcast4(rows4(tm, [](float a, float b) { return fmax2(a, b); }), tm);
#pragma unroll
  for (int i = 0; i < 4; ++i) {
    l0[i] = __builtin_amdgcn_exp2f((l0[i] - tm[i]) * cs);
    l1[i] = __builtin_amdgcn_exp2f((l1[i] - tm[i]) * cs);
    tm[i] = l0[i] + l1[i];
  }
  bcast4(__builtin_amdgcn_rcpf(rows4(tm, [](float a, float b) { return a + b; })), tm);
  if (WM == 0) asm volatile("s_waitcnt lgkmcnt(0)" : "+v"(vv[0])::"memory");
#pragma unroll
  for (int i = 0; i < 4; ++i) {
    const unsigned pp = pk_bf16(l0[i] * tm[i], l1[i] * tm[i]);
    const u32x4 pr = {__builtin_amdgcn_perm(pp, pp, sel[0]), __builtin_amdgcn_perm(pp, pp, sel[1]),
                      __builtin_amdgcn_perm(pp, pp, sel[2]), __builtin_amdgcn_perm(pp, pp, sel[3])};
    const u32x4 ov = pack8(mfma0(u32x4{vv[i + 1][0], vv[i + 1][1], vv[i][0], vv[i][1]}, pr), mfma0(u32x4{vv[i + 1][2], vv[i + 1][3], vv[i][2], vv[i][3]}, pr));
    asm volatile("buffer_store_dwordx4 %0, %1, %2, 0 offen offset:%3 sc1\n\ts_nop 1" ::"v"(ov), "v"(lane * 16), "s"(rsO), "i"(i * 1024) : "memory");
  }
  STAMP(2, 3);
}

template <class GST>
DI void attn_phase(unsigned smem_base, int wid, bf16* H, const bf16* __restrict__ W, bf16* O,
                   const bf16* __restrict__ wo, const bf16* __restrict__ X, float* modp, bf16* Y, float* stat,
                   unsigned* flags, const bf16* __restrict__ wg, const bf16* __restrict__ wu, float* xbuf,
                   unsigned* ctab, const bf16* __restrict__ cnd, const bf16* __restrict__ wmod, rsrc_t rsC, GST gst) {
  const int tid = threadIdx.x, lane = tid & 63;
  const int xcd = blockIdx.x & 7, idx = blockIdx.x >> 3;
  const int bm = (xcd >> 2) * 8 + (idx & 7), bn = (xcd & 3) * 4 + (idx >> 3);
  const int rb = xcd * 2 + (idx & 1), cg = idx >> 1;
  const unsigned rot = __builtin_amdgcn_readfirstlane(GMAP ? (cg & 4) : (cg >> 2) * 2);
  const unsigned cnt = smem_base + QX_CNT, cntl = cnt + lane * 4;
  const rsrc_t rsW = mk_rsrc(W, D_DIM * 3 * D_DIM * 2);
  first_phase(smem_base, wid, lane, X, cnd, wmod, W, modp, ctab, H, flags, [&](int w) {
#pragma unroll
    for (int i = 0; i < 3; ++i) {
      const int p = w + 8 * i < 21 ? w + 8 * i : 20, u = (p * 11) >> 5;
      qkv_ldw(smem_base, lane, p - 3 * u, bn * 64, rsW, u * Q_SLOT, u);
    }
  });
  if (wid < 4) {
    if (wid & 1) qkv_mfma<1>(smem_base, lane, wid >> 1, bm, bn, H, O);
    else qkv_mfma<0>(smem_base, lane, wid >> 1, bm, bn, H, O);
  } else {
    const rsrc_t rsO = mk_rsrc(wo, D_DIM * D_DIM * 2);
    const unsigned voffO = (unsigned)((lane >> 3) * 2048 + cg * 128 + ((((lane & 7) >> 1) ^ (lane >> 4)) * 32) + (lane & 1) * 16);
    if (wid < 7) qkv_loader(smem_base, lane, wid - 4, bm * 128, bn * 64, H, rsW, rot, voffO, rsO);
    else {
  const unsigned vgu = idx < 16 ? 0x40000000u : (unsigned)((((idx - 16) * 64 + lane) / 7) * (F_DIM * 2) + xcd * 896 + (((idx - 16) * 64 + lane) % 7) * 128);
  const int ql = idx * 64 + lane;
  const unsigned vq = idx >= 24 ? 0x40000000u : (unsigned)((ql / 12) * 6144 + ((ql % 12) >> 2) * 2048 + (xcd & 3) * 512 + (ql & 3) * 128);
  auto q_touch = [&](unsigned j) { touch_lds(smem_base + QX_SINK, vq, mk_rsrc(W, D_DIM * 3 * D_DIM * 2), j * 786432u); };
      const poll_t ph = poll(0u, flags + (FL_H + bm * 4 + (xcd & 3)) * 32, lane, 1, bm ? 9u : 8u);
      STAMPT(1, 6, 448);
      asm volatile("s_barrier" ::: "memory");
      SCHED0();
      unsigned* r1 = flags + (FL_M + (1 + xcd) * 32 + 24) * 32;
      const sig_t gi = drain(idx < 8 ? r1 : flags + (FL_M + 24 + xcd) * 32, lane, idx < 16 ? 1 : 0, 4);
      poll_end(ph);
      signal(gi);
      if (idx < 8) {
        const int v = xcd * 8 + idx;
        unsigned t = 0;
        for (int i = 0; i < 4; ++i) __builtin_amdgcn_s_barrier();
        asm volatile("buffer_atomic_add %0, %1, %2, 0 offen sc0 sc1\n\ts_barrier\n\ts_barrier\n\ts_barrier\n\ts_barrier\n\ts_waitcnt vmcnt(0)" : "+v"(t) : "v"(lane * 128), "s"(mk_rsrc(r1, 128)) : "memory");
#pragma unroll 1
        for (unsigned spins = 0; __builtin_amdgcn_readfirstlane(t) < 8u && spins < (1u << 20); ++spins) t = cnt_read(r1, lane, 1);
        static_for<2>([&](auto I) { bload_lds_sc1<I.value * 1024>(smem_base + G3_CT, lane * 16, mk_rsrc(modp + 16384 + v * 512, 2048), 0); });
        for (int i = 0; i < 4; ++i) __builtin_amdgcn_s_barrier();
        wait_vm<0>();
        float sum = red_sum<8>(smem_base + G3_CT, lane);
        sum = (v >> 4) == 2 ? gain_nz(sum) : rbf(sum);
        asm volatile("buffer_store_dword %0, %1, %2, 0 offen sc1" ::"v"(sum), "v"(lane * 4), "s"(mk_rsrc(modp + 49152 + v * 64, 256)) : "memory");
        asm volatile("buffer_store_short_d16_hi %0, %1, %2, 0 offen sc1" ::"v"(sum), "v"((lane >> 5) * 128 + (lane & 31) * 2),
                     "s"(mk_rsrc(ctab + (v & 15) * 64 + (v & 16), (v >> 4) == 1 || (v >> 4) == 2 ? 256 : 0)) : "memory");
        for (int i = 0; i < 4; ++i) __builtin_amdgcn_s_barrier();
        signal(drain(flags + (FL_M + 16 + idx) * 32, lane, 9, 4096));
      }
      {
        int nxt = TS + idx % TP, ev = 0;
        const rsrc_t rsE = mk_rsrc((char*)xbuf + (size_t)blockIdx.x * 32768, 32768);
        int qj = QT0;
#pragma unroll 1
        for (int i = idx < 8 ? 16 : 0; i < 27; ++i) {
          if (G4S == 2 && i >= SM0 && i < SM0 + 32 / SMN) {
#pragma unroll
            for (int j = 0; j < SMN; ++j) bstore_mark(u32x4{~0u, ~0u, ~0u, ~0u}, lane * 16, rsE, (unsigned)((i - SM0) * SMN + j) * 1024u);
          }
          if (QT0 < 8 && i >= QS0 && (i - QS0) % QP == 0) {
#pragma unroll
            for (int j = 0; j < QN; ++j) { if (qj < 8) q_touch(qj); ++qj; }
          }
          if (i == WOS && idx >= 16) {
            touch_lds(smem_base + QX_SINK, (blockIdx.x - 128) * 16384 + lane * 128, rsO, 0);
            touch_lds(smem_base + QX_SINK, (blockIdx.x - 128) * 16384 + lane * 128, rsO, 8192);
          }
          if (CT2 && i == CT2) touch_lds(smem_base + QX_SINK, lane * 32 + idx * 2048u + 8192u, rsC, 0);
          if (TW > 0 && i == nxt && ev < TN) {
#pragma unroll
            for (int j = 0; j < TW; ++j) gu_touch(smem_base, ev * TW + j, vgu, wg, wu, GNT & 1);
            nxt += TP; ++ev;
          }
          __builtin_amdgcn_s_barrier();
        }
      }
      unsigned* cr = flags + (FL_M + (idx == 15 ? 0 : 1 + xcd) * 32 + 16) * 32;
      const int nl = idx == 15 ? 16 : 8;
      unsigned t = 0;
      asm volatile("buffer_atomic_add %0, %1, %2, 0 offen sc0 sc1" : "+v"(t) : "v"(lane * 128), "s"(mk_rsrc(cr, nl * 128)) : "memory");
      wo_piece(smem_base, 63u, rot, voffO, rsO);
      __builtin_amdgcn_s_barrier();
      __builtin_amdgcn_s_barrier();
      asm volatile("s_waitcnt vmcnt(1)" : "+v"(t)::"memory");
      for (unsigned spins = 0; __builtin_amdgcn_ballot_w64(t < (lane < nl ? 8u : 0u)) != 0ull && spins < (1u << 20); ++spins) {
        if (idx == 15) set_words(flags, lane);
        t = cnt_read(cr, lane, nl);
      }
      static_for<4>([&](auto I) { bload_lds_sc1<I.value * 1024>(smem_base + G3_CT, lane * 16, mk_rsrc(ctab, 4096), 0); });
#pragma unroll
      for (int i = 0; i < 2; ++i)
        asm volatile("s_mov_b32 m0, %0\n\ts_nop 0\n\tbuffer_load_dword %1, %2, 0 offen sc1 lds" ::"s"(smem_base + QX_GT + i * 256), "v"(lane * 4),
                     "s"(mk_rsrc(modp + 49152 + i * 2048 + cg * 64, 256)) : "memory");
      for (int i = 30; i < 33; ++i) __builtin_amdgcn_s_barrier();
      wo_piece(smem_base, 127u, rot, voffO, rsO);
    }
    __builtin_amdgcn_s_barrier();
    wait_vm<0>();
    lds_add(cntl, 1u);
  }
  const int fr = lane & 15, fq = lane >> 4;
  const int kq = wid & 3, rg = rb * 2 + (wid >> 2);
  const int ko = GROT ? (kq + cg) & 3 : kq;
  lds_wait<2>(cnt, 4);
  u32x2 wl[8][4], wh[8][4];
  {
    const int q = (lane >> 2) & 3, p = lane & 3, r = 4 * fq + q;
    const unsigned wb = smem_base + (kq < 2 ? QX_WH + kq * 32768 : (kq - 2) * 32768) + r * 128 + (((r >> 1) & 3) * 32) + 8 * p;
    unsigned wa[4];
#pragma unroll
    for (int f = 0; f < 4; ++f) wa[f] = wb ^ (f * 32);
    static_for<32>([&](auto E) {
      constexpr int s = E.value >> 2, f = E.value & 3;
      dsr_tr<s * 4096>(wl[s][f], wa[f]);
      dsr_tr<s * 4096 + 2048>(wh[s][f], wa[f]);
    });
  }
  const rsrc_t rsA = mk_rsrc((const char*)O + (size_t)rg * 131072, 131072);
  unsigned voffA[4], hs[8];
#pragma unroll
  for (int i = 0; i < 4; ++i) { voffA[i] = lane * 16 + ((ko + 1 + i) & 3) * 1024; asm volatile("" : "+v"(voffA[i])); }
#pragma unroll
  for (int i = 0; i < 8; ++i) { hs[i] = __builtin_amdgcn_readfirstlane((kq * 8 + ((i + rot) & 7)) * 4096); asm volatile("" : "+s"(hs[i])); }
  asm volatile("" ::"s"(rsA));
  if (wid < 4) {
    const sig_t g = drain(flags + (FL_O + bm * 8 + (bn & 1)) * 32, lane, 4, 256);
    if (lds_add(cntl + 256, 1u) == 3u) signal(g);
    STAMP(2, 4);
  }
  const poll_t pl = poll(wid - 7, flags + (FL_O + rb * 8 + (cg & 3) * 2) * 32, lane, 2, 8u);
  asm volatile("s_waitcnt lgkmcnt(0)" ::: "memory");
  __builtin_amdgcn_s_barrier();
  u32x4 wf[8][4];
#pragma unroll
  for (int s = 0; s < 8; ++s)
#pragma unroll
    for (int f = 0; f < 4; ++f) {
      asm volatile("" : "+v"(wl[s][f]), "+v"(wh[s][f]));
      wf[s][f] = u32x4{wl[s][f][0], wl[s][f][1], wh[s][f][0], wh[s][f][1]};
    }
  STAMP(3, 0);
  const unsigned voffL = lane * 16;
  u32x4 af[32], xv[2], pr[3][4], gt[4], ct[4];
  f32x4 acc[4][4];
  auto aload = [&](auto N) {
    constexpr int n = N.value;
    asm volatile("" : "=v"(af[n]));
    bload_sc1<0>(af[n], voffA[n >> 3], rsA, hs[n & 7]);
  };
  static_for<GDEP>([&](auto N) { aload(N); });
  const unsigned sl = smem_base + 49152 + voffL + (wid >> 2) * 49152;
  const int sw = ((fq & 1) << 1) | (fq >> 1);
  SCHED0();
  static_for<32>([&](auto N) {
    constexpr int n = N.value, ps = n >> 3, s = n & 7, XG = 32 - GDEP;
    constexpr int last = n - 1 + GDEP < 31 ? n - 1 + GDEP : 31;
    asm volatile("s_waitcnt vmcnt(%1)" : "+v"(af[n]) : "n"(last - n + (n > XG ? 2 : 0) + (n > GS0 + 3 ? 6 : (n > GS0 ? 2 * (n - GS0) : 0))) : "memory");
    if constexpr (n == 0) poll_end(pl);
    if constexpr (n == 28) { WAIT_LGKM0(); __builtin_amdgcn_s_barrier(); }
    static_for<4>([&](auto F) {
      constexpr int f = F.value;
      if constexpr (s == 0) asm volatile("v_mfma_f32_16x16x32_bf16 %0, %1, %2, 0" : "=&v"(acc[ps][f]) : "v"(wf[s][f]), "v"(af[n]));
      else MFMA_AV(acc[ps][f], wf[s][f], af[n]);
      if constexpr (f == 0 && n + GDEP < 32) aload(ic<(n + GDEP < 32 ? n + GDEP : 0)>{});
      if constexpr (f == 0 && n == XG) {
        const rsrc_t rsX = mk_rsrc(X, S_LEN * D_DIM * 2);
        const unsigned vx = (unsigned)(((rg * 64 + ko * 16 + fr) * D_DIM + cg * 64 + sw * 8) * 2);
        asm volatile("" : "=v"(xv[0]), "=v"(xv[1]));
        bload_v<0>(xv[0], vx, rsX, 0); bload_v<64>(xv[1], vx, rsX, 0);
      }
      if constexpr ((f & 1) && n >= GS0 && n < GS0 + 3) gst(ic<(n >= GS0 ? n - GS0 : 0)>{}, ic<(f >> 1)>{});
      if constexpr (ps > 0 && (s == 2 || s == 3) && (f & 1)) {
        constexpr int e = (s - 2) * 2 + (f >> 1);
        dsw128<(ps - 1) * 4096 + e * 1024>(sl + ((kq + ps) & 3) * 12288, __builtin_bit_cast(u32x4, acc[ps - 1][e]));
      }
      if constexpr (n >= 28 && n < 31) dsr128<(n - 28) * 4096 + f * 1024>(pr[n - 28][f], sl + kq * 12288);
      if constexpr (n == 31) { dsr128<f * 64>(gt[f], smem_base + QX_GT + fq * 16); dsr128<256 + f * 64>(ct[f], smem_base + QX_GT + fq * 16); }
    });
  });
  asm volatile("s_waitcnt vmcnt(6)" : "+v"(xv[0]), "+v"(xv[1])::"memory");
  asm volatile("s_waitcnt lgkmcnt(0)\n\ts_nop 4" ::: "memory");
  STAMP(3, 2);
  f32x4 fin[4], yv[4];
#pragma unroll
  for (int f = 0; f < 4; ++f) asm volatile("" : "+v"(acc[3][f]), "+v"(pr[0][f]), "+v"(pr[1][f]), "+v"(pr[2][f]), "+v"(gt[f]), "+v"(ct[f]));
#pragma unroll
  for (int c = 0; c < 2; ++c) {
    const auto x0 = __builtin_amdgcn_permlane16_swap(xv[c][0], xv[c][2], false, false);
    const auto x1 = __builtin_amdgcn_permlane16_swap(xv[c][1], xv[c][3], false, false);
#pragma unroll
    for (int h = 0; h < 2; ++h) {
      const int f = 2 * c + h;
      const f32x4 a = (acc[3][f] + __builtin_bit_cast(f32x4, pr[0][f])) + (__builtin_bit_cast(f32x4, pr[1][f]) + __builtin_bit_cast(f32x4, pr[2][f]));
      const f32x4 xf = {bf_lo(x0[h]), bf_hi(x0[h]), bf_lo(x1[h]), bf_hi(x1[h])};
      fin[f] = xf + __builtin_bit_cast(f32x4, gt[f]) * a;
      yv[f] = fin[f] * __builtin_bit_cast(f32x4, ct[f]);
    }
    const auto y0 = __builtin_amdgcn_permlane16_swap(pk_bf16(yv[2 * c][0], yv[2 * c][1]), pk_bf16(yv[2 * c + 1][0], yv[2 * c + 1][1]), false, false);
    const auto y1 = __builtin_amdgcn_permlane16_swap(pk_bf16(yv[2 * c][2], yv[2 * c][3]), pk_bf16(yv[2 * c + 1][2], yv[2 * c + 1][3]), false, false);
    gst128((char*)Y + ((size_t)(rg >> 2) * 32 + cg * 2 + c) * 16384 + ((rg & 3) * 4 + ko) * 1024 + (sw * 16 + fr) * 16,
           u32x4{y0[0], y1[0], y0[1], y1[1]});
  }
  const f32x4 s4 = (fin[0] + fin[1]) + (fin[2] + fin[3]);
  const float s = rowsum4((s4[0] + s4[1]) + (s4[2] + s4[3]));
  const float mb = s * (1.0f / 64.0f);
  f32x4 q4 = {0, 0, 0, 0};
#pragma unroll
  for (int f = 0; f < 4; ++f) { const f32x4 d = fin[f] - mb; q4 += d * d; }
  const float m2 = rowsum4((q4[0] + q4[1]) + (q4[2] + q4[3]));
  asm volatile("buffer_store_dwordx2 %0, %1, %2, 0 offen sc1" ::"v"(f32x2{s, m2}), "v"(lane * 8),
               "s"(mk_rsrc(stat + ((size_t)cg * S_LEN + rg * 64 + ko * 16) * 2, 128)) : "memory");
  STAMP(3, 3);
}

#define G3NT 1
#define TPOS 6
#define SPOS 9
#define NBC 1
#define G3_STAGE(SLOT, P, U)                                                                               \
  do {                                                                                                     \
    if ((P) == 0) bload_lds_sc1<0>(m0w + 65536 + (SLOT) * 16384, voffA, rsA, (unsigned)(U) * 16384u);      \
    else if ((P) == 1) bload_lds_sc1<1024>(m0w + 65536 + (SLOT) * 16384, voffA, rsA, (unsigned)(U) * 16384u);  \
    else if ((P) == 2) bload_lds_nt<0>(m0w + (SLOT) * 16384, voffB0, rsB, (unsigned)(U) * 229376u);        \
    else bload_lds_nt<0>(m0w + (SLOT) * 16384 + 1024, voffB1, rsB, (unsigned)(U) * 229376u);               \
  } while (0)

#define G3ROT 1
DI int g3_line(int gx, int p) {
  const int t = p + (G3ROT ? (3 + gx) & 7 : 0);
  return 7 * gx + (t >= 7 ? t - 7 : t);
}
template <int FN, class WST, class HOOK, class PRE>
DI void g3_body(char* smem, unsigned smem_base, int wm, int wn, int wid, int lane, int bm, int bn, rsrc_t rsA, rsrc_t rsB,
                rsrc_t rsP, rsrc_t rsE, unsigned voffA, unsigned voffB0, unsigned voffB1, bf16* act, WST wst, HOOK hook, PRE pre) {
  constexpr int NB = 2 * FN, NM = 4 * NB, RD = 5 + 2 * NB;
  int fr = lane & 15, fq = lane >> 4;
  f32x4 acc[4][NB], accC[2];
  accC[0] = f32x4{0, 0, 0, 0}; accC[1] = f32x4{0, 0, 0, 0};
  asm volatile("" : "+v"(accC[0]), "+v"(accC[1]));
  const int rot = wm % FN;
  unsigned cad = ZL && (fr >= 2 || wm >= FN) ? smem_base + X_CN + 1536 : smem_base + G3_CT + (fr & 1) * 64 + fq * 16;
  unsigned cin = ZL && (fr >= 2 || wm >= FN) ? 0u : 512u;
  if (ZL) asm volatile("" : "+v"(cin));
#pragma unroll
  for (int i = 0; i < 4; ++i)
#pragma unroll
    for (int j = 0; j < NB; ++j) { acc[i][j] = f32x4{0, 0, 0, 0}; asm volatile("" : "+v"(acc[i][j])); }
  unsigned aad = smem_base + 65536 + wm * 4096 + lane * 16;
  unsigned bad[FN];
  {
    const int q = (lane >> 2) & 3, p = lane & 3;
    const int r = 8 * fq + q;
    const int sw = (r & 3) | (((r >> 3) & 1) << 2);
#pragma unroll
    for (int j = 0; j < FN; ++j) {
      const int gi = wn * 4 + (j + rot) % FN + (bn & 1);
      bad[j] = smem_base + r * 256 + ((gi ^ sw) * 32) + 8 * p;
    }
  }
  u32x4 aX[5], aY[5];
  u32x2 bXl[NB], bXh[NB], bYl[NB], bYh[NB];
  const unsigned m0w = smem_base + wid * 2048;
  const unsigned voffP = lane * 16;
  u32x4 ones = {~0u, ~0u, ~0u, ~0u};
  if (G4S == 1 && FN == 3) asm volatile("" : "+v"(ones));
#define G3_READ(AR, BL, BH, SLOT, R)                                                                       \
  do {                                                                                                     \
    if ((R) < 2 * NB) {                                                                                    \
      constexpr int jb = ((R) < 2 * NB ? (R) : 0) >> 1;                                                    \
      constexpr int off = (SLOT) * 16384 + (jb / FN) * 8192 + ((R) & 1) * 1024;                            \
      if ((R) & 1) dsr_tr<off>(BH[jb], bad[jb % FN]);                                                       \
      else dsr_tr<off>(BL[jb], bad[jb % FN]);                                                               \
    } else if ((R) < 2 * NB + 4) {                                                                         \
      constexpr int ia = ((R) >= 2 * NB && (R) < 2 * NB + 4) ? (R) - 2 * NB : 0;                           \
      dsr128<(SLOT) * 16384 + ia * 1024>(AR[ia], aad);                                                      \
    } else {                                                                                               \
      if ((SLOT) == 0) cad += cin;                                       \
      dsr128<(SLOT) * 128>(AR[4], cad);                                                                    \
    }                                                                                                      \
  } while (0)
  auto rd = [&](auto SL, auto RR, u32x4 (&AR)[5], u32x2 (&BL)[NB], u32x2 (&BH)[NB]) { G3_READ(AR, BL, BH, SL.value, RR.value); };
#define G3_UNIT(AR, BL, BH, NAR, NBL, NBH, SLOT, U, LI)                                                    \
  do {                                                                                                     \
    WAIT_LGKM0();                                                                                          \
    u32x4 bb[NB];                                                                                          \
    _Pragma("unroll") for (int j = 0; j < NB; ++j) bb[j] = u32x4{BL[j][0], BL[j][1], BH[j][0], BH[j][1]};  \
    SCHED0();                                                                                              \
    asm volatile("s_nop 1");                                                                               \
    static_for<NM>([&](auto N) {                                                                           \
      constexpr int n = N.value;                                                                           \
      if constexpr (n == NM / 2 && !(LI && (SLOT) == 3)) {                                                 \
        wait_vm<(LI && (SLOT) == 0 && FN == 3) ? 25 : 8>(); __builtin_amdgcn_s_barrier();                  \
      }                                                                                                    \
      if constexpr (n == NM / 2 + TPOS && !LI) {                                                           \
                      \
        if (((U) & 7) == wid) bload_lds<0>(smem_base + 131072 + 4096, voffP, rsP, (unsigned)(U) * 1024u);  \
        if (CT3 && FN == 3 && (SLOT) == 0 && (U) * 8 + wid == CT3 * 8 + 7) touch_lds(smem_base + 131072 + 4096, voffP * 2 + (blockIdx.x >> 3) * 2048u + 8192u, rsE, 0);  \
      }                                                                                                    \
      if constexpr (G4S == 1 && FN == 3 && n == NM / 2 + SPOS) {                                               \
                                          \
        if ((((SLOT) + 2) & 3) == wm) bstore_mark(ones, voffP, rsE, (unsigned)(U) * 1024u);                \
      }                                                                                                    \
      MFMA_AV(acc[n & 3][n >> 2], bb[n >> 2], AR[n & 3]);                                                  \
      if constexpr (n < NM / 2) {                                                                          \
        if constexpr (LI && (SLOT) == 0 && n == 0) hook(ic<0>{});                                          \
        if constexpr (n % 3 == 1 && n / 3 < 4) {                                                           \
          if constexpr (LI && (SLOT) > 0) wst(ic<((SLOT) + 3) & 3>{}, ic<(n / 3) & 3>{});                  \
          else G3_STAGE(((SLOT) + 3) & 3, n / 3, (U) + 3);                                                 \
        }                                                                                                  \
      } else if constexpr (LI && (SLOT) == 3) {                                                            \
        if constexpr (n == NM / 2) hook(ic<1>{});                                                          \
      } else {                                                                                             \
        constexpr int r0 = (n - NM / 2) * 2;                                                               \
        if constexpr (r0 < RD) rd(ic<((SLOT) + 1) & 3>{}, ic<(r0 < RD ? r0 : 0)>{}, NAR, NBL, NBH);        \
        if constexpr (r0 + 1 < RD) rd(ic<((SLOT) + 1) & 3>{}, ic<(r0 + 1 < RD ? r0 + 1 : 0)>{}, NAR, NBL, NBH); \
      }                                                                                                    \
    });                                                                                                    \
    if (NBC || FN == 4 || wm < 3) {                                                                        \
      MFMA_AV(accC[0], bb[0], AR[4]);                                                                      \
      MFMA_AV(accC[1], bb[FN], AR[4]);                                                                     \
    }                                                                                                      \
    SCHED0();                                                                                              \
  } while (0)

  unsigned v0 = wm * 4096 + lane * 16;
#pragma unroll
  for (int j = 0; j < FN; ++j) asm volatile("" : "+v"(bad[j]));
  asm volatile("" : "+v"(aad), "+v"(cad), "+v"(voffA), "+v"(v0) : "s"(rsA), "s"(rsP), "s"(m0w));
  const poll_t pl = pre();
  STAMP(5, 0);
  static_for<4>([&](auto I) { asm volatile("" : "=v"(aX[I.value])); bload_sc1<I.value * 1024>(aX[I.value], v0, rsA, 0); });
  static_for<2 * NB>([&](auto RR) { rd(ic<0>{}, RR, aX, bXl, bXh); });
  dsr128<0>(aX[4], cad);
  G3_STAGE(1, 0, 1); G3_STAGE(1, 1, 1); G3_STAGE(2, 0, 2); G3_STAGE(2, 1, 2);
#pragma unroll
  for (int i = 0; i < 2; ++i) bload_lds<0>(smem_base + 131072 + 4096, voffA, rsP, 0x40000000u);
  asm volatile("s_waitcnt vmcnt(6)" : "+v"(aX[0]), "+v"(aX[1]), "+v"(aX[2]), "+v"(aX[3])::"memory");
  poll_end(pl);
  STAMPDECL;
  STAMPNW(5, 1);
  for (int u = 0; u < 28; u += 4) {
    G3_UNIT(aX, bXl, bXh, aY, bYl, bYh, 0, u, false);
    G3_UNIT(aY, bYl, bYh, aX, bXl, bXh, 1, u + 1, false);
    G3_UNIT(aX, bXl, bXh, aY, bYl, bYh, 2, u + 2, false);
    G3_UNIT(aY, bYl, bYh, aX, bXl, bXh, 3, u + 3, false);
  }
  G3_UNIT(aX, bXl, bXh, aY, bYl, bYh, 0, 28, true);
  G3_UNIT(aY, bYl, bYh, aX, bXl, bXh, 1, 29, true);
  G3_UNIT(aX, bXl, bXh, aY, bYl, bYh, 2, 30, true);
  G3_UNIT(aY, bYl, bYh, aX, bXl, bXh, 3, 31, true);
  asm volatile("s_nop 15" ::: "memory");
  SCHED0();
  STAMP(5, 2);
  STAMPFLUSH(5, 1);
  lane = fresh_lane(); fr = lane & 15; fq = lane >> 4;
  const f32x2* rstat = reinterpret_cast<const f32x2*>(smem + 131072);
  float* colc = reinterpret_cast<float*>(smem + 131072 + 2048);
  constexpr float KG = -1.4426950408889634f, KU = -0.6931471805599453f;
  asm volatile("" : "+v"(accC[0]), "+v"(accC[1]));
  if (fr < 2 && wm < FN) {
#pragma unroll
    for (int m = 0; m < 2; ++m) *reinterpret_cast<f32x4*>(colc + (m * 2 + fr) * 128 + (wn * 4 + rot) * 16 + fq * 4) = EFOLD ? accC[m] * (m ? KU : KG) : accC[m];
  }
  asm volatile("s_waitcnt lgkmcnt(0)" ::: "memory");
  __builtin_amdgcn_s_barrier();
  asm volatile("" ::: "memory");
  if (EPRI == FN) __builtin_amdgcn_s_setprio(1);
  static_for<4>([&](auto J) { wst(ic<3>{}, J); });
  f32x2 rs[4];
#pragma unroll
  for (int i = 0; i < 4; ++i) rs[i] = rstat[wm * 64 + i * 16 + fr];
#pragma unroll
  for (int j = 0; j < FN; ++j) {
    const int jg = (j + rot) % FN;
    const int cl = (wn * 4 + jg) * 16 + fq * 4;
    const f32x4 csg = *reinterpret_cast<const f32x4*>(colc + cl), bg = *reinterpret_cast<const f32x4*>(colc + 128 + cl);
    const f32x4 csu = *reinterpret_cast<const f32x4*>(colc + 256 + cl), bu = *reinterpret_cast<const f32x4*>(colc + 384 + cl);
    u32x2 pk[4];
#pragma unroll
    for (int i = 0; i < (EP ? 0 : 4); ++i) {
#pragma unroll
      for (int hh = 0; hh < 2; ++hh) {
        const f32x2 ag = hh ? hi2(acc[i][j]) : lo2(acc[i][j]), au = hh ? hi2(acc[i][FN + j]) : lo2(acc[i][FN + j]);
        const f32x2 g = (EFOLD ? rs[i][0] * KG : rs[i][0]) * ag + (rs[i][1] * (hh ? hi2(csg) : lo2(csg)) + (hh ? hi2(bg) : lo2(bg)));
        const f32x2 uu = (EFOLD ? rs[i][0] * KU : rs[i][0]) * au + (rs[i][1] * (hh ? hi2(csu) : lo2(csu)) + (hh ? hi2(bu) : lo2(bu)));
        const f32x2 ea = EFOLD ? g : g * -1.4426950408889634f;
        f32x2 e = {__builtin_amdgcn_exp2f(ea[0]), __builtin_amdgcn_exp2f(ea[1])};
        e = e + 1.0f;
        const f32x2 rc = {__builtin_amdgcn_rcpf(e[0]), __builtin_amdgcn_rcpf(e[1])};
        const f32x2 o = (g * uu) * rc;
        pk[i][hh] = pk_bf16(o[0], o[1]);
      }
    }
    const int Jv = (bn & 3) * 7 + wn * 4 + jg;
    const int J = g3_line(bn >> 2, Jv >> 2) * 4 + (Jv & 3);
    char* dst = (char*)act + ((size_t)((bm * 4 + wm) * (F_DIM / 32) + (J >> 1)) * 4 + (fq & 1)) * 1024 + (((J & 1) * 2 + (fq >> 1)) * 16 + fr) * 16;
#pragma unroll
    for (int i = 0; i < 4; i += 2) {
      const auto s0 = __builtin_amdgcn_permlane16_swap(pk[i][0], pk[i + 1][0], false, false);
      const auto s1 = __builtin_amdgcn_permlane16_swap(pk[i][1], pk[i + 1][1], false, false);
      if (ENOST) asm volatile("" ::"v"(dst + i * 1024), "v"(u32x4{s0[0], s1[0], s0[1], s1[1]}));
      else gst128(dst + i * 1024, u32x4{s0[0], s1[0], s0[1], s1[1]});
    }
  }
  if (EPRI == FN) __builtin_amdgcn_s_setprio(0);
}

#define G4PRI 2
#define XM 2
#define XB 1
#define G4AD 3
namespace g4 {
constexpr int NP = XB ? 4 : 8;
constexpr int NU = 14, SL = 4, AD = G4AD;
constexpr int CN = X_CN;
constexpr int GT = 131072 + 5120;
constexpr int na(int t) { return t + AD - 1 < NU ? 4 : 0; }
constexpr int nb(int mh, int t) { return t < 0 ? 0 : (t + SL < NU ? 4 : (t == NU - 3 && mh ? 4 : 0)); }
constexpr int behind(int mh, int t0, int u, int b) {
  int s = b ? na(u) : 0;
  for (int t = t0 + 1; t < u; ++t) s += na(t) + nb(mh, t);
  return s;
}
constexpr int vm(int mh, int u, int b) {
  const int wa = nb(mh, u - AD + 1) + behind(mh, u - AD + 1, u, b), wb = behind(mh, u == NU - 2 - G4SK ? NU - 1 - SL : u + 1 - SL, u, b);
  return b == 0 ? wa + 2 : (u + 1 < NU && u + 1 >= SL && wb < wa ? wb : wa);
}
}

template <int MH, class WST, class PRE, class EXTRA>
DI void g4_loop(f32x4 (&acc)[2][8], unsigned smem_base, int lane, int wid, int kb0, rsrc_t rsA, WST ldb, PRE pre, EXTRA extra) {
  using namespace g4;
  const int fq = lane >> 4, k2 = wid & 1;
  unsigned voffA[2], bad[8];
#pragma unroll
  for (int i = 0; i < 2; ++i) voffA[i] = lane * 16 + ((wid & 3) ^ i) * 1024;
  {
    const int q = (lane >> 2) & 3, r = 8 * fq + q, sw = q | ((fq & 1) << 2);
#pragma unroll
    for (int f = 0; f < 8; ++f) bad[f] = smem_base + k2 * 65536 + r * 256 + ((f ^ sw) * 32) + 8 * (lane & 3);
  }
  const unsigned sA = (unsigned)(kb0 + 2 * k2) * 4096u;
  u32x4 af[AD][4];
  u32x2 bl[2][8], bh[2][8];
  auto lda = [&](auto V, auto J) {
    constexpr int v = V.value, j = J.value;
    asm volatile("" : "=v"(af[v % AD][j]));
    bload_sc1<0>(af[v % AD][j], voffA[j & 1], rsA, sA + (4 * v + (j >> 1)) * 4096u);
  };
  auto rdb = [&](auto V, auto B, auto E) {
    constexpr int o = (V.value & 3) * 16384 + B.value * 8192, f = E.value >> 1;
    if constexpr (E.value & 1) dsr_tr<o + 1024>(bh[B.value][f], bad[f]);
    else dsr_tr<o>(bl[B.value][f], bad[f]);
  };
  unsigned wp = smem_base + k2 * 65536 + 32768 + (wid >> 1) * 8192 + lane * 16;
#pragma unroll
  for (int f = 0; f < 8; ++f) asm volatile("" : "+v"(bad[f]));
  asm volatile("" : "+v"(voffA[0]), "+v"(voffA[1]), "+v"(wp) : "s"(rsA), "s"(sA));
  const poll_t pl = pre();
  static_for<4 * (AD - 1)>([&](auto J) { lda(ic<J.value / 4>{}, ic<J.value % 4>{}); });
  STAMPDECL;
  STAMPNW(6, 1);
  static_for<16>([&](auto E) { rdb(ic<0>{}, ic<0>{}, E); });
  static_for<NU>([&](auto U) {
    constexpr int u = U.value;
    constexpr bool last = u == NU - 1;
    static_for<32>([&](auto N) {
      constexpr int n = N.value, b = n >> 4, m = n & 15;
      constexpr int f = (last && b) ? (m & 7) : (m >> 1), i = (last && b) ? 1 - (m >> 3) : (m & 1);
      if constexpr (m == 0) {
        asm volatile("s_waitcnt vmcnt(%2) lgkmcnt(0)" : "+v"(af[u % AD][2 * b]), "+v"(af[u % AD][2 * b + 1])
                     : "n"(vm(MH, u, b)) : "memory");
        if constexpr (u == 0 && b == 0) poll_end(pl);
#pragma unroll
        for (int g = 0; g < 8; ++g) asm volatile("" : "+v"(bl[b][g]), "+v"(bh[b][g]));
        if constexpr (b == 1 && u <= NU - 2 - G4SK) __builtin_amdgcn_s_barrier();
        if constexpr (b == 1 && u == NU - 2 - G4SK && MH == 0) __builtin_amdgcn_s_setprio(G4PRI);
      }
      const u32x4 bb = {bl[b][f][0], bl[b][f][1], bh[b][f][0], bh[b][f][1]};
      if constexpr (u == 0 && b == 0) asm volatile("v_mfma_f32_16x16x32_bf16 %0, %1, %2, 0" : "=&v"(acc[i][f]) : "v"(bb), "v"(af[u % AD][i]));
      else MFMA_AV(acc[i][f], bb, af[u % AD][2 * b + i]);
      if constexpr (m < 8 && !(last && b)) {
        rdb(ic<u + b>{}, ic<1 - b>{}, ic<2 * m>{});
        rdb(ic<u + b>{}, ic<1 - b>{}, ic<2 * m + 1>{});
      }
      if constexpr ((m & 3) == 2) {
        if constexpr (b == 0 && na(u)) lda(ic<u + AD - 1>{}, ic<(m >> 2)>{});
        if constexpr (b == 1 && u + SL < NU) ldb(ic<u + SL>{}, ic<(m >> 2)>{});
        if constexpr (b == 1 && u + 3 == NU) extra(ic<(m >> 2)>{});
      }
      if constexpr (last && n >= 24) dsw128<(m & 7) * 1024>(wp, __builtin_bit_cast(u32x4, acc[1][m & 7]));
    });
  });
  dsw32<0>(smem_base + CN + wid * 4, 1u);
  STAMPFLUSH(6, 1);
  const unsigned rp = wp + 65536 - k2 * 131072;
  u32x4 pq[8];
  for (;;) {
    unsigned fl;
    asm volatile("ds_read_b32 %0, %1" : "=v"(fl) : "v"(smem_base + CN + (wid ^ 1) * 4) : "memory");
    static_for<8>([&](auto F) { dsr128<F.value * 1024>(pq[F.value], rp); });
    asm volatile("s_waitcnt lgkmcnt(0)" : "+v"(fl), "+v"(pq[0]), "+v"(pq[1]), "+v"(pq[2]), "+v"(pq[3]), "+v"(pq[4]), "+v"(pq[5]), "+v"(pq[6]), "+v"(pq[7])::"memory");
    if (__builtin_amdgcn_readfirstlane(fl) != 0u) break;
  }
#pragma unroll
  for (int f = 0; f < 8; ++f) {
    asm volatile("" : "+v"(acc[0][f]));
    acc[0][f] += __builtin_bit_cast(f32x4, pq[f]);
  }
}

DI bool unset8(const u32x4 (&v)[8]) {
  unsigned m = 0;
#pragma unroll
  for (int f = 0; f < g4::NP; ++f) m = max(max(m, max(v[f][0], v[f][1])), max(v[f][2], v[f][3]));
  return __builtin_amdgcn_ballot_w64(m == ~0u) != 0ull;
}
template <int MH, class WST>
DI void g4_phase(unsigned smem_base, int wid, const bf16* At, WST wst, const bf16* Yt,
                 bf16* __restrict__ C, float* xbuf, unsigned* flags) {
  using namespace g4;
  constexpr int KB = F_DIM / 32;
  STAMP(6, 0);
  const int lane = fresh_lane();
  constexpr int mh = MH;
  const int kq = wid & 3;
  const int xcd = blockIdx.x & 7, idx = blockIdx.x >> 3;
  const int kh = xcd & 1, bm = ((xcd >> 1) & 1) * 8 + (idx & 7), bn = (xcd >> 2) * 4 + (idx >> 3);
  const unsigned partner = blockIdx.x ^ 1;
  const int n0 = bn * 128, bm64 = bm * 2 + kh;
  const int fr = lane & 15, fq = lane >> 4;
  const unsigned voffL = lane * 16;
  const rsrc_t rsA = mk_rsrc((const char*)At + (size_t)(bm64 ^ mh ^ 1) * KB * 4096, KB * 4096);
  const rsrc_t rsEx = mk_rsrc(xbuf, 256 * 32768);
  const int kb0 = kh * (KB / 2);
  f32x4 acc[2][8];
  const unsigned oa = arrive<5, 8, 4, 256>(smem_base + CN + 512, lane, flags + (FL_A + ((idx & 7) * 2 + (xcd >> 2)) * 8 + (xcd & 1)) * 32);
  auto pre = [&] { return await<5>(oa, lane, flags + (FL_A + ((bm >> 1) * 2 + kh) * 8 + (idx >> 3) * 2) * 32, 2, 8u); };
  if constexpr (mh == 0) {
    g4_loop<0>(acc, smem_base, lane, wid, kb0, rsA, wst, pre, [](auto) {});
    STAMP2(1);
    static_for<NP>([&](auto F) {
      constexpr int f = F.value;
      u32x4 t;
      if constexpr (XB) t = pack8(acc[0][2 * f], acc[0][2 * f + 1]);
      else t = __builtin_bit_cast(u32x4, acc[0][f]);
      if (XM != 2) {
#pragma unroll
        for (int e = 0; e < 4; ++e) t[e] = min(t[e], 0xfffffffeu);
      }
      bstore_sc1<(f & 3) * 1024>(t, voffL, rsEx, blockIdx.x * 32768u + kq * 8192u + (f >> 2) * 4096u);
    });
    if (XM == 2) {
      STAMP2(2);
      signal(drain(flags + (FL_P + blockIdx.x) * 32 + kq, lane, 1, 4));
      STAMP2(3);
      STAMP2F(7, 1, 4);
      return;
    }
    STAMP2(2);
    STAMP2(3);
    STAMP2F(7, 1, 4);
    return;
  } else {
  u32x4 yv[4];
  const rsrc_t rsX = mk_rsrc(Yt, S_LEN * D_DIM * 2);
  const unsigned vx = (unsigned)(((bm64 >> 2) * 32 + bn * 4) * 16384 + ((bm64 & 3) * 4 + kq) * 1024 + ((((fq & 1) << 1) | (fq >> 1)) * 16 + fr) * 16);
  g4_loop<1>(acc, smem_base, lane, wid, kb0, rsA, wst, pre,
             [&](auto J) { asm volatile("" : "=v"(yv[J.value])); bload_sc1<0>(yv[J.value], vx, rsX, J.value * 16384); });
  STAMP2(2);
  u32x4 pv[8], gt[8], ct[8];
  auto fetch = [&] {
    static_for<NP>([&](auto F) {
      constexpr int f = F.value;
      bload_sc1<(f & 3) * 1024>(pv[f], voffL, rsEx, partner * 32768u + kq * 8192u + (f >> 2) * 4096u);
    });
  };
#pragma unroll
  for (int f = 0; f < 8; ++f) asm volatile("" : "=v"(pv[f]));
  if (XM == 0) fetch();
  static_for<8>([&](auto F) { dsr128<F.value * 64>(gt[F.value], smem_base + GT + fq * 16); dsr128<512 + F.value * 64>(ct[F.value], smem_base + GT + fq * 16); });
  asm volatile("s_waitcnt vmcnt(%4) lgkmcnt(0)" : "+v"(yv[0]), "+v"(yv[1]), "+v"(yv[2]), "+v"(yv[3]) : "n"(XM == 0 ? NP : 0) : "memory");
#pragma unroll
  for (int c = 0; c < 4; ++c) {
    const auto x0 = __builtin_amdgcn_permlane16_swap(yv[c][0], yv[c][2], false, false);
    const auto x1 = __builtin_amdgcn_permlane16_swap(yv[c][1], yv[c][3], false, false);
#pragma unroll
    for (int h = 0; h < 2; ++h) {
      const int f = 2 * c + h;
      asm volatile("" : "+v"(gt[f]), "+v"(ct[f]));
      acc[0][f] = f32x4{bf_lo(x0[h]), bf_hi(x0[h]), bf_lo(x1[h]), bf_hi(x1[h])} * __builtin_bit_cast(f32x4, ct[f]) + __builtin_bit_cast(f32x4, gt[f]) * acc[0][f];
      asm volatile("" : "+v"(acc[0][f]));
    }
  }
  char* cp = (char*)C + (size_t)(bm64 * 64 + kq * 16 + (fr & 7)) * (D_DIM * 2) + n0 * 2 + (((fq & 1) << 1) | (fq >> 1)) * 16 + (fr >> 3) * 64;
  asm volatile("" : "+v"(cp));
  STAMP2(5);
  unsigned spins = 0;
  auto landed = [&] { asm volatile("s_waitcnt vmcnt(0)" : "+v"(pv[0]), "+v"(pv[1]), "+v"(pv[2]), "+v"(pv[3]), "+v"(pv[4]), "+v"(pv[5]), "+v"(pv[6]), "+v"(pv[7])::"memory"); };
  const poll_t pl = poll(0u, flags + (FL_P + partner) * 32 + kq, lane, 1, 1u);
  STAMP2(7);
  if (XM != 0) fetch();
  landed();
  poll_end(pl);
  while (XM != 2 && unset8(pv) && ++spins < (1u << 20)) {
    fetch();
    landed();
  }
  STAMP2(6);
  static_for<2>([&](auto L) {
    constexpr int l = L.value;
    u32x4 ov[2];
#pragma unroll
    for (int c = 0; c < 2; ++c) {
      const int f = 4 * l + 2 * c;
      const u32x4 pb = pv[f >> 1];
      const f32x4 p0 = XB ? f32x4{bf_lo(pb[0]), bf_hi(pb[0]), bf_lo(pb[1]), bf_hi(pb[1])} : __builtin_bit_cast(f32x4, pv[f]);
      const f32x4 p1 = XB ? f32x4{bf_lo(pb[2]), bf_hi(pb[2]), bf_lo(pb[3]), bf_hi(pb[3])} : __builtin_bit_cast(f32x4, pv[f + 1]);
      const f32x4 o0 = acc[0][f] + __builtin_bit_cast(f32x4, gt[f]) * p0;
      const f32x4 o1 = acc[0][f + 1] + __builtin_bit_cast(f32x4, gt[f + 1]) * p1;
      const auto s0 = __builtin_amdgcn_permlane16_swap(pk_bf16(o0[0], o0[1]), pk_bf16(o1[0], o1[1]), false, false);
      const auto s1 = __builtin_amdgcn_permlane16_swap(pk_bf16(o0[2], o0[3]), pk_bf16(o1[2], o1[3]), false, false);
      ov[c] = u32x4{s0[0], s1[0], s0[1], s1[1]};
    }
    u32x4 lo, hi;
#pragma unroll
    for (int e = 0; e < 4; ++e) {
      lo[e] = __builtin_amdgcn_update_dpp(ov[0][e], ov[1][e], 0x128, 0xf, 0xc, false);
      hi[e] = __builtin_amdgcn_update_dpp(ov[1][e], ov[0][e], 0x128, 0xf, 0x3, false);
    }
    gst128(cp + l * 128, lo);
    gst128(cp + 8 * (D_DIM * 2) + l * 128, hi);
  });
  STAMP2(3);
  STAMP2(4);
  STAMP2F(6, 2, 7);
}
}

__global__ __launch_bounds__(512, 2) void block_kernel(char* ws, const bf16* X, const bf16* cnd, const bf16* wmod, const bf16* Wq,
                                                       const bf16* Wo, const bf16* Wg, const bf16* Wu, const bf16* Wd, bf16* C) {
  __shared__ __attribute__((aligned(1024))) char smem[Q_LDS];
  unsigned long long pc;
  asm volatile("s_getpc_b64 %0" : "=s"(pc));
  asm volatile("s_cmp_ge_u32 %0, 9\n\ts_cbranch_scc1 1f\n\ts_mov_b32 m0, %1\n\ts_nop 0\n\tbuffer_load_dword %2, %3, 0 offen lds\n1:"
               ::"s"((blockIdx.x >> 3) + __builtin_amdgcn_readfirstlane(threadIdx.x ^ 64)), "s"((unsigned)(uintptr_t)smem + QX_SINK), "v"((threadIdx.x & 63) * 128 + (blockIdx.x >> 3) * 8192),
                 "s"(mk_rsrc((const void*)(uintptr_t)(pc & ~255ull), CODEPF * 1024)) : "memory", "scc");
  asm volatile("" : "+s"(ws) : "s"(X), "s"(cnd), "s"(wmod), "s"(Wq), "s"(Wo), "s"(Wg), "s"(Wu), "s"(Wd), "s"(C));
  float* modp = (float*)ws;
  unsigned* ctab = (unsigned*)(ws + 212992);
  unsigned* flags = (unsigned*)(ws + 262144);
  float* stat = (float*)(ws + 524288);
  bf16* O = (bf16*)(ws + (1 << 20));
  bf16* Y = (bf16*)(ws + (9 << 20));
  bf16* H = (bf16*)(ws + (13 << 20));
  bf16* act = (bf16*)(ws + (17 << 20));
  float* xbuf = (float*)(ws + (32 << 20));
  STAMP(2, 0);
  const int wid = __builtin_amdgcn_readfirstlane(threadIdx.x >> 6);
  const int wm = wid & 3, wn = wid >> 2;
  const int xcd = blockIdx.x & 7, idx = blockIdx.x >> 3;
  const int bm = idx & 7, bn = xcd * 4 + (idx >> 3);
  const unsigned smem_base = (unsigned)(uintptr_t)smem;
  const rsrc_t rsA = mk_rsrc((const char*)Y + (size_t)bm * 32 * 16384, 32 * 16384);
  const rsrc_t rsB = mk_rsrc(wn ? Wu : Wg, D_DIM * F_DIM * 2);
  const rsrc_t rsP = mk_rsrc((const char*)Wd + (size_t)blockIdx.x * 28672, 28672);
  const rsrc_t rsE = mk_rsrc((const void*)(uintptr_t)(pc & ~255ull), CODEPF * 1024);
  const unsigned m0w = smem_base + wid * 2048;
  unsigned voffB0 = 0, voffB1 = 0;
  attn_phase(smem_base, wid, H, Wq, O, Wo, X, modp, Y, stat, flags, Wg, Wu, xbuf, ctab, cnd, wmod, rsE, [&](auto S, auto I) {
    if constexpr (S.value == 0) {
      const int l2 = fresh_lane();
      const int r = (wm * 2 + I.value) * 4 + (l2 >> 4);
      const int sw = (r & 3) | (((r >> 3) & 1) << 2);
      const int c16 = l2 & 15;
      const int vb = (((bn & 3) * 224) & ~63) + ((c16 >> 1) ^ sw) * 32 + (c16 & 1) * 16;
      (I.value ? voffB1 : voffB0) = (unsigned)(r * (F_DIM * 2) + g3_line(bn >> 2, vb >> 7) * 128 + (vb & 127));
    }
    if (GSNT) bload_lds_nt<0>(m0w + S.value * 16384 + I.value * 1024, I.value ? voffB1 : voffB0, rsB, S.value * 229376u);
    else bload_lds<0>(m0w + S.value * 16384 + I.value * 1024, I.value ? voffB1 : voffB0, rsB, S.value * 229376u);
  });
  __builtin_amdgcn_s_setprio(0);
  const int lane = fresh_lane();
  const unsigned voffA = wid * 2048 + lane * 16;
  const unsigned oy = arrive<3, 8, 8, 512>(smem_base + X_CN + 768, lane, flags + (FL_Y + xcd * 32 + (idx & 3)) * 32);
  auto pl = [&] { return await<3>(oy, lane, flags + (FL_Y + bm * 32 + xcd * 4) * 32, 4, 8u); };
  const int n0 = ((xcd >> 2) * 4 + (idx >> 3)) * 128;
  unsigned vo = 0;
  auto wst = [&](auto V, auto J) {
    constexpr int v = V.value, j = J.value;
    if constexpr (v == 0 && j == 0) {
      const int l2 = fresh_lane();
      vo = (l2 >> 4) * 2048 + ((((l2 & 15) >> 1) ^ ((l2 >> 4) | ((wid & 1) << 2))) * 32) + (l2 & 1) * 16;
    }
    bload_lds<0>(m0w + (j >> 1) * 65536 + (v & 3) * 16384 + (j & 1) * 1024, vo, mk_rsrc((const char*)Wd + n0 * 2, F_DIM * D_DIM * 2 - n0 * 2),
                 (unsigned)(((xcd & 1) * (F_DIM / 64) + wn) * 65536 + wm * 16384) + (4 * v + 2 * (j >> 1)) * 65536u + (j & 1) * 8192u);
  };
  if (wn == 0) {
    g3_body<4>(smem, smem_base, wm, wn, wid, lane, bm, bn, rsA, rsB, rsP, rsE, voffA, voffB0, voffB1, act, wst, [](auto) {}, pl);
    STAMP(5, 3);
    g4_phase<0>(smem_base, wid, act, wst, Y, C, xbuf, flags);
  } else {
    u32x2 tv[16];
    unsigned gv;
    g3_body<3>(smem, smem_base, wm, wn, wid, lane, bm, bn, rsA, rsB, rsP, rsE, voffA, voffB0, voffB1, act, wst, [&](auto K) {
      const int tid = wm * 64 + fresh_lane();
      if constexpr (K.value == 0) {
        asm volatile("" : "=v"(gv));
        bload1_sc1(gv, (unsigned)((n0 + (tid & 127)) * 4), mk_rsrc(modp + 49152 + (wm >= 2 ? 2048 : 3072), 4096));
        const rsrc_t rsS = mk_rsrc(stat, S_LEN * 128);
#pragma unroll
        for (int i = 0; i < 16; ++i)
          asm volatile("buffer_load_dwordx2 %0, %1, %2, %3 offen sc1" : "=v"(tv[i]) : "v"((bm * 256 + tid) * 8), "s"(rsS), "s"(i * 16384) : "memory");
      } else {
#pragma unroll
        for (int i = 0; i < 16; ++i) asm volatile("" : "+v"(tv[i]));
        float s = 0;
#pragma unroll
        for (int i = 0; i < 16; i += 2) s += __uint_as_float(tv[i][0]) + __uint_as_float(tv[i + 1][0]);
        const float mean = s * (1.0f / D_DIM);
        float m2 = 0;
#pragma unroll
        for (int i = 0; i < 16; i += 2) {
          const float d0 = __uint_as_float(tv[i][0]) * (1.0f / 64.0f) - mean, d1 = __uint_as_float(tv[i + 1][0]) * (1.0f / 64.0f) - mean;
          m2 += __uint_as_float(tv[i][1]) + __uint_as_float(tv[i + 1][1]) + 64.0f * (d0 * d0 + d1 * d1);
        }
        const float rstd = rsqrtf(m2 * (1.0f / D_DIM) + 1e-5f);
        reinterpret_cast<f32x2*>(smem + 131072)[tid & 255] = f32x2{rstd, -mean * rstd};
        asm volatile("" : "+v"(gv));
        const float sum = (wm >= 2) ? 1.0f / __uint_as_float(gv) : __uint_as_float(gv);
        dsw32<0>(smem_base + g4::GT + (tid & 255) * 4, __float_as_uint(sum));
      }
    }, pl);
    g4_phase<1>(smem_base, wid, act, wst, Y, C, xbuf, flags);
  }
}

extern "C" void kernel_launch(void* const* d_in, const int* in_sizes, int n_in, void* d_out, int out_size,
                              void* d_ws, size_t ws_size, hipStream_t stream) {
  const bf16* x = (const bf16*)d_in[0];
  const bf16* wqkv = (const bf16*)d_in[1];
  const bf16* wo = (const bf16*)d_in[2];
  const bf16* wg = (const bf16*)d_in[4];
  const bf16* wu = (const bf16*)d_in[5];
  const bf16* wd = (const bf16*)d_in[6];
  const bf16* c = (const bf16*)d_in[7];
  const bf16* wmod = (const bf16*)d_in[8];
  bf16* out = (bf16*)d_out;

  block_kernel<<<256, 512, 0, stream>>>((char*)d_ws, x, c, wmod, wqkv, wo, wg, wu, wd, out);
}
```

```cpp
#include <hip/hip_runtime.h>
#include <hip/hip_bf16.h>
#include <stdint.h>

typedef __hip_bfloat16 bf16;
typedef __attribute__((ext_vector_type(8))) short bf16x8;
typedef __attribute__((ext_vector_type(4))) float f32x4;
typedef __attribute__((ext_vector_type(2))) float f32x2;
typedef __attribute__((ext_vector_type(4))) unsigned u32x4;
typedef __attribute__((ext_vector_type(2))) unsigned u32x2;
typedef __attribute__((ext_vector_type(2))) __bf16 bf16x2_t;
typedef __amdgpu_buffer_rsrc_t rsrc_t;

#define DI __device__ __forceinline__
constexpr int CODEPF = 67;

constexpr int S_LEN = 2048;
constexpr int D_DIM = 1024;
constexpr int F_DIM = 3584;
constexpr int QBAR = 2;
constexpr int NP1 = 5;
constexpr int PSLP = 4;
constexpr int NDLY = 20, TDLY = 15;
constexpr int TS = 12, TP = 6;
constexpr int WOS = 6;
constexpr int GDEP = 12, GS0 = 28;
constexpr int TPOS = 6, CT3 = 24;

template <int V> struct ic { static constexpr int value = V; };
template <int N, int I = 0, class F>
DI void static_for(F&& f) {
  if constexpr (I < N) { f(ic<I>{}); static_for<N, I + 1>(f); }
}

DI float bf_lo(unsigned u) { return __uint_as_float(u << 16); }
DI float bf_hi(unsigned u) { return __uint_as_float(u & 0xffff0000u); }
DI unsigned pk_bf16(float lo, float hi) {
  const f32x2 v = {lo, hi};
  return __builtin_bit_cast(unsigned, __builtin_convertvector(v, bf16x2_t));
}
DI rsrc_t mk_rsrc(const void* p, unsigned bytes) { return __builtin_amdgcn_make_buffer_rsrc((void*)p, (short)0, (int)bytes, 0x00020000); }

template <int N>
DI void wait_vm() { asm volatile("s_waitcnt vmcnt(%0)" ::"n"(N > 63 ? 63 : N) : "memory"); }
#define WAIT_LGKM0() do { asm volatile("s_waitcnt lgkmcnt(0)" ::: "memory"); __builtin_amdgcn_sched_barrier(0); } while (0)
#define SCHED0() __builtin_amdgcn_sched_barrier(0)

#define MFMA_AV(C, B, A) asm volatile("v_mfma_f32_16x16x32_bf16 %0, %1, %2, %0" : "+v"(C) : "v"(B), "v"(A))
template <int OFF>
DI void bload_v(u32x4& d, unsigned voff, rsrc_t rs, unsigned soff) {
  asm volatile("buffer_load_dwordx4 %0, %1, %2, %3 offen offset:%4" : "+v"(d) : "v"(voff), "s"(rs), "s"(soff), "i"(OFF) : "memory");
}
template <int OFF>
DI void bload_lds(unsigned m0v, unsigned voff, rsrc_t rs, unsigned soff) {
  asm volatile("s_mov_b32 m0, %0\n\ts_nop 0\n\tbuffer_load_dwordx4 %1, %2, %3 offen offset:%4 lds" ::"s"(m0v), "v"(voff), "s"(rs), "s"(soff), "i"(OFF) : "memory");
}
DI void touch_lds(unsigned m0v, unsigned voff, rsrc_t rs, unsigned soff) {
  asm volatile("s_mov_b32 m0, %0\n\ts_nop 0\n\tbuffer_load_dword %1, %2, %3 offen lds" ::"s"(m0v), "v"(voff), "s"(rs), "s"(soff) : "memory");
}
template <int OFF>
DI void bload_lds_nt(unsigned m0v, unsigned voff, rsrc_t rs, unsigned soff) {
  asm volatile("s_mov_b32 m0, %0\n\ts_nop 0\n\tbuffer_load_dwordx4 %1, %2, %3 offen offset:%4 nt lds" ::"s"(m0v), "v"(voff), "s"(rs), "s"(soff), "i"(OFF) : "memory");
}
template <int OFF>
DI void bload_lds_sc1(unsigned m0v, unsigned voff, rsrc_t rs, unsigned soff) {
  asm volatile("s_mov_b32 m0, %0\n\ts_nop 0\n\tbuffer_load_dwordx4 %1, %2, %3 offen offset:%4 sc1 lds" ::"s"(m0v), "v"(voff), "s"(rs), "s"(soff), "i"(OFF) : "memory");
}
template <int OFF>
DI void dsr128(u32x4& d, unsigned addr) { asm volatile("ds_read_b128 %0, %1 offset:%2" : "=v"(d) : "v"(addr), "i"(OFF) : "memory"); }
template <int OFF>
DI void dsr32(unsigned& d, unsigned addr) { asm volatile("ds_read_b32 %0, %1 offset:%2" : "=v"(d) : "v"(addr), "i"(OFF) : "memory"); }
DI void bload_nt(u32x4& d, unsigned voff, rsrc_t rs, unsigned soff) {
  asm volatile("buffer_load_dwordx4 %0, %1, %2, %3 offen nt" : "=v"(d) : "v"(voff), "s"(rs), "s"(soff) : "memory");
}
template <int OFF>
DI void dsr64(u32x2& d, unsigned addr) { asm volatile("ds_read_b64 %0, %1 offset:%2" : "=v"(d) : "v"(addr), "i"(OFF) : "memory"); }
template <int OFF>
DI void dsr_tr(u32x2& d, unsigned addr) { asm volatile("ds_read_b64_tr_b16 %0, %1 offset:%2" : "=v"(d) : "v"(addr), "i"(OFF) : "memory"); }
template <int OFF>
DI void dsw128(unsigned addr, u32x4 d) { asm volatile("ds_write_b128 %0, %1 offset:%2" ::"v"(addr), "v"(d), "i"(OFF) : "memory"); }
template <int OFF>
DI void dsw32(unsigned addr, unsigned d) { asm volatile("ds_write_b32 %0, %1 offset:%2" ::"v"(addr), "v"(d), "i"(OFF) : "memory"); }

DI void gst128(void* p, u32x4 v) {
  asm volatile("global_store_dwordx4 %0, %1, off sc1\n\ts_nop 1" ::"v"(p), "v"(v) : "memory");
}
DI int fresh_lane() {
  int l;
  asm volatile("v_mbcnt_lo_u32_b32 %0, -1, 0\n\tv_mbcnt_hi_u32_b32 %0, -1, %0" : "=v"(l));
  return l;
}
DI f32x2 lo2(f32x4 v) { return __builtin_shufflevector(v, v, 0, 1); }
DI f32x2 hi2(f32x4 v) { return __builtin_shufflevector(v, v, 2, 3); }
DI float rowsum4(float v) {
  const unsigned u = __float_as_uint(v);
  const auto a = __builtin_amdgcn_permlane16_swap(u, u, false, false);
  const unsigned t = __float_as_uint(__uint_as_float(a[0]) + __uint_as_float(a[1]));
  const auto b = __builtin_amdgcn_permlane32_swap(t, t, false, false);
  return __uint_as_float(b[0]) + __uint_as_float(b[1]);
}
DI float silu_f(float v) { return v * __builtin_amdgcn_rcpf(1.0f + __builtin_amdgcn_exp2f(v * -1.4426950408889634f)); }
DI float rbf(float v) { return bf_lo(pk_bf16(v, v)); }
DI float gain_nz(float m) {
  const float g = rbf(1.0f + rbf(m));
  return __builtin_fabsf(g) < 8.67361738e-19f ? 8.67361738e-19f : g;
}
template <int OFF>
DI void bload_sc1(u32x4& d, unsigned voff, rsrc_t rs, unsigned soff) {
  asm volatile("buffer_load_dwordx4 %0, %1, %2, %3 offen offset:%4 sc1" : "+v"(d) : "v"(voff), "s"(rs), "s"(soff), "i"(OFF) : "memory");
}
template <int OFF>
DI void bstore_sc1(u32x4 d, unsigned voff, rsrc_t rs, unsigned soff) {
  asm volatile("buffer_store_dwordx4 %0, %1, %2, %3 offen offset:%4 sc1\n\ts_nop 1" ::"v"(d), "v"(voff), "s"(rs), "s"(soff), "i"(OFF) : "memory");
}
DI void bload1_sc1(unsigned& d, unsigned voff, rsrc_t rs) {
  asm volatile("buffer_load_dword %0, %1, %2, 0 offen sc1" : "+v"(d) : "v"(voff), "s"(rs) : "memory");
}

constexpr int Q_SLOT = 21504, Q_NSL = 7;
constexpr int QX_WH = 4 * Q_SLOT;
constexpr int QX_HX = 65536;
constexpr int QX_GT = QX_WH + 65536;
constexpr int QX_CNT = QX_GT + 512;
constexpr int QX_SINK = QX_CNT + 512;
constexpr int G3_CT = QX_SINK + 1024;
constexpr int X_CN = G3_CT + 4096;
constexpr int Q_LDS = X_CN + 2048;
constexpr int P_TAB = 4 * Q_SLOT, P_RED = 5 * Q_SLOT, P_ST = P_RED + 4096;

DI unsigned lds_add(unsigned addr, unsigned v0) {
  unsigned o;
  asm volatile("ds_add_rtn_u32 %0, %1, %2\n\ts_waitcnt lgkmcnt(0)" : "=v"(o) : "v"(addr), "v"(v0) : "memory");
  return __builtin_amdgcn_readfirstlane(o);
}
template <int SLP>
DI void lds_wait(unsigned addr, unsigned n) {
  for (;;) {
    unsigned v;
    asm volatile("ds_read_b32 %0, %1\n\ts_waitcnt lgkmcnt(0)" : "=v"(v) : "v"(addr) : "memory");
    if (__builtin_amdgcn_readfirstlane(v) >= n) break;
    __builtin_amdgcn_s_sleep(SLP);
  }
}
struct poll_t { unsigned a, b; };
struct sig_t { rsrc_t rs; unsigned vo; };
DI sig_t drain(unsigned* out, int lane, int no, int so) {
  sig_t g = {mk_rsrc(out, (no - 1) * so + 4), (unsigned)(lane * so)};
  asm volatile("s_waitcnt vmcnt(0)" : "+v"(g.vo) : "s"(g.rs) : "memory");
  return g;
}
DI void signal(const sig_t& g) { asm volatile("buffer_atomic_add %0, %1, %2, 0 offen sc1" ::"v"(1u), "v"(g.vo), "s"(g.rs) : "memory"); }
#define POLL_RD(r) "v_mov_b32 " r ", 0\n\tbuffer_atomic_add " r ", %3, %4, 0 offen sc0 sc1\n\t"
#define POLL_CK(r) "s_waitcnt vmcnt(1)\n\tv_cmp_lt_u32 vcc, " r ", %5\n\ts_cbranch_vccz 2f\n\t"
DI poll_t poll(unsigned skip, unsigned* in, int lane, int nl, unsigned n) {
  poll_t r;
  asm volatile("s_cmp_lg_u32 %2, 0\n\ts_cbranch_scc1 2f\n\t" POLL_RD("%0") "s_sleep %6\n1:\n\t" POLL_RD("%1") POLL_CK("%0") POLL_RD("%0") POLL_CK("%1")
               "s_add_u32 %2, %2, 1\n\ts_cmp_lt_u32 %2, 0x80000\n\ts_cbranch_scc1 1b\n2:"
               : "=&v"(r.a), "=&v"(r.b), "+s"(skip) : "v"(lane * 128), "s"(mk_rsrc(in, nl * 128)), "v"(lane < nl ? n : 0u), "n"(PSLP) : "memory", "vcc", "scc");
  return r;
}
DI void poll_end(const poll_t& r) { asm volatile("" ::"v"(r.a), "v"(r.b)); }
template <int NW, int NO, int SO>
DI unsigned arrive(unsigned lcnt, int lane, unsigned* out) {
  asm volatile("" ::"v"(lcnt + lane * 4));
  const sig_t g = drain(out, lane, NO, SO);
  const unsigned o = lds_add(lcnt + lane * 4, 1u);
  if (o == NW - 1) {
    signal(g);
  }
  return o;
}
DI poll_t await(unsigned o, int lane, unsigned* in, int nl, unsigned n) {
  const poll_t r = poll(o, in, lane, nl, n);
  __builtin_amdgcn_s_barrier();
  return r;
}
constexpr int FL_M = 0, FL_H = 544, FL_O = 608, FL_P = 736, FL_Y = 992, FL_A = 1248, FL_S = 1376;

DI f32x4 mfma0(u32x4 a, u32x4 b) {
  return __builtin_amdgcn_mfma_f32_16x16x32_bf16(__builtin_bit_cast(bf16x8, a), __builtin_bit_cast(bf16x8, b), f32x4{0, 0, 0, 0}, 0, 0, 0);
}
DI u32x4 pack8(const f32x4 a, const f32x4 b) { return u32x4{pk_bf16(a[0], a[1]), pk_bf16(a[2], a[3]), pk_bf16(b[0], b[1]), pk_bf16(b[2], b[3])}; }
template <class OP>
DI float rows4(const float (&v)[4], OP op) {
  const auto a = __builtin_amdgcn_permlane16_swap(__float_as_uint(v[0]), __float_as_uint(v[1]), false, false);
  const auto b = __builtin_amdgcn_permlane16_swap(__float_as_uint(v[2]), __float_as_uint(v[3]), false, false);
  const float t0 = op(__uint_as_float(a[0]), __uint_as_float(a[1])), t1 = op(__uint_as_float(b[0]), __uint_as_float(b[1]));
  const auto c = __builtin_amdgcn_permlane32_swap(__float_as_uint(t0), __float_as_uint(t1), false, false);
  return op(__uint_as_float(c[0]), __uint_as_float(c[1]));
}
DI void bcast4(float t, float (&v)[4]) {
  const auto c = __builtin_amdgcn_permlane32_swap(__float_as_uint(t), __float_as_uint(t), false, false);
  const auto a = __builtin_amdgcn_permlane16_swap(c[0], c[0], false, false);
  const auto b = __builtin_amdgcn_permlane16_swap(c[1], c[1], false, false);
  v[0] = __uint_as_float(a[0]); v[1] = __uint_as_float(a[1]); v[2] = __uint_as_float(b[0]); v[3] = __uint_as_float(b[1]);
}
DI float fmax2(float a, float b) { return __builtin_amdgcn_fmed3f(a, b, __builtin_inff()); }

DI float sum16(float v) {
  v += __builtin_bit_cast(float, __builtin_amdgcn_update_dpp(0, __builtin_bit_cast(int, v), 0x128, 0xf, 0xf, false));
  v += __builtin_bit_cast(float, __builtin_amdgcn_update_dpp(0, __builtin_bit_cast(int, v), 0x124, 0xf, 0xf, false));
  v += __builtin_bit_cast(float, __builtin_amdgcn_update_dpp(0, __builtin_bit_cast(int, v), 0x122, 0xf, 0xf, false));
  v += __builtin_bit_cast(float, __builtin_amdgcn_update_dpp(0, __builtin_bit_cast(int, v), 0x121, 0xf, 0xf, false));
  return v;
}
DI f32x4 sum4(const u32x4* p) {
  return (__builtin_bit_cast(f32x4, p[0]) + __builtin_bit_cast(f32x4, p[1])) + (__builtin_bit_cast(f32x4, p[2]) + __builtin_bit_cast(f32x4, p[3]));
}
template <int NB, int NMID, class MID>
DI void mod_gemv(unsigned red, int lane, int col0, int k0, const bf16* __restrict__ c, const bf16* __restrict__ wmod, MID mid) {
  const int fr = lane & 15, fq = lane >> 4;
  const rsrc_t rsW = mk_rsrc(wmod, 1024 * 6144 * 2);
  const unsigned vw = (unsigned)(((k0 + (fr >> 3) * 32 + fq * 8) * 6144 + col0 + (fr & 7) * 8) * 2);
  u32x4 raw[NB][8], cv[NB];
  static_for<NB>([&](auto B) {
    asm volatile("" : "=v"(cv[B.value]));
    bload_v<B.value * 128>(cv[B.value], (unsigned)((k0 + (fr & 1) * 32 + fq * 8) * 2), mk_rsrc(c, 2048), 0);
  });
  static_for<NB * 8>([&](auto N) {
    constexpr int b = N.value >> 3, j = N.value & 7;
    bload_nt(raw[b][j], vw, rsW, (b * 64 + j) * 12288);
  });
  __builtin_amdgcn_s_barrier();
  f32x4 acc[8];
#pragma unroll
  for (int n = 0; n < 8; ++n) acc[n] = f32x4{0, 0, 0, 0};
  u32x4 cf[NB];
  static_for<NB>([&](auto B) {
    constexpr int b = B.value;
    asm volatile("s_waitcnt vmcnt(%1)" : "+v"(cv[b]) : "n"(NB * 8) : "memory");
#pragma unroll
    for (int e = 0; e < 4; ++e) cf[b][e] = pk_bf16(silu_f(bf_lo(cv[b][e])), silu_f(bf_hi(cv[b][e])));
    asm volatile("" : "+v"(cf[b]));
  });
  static_for<NB>([&](auto B) {
    constexpr int b = B.value;
    if constexpr (b == 1) {
#pragma unroll
      for (int n = 0; n < 8; ++n) asm volatile("" : "+v"(acc[n]));
      mid();
    }
    asm volatile("s_waitcnt vmcnt(%8)" : "+v"(raw[b][0]), "+v"(raw[b][1]), "+v"(raw[b][2]), "+v"(raw[b][3]), "+v"(raw[b][4]), "+v"(raw[b][5]), "+v"(raw[b][6]), "+v"(raw[b][7])
                 : "n"(b ? NMID : (NB - 1) * 8) : "memory");
#pragma unroll
    for (int p = 0; p < 4; ++p) {
      u32x4 lo, hi;
#pragma unroll
      for (int t = 0; t < 4; ++t) {
        lo[t] = __builtin_amdgcn_perm(raw[b][2 * t + 1][p], raw[b][2 * t][p], 0x05040100u);
        hi[t] = __builtin_amdgcn_perm(raw[b][2 * t + 1][p], raw[b][2 * t][p], 0x07060302u);
      }
      acc[2 * p] = __builtin_amdgcn_mfma_f32_16x16x32_bf16(__builtin_bit_cast(bf16x8, lo), __builtin_bit_cast(bf16x8, cf[b]), acc[2 * p], 0, 0, 0);
      acc[2 * p + 1] = __builtin_amdgcn_mfma_f32_16x16x32_bf16(__builtin_bit_cast(bf16x8, hi), __builtin_bit_cast(bf16x8, cf[b]), acc[2 * p + 1], 0, 0, 0);
    }
  });
  if (fr == (fq >> 1)) {
    static_for<8>([&](auto R) {
      constexpr int r = R.value >> 1, h = (R.value & 1) * 4;
      dsw128<R.value * 16>(red + fq * 128, __builtin_bit_cast(u32x4, f32x4{acc[h][r], acc[h + 1][r], acc[h + 2][r], acc[h + 3][r]}));
    });
  }
}
template <int NR>
DI float red_sum(unsigned red, int lane) {
  unsigned v[NR];
  static_for<NR>([&](auto R) { dsr32<R.value * 256>(v[R.value], red + lane * 4); });
  asm volatile("s_waitcnt lgkmcnt(0)" ::: "memory");
  float s = 0;
#pragma unroll
  for (int r = 0; r < NR; r += 2) {
    asm volatile("" : "+v"(v[r]), "+v"(v[r + 1]));
    s += __uint_as_float(v[r]) + __uint_as_float(v[r + 1]);
  }
  return s;
}
DI unsigned cnt_read(unsigned* in, int lane, int nl) {
  unsigned t = 0;
  asm volatile("buffer_atomic_add %0, %1, %2, 0 offen sc0 sc1\n\ts_waitcnt vmcnt(0)" : "+v"(t) : "v"(lane * 128), "s"(mk_rsrc(in, nl * 128)) : "memory");
  return t;
}
DI bool words_set(unsigned* word, int lane) {
  unsigned t = 0;
  asm volatile("buffer_atomic_add %0, %1, %2, 0 offen sc0 sc1\n\ts_waitcnt vmcnt(0)" : "+v"(t) : "v"(lane * 8192), "s"(mk_rsrc(word, 7 * 8192 + 4)) : "memory");
  return (__builtin_amdgcn_ballot_w64(t == 0u) & 255u) == 0u;
}
DI void confirm(unsigned* word, int lane) {
#pragma unroll 1
  for (unsigned spins = 0; !words_set(word, lane) && ++spins < (1u << 20);) __builtin_amdgcn_s_sleep(2);
}
DI void set_words(unsigned* flags, int lane) {
  bstore_sc1<0>(u32x4{1u, 1u, 1u, 1u}, (unsigned)(lane * 128), mk_rsrc(flags + (FL_S + (blockIdx.x & 7) * 64) * 32, 64 * 128), 0);
}

template <class PRE>
DI void first_phase(unsigned smem_base, int wid, int lane, const bf16* __restrict__ x, const bf16* __restrict__ c, const bf16* __restrict__ wmod,
                    const bf16* __restrict__ W, float* modp, bf16* H, unsigned* flags, PRE prefill) {
  const int xcd = blockIdx.x & 7, idx = blockIdx.x >> 3;
  const int fr = lane & 15, fq = lane >> 4;
  const int u = xcd * 16 + (idx & 15), v = xcd * 8 + idx;
  unsigned* word = flags + __builtin_amdgcn_readfirstlane((FL_S + (blockIdx.x >> 2)) * 32 + (blockIdx.x & 3));
  const bool master = idx == 15;
  if (wid == (idx >= 16 ? 0 : 7)) {
    asm volatile("buffer_store_dword %0, %1, %2, 0 offen sc1" ::"v"(0u), "v"(lane * 8192), "s"(mk_rsrc(word, 7 * 8192 + 4)) : "memory");
  }
  if (master) {
    bstore_sc1<0>(u32x4{0u, 0u, 0u, 0u}, (unsigned)(wid * 2048 + lane * 16), mk_rsrc(flags + xcd * (FL_O / 8) * 32, FL_O / 8 * 128), 0);
    bstore_sc1<1024>(u32x4{0u, 0u, 0u, 0u}, (unsigned)(wid * 2048 + lane * 16), mk_rsrc(flags + xcd * (FL_O / 8) * 32, FL_O / 8 * 128), 0);
  }
  if (wid == 7) {
    dsw32<0>(smem_base + QX_CNT + lane * 4, 0u); dsw32<256>(smem_base + QX_CNT + lane * 4, 0u);
    dsw128<0>(smem_base + X_CN + lane * 16, u32x4{0u, 0u, 0u, 0u}); dsw128<1024>(smem_base + X_CN + lane * 16, u32x4{0u, 0u, 0u, 0u});
    asm volatile("s_waitcnt lgkmcnt(0)" ::: "memory");
  }
  if (__builtin_expect(idx >= 16, 1)) {
    unsigned o = 0;
    if (wid < 4) {
      const int col0 = (u >> 3) * 128, kp = u & 7;
      mod_gemv<1, 0>(smem_base + P_RED + wid * 512, lane, col0 + (wid & 1) * 64, kp * 128 + (wid >> 1) * 64, c, wmod, [] {});
      const sig_t g = {mk_rsrc(flags + (FL_M + (u & 15)) * 32, 16 * 4096 + 4), (unsigned)(lane * 4096)};
      asm volatile("" ::"s"(g.rs), "v"(g.vo));
      o = lds_add(smem_base + X_CN + 1280 + lane * 4, 1u);
      if (o == 3) {
        u32x2 r[4];
        static_for<4>([&](auto R) { dsr64<(R.value >> 1) * 1024 + (R.value & 1) * 256>(r[R.value], smem_base + P_RED + (lane >> 5) * 512 + (lane & 31) * 8); });
        asm volatile("s_waitcnt lgkmcnt(0)" : "+v"(r[0]), "+v"(r[1]), "+v"(r[2]), "+v"(r[3])::"memory");
        const f32x2 sum = (__builtin_bit_cast(f32x2, r[0]) + __builtin_bit_cast(f32x2, r[1])) + (__builtin_bit_cast(f32x2, r[2]) + __builtin_bit_cast(f32x2, r[3]));
        asm volatile("buffer_store_dwordx2 %0, %1, %2, 0 offen sc1" ::"v"(sum), "v"(lane * 8), "s"(mk_rsrc(modp + kp * 2048 + col0, 512)) : "memory");
        confirm(word, lane);
        signal(g);
      }
    } else {
      const rsrc_t rsX = mk_rsrc(x, S_LEN * D_DIM * 2);
      const int r0 = (wid - 4) * 4;
      u32x4 a[8];
      static_for<8>([&](auto N) {
        asm volatile("" : "=v"(a[N.value]));
        bload_v<(N.value & 1) * 1024>(a[N.value], (unsigned)((u * 16 + r0) * 2048 + lane * 16), rsX, (N.value >> 1) * 2048);
      });
      __builtin_amdgcn_s_barrier();
      asm volatile("s_waitcnt vmcnt(0)" : "+v"(a[0]), "+v"(a[1]), "+v"(a[2]), "+v"(a[3]), "+v"(a[4]), "+v"(a[5]), "+v"(a[6]), "+v"(a[7])::"memory");
      float sm[4], q[4];
      f32x2 xf[4][8];
#pragma unroll
      for (int i = 0; i < 4; ++i) {
        const unsigned ad = smem_base + (lane >> 5) * Q_SLOT + (lane & 31) * 256 + (((r0 + i) ^ (lane & 7)) * 16);
        dsw128<0>(ad, a[2 * i]); dsw128<2 * Q_SLOT>(ad, a[2 * i + 1]);
#pragma unroll
        for (int j = 0; j < 8; ++j) xf[i][j] = f32x2{bf_lo(a[2 * i + (j >> 2)][j & 3]), bf_hi(a[2 * i + (j >> 2)][j & 3])};
        const f32x2 t = ((xf[i][0] + xf[i][1]) + (xf[i][2] + xf[i][3])) + ((xf[i][4] + xf[i][5]) + (xf[i][6] + xf[i][7]));
        sm[i] = t[0] + t[1];
      }
      const float mrow = sum16(rows4(sm, [](float p, float r) { return p + r; })) * (1.0f / D_DIM);
      bcast4(mrow, sm);
#pragma unroll
      for (int i = 0; i < 4; ++i) {
        f32x2 t0 = xf[i][0] - sm[i], t1 = xf[i][1] - sm[i];
        t0 *= t0; t1 *= t1;
#pragma unroll
        for (int j = 2; j < 8; j += 2) {
          const f32x2 d0 = xf[i][j] - sm[i], d1 = xf[i][j + 1] - sm[i];
          t0 += d0 * d0; t1 += d1 * d1;
        }
        t0 += t1;
        q[i] = t0[0] + t0[1];
      }
      const float rstd = __builtin_amdgcn_rsqf(sum16(rows4(q, [](float p, float r) { return p + r; })) * (1.0f / D_DIM) + 1e-5f);
      asm volatile("ds_write_b64 %0, %1\n\ts_waitcnt lgkmcnt(0)" ::"v"(smem_base + P_ST + (r0 + fq) * 8), "v"(f32x2{rstd, -mrow * rstd}) : "memory");
      prefill(wid - 4); prefill(wid);
    }
    u32x4 mp[8], xv[4], tb[4][4];
    u32x2 rr;
    const rsrc_t rsM = mk_rsrc(modp, 8 * 2048 * 4);
    const unsigned vm = (unsigned)((lane >> 5) * 4096 + wid * 512 + (lane & 31) * 16);
    asm volatile("" ::"s"(rsM), "v"(vm));
    const poll_t pm = poll(3 - o, flags + (FL_M + (1 + (u >> 3)) * 32) * 32, lane, 16, 8u);
    __builtin_amdgcn_s_barrier();
#pragma unroll
    for (int i = 0; i < 8; ++i) { asm volatile("" : "=v"(mp[i])); bload_sc1<0>(mp[i], vm, rsM, i * 8192); }
    const unsigned xa = smem_base + (wid >> 1) * Q_SLOT + (wid & 1) * 4096 + fq * 256 + ((fr ^ fq) * 16);
    dsr128<0>(xv[0], xa); dsr128<1024>(xv[1], xa ^ 64u); dsr128<2048>(xv[2], xa); dsr128<3072>(xv[3], xa ^ 64u);
    dsr64<0>(rr, smem_base + P_ST + fr * 8);
    asm volatile("s_waitcnt lgkmcnt(0)" : "+v"(xv[0]), "+v"(xv[1]), "+v"(xv[2]), "+v"(xv[3]), "+v"(rr)::"memory");
    f32x2 xn[4][4];
#pragma unroll
    for (int i = 0; i < 4; ++i)
#pragma unroll
      for (int d = 0; d < 4; ++d) {
        xn[i][d] = f32x2{bf_lo(xv[i][d]), bf_hi(xv[i][d])} * __uint_as_float(rr[0]) + __uint_as_float(rr[1]);
        asm volatile("" : "+v"(xn[i][d]));
      }
    char* hp = (char*)H + ((size_t)u * 32 + wid * 4) * 1024 + lane * 16;
    asm volatile("s_waitcnt vmcnt(0)" : "+v"(mp[0]), "+v"(mp[1]), "+v"(mp[2]), "+v"(mp[3]), "+v"(mp[4]), "+v"(mp[5]), "+v"(mp[6]), "+v"(mp[7]), "+v"(hp)::"memory");
    poll_end(pm);
    f32x4 mv = sum4(mp) + sum4(mp + 4);
#pragma unroll
    for (int e = 0; e < 4; ++e) {
      mv[e] = rbf(mv[e]);
      if (lane & 32) mv[e] = rbf(1.0f + mv[e]);
    }
    dsw128<0>(smem_base + P_TAB + wid * 1024 + lane * 16, __builtin_bit_cast(u32x4, mv));
    static_for<16>([&](auto N) {
      constexpr int i = N.value >> 2, j = N.value & 3;
      dsr128<i * 128 + (j >> 1) * 512 + (j & 1) * 16>(tb[i][j], smem_base + P_TAB + wid * 1024 + fq * 32);
    });
    static_for<4>([&](auto I) {
      constexpr int i = I.value;
      asm volatile("s_waitcnt lgkmcnt(%4)" : "+v"(tb[i][0]), "+v"(tb[i][1]), "+v"(tb[i][2]), "+v"(tb[i][3]) : "n"(12 - 4 * i) : "memory");
      u32x4 ho;
#pragma unroll
      for (int d = 0; d < 4; ++d) {
        const f32x4 gg = __builtin_bit_cast(f32x4, tb[i][2 + (d >> 1)]), ss = __builtin_bit_cast(f32x4, tb[i][d >> 1]);
        const f32x2 hv = xn[i][d] * ((d & 1) ? hi2(gg) : lo2(gg)) + ((d & 1) ? hi2(ss) : lo2(ss));
        ho[d] = pk_bf16(hv[0], hv[1]);
      }
      gst128(hp + i * 1024, ho);
    });
    const sig_t g = drain(flags + (FL_H + (u >> 3) * 4) * 32, lane, ((u & 7) == 7 && u != 127) ? 8 : 4, 128);
    if (lds_add(smem_base + X_CN + 1024 + lane * 4, 1u) == 7) {
      signal(g);
    }
  } else {
    const rsrc_t rsS = mk_rsrc(word, wid == 7 ? 7 * 8192 + 4 : 0);
    unsigned seen = 0;
    auto mid = [&] { asm volatile("buffer_atomic_add %0, %1, %2, 0 offen sc0 sc1" : "+v"(seen) : "v"(lane * 8192), "s"(rsS) : "memory"); };
    if (!master) {
      const int ql = (wid * 7 + idx - 8) * 64 + lane;
      if (idx >= 8 && wid < 3) touch_lds(smem_base + QX_SINK, (unsigned)((ql >> 1) * 2048 + (xcd & 3) * 512 + 256 + (ql & 1) * 128), mk_rsrc(W, D_DIM * 3 * D_DIM * 2), 0);
      prefill(wid);
    }
    if (idx < 8) {
      if (wid == 0)
        bstore_sc1<0>(u32x4{0u, 0u, 0u, 0u}, (unsigned)(lane * 128), mk_rsrc(flags + (FL_O + v * ((FL_S - FL_O) / 64)) * 32, (FL_S - FL_O) / 64 * 128), 0);
      __builtin_amdgcn_s_sleep(NDLY);
      mod_gemv<2, 1>(smem_base + P_RED + wid * 512, lane, 2048 + (xcd * 8 + wid) * 64, idx * 128, c, wmod, mid);
      const float sum = red_sum<2>(smem_base + P_RED + wid * 512, lane);
      asm volatile("buffer_store_dword %1, %2, %3, 0 offen sc1\n\ts_waitcnt vmcnt(1)" : "+v"(seen) : "v"(sum), "v"(lane * 4), "s"(mk_rsrc(modp + 16384 + ((xcd * 8 + wid) * 8 + idx) * 64, 256)) : "memory");
    } else if (!master) {
      __builtin_amdgcn_s_sleep(TDLY);
#pragma unroll
      for (int q = 0; q < 3; ++q) {
        const int ql = ((q * 8 + wid) * 7 + idx - 8) * 64 + lane + 224 * 12;
        touch_lds(smem_base + QX_SINK, (unsigned)((ql >> 2) * 2048 + (xcd & 3) * 512 + (ql & 3) * 128), mk_rsrc(W, D_DIM * 3 * D_DIM * 2), 0);
      }
      __builtin_amdgcn_s_barrier();
      wait_vm<3>();
      mid();
      asm volatile("s_waitcnt vmcnt(0)" : "+v"(seen)::"memory");
    } else {
      wait_vm<0>();
      __builtin_amdgcn_s_barrier();
      bstore_sc1<0>(u32x4{1u, 1u, 1u, 1u}, (unsigned)(wid * 1024 + lane * 16), mk_rsrc(flags + (FL_S + xcd * 64) * 32, 64 * 128), 0);
      unsigned spins = 0;
      if (wid == 7) {
#pragma unroll 1
        do set_words(flags, lane); while (!words_set(word, lane) && ++spins < (1u << 20));
      }
      __builtin_amdgcn_s_barrier();
      prefill(wid);
      if (wid == 7) {
#pragma unroll 1
        do set_words(flags, lane); while (__builtin_amdgcn_ballot_w64(cnt_read(flags + FL_M * 32, lane, 16) < (lane < 16 ? 8u : 0u)) != 0ull && ++spins < (1u << 20));
      }
      seen = 1;
    }
    if (wid != 7) wait_vm<0>();
    else if ((__builtin_amdgcn_ballot_w64(seen == 0u) & 255u) != 0u) confirm(word, lane);
  }
}
DI void wo_piece(unsigned smem_base, unsigned p, unsigned rot, unsigned voff, rsrc_t rs) {
  const unsigned pb = p >> 2, kb = (pb & 24u) | ((pb + rot) & 7u);
  bload_lds<0>(smem_base + (p < 64u ? QX_WH + p * 1024u : (p - 64u) * 1024u), voff, rs, kb * 65536u + (p & 3u) * 16384u);
}

DI void qkv_ldw(unsigned smem_base, int lane, int sw, int n0, rsrc_t rsW, unsigned so, unsigned u) {
#pragma unroll
  for (int i = 0; i < 4; ++i) {
    const int r = (i & 1) * 8 + (lane >> 3), ch = lane & 7;
    const int g = ((r >> 1) & 1) | (((r >> 3) & 1) << 1);
    bload_lds<0>(smem_base + 9216 + sw * 4096 + so + i * 1024, (unsigned)(r * 6144 + sw * 2048 + (n0 + (((ch >> 1) ^ g) * 16) + (ch & 1) * 8) * 2), rsW,
                 u * 196608u + (i >> 1) * 98304u);
  }
}
DI void qkv_loader(unsigned smem_base, int lane, int sw, int m0, int n0, const bf16* H, rsrc_t rsW,
                   unsigned rot, unsigned voffO, rsrc_t rsO) {
  const rsrc_t rsH = mk_rsrc(H, S_LEN * D_DIM * 2);
  int fb0 = (m0 >> 4) - 1 + 3 * sw; fb0 = fb0 < 0 ? 0 : fb0;
  const unsigned voffH0 = (unsigned)(fb0 * 32768 + lane * 16);
  const unsigned voffH1 = (unsigned)(((m0 >> 4) + 3 * sw) * 32768 + lane * 16);
  const unsigned m0a = smem_base + sw * 3072;
  auto ldh = [&](unsigned so, unsigned u) {
    bload_lds_sc1<0>(m0a + so, voffH0, rsH, u * 1024u);
    bload_lds_sc1<0>(m0a + so + 1024, voffH1, rsH, u * 1024u);
    bload_lds_sc1<0>(m0a + so + 2048, voffH1, rsH, u * 1024u + 32768u);
  };
  auto ldw = [&](unsigned so, unsigned u) { qkv_ldw(smem_base, lane, sw, n0, rsW, so, u); };
  asm volatile("" ::"v"(voffH0), "v"(voffH1), "s"(rsH), "s"(m0a));
  __builtin_amdgcn_s_barrier();
#pragma unroll
  for (int u = 1; u <= NP1; ++u) ldh(u * Q_SLOT, u);
  wait_vm<3 * (NP1 - 1)>();
  __builtin_amdgcn_s_barrier();
#pragma unroll
  for (int u = NP1 + 1; u < Q_NSL; ++u) ldh(u * Q_SLOT, u);
  static_for<Q_NSL - 2, 1>([&](auto U) {
    constexpr int u = U.value;
    ldh((u - 1) * Q_SLOT, u + Q_NSL - 1); ldw((u - 1) * Q_SLOT, u + Q_NSL - 1);
    wait_vm<3 * (Q_NSL - 2 - u) + 7 * u>();
    __builtin_amdgcn_s_barrier();
  });
  unsigned sb = (Q_NSL - 3) * Q_SLOT;
  for (int u = Q_NSL - 2; u < 32 - (Q_NSL - 1); ++u) {
    ldh(sb, u + Q_NSL - 1); ldw(sb, u + Q_NSL - 1);
    sb = (sb == (Q_NSL - 1) * Q_SLOT) ? 0u : sb + Q_SLOT;
    wait_vm<(Q_NSL - 2) * 7>();
    __builtin_amdgcn_s_barrier();
  }
  for (int t = 0; t < 6; ++t) {
    const unsigned p0 = (unsigned)(t * 21 + (t >= 3 ? 1 : 0) + sw * 7);
#pragma unroll
    for (int i = 0; i < 7; ++i) wo_piece(smem_base, p0 + i, rot, voffO, rsO);
    if (t < 5) wait_vm<(Q_NSL - 2) * 7>();
    __builtin_amdgcn_s_barrier();
  }
}

template <int WM>
DI void qkv_mfma(unsigned smem_base, int lane, int wn, int bm, int bn, const bf16* H, bf16* __restrict__ O) {
  const int fr = lane & 15, fq = lane >> 4;
  unsigned bb0, bb1;
  const unsigned ab = smem_base + lane * 16;
  {
    const int q = (lane >> 2) & 3, p = lane & 3;
    const int r = 8 * fq + q;
    const int g = ((r >> 1) & 1) | (((r >> 3) & 1) << 1);
    bb0 = smem_base + 9216 + r * 128 + (((wn * 2) ^ g) * 32) + 8 * p;
    bb1 = bb0 ^ 32u;
  }
  const bool par = fr & 1, c0 = 4 * fq + (fr & 1) <= fr, c1 = 4 * fq + 2 + (fr & 1) <= fr;
  const unsigned s0 = par ? 0x01000c0cu : 0x0c0c0100u, s1 = par ? 0x03020c0cu : 0x0c0c0302u;
  u32x4 sel = {c0 ? s0 : 0x0c0c0c0cu, c1 ? s1 : 0x0c0c0c0cu, c0 ? 0x0c0c0c0cu : s0, c1 ? 0x0c0c0c0cu : s1};
  unsigned hx = ab + QX_HX + wn * 3072;
  asm volatile("" : "+v"(sel), "+v"(hx));
  const rsrc_t rsO = mk_rsrc((char*)O + (size_t)((bm * 2 + WM) * 32 + bn * 2 + wn) * 4096, 4096);
  f32x4 acc[4][6], acch[2];
#pragma unroll
  for (int i = 0; i < 4; ++i)
#pragma unroll
    for (int j = 0; j < 6; ++j) { acc[i][j] = f32x4{0, 0, 0, 0}; asm volatile("" : "+v"(acc[i][j])); }
#pragma unroll
  for (int j = 0; j < 2; ++j) { acch[j] = f32x4{0, 0, 0, 0}; asm volatile("" : "+v"(acch[j])); }
  __builtin_amdgcn_s_setprio(2);
  u32x4 aX[5], aY[5];
  u32x2 bXl[6], bXh[6], bYl[6], bYh[6];
  unsigned ra = ab, rb0 = bb0, rb1 = bb1, so = 0;
#define Q_READ(AR, BL, BH, R)                                                                                    \
  do {                                                                                                           \
    if ((R) < 12) {                                                                                              \
      constexpr int jb = ((R) < 12 ? (R) : 0) >> 1;                                                              \
      if ((R) & 1) dsr_tr<(jb >> 1) * 4096 + 512>(BH[jb], (jb & 1) ? rb1 : rb0);                                 \
      else dsr_tr<(jb >> 1) * 4096>(BL[jb], (jb & 1) ? rb1 : rb0);                                               \
    } else if ((R) < 16) {                                                                                       \
      constexpr int ia = ((R) >= 12 && (R) < 16) ? (R) - 12 : 0;                                                 \
      dsr128<1024 + WM * 4096 + ia * 1024>(AR[ia], ra);                                                          \
    } else {                                                                                                     \
      dsr128<0>(AR[4], ra);                                                                                      \
    }                                                                                                            \
  } while (0)
#define Q_MFMA(AR, BB, N)                                                                                        \
  do {                                                                                                           \
    if ((N) < 16) MFMA_AV(acc[(N) & 3][((N) < 24 ? (N) : 0) >> 2], BB[((N) < 24 ? (N) : 0) >> 2], AR[(N) & 3]);  \
    else if ((N) < 24) MFMA_AV(acc[(N) & 3][((N) < 24 ? (N) : 0) >> 2], AR[(N) & 3], BB[((N) < 24 ? (N) : 0) >> 2]); \
    else if (WM == 0) MFMA_AV(acch[(N) & 1], BB[2 + ((N) & 1)], AR[4]);                                          \
    else MFMA_AV(acch[(N) & 1], AR[4], BB[4 + ((N) & 1)]);                                                       \
  } while (0)
#define Q_ADV(K)                                                                                                 \
  do {                                                                                                           \
    if ((K) == 0) asm volatile("s_add_u32 %0, %0, %1\n\ts_cmp_eq_u32 %0, %2\n\ts_cselect_b32 %0, 0, %0" : "+s"(so) : "n"(Q_SLOT), "n"(Q_NSL * Q_SLOT) : "scc"); \
    else if ((K) == 1) asm volatile("v_add_u32 %0, %1, %2" : "=v"(rb0) : "s"(so), "v"(bb0));                     \
    else if ((K) == 2) asm volatile("v_add_u32 %0, %1, %2" : "=v"(rb1) : "s"(so), "v"(bb1));                     \
    else asm volatile("v_add_u32 %0, %1, %2" : "=v"(ra) : "s"(so), "v"(ab));                                     \
  } while (0)
#define Q_UNIT(AR, BL, BH, NAR, NBL, NBH)                                                                        \
  do {                                                                                                           \
    WAIT_LGKM0();                                                                                                \
    u32x4 bb[6];                                                                                                 \
    _Pragma("unroll") for (int j = 0; j < 6; ++j) bb[j] = u32x4{BL[j][0], BL[j][1], BH[j][0], BH[j][1]};         \
    SCHED0();                                                                                                    \
    static_for<26>([&](auto N) {                                                                                 \
      constexpr int n = N.value;                                                                                 \
      if constexpr (n == QBAR) __builtin_amdgcn_s_barrier();                                                     \
      Q_MFMA(AR, bb, n);                                                                                         \
      if constexpr (n >= QBAR && n < QBAR + 17) Q_READ(NAR, NBL, NBH, n - QBAR);                                 \
      if constexpr (n >= QBAR + 18 && n < QBAR + 22) Q_ADV(n - QBAR - 18);                                       \
    });                                                                                                          \
    SCHED0();                                                                                                    \
  } while (0)
  {
    const rsrc_t rsH = mk_rsrc(H, S_LEN * D_DIM * 2);
    unsigned vh = lane * 16 + (bm * 8 + WM * 4) * 32768, vl = lane * 16 + (bm ? bm * 8 - 1 : 0) * 32768;
    asm volatile("" : "+v"(vh), "+v"(vl), "+v"(rb0), "+v"(rb1) : "s"(rsH));
    __builtin_amdgcn_s_barrier();
    static_for<5>([&](auto I) { asm volatile("" : "=v"(aX[I.value])); bload_sc1<0>(aX[I.value], I.value < 4 ? vh : vl, rsH, (I.value & 3) * 32768); });
    static_for<12>([&](auto R) { Q_READ(aX, bXl, bXh, R.value); });
    static_for<4>([&](auto K) { Q_ADV(K.value); });
    asm volatile("s_waitcnt vmcnt(0)" : "+v"(aX[0]), "+v"(aX[1]), "+v"(aX[2]), "+v"(aX[3]), "+v"(aX[4])::"memory");
  }
  for (int u = 0; u < 32; u += 2) {
    Q_UNIT(aX, bXl, bXh, aY, bYl, bYh);
    Q_UNIT(aY, bYl, bYh, aX, bXl, bXh);
  }
  asm volatile("s_waitcnt lgkmcnt(0)\n\ts_nop 7" ::: "memory");
#pragma unroll
  for (int i = 0; i < 5; ++i) asm volatile("" : "+v"(aX[i]));
#pragma unroll
  for (int j = 0; j < 6; ++j) asm volatile("" : "+v"(bXl[j]), "+v"(bXh[j]));
#pragma unroll
  for (int i = 0; i < 4; ++i)
#pragma unroll
    for (int j = 0; j < 6; ++j) asm volatile("" : "+v"(acc[i][j]));
  asm volatile("" : "+v"(acch[0]), "+v"(acch[1]));
  SCHED0();
  u32x4 kk[5], vv[5];
  if (WM == 0) {
    kk[4] = pack8(acc[3][2], acc[3][3]); vv[4] = pack8(acc[3][4], acc[3][5]);
    dsw128<0>(hx, kk[4]); dsw128<1024>(hx, vv[4]);
  } else {
    dsw128<2048>(hx, pack8(acch[0], acch[1]));
  }
  asm volatile("s_waitcnt lgkmcnt(0)" ::: "memory");
  __builtin_amdgcn_s_barrier();
  if (WM == 0) dsr128<2048>(vv[0], hx);
  else { dsr128<0>(kk[0], hx); dsr128<1024>(vv[0], hx); }
  SCHED0();
  if (WM == 0) kk[0] = pack8(acch[0], acch[1]);
#pragma unroll
  for (int i = 0; i < (WM == 0 ? 3 : 4); ++i) kk[i + 1] = pack8(acc[i][2], acc[i][3]);
  const float cs = 0.17677669529663687f * 1.4426950408889634f;
  float l0[4], l1[4], tm[4];
#pragma unroll
  for (int ii = 1; ii < 5; ++ii) {
    const int i = ii & 3;
    const u32x4 q = pack8(acc[i][0], acc[i][1]);
    if (WM == 1 && i == 0) asm volatile("s_waitcnt lgkmcnt(0)" : "+v"(kk[0]), "+v"(vv[0])::"memory");
    const f32x4 sc = mfma0(kk[i + 1], q), sp = mfma0(kk[i], q);
    float p0 = par ? sp[1] : sp[0], p1 = par ? sp[3] : sp[2];
    if (WM == 0 && i == 0 && bm == 0) { p0 = -5.656854249e9f; p1 = -5.656854249e9f; }
    l0[i] = c0 ? (par ? sc[1] : sc[0]) : p0;
    l1[i] = c1 ? (par ? sc[3] : sc[2]) : p1;
    tm[i] = fmax2(l0[i], l1[i]);
  }
#pragma unroll
  for (int i = 0; i < (WM == 0 ? 3 : 4); ++i) vv[i + 1] = pack8(acc[i][4], acc[i][5]);
  bcast4(rows4(tm, [](float a, float b) { return fmax2(a, b); }), tm);
#pragma unroll
  for (int i = 0; i < 4; ++i) {
    l0[i] = __builtin_amdgcn_exp2f((l0[i] - tm[i]) * cs);
    l1[i] = __builtin_amdgcn_exp2f((l1[i] - tm[i]) * cs);
    tm[i] = l0[i] + l1[i];
  }
  bcast4(__builtin_amdgcn_rcpf(rows4(tm, [](float a, float b) { return a + b; })), tm);
  if (WM == 0) asm volatile("s_waitcnt lgkmcnt(0)" : "+v"(vv[0])::"memory");
#pragma unroll
  for (int i = 0; i < 4; ++i) {
    const unsigned pp = pk_bf16(l0[i] * tm[i], l1[i] * tm[i]);
    const u32x4 pr = {__builtin_amdgcn_perm(pp, pp, sel[0]), __builtin_amdgcn_perm(pp, pp, sel[1]),
                      __builtin_amdgcn_perm(pp, pp, sel[2]), __builtin_amdgcn_perm(pp, pp, sel[3])};
    const u32x4 ov = pack8(mfma0(u32x4{vv[i + 1][0], vv[i + 1][1], vv[i][0], vv[i][1]}, pr), mfma0(u32x4{vv[i + 1][2], vv[i + 1][3], vv[i][2], vv[i][3]}, pr));
    asm volatile("buffer_store_dwordx4 %0, %1, %2, 0 offen offset:%3 sc1\n\ts_nop 1" ::"v"(ov), "v"(lane * 16), "s"(rsO), "i"(i * 1024) : "memory");
  }
}

template <class GST>
DI void attn_phase(unsigned smem_base, int wid, bf16* H, const bf16* __restrict__ W, bf16* O,
                   const bf16* __restrict__ wo, const bf16* __restrict__ X, float* modp, bf16* Y, float* stat,
                   unsigned* flags, const bf16* __restrict__ wg, const bf16* __restrict__ wu,
                   unsigned* ctab, const bf16* __restrict__ cnd, const bf16* __restrict__ wmod, GST gst) {
  const int tid = threadIdx.x, lane = tid & 63;
  const int xcd = blockIdx.x & 7, idx = blockIdx.x >> 3;
  const int bm = (xcd >> 2) * 8 + (idx & 7), bn = (xcd & 3) * 4 + (idx >> 3);
  const int rb = xcd * 2 + (idx & 1), cg = idx >> 1;
  const unsigned rot = __builtin_amdgcn_readfirstlane((cg >> 2) * 2);
  const unsigned cnt = smem_base + QX_CNT, cntl = cnt + lane * 4;
  const rsrc_t rsW = mk_rsrc(W, D_DIM * 3 * D_DIM * 2);
  first_phase(smem_base, wid, lane, X, cnd, wmod, W, modp, H, flags, [&](int w) {
#pragma unroll
    for (int i = 0; i < 3; ++i) {
      const int p = w + 8 * i < 21 ? w + 8 * i : 20, u = (p * 11) >> 5;
      qkv_ldw(smem_base, lane, p - 3 * u, bn * 64, rsW, u * Q_SLOT, u);
    }
  });
  if (wid < 4) {
    if (wid & 1) qkv_mfma<1>(smem_base, lane, wid >> 1, bm, bn, H, O);
    else qkv_mfma<0>(smem_base, lane, wid >> 1, bm, bn, H, O);
  } else {
    const rsrc_t rsO = mk_rsrc(wo, D_DIM * D_DIM * 2);
    const unsigned voffO = (unsigned)((lane >> 3) * 2048 + cg * 128 + ((((lane & 7) >> 1) ^ (lane >> 4)) * 32) + (lane & 1) * 16);
    if (wid < 7) qkv_loader(smem_base, lane, wid - 4, bm * 128, bn * 64, H, rsW, rot, voffO, rsO);
    else {
      const unsigned vgu = idx < 16 ? 0x40000000u : (unsigned)((((idx - 16) * 64 + lane) / 7) * (F_DIM * 2) + xcd * 896 + (((idx - 16) * 64 + lane) % 7) * 128);
      const poll_t ph = poll(0u, flags + (FL_H + bm * 4 + (xcd & 3)) * 32, lane, 1, bm ? 9u : 8u);
      asm volatile("s_barrier" ::: "memory");
      SCHED0();
      unsigned* r1 = flags + (FL_M + (1 + xcd) * 32 + 24) * 32;
      const sig_t gi = drain(idx < 8 ? r1 : flags + (FL_M + 24 + xcd) * 32, lane, idx < 16 ? 1 : 0, 4);
      poll_end(ph);
      signal(gi);
      if (idx < 8) {
        const int v = xcd * 8 + idx;
        unsigned t = 0;
        for (int i = 0; i < 4; ++i) __builtin_amdgcn_s_barrier();
        asm volatile("buffer_atomic_add %0, %1, %2, 0 offen sc0 sc1\n\ts_barrier\n\ts_barrier\n\ts_barrier\n\ts_barrier\n\ts_waitcnt vmcnt(0)" : "+v"(t) : "v"(lane * 128), "s"(mk_rsrc(r1, 128)) : "memory");
#pragma unroll 1
        for (unsigned spins = 0; __builtin_amdgcn_readfirstlane(t) < 8u && spins < (1u << 20); ++spins) t = cnt_read(r1, lane, 1);
        static_for<2>([&](auto I) { bload_lds_sc1<I.value * 1024>(smem_base + G3_CT, lane * 16, mk_rsrc(modp + 16384 + v * 512, 2048), 0); });
        for (int i = 0; i < 4; ++i) __builtin_amdgcn_s_barrier();
        wait_vm<0>();
        float sum = red_sum<8>(smem_base + G3_CT, lane);
        sum = (v >> 4) == 2 ? gain_nz(sum) : rbf(sum);
        asm volatile("buffer_store_dword %0, %1, %2, 0 offen sc1" ::"v"(sum), "v"(lane * 4), "s"(mk_rsrc(modp + 49152 + v * 64, 256)) : "memory");
        asm volatile("buffer_store_short_d16_hi %0, %1, %2, 0 offen sc1" ::"v"(sum), "v"((lane >> 5) * 128 + (lane & 31) * 2),
                     "s"(mk_rsrc(ctab + (v & 15) * 64 + (v & 16), (v >> 4) == 1 || (v >> 4) == 2 ? 256 : 0)) : "memory");
        for (int i = 0; i < 4; ++i) __builtin_amdgcn_s_barrier();
        signal(drain(flags + (FL_M + 16 + idx) * 32, lane, 9, 4096));
      }
      {
        int nxt = TS + idx % TP, ev = 0;
#pragma unroll 1
        for (int i = idx < 8 ? 16 : 0; i < 27; ++i) {
          if (i == WOS && idx >= 16) {
            touch_lds(smem_base + QX_SINK, (blockIdx.x - 128) * 16384 + lane * 128, rsO, 0);
            touch_lds(smem_base + QX_SINK, (blockIdx.x - 128) * 16384 + lane * 128, rsO, 8192);
          }
          if (i == nxt && ev < 2) {
            touch_lds(smem_base + QX_SINK, vgu, mk_rsrc(ev ? wu : wg, D_DIM * F_DIM * 2), 0);
            nxt += TP; ++ev;
          }
          __builtin_amdgcn_s_barrier();
        }
      }
      unsigned* cr = flags + (FL_M + (idx == 15 ? 0 : 1 + xcd) * 32 + 16) * 32;
      const int nl = idx == 15 ? 16 : 8;
      unsigned t = 0;
      asm volatile("buffer_atomic_add %0, %1, %2, 0 offen sc0 sc1" : "+v"(t) : "v"(lane * 128), "s"(mk_rsrc(cr, nl * 128)) : "memory");
      wo_piece(smem_base, 63u, rot, voffO, rsO);
      __builtin_amdgcn_s_barrier();
      __builtin_amdgcn_s_barrier();
      asm volatile("s_waitcnt vmcnt(1)" : "+v"(t)::"memory");
      for (unsigned spins = 0; __builtin_amdgcn_ballot_w64(t < (lane < nl ? 8u : 0u)) != 0ull && spins < (1u << 20); ++spins) {
        if (idx == 15) set_words(flags, lane);
        t = cnt_read(cr, lane, nl);
      }
      static_for<4>([&](auto I) { bload_lds_sc1<I.value * 1024>(smem_base + G3_CT, lane * 16, mk_rsrc(ctab, 4096), 0); });
#pragma unroll
      for (int i = 0; i < 2; ++i)
        asm volatile("s_mov_b32 m0, %0\n\ts_nop 0\n\tbuffer_load_dword %1, %2, 0 offen sc1 lds" ::"s"(smem_base + QX_GT + i * 256), "v"(lane * 4),
                     "s"(mk_rsrc(modp + 49152 + i * 2048 + cg * 64, 256)) : "memory");
      for (int i = 30; i < 33; ++i) __builtin_amdgcn_s_barrier();
      wo_piece(smem_base, 127u, rot, voffO, rsO);
    }
    __builtin_amdgcn_s_barrier();
    wait_vm<0>();
    lds_add(cntl, 1u);
  }
  const int fr = lane & 15, fq = lane >> 4;
  const int kq = wid & 3, rg = rb * 2 + (wid >> 2);
  const int ko = (kq + cg) & 3;
  lds_wait<2>(cnt, 4);
  u32x2 wl[8][4], wh[8][4];
  {
    const int q = (lane >> 2) & 3, p = lane & 3, r = 4 * fq + q;
    const unsigned wb = smem_base + (kq < 2 ? QX_WH + kq * 32768 : (kq - 2) * 32768) + r * 128 + (((r >> 1) & 3) * 32) + 8 * p;
    unsigned wa[4];
#pragma unroll
    for (int f = 0; f < 4; ++f) wa[f] = wb ^ (f * 32);
    static_for<32>([&](auto E) {
      constexpr int s = E.value >> 2, f = E.value & 3;
      dsr_tr<s * 4096>(wl[s][f], wa[f]);
      dsr_tr<s * 4096 + 2048>(wh[s][f], wa[f]);
    });
  }
  const rsrc_t rsA = mk_rsrc((const char*)O + (size_t)rg * 131072, 131072);
  unsigned voffA[4], hs[8];
#pragma unroll
  for (int i = 0; i < 4; ++i) { voffA[i] = lane * 16 + ((ko + 1 + i) & 3) * 1024; asm volatile("" : "+v"(voffA[i])); }
#pragma unroll
  for (int i = 0; i < 8; ++i) { hs[i] = __builtin_amdgcn_readfirstlane((kq * 8 + ((i + rot) & 7)) * 4096); asm volatile("" : "+s"(hs[i])); }
  asm volatile("" ::"s"(rsA));
  if (wid < 4) {
    const sig_t g = drain(flags + (FL_O + bm * 8 + (bn & 1)) * 32, lane, 4, 256);
    if (lds_add(cntl + 256, 1u) == 3u) signal(g);
  }
  const poll_t pl = poll(wid - 7, flags + (FL_O + rb * 8 + (cg & 3) * 2) * 32, lane, 2, 8u);
  asm volatile("s_waitcnt lgkmcnt(0)" ::: "memory");
  __builtin_amdgcn_s_barrier();
  u32x4 wf[8][4];
#pragma unroll
  for (int s = 0; s < 8; ++s)
#pragma unroll
    for (int f = 0; f < 4; ++f) {
      asm volatile("" : "+v"(wl[s][f]), "+v"(wh[s][f]));
      wf[s][f] = u32x4{wl[s][f][0], wl[s][f][1], wh[s][f][0], wh[s][f][1]};
    }
  const unsigned voffL = lane * 16;
  u32x4 af[32], xv[2], pr[3][4], gt[4], ct[4];
  f32x4 acc[4][4];
  auto aload = [&](auto N) {
    constexpr int n = N.value;
    asm volatile("" : "=v"(af[n]));
    bload_sc1<0>(af[n], voffA[n >> 3], rsA, hs[n & 7]);
  };
  static_for<GDEP>([&](auto N) { aload(N); });
  const unsigned sl = smem_base + 49152 + voffL + (wid >> 2) * 49152;
  const int sw = ((fq & 1) << 1) | (fq >> 1);
  SCHED0();
  static_for<32>([&](auto N) {
    constexpr int n = N.value, ps = n >> 3, s = n & 7, XG = 32 - GDEP;
    constexpr int last = n - 1 + GDEP < 31 ? n - 1 + GDEP : 31;
    asm volatile("s_waitcnt vmcnt(%1)" : "+v"(af[n]) : "n"(last - n + (n > XG ? 2 : 0) + (n > GS0 + 3 ? 6 : (n > GS0 ? 2 * (n - GS0) : 0))) : "memory");
    if constexpr (n == 0) poll_end(pl);
    if constexpr (n == 28) { WAIT_LGKM0(); __builtin_amdgcn_s_barrier(); }
    static_for<4>([&](auto F) {
      constexpr int f = F.value;
      if constexpr (s == 0) asm volatile("v_mfma_f32_16x16x32_bf16 %0, %1, %2, 0" : "=&v"(acc[ps][f]) : "v"(wf[s][f]), "v"(af[n]));
      else MFMA_AV(acc[ps][f], wf[s][f], af[n]);
      if constexpr (f == 0 && n + GDEP < 32) aload(ic<(n + GDEP < 32 ? n + GDEP : 0)>{});
      if constexpr (f == 0 && n == XG) {
        const rsrc_t rsX = mk_rsrc(X, S_LEN * D_DIM * 2);
        const unsigned vx = (unsigned)(((rg * 64 + ko * 16 + fr) * D_DIM + cg * 64 + sw * 8) * 2);
        asm volatile("" : "=v"(xv[0]), "=v"(xv[1]));
        bload_v<0>(xv[0], vx, rsX, 0); bload_v<64>(xv[1], vx, rsX, 0);
      }
      if constexpr ((f & 1) && n >= GS0 && n < GS0 + 3) gst(ic<(n >= GS0 ? n - GS0 : 0)>{}, ic<(f >> 1)>{});
      if constexpr (ps > 0 && (s == 2 || s == 3) && (f & 1)) {
        constexpr int e = (s - 2) * 2 + (f >> 1);
        dsw128<(ps - 1) * 4096 + e * 1024>(sl + ((kq + ps) & 3) * 12288, __builtin_bit_cast(u32x4, acc[ps - 1][e]));
      }
      if constexpr (n >= 28 && n < 31) dsr128<(n - 28) * 4096 + f * 1024>(pr[n - 28][f], sl + kq * 12288);
      if constexpr (n == 31) { dsr128<f * 64>(gt[f], smem_base + QX_GT + fq * 16); dsr128<256 + f * 64>(ct[f], smem_base + QX_GT + fq * 16); }
    });
  });
  asm volatile("s_waitcnt vmcnt(6)" : "+v"(xv[0]), "+v"(xv[1])::"memory");
  asm volatile("s_waitcnt lgkmcnt(0)\n\ts_nop 4" ::: "memory");
  f32x4 fin[4], yv[4];
#pragma unroll
  for (int f = 0; f < 4; ++f) asm volatile("" : "+v"(acc[3][f]), "+v"(pr[0][f]), "+v"(pr[1][f]), "+v"(pr[2][f]), "+v"(gt[f]), "+v"(ct[f]));
#pragma unroll
  for (int c = 0; c < 2; ++c) {
    const auto x0 = __builtin_amdgcn_permlane16_swap(xv[c][0], xv[c][2], false, false);
    const auto x1 = __builtin_amdgcn_permlane16_swap(xv[c][1], xv[c][3], false, false);
#pragma unroll
    for (int h = 0; h < 2; ++h) {
      const int f = 2 * c + h;
      const f32x4 a = (acc[3][f] + __builtin_bit_cast(f32x4, pr[0][f])) + (__builtin_bit_cast(f32x4, pr[1][f]) + __builtin_bit_cast(f32x4, pr[2][f]));
      const f32x4 xf = {bf_lo(x0[h]), bf_hi(x0[h]), bf_lo(x1[h]), bf_hi(x1[h])};
      fin[f] = xf + __builtin_bit_cast(f32x4, gt[f]) * a;
      yv[f] = fin[f] * __builtin_bit_cast(f32x4, ct[f]);
    }
    const auto y0 = __builtin_amdgcn_permlane16_swap(pk_bf16(yv[2 * c][0], yv[2 * c][1]), pk_bf16(yv[2 * c + 1][0], yv[2 * c + 1][1]), false, false);
    const auto y1 = __builtin_amdgcn_permlane16_swap(pk_bf16(yv[2 * c][2], yv[2 * c][3]), pk_bf16(yv[2 * c + 1][2], yv[2 * c + 1][3]), false, false);
    gst128((char*)Y + ((size_t)(rg >> 2) * 32 + cg * 2 + c) * 16384 + ((rg & 3) * 4 + ko) * 1024 + (sw * 16 + fr) * 16,
           u32x4{y0[0], y1[0], y0[1], y1[1]});
  }
  const f32x4 s4 = (fin[0] + fin[1]) + (fin[2] + fin[3]);
  const float s = rowsum4((s4[0] + s4[1]) + (s4[2] + s4[3]));
  const float mb = s * (1.0f / 64.0f);
  f32x4 q4 = {0, 0, 0, 0};
#pragma unroll
  for (int f = 0; f < 4; ++f) { const f32x4 d = fin[f] - mb; q4 += d * d; }
  const float m2 = rowsum4((q4[0] + q4[1]) + (q4[2] + q4[3]));
  asm volatile("buffer_store_dwordx2 %0, %1, %2, 0 offen sc1" ::"v"(f32x2{s, m2}), "v"(lane * 8),
               "s"(mk_rsrc(stat + ((size_t)cg * S_LEN + rg * 64 + ko * 16) * 2, 128)) : "memory");
}

#define G3_STAGE(SLOT, P, U)                                                                               \
  do {                                                                                                     \
    if ((P) == 0) bload_lds_sc1<0>(m0w + 65536 + (SLOT) * 16384, voffA, rsA, (unsigned)(U) * 16384u);      \
    else if ((P) == 1) bload_lds_sc1<1024>(m0w + 65536 + (SLOT) * 16384, voffA, rsA, (unsigned)(U) * 16384u);  \
    else if ((P) == 2) bload_lds_nt<0>(m0w + (SLOT) * 16384, voffB0, rsB, (unsigned)(U) * 229376u);        \
    else bload_lds_nt<0>(m0w + (SLOT) * 16384 + 1024, voffB1, rsB, (unsigned)(U) * 229376u);               \
  } while (0)

DI int g3_line(int gx, int p) {
  const int t = p + ((3 + gx) & 7);
  return 7 * gx + (t >= 7 ? t - 7 : t);
}
template <int FN, class WST, class HOOK, class PRE>
DI void g3_body(char* smem, unsigned smem_base, int wm, int wn, int wid, int lane, int bm, int bn, rsrc_t rsA, rsrc_t rsB,
                rsrc_t rsP, rsrc_t rsE, unsigned voffA, unsigned voffB0, unsigned voffB1, bf16* act, WST wst, HOOK hook, PRE pre) {
  constexpr int NB = 2 * FN, NM = 4 * NB, RD = 5 + 2 * NB;
  int fr = lane & 15, fq = lane >> 4;
  f32x4 acc[4][NB], accC[2];
  accC[0] = f32x4{0, 0, 0, 0}; accC[1] = f32x4{0, 0, 0, 0};
  asm volatile("" : "+v"(accC[0]), "+v"(accC[1]));
  const int rot = wm % FN;
  unsigned cad = fr >= 2 || wm >= FN ? smem_base + X_CN + 1536 : smem_base + G3_CT + (fr & 1) * 64 + fq * 16;
  unsigned cin = fr >= 2 || wm >= FN ? 0u : 512u;
  asm volatile("" : "+v"(cin));
#pragma unroll
  for (int i = 0; i < 4; ++i)
#pragma unroll
    for (int j = 0; j < NB; ++j) { acc[i][j] = f32x4{0, 0, 0, 0}; asm volatile("" : "+v"(acc[i][j])); }
  unsigned aad = smem_base + 65536 + wm * 4096 + lane * 16;
  unsigned bad[FN];
  {
    const int q = (lane >> 2) & 3, p = lane & 3;
    const int r = 8 * fq + q;
    const int sw = (r & 3) | (((r >> 3) & 1) << 2);
#pragma unroll
    for (int j = 0; j < FN; ++j) {
      const int gi = wn * 4 + (j + rot) % FN + (bn & 1);
      bad[j] = smem_base + r * 256 + ((gi ^ sw) * 32) + 8 * p;
    }
  }
  u32x4 aX[5], aY[5];
  u32x2 bXl[NB], bXh[NB], bYl[NB], bYh[NB];
  const unsigned m0w = smem_base + wid * 2048;
  const unsigned voffP = lane * 16;
#define G3_READ(AR, BL, BH, SLOT, R)                                                                       \
  do {                                                                                                     \
    if ((R) < 2 * NB) {                                                                                    \
      constexpr int jb = ((R) < 2 * NB ? (R) : 0) >> 1;                                                    \
      constexpr int off = (SLOT) * 16384 + (jb / FN) * 8192 + ((R) & 1) * 1024;                            \
      if ((R) & 1) dsr_tr<off>(BH[jb], bad[jb % FN]);                                                       \
      else dsr_tr<off>(BL[jb], bad[jb % FN]);                                                               \
    } else if ((R) < 2 * NB + 4) {                                                                         \
      constexpr int ia = ((R) >= 2 * NB && (R) < 2 * NB + 4) ? (R) - 2 * NB : 0;                           \
      dsr128<(SLOT) * 16384 + ia * 1024>(AR[ia], aad);                                                      \
    } else {                                                                                               \
      if ((SLOT) == 0) cad += cin;                                       \
      dsr128<(SLOT) * 128>(AR[4], cad);                                                                    \
    }                                                                                                      \
  } while (0)
  auto rd = [&](auto SL, auto RR, u32x4 (&AR)[5], u32x2 (&BL)[NB], u32x2 (&BH)[NB]) { G3_READ(AR, BL, BH, SL.value, RR.value); };
#define G3_UNIT(AR, BL, BH, NAR, NBL, NBH, SLOT, U, LI)                                                    \
  do {                                                                                                     \
    WAIT_LGKM0();                                                                                          \
    u32x4 bb[NB];                                                                                          \
    _Pragma("unroll") for (int j = 0; j < NB; ++j) bb[j] = u32x4{BL[j][0], BL[j][1], BH[j][0], BH[j][1]};  \
    SCHED0();                                                                                              \
    asm volatile("s_nop 1");                                                                               \
    static_for<NM>([&](auto N) {                                                                           \
      constexpr int n = N.value;                                                                           \
      if constexpr (n == NM / 2 && !(LI && (SLOT) == 3)) {                                                 \
        wait_vm<(LI && (SLOT) == 0 && FN == 3) ? 25 : 8>(); __builtin_amdgcn_s_barrier();                  \
      }                                                                                                    \
      if constexpr (n == NM / 2 + TPOS && !LI) {                                                           \
                      \
        if (((U) & 7) == wid) bload_lds<0>(smem_base + 131072 + 4096, voffP, rsP, (unsigned)(U) * 1024u);  \
        if (FN == 3 && (SLOT) == 0 && (U) * 8 + wid == CT3 * 8 + 7) touch_lds(smem_base + 131072 + 4096, voffP * 2 + (blockIdx.x >> 3) * 2048u + 8192u, rsE, 0);  \
      }                                                                                                    \
      MFMA_AV(acc[n & 3][n >> 2], bb[n >> 2], AR[n & 3]);                                                  \
      if constexpr (n < NM / 2) {                                                                          \
        if constexpr (LI && (SLOT) == 0 && n == 0) hook(ic<0>{});                                          \
        if constexpr (n % 3 == 1 && n / 3 < 4) {                                                           \
          if constexpr (LI && (SLOT) > 0) wst(ic<((SLOT) + 3) & 3>{}, ic<(n / 3) & 3>{});                  \
          else G3_STAGE(((SLOT) + 3) & 3, n / 3, (U) + 3);                                                 \
        }                                                                                                  \
      } else if constexpr (LI && (SLOT) == 3) {                                                            \
        if constexpr (n == NM / 2) hook(ic<1>{});                                                          \
      } else {                                                                                             \
        constexpr int r0 = (n - NM / 2) * 2;                                                               \
        if constexpr (r0 < RD) rd(ic<((SLOT) + 1) & 3>{}, ic<(r0 < RD ? r0 : 0)>{}, NAR, NBL, NBH);        \
        if constexpr (r0 + 1 < RD) rd(ic<((SLOT) + 1) & 3>{}, ic<(r0 + 1 < RD ? r0 + 1 : 0)>{}, NAR, NBL, NBH); \
      }                                                                                                    \
    });                                                                                                    \
    MFMA_AV(accC[0], bb[0], AR[4]);                                                                        \
    MFMA_AV(accC[1], bb[FN], AR[4]);                                                                       \
    SCHED0();                                                                                              \
  } while (0)

  unsigned v0 = wm * 4096 + lane * 16;
#pragma unroll
  for (int j = 0; j < FN; ++j) asm volatile("" : "+v"(bad[j]));
  asm volatile("" : "+v"(aad), "+v"(cad), "+v"(voffA), "+v"(v0) : "s"(rsA), "s"(rsP), "s"(m0w));
  const poll_t pl = pre();
  static_for<4>([&](auto I) { asm volatile("" : "=v"(aX[I.value])); bload_sc1<I.value * 1024>(aX[I.value], v0, rsA, 0); });
  static_for<2 * NB>([&](auto RR) { rd(ic<0>{}, RR, aX, bXl, bXh); });
  dsr128<0>(aX[4], cad);
  G3_STAGE(1, 0, 1); G3_STAGE(1, 1, 1); G3_STAGE(2, 0, 2); G3_STAGE(2, 1, 2);
#pragma unroll
  for (int i = 0; i < 2; ++i) bload_lds<0>(smem_base + 131072 + 4096, voffA, rsP, 0x40000000u);
  asm volatile("s_waitcnt vmcnt(6)" : "+v"(aX[0]), "+v"(aX[1]), "+v"(aX[2]), "+v"(aX[3])::"memory");
  poll_end(pl);
  for (int u = 0; u < 28; u += 4) {
    G3_UNIT(aX, bXl, bXh, aY, bYl, bYh, 0, u, false);
    G3_UNIT(aY, bYl, bYh, aX, bXl, bXh, 1, u + 1, false);
    G3_UNIT(aX, bXl, bXh, aY, bYl, bYh, 2, u + 2, false);
    G3_UNIT(aY, bYl, bYh, aX, bXl, bXh, 3, u + 3, false);
  }
  G3_UNIT(aX, bXl, bXh, aY, bYl, bYh, 0, 28, true);
  G3_UNIT(aY, bYl, bYh, aX, bXl, bXh, 1, 29, true);
  G3_UNIT(aX, bXl, bXh, aY, bYl, bYh, 2, 30, true);
  G3_UNIT(aY, bYl, bYh, aX, bXl, bXh, 3, 31, true);
  asm volatile("s_nop 15" ::: "memory");
  SCHED0();
  lane = fresh_lane(); fr = lane & 15; fq = lane >> 4;
  const f32x2* rstat = reinterpret_cast<const f32x2*>(smem + 131072);
  float* colc = reinterpret_cast<float*>(smem + 131072 + 2048);
  constexpr float KG = -1.4426950408889634f, KU = -0.6931471805599453f;
  asm volatile("" : "+v"(accC[0]), "+v"(accC[1]));
  if (fr < 2 && wm < FN) {
#pragma unroll
    for (int m = 0; m < 2; ++m) *reinterpret_cast<f32x4*>(colc + (m * 2 + fr) * 128 + (wn * 4 + rot) * 16 + fq * 4) = accC[m] * (m ? KU : KG);
  }
  asm volatile("s_waitcnt lgkmcnt(0)" ::: "memory");
  __builtin_amdgcn_s_barrier();
  asm volatile("" ::: "memory");
  static_for<4>([&](auto J) { wst(ic<3>{}, J); });
  f32x2 rs[4];
#pragma unroll
  for (int i = 0; i < 4; ++i) rs[i] = rstat[wm * 64 + i * 16 + fr];
#pragma unroll
  for (int j = 0; j < FN; ++j) {
    const int jg = (j + rot) % FN;
    const int cl = (wn * 4 + jg) * 16 + fq * 4;
    const f32x4 csg = *reinterpret_cast<const f32x4*>(colc + cl), bg = *reinterpret_cast<const f32x4*>(colc + 128 + cl);
    const f32x4 csu = *reinterpret_cast<const f32x4*>(colc + 256 + cl), bu = *reinterpret_cast<const f32x4*>(colc + 384 + cl);
    u32x2 pk[4];
#pragma unroll
    for (int i = 0; i < 4; ++i) {
#pragma unroll
      for (int hh = 0; hh < 2; ++hh) {
        const f32x2 ag = hh ? hi2(acc[i][j]) : lo2(acc[i][j]), au = hh ? hi2(acc[i][FN + j]) : lo2(acc[i][FN + j]);
        const f32x2 g = (rs[i][0] * KG) * ag + (rs[i][1] * (hh ? hi2(csg) : lo2(csg)) + (hh ? hi2(bg) : lo2(bg)));
        const f32x2 uu = (rs[i][0] * KU) * au + (rs[i][1] * (hh ? hi2(csu) : lo2(csu)) + (hh ? hi2(bu) : lo2(bu)));
        f32x2 e = {__builtin_amdgcn_exp2f(g[0]), __builtin_amdgcn_exp2f(g[1])};
        e = e + 1.0f;
        const f32x2 rc = {__builtin_amdgcn_rcpf(e[0]), __builtin_amdgcn_rcpf(e[1])};
        const f32x2 o = (g * uu) * rc;
        pk[i][hh] = pk_bf16(o[0], o[1]);
      }
    }
    const int Jv = (bn & 3) * 7 + wn * 4 + jg;
    const int J = g3_line(bn >> 2, Jv >> 2) * 4 + (Jv & 3);
    char* dst = (char*)act + ((size_t)((bm * 4 + wm) * (F_DIM / 32) + (J >> 1)) * 4 + (fq & 1)) * 1024 + (((J & 1) * 2 + (fq >> 1)) * 16 + fr) * 16;
#pragma unroll
    for (int i = 0; i < 4; i += 2) {
      const auto s0 = __builtin_amdgcn_permlane16_swap(pk[i][0], pk[i + 1][0], false, false);
      const auto s1 = __builtin_amdgcn_permlane16_swap(pk[i][1], pk[i + 1][1], false, false);
      gst128(dst + i * 1024, u32x4{s0[0], s1[0], s0[1], s1[1]});
    }
  }
}

namespace g4 {
constexpr int NU = 14, SL = 4, AD = 3;
constexpr int CN = X_CN;
constexpr int GT = 131072 + 5120;
constexpr int na(int t) { return t + AD - 1 < NU ? 4 : 0; }
constexpr int nb(int mh, int t) { return t < 0 ? 0 : (t + SL < NU ? 4 : (t == NU - 3 && mh ? 4 : 0)); }
constexpr int behind(int mh, int t0, int u, int b) {
  int s = b ? na(u) : 0;
  for (int t = t0 + 1; t < u; ++t) s += na(t) + nb(mh, t);
  return s;
}
constexpr int vm(int mh, int u, int b) {
  const int wa = nb(mh, u - AD + 1) + behind(mh, u - AD + 1, u, b), wb = behind(mh, u == NU - 2 ? NU - 1 - SL : u + 1 - SL, u, b);
  return b == 0 ? wa + 2 : (u + 1 < NU && u + 1 >= SL && wb < wa ? wb : wa);
}
}

template <int MH, class WST, class PRE, class EXTRA>
DI void g4_loop(f32x4 (&acc)[2][8], unsigned smem_base, int lane, int wid, int kb0, rsrc_t rsA, WST ldb, PRE pre, EXTRA extra) {
  using namespace g4;
  const int fq = lane >> 4, k2 = wid & 1;
  unsigned voffA[2], bad[8];
#pragma unroll
  for (int i = 0; i < 2; ++i) voffA[i] = lane * 16 + ((wid & 3) ^ i) * 1024;
  {
    const int q = (lane >> 2) & 3, r = 8 * fq + q, sw = q | ((fq & 1) << 2);
#pragma unroll
    for (int f = 0; f < 8; ++f) bad[f] = smem_base + k2 * 65536 + r * 256 + ((f ^ sw) * 32) + 8 * (lane & 3);
  }
  const unsigned sA = (unsigned)(kb0 + 2 * k2) * 4096u;
  u32x4 af[AD][4];
  u32x2 bl[2][8], bh[2][8];
  auto lda = [&](auto V, auto J) {
    constexpr int v = V.value, j = J.value;
    asm volatile("" : "=v"(af[v % AD][j]));
    bload_sc1<0>(af[v % AD][j], voffA[j & 1], rsA, sA + (4 * v + (j >> 1)) * 4096u);
  };
  auto rdb = [&](auto V, auto B, auto E) {
    constexpr int o = (V.value & 3) * 16384 + B.value * 8192, f = E.value >> 1;
    if constexpr (E.value & 1) dsr_tr<o + 1024>(bh[B.value][f], bad[f]);
    else dsr_tr<o>(bl[B.value][f], bad[f]);
  };
  unsigned wp = smem_base + k2 * 65536 + 32768 + (wid >> 1) * 8192 + lane * 16;
#pragma unroll
  for (int f = 0; f < 8; ++f) asm volatile("" : "+v"(bad[f]));
  asm volatile("" : "+v"(voffA[0]), "+v"(voffA[1]), "+v"(wp) : "s"(rsA), "s"(sA));
  const poll_t pl = pre();
  static_for<4 * (AD - 1)>([&](auto J) { lda(ic<J.value / 4>{}, ic<J.value % 4>{}); });
  static_for<16>([&](auto E) { rdb(ic<0>{}, ic<0>{}, E); });
  static_for<NU>([&](auto U) {
    constexpr int u = U.value;
    constexpr bool last = u == NU - 1;
    static_for<32>([&](auto N) {
      constexpr int n = N.value, b = n >> 4, m = n & 15;
      constexpr int f = (last && b) ? (m & 7) : (m >> 1), i = (last && b) ? 1 - (m >> 3) : (m & 1);
      if constexpr (m == 0) {
        asm volatile("s_waitcnt vmcnt(%2) lgkmcnt(0)" : "+v"(af[u % AD][2 * b]), "+v"(af[u % AD][2 * b + 1])
                     : "n"(vm(MH, u, b)) : "memory");
        if constexpr (u == 0 && b == 0) poll_end(pl);
#pragma unroll
        for (int g = 0; g < 8; ++g) asm volatile("" : "+v"(bl[b][g]), "+v"(bh[b][g]));
        if constexpr (b == 1 && u <= NU - 2) __builtin_amdgcn_s_barrier();
        if constexpr (b == 1 && u == NU - 2 && MH == 0) __builtin_amdgcn_s_setprio(2);
      }
      const u32x4 bb = {bl[b][f][0], bl[b][f][1], bh[b][f][0], bh[b][f][1]};
      if constexpr (u == 0 && b == 0) asm volatile("v_mfma_f32_16x16x32_bf16 %0, %1, %2, 0" : "=&v"(acc[i][f]) : "v"(bb), "v"(af[u % AD][i]));
      else MFMA_AV(acc[i][f], bb, af[u % AD][2 * b + i]);
      if constexpr (m < 8 && !(last && b)) {
        rdb(ic<u + b>{}, ic<1 - b>{}, ic<2 * m>{});
        rdb(ic<u + b>{}, ic<1 - b>{}, ic<2 * m + 1>{});
      }
      if constexpr ((m & 3) == 2) {
        if constexpr (b == 0 && na(u)) lda(ic<u + AD - 1>{}, ic<(m >> 2)>{});
        if constexpr (b == 1 && u + SL < NU) ldb(ic<u + SL>{}, ic<(m >> 2)>{});
        if constexpr (b == 1 && u + 3 == NU) extra(ic<(m >> 2)>{});
      }
      if constexpr (last && n >= 24) dsw128<(m & 7) * 1024>(wp, __builtin_bit_cast(u32x4, acc[1][m & 7]));
    });
  });
  dsw32<0>(smem_base + CN + wid * 4, 1u);
  const unsigned rp = wp + 65536 - k2 * 131072;
  u32x4 pq[8];
  for (;;) {
    unsigned fl;
    asm volatile("ds_read_b32 %0, %1" : "=v"(fl) : "v"(smem_base + CN + (wid ^ 1) * 4) : "memory");
    static_for<8>([&](auto F) { dsr128<F.value * 1024>(pq[F.value], rp); });
    asm volatile("s_waitcnt lgkmcnt(0)" : "+v"(fl), "+v"(pq[0]), "+v"(pq[1]), "+v"(pq[2]), "+v"(pq[3]), "+v"(pq[4]), "+v"(pq[5]), "+v"(pq[6]), "+v"(pq[7])::"memory");
    if (__builtin_amdgcn_readfirstlane(fl) != 0u) break;
  }
#pragma unroll
  for (int f = 0; f < 8; ++f) {
    asm volatile("" : "+v"(acc[0][f]));
    acc[0][f] += __builtin_bit_cast(f32x4, pq[f]);
  }
}

template <int MH, class WST>
DI void g4_phase(unsigned smem_base, int wid, const bf16* At, WST wst, const bf16* Yt,
                 bf16* __restrict__ C, float* xbuf, unsigned* flags) {
  using namespace g4;
  constexpr int KB = F_DIM / 32;
  const int lane = fresh_lane();
  constexpr int mh = MH;
  const int kq = wid & 3;
  const int xcd = blockIdx.x & 7, idx = blockIdx.x >> 3;
  const int kh = xcd & 1, bm = ((xcd >> 1) & 1) * 8 + (idx & 7), bn = (xcd >> 2) * 4 + (idx >> 3);
  const unsigned partner = blockIdx.x ^ 1;
  const int n0 = bn * 128, bm64 = bm * 2 + kh;
  const int fr = lane & 15, fq = lane >> 4;
  const unsigned voffL = lane * 16;
  const rsrc_t rsA = mk_rsrc((const char*)At + (size_t)(bm64 ^ mh ^ 1) * KB * 4096, KB * 4096);
  const rsrc_t rsEx = mk_rsrc(xbuf, 256 * 32768);
  const int kb0 = kh * (KB / 2);
  f32x4 acc[2][8];
  const unsigned oa = arrive<8, 4, 256>(smem_base + CN + 512, lane, flags + (FL_A + ((idx & 7) * 2 + (xcd >> 2)) * 8 + (xcd & 1)) * 32);
  auto pre = [&] { return await(oa, lane, flags + (FL_A + ((bm >> 1) * 2 + kh) * 8 + (idx >> 3) * 2) * 32, 2, 8u); };
  if constexpr (mh == 0) {
    g4_loop<0>(acc, smem_base, lane, wid, kb0, rsA, wst, pre, [](auto) {});
    static_for<4>([&](auto F) {
      constexpr int f = F.value;
      bstore_sc1<f * 1024>(pack8(acc[0][2 * f], acc[0][2 * f + 1]), voffL, rsEx, blockIdx.x * 32768u + kq * 8192u);
    });
    signal(drain(flags + (FL_P + blockIdx.x) * 32 + kq, lane, 1, 4));
  } else {
  u32x4 yv[4];
  const rsrc_t rsX = mk_rsrc(Yt, S_LEN * D_DIM * 2);
  const unsigned vx = (unsigned)(((bm64 >> 2) * 32 + bn * 4) * 16384 + ((bm64 & 3) * 4 + kq) * 1024 + ((((fq & 1) << 1) | (fq >> 1)) * 16 + fr) * 16);
  g4_loop<1>(acc, smem_base, lane, wid, kb0, rsA, wst, pre,
             [&](auto J) { asm volatile("" : "=v"(yv[J.value])); bload_sc1<0>(yv[J.value], vx, rsX, J.value * 16384); });
  u32x4 pv[4], gt[8], ct[8];
  static_for<8>([&](auto F) { dsr128<F.value * 64>(gt[F.value], smem_base + GT + fq * 16); dsr128<512 + F.value * 64>(ct[F.value], smem_base + GT + fq * 16); });
  asm volatile("s_waitcnt vmcnt(0) lgkmcnt(0)" : "+v"(yv[0]), "+v"(yv[1]), "+v"(yv[2]), "+v"(yv[3])::"memory");
#pragma unroll
  for (int c = 0; c < 4; ++c) {
    const auto x0 = __builtin_amdgcn_permlane16_swap(yv[c][0], yv[c][2], false, false);
    const auto x1 = __builtin_amdgcn_permlane16_swap(yv[c][1], yv[c][3], false, false);
#pragma unroll
    for (int h = 0; h < 2; ++h) {
      const int f = 2 * c + h;
      asm volatile("" : "+v"(gt[f]), "+v"(ct[f]));
      acc[0][f] = f32x4{bf_lo(x0[h]), bf_hi(x0[h]), bf_lo(x1[h]), bf_hi(x1[h])} * __builtin_bit_cast(f32x4, ct[f]) + __builtin_bit_cast(f32x4, gt[f]) * acc[0][f];
      asm volatile("" : "+v"(acc[0][f]));
    }
  }
  char* cp = (char*)C + (size_t)(bm64 * 64 + kq * 16 + (fr & 7)) * (D_DIM * 2) + n0 * 2 + (((fq & 1) << 1) | (fq >> 1)) * 16 + (fr >> 3) * 64;
  asm volatile("" : "+v"(cp));
  const poll_t pl = poll(0u, flags + (FL_P + partner) * 32 + kq, lane, 1, 1u);
  static_for<4>([&](auto F) {
    asm volatile("" : "=v"(pv[F.value]));
    bload_sc1<F.value * 1024>(pv[F.value], voffL, rsEx, partner * 32768u + kq * 8192u);
  });
  asm volatile("s_waitcnt vmcnt(0)" : "+v"(pv[0]), "+v"(pv[1]), "+v"(pv[2]), "+v"(pv[3])::"memory");
  poll_end(pl);
  static_for<2>([&](auto L) {
    constexpr int l = L.value;
    u32x4 ov[2];
#pragma unroll
    for (int c = 0; c < 2; ++c) {
      const int f = 4 * l + 2 * c;
      const u32x4 pb = pv[f >> 1];
      const f32x4 p0 = {bf_lo(pb[0]), bf_hi(pb[0]), bf_lo(pb[1]), bf_hi(pb[1])};
      const f32x4 p1 = {bf_lo(pb[2]), bf_hi(pb[2]), bf_lo(pb[3]), bf_hi(pb[3])};
      const f32x4 o0 = acc[0][f] + __builtin_bit_cast(f32x4, gt[f]) * p0;
      const f32x4 o1 = acc[0][f + 1] + __builtin_bit_cast(f32x4, gt[f + 1]) * p1;
      const auto s0 = __builtin_amdgcn_permlane16_swap(pk_bf16(o0[0], o0[1]), pk_bf16(o1[0], o1[1]), false, false);
      const auto s1 = __builtin_amdgcn_permlane16_swap(pk_bf16(o0[2], o0[3]), pk_bf16(o1[2], o1[3]), false, false);
      ov[c] = u32x4{s0[0], s1[0], s0[1], s1[1]};
    }
    u32x4 lo, hi;
#pragma unroll
    for (int e = 0; e < 4; ++e) {
      lo[e] = __builtin_amdgcn_update_dpp(ov[0][e], ov[1][e], 0x128, 0xf, 0xc, false);
      hi[e] = __builtin_amdgcn_update_dpp(ov[1][e], ov[0][e], 0x128, 0xf, 0x3, false);
    }
    gst128(cp + l * 128, lo);
    gst128(cp + 8 * (D_DIM * 2) + l * 128, hi);
  });
}
}

__global__ __launch_bounds__(512, 2) void block_kernel(char* ws, const bf16* X, const bf16* cnd, const bf16* wmod, const bf16* Wq,
                                                       const bf16* Wo, const bf16* Wg, const bf16* Wu, const bf16* Wd, bf16* C) {
  __shared__ __attribute__((aligned(1024))) char smem[Q_LDS];
  unsigned long long pc;
  asm volatile("s_getpc_b64 %0" : "=s"(pc));
  asm volatile("s_cmp_ge_u32 %0, 9\n\ts_cbranch_scc1 1f\n\ts_mov_b32 m0, %1\n\ts_nop 0\n\tbuffer_load_dword %2, %3, 0 offen lds\n1:"
               ::"s"((blockIdx.x >> 3) + __builtin_amdgcn_readfirstlane(threadIdx.x ^ 64)), "s"((unsigned)(uintptr_t)smem + QX_SINK), "v"((threadIdx.x & 63) * 128 + (blockIdx.x >> 3) * 8192),
                 "s"(mk_rsrc((const void*)(uintptr_t)(pc & ~255ull), CODEPF * 1024)) : "memory", "scc");
  asm volatile("" : "+s"(ws) : "s"(X), "s"(cnd), "s"(wmod), "s"(Wq), "s"(Wo), "s"(Wg), "s"(Wu), "s"(Wd), "s"(C));
  float* modp = (float*)ws;
  unsigned* ctab = (unsigned*)(ws + 212992);
  unsigned* flags = (unsigned*)(ws + 262144);
  float* stat = (float*)(ws + 524288);
  bf16* O = (bf16*)(ws + (1 << 20));
  bf16* Y = (bf16*)(ws + (9 << 20));
  bf16* H = (bf16*)(ws + (13 << 20));
  bf16* act = (bf16*)(ws + (17 << 20));
  float* xbuf = (float*)(ws + (32 << 20));
  const int wid = __builtin_amdgcn_readfirstlane(threadIdx.x >> 6);
  const int wm = wid & 3, wn = wid >> 2;
  const int xcd = blockIdx.x & 7, idx = blockIdx.x >> 3;
  const int bm = idx & 7, bn = xcd * 4 + (idx >> 3);
  const unsigned smem_base = (unsigned)(uintptr_t)smem;
  const rsrc_t rsA = mk_rsrc((const char*)Y + (size_t)bm * 32 * 16384, 32 * 16384);
  const rsrc_t rsB = mk_rsrc(wn ? Wu : Wg, D_DIM * F_DIM * 2);
  const rsrc_t rsP = mk_rsrc((const char*)Wd + (size_t)blockIdx.x * 28672, 28672);
  const rsrc_t rsE = mk_rsrc((const void*)(uintptr_t)(pc & ~255ull), CODEPF * 1024);
  const unsigned m0w = smem_base + wid * 2048;
  unsigned voffB0 = 0, voffB1 = 0;
  attn_phase(smem_base, wid, H, Wq, O, Wo, X, modp, Y, stat, flags, Wg, Wu, ctab, cnd, wmod, [&](auto S, auto I) {
    if constexpr (S.value == 0) {
      const int l2 = fresh_lane();
      const int r = (wm * 2 + I.value) * 4 + (l2 >> 4);
      const int sw = (r & 3) | (((r >> 3) & 1) << 2);
      const int c16 = l2 & 15;
      const int vb = (((bn & 3) * 224) & ~63) + ((c16 >> 1) ^ sw) * 32 + (c16 & 1) * 16;
      (I.value ? voffB1 : voffB0) = (unsigned)(r * (F_DIM * 2) + g3_line(bn >> 2, vb >> 7) * 128 + (vb & 127));
    }
    bload_lds<0>(m0w + S.value * 16384 + I.value * 1024, I.value ? voffB1 : voffB0, rsB, S.value * 229376u);
  });
  __builtin_amdgcn_s_setprio(0);
  const int lane = fresh_lane();
  const unsigned voffA = wid * 2048 + lane * 16;
  const unsigned oy = arrive<8, 8, 512>(smem_base + X_CN + 768, lane, flags + (FL_Y + xcd * 32 + (idx & 3)) * 32);
  auto pl = [&] { return await(oy, lane, flags + (FL_Y + bm * 32 + xcd * 4) * 32, 4, 8u); };
  const int n0 = ((xcd >> 2) * 4 + (idx >> 3)) * 128;
  unsigned vo = 0;
  auto wst = [&](auto V, auto J) {
    constexpr int v = V.value, j = J.value;
    if constexpr (v == 0 && j == 0) {
      const int l2 = fresh_lane();
      vo = (l2 >> 4) * 2048 + ((((l2 & 15) >> 1) ^ ((l2 >> 4) | ((wid & 1) << 2))) * 32) + (l2 & 1) * 16;
    }
    bload_lds<0>(m0w + (j >> 1) * 65536 + (v & 3) * 16384 + (j & 1) * 1024, vo, mk_rsrc((const char*)Wd + n0 * 2, F_DIM * D_DIM * 2 - n0 * 2),
                 (unsigned)(((xcd & 1) * (F_DIM / 64) + wn) * 65536 + wm * 16384) + (4 * v + 2 * (j >> 1)) * 65536u + (j & 1) * 8192u);
  };
  if (wn == 0) {
    g3_body<4>(smem, smem_base, wm, wn, wid, lane, bm, bn, rsA, rsB, rsP, rsE, voffA, voffB0, voffB1, act, wst, [](auto) {}, pl);
    g4_phase<0>(smem_base, wid, act, wst, Y, C, xbuf, flags);
  } else {
    u32x2 tv[16];
    unsigned gv;
    g3_body<3>(smem, smem_base, wm, wn, wid, lane, bm, bn, rsA, rsB, rsP, rsE, voffA, voffB0, voffB1, act, wst, [&](auto K) {
      const int tid = wm * 64 + fresh_lane();
      if constexpr (K.value == 0) {
        asm volatile("" : "=v"(gv));
        bload1_sc1(gv, (unsigned)((n0 + (tid & 127)) * 4), mk_rsrc(modp + 49152 + (wm >= 2 ? 2048 : 3072), 4096));
        const rsrc_t rsS = mk_rsrc(stat, S_LEN * 128);
#pragma unroll
        for (int i = 0; i < 16; ++i)
          asm volatile("buffer_load_dwordx2 %0, %1, %2, %3 offen sc1" : "=v"(tv[i]) : "v"((bm * 256 + tid) * 8), "s"(rsS), "s"(i * 16384) : "memory");
      } else {
#pragma unroll
        for (int i = 0; i < 16; ++i) asm volatile("" : "+v"(tv[i]));
        float s = 0;
#pragma unroll
        for (int i = 0; i < 16; i += 2) s += __uint_as_float(tv[i][0]) + __uint_as_float(tv[i + 1][0]);
        const float mean = s * (1.0f / D_DIM);
        float m2 = 0;
#pragma unroll
        for (int i = 0; i < 16; i += 2) {
          const float d0 = __uint_as_float(tv[i][0]) * (1.0f / 64.0f) - mean, d1 = __uint_as_float(tv[i + 1][0]) * (1.0f / 64.0f) - mean;
          m2 += __uint_as_float(tv[i][1]) + __uint_as_float(tv[i + 1][1]) + 64.0f * (d0 * d0 + d1 * d1);
        }
        const float rstd = rsqrtf(m2 * (1.0f / D_DIM) + 1e-5f);
        reinterpret_cast<f32x2*>(smem + 131072)[tid & 255] = f32x2{rstd, -mean * rstd};
        asm volatile("" : "+v"(gv));
        const float sum = (wm >= 2) ? 1.0f / __uint_as_float(gv) : __uint_as_float(gv);
        dsw32<0>(smem_base + g4::GT + (tid & 255) * 4, __float_as_uint(sum));
      }
    }, pl);
    g4_phase<1>(smem_base, wid, act, wst, Y, C, xbuf, flags);
  }
}

extern "C" void kernel_launch(void* const* d_in, const int* in_sizes, int n_in, void* d_out, int out_size,
                              void* d_ws, size_t ws_size, hipStream_t stream) {
  const bf16* x = (const bf16*)d_in[0];
  const bf16* wqkv = (const bf16*)d_in[1];
  const bf16* wo = (const bf16*)d_in[2];
  const bf16* wg = (const bf16*)d_in[4];
  const bf16* wu = (const bf16*)d_in[5];
  const bf16* wd = (const bf16*)d_in[6];
  const bf16* c = (const bf16*)d_in[7];
  const bf16* wmod = (const bf16*)d_in[8];
  bf16* out = (bf16*)d_out;

  block_kernel<<<256, 512, 0, stream>>>((char*)d_ws, x, c, wmod, wqkv, wo, wg, wu, wd, out);
}
```

```cpp
#include <hip/hip_runtime.h>
#include <hip/hip_bf16.h>
#include <stdint.h>

typedef __hip_bfloat16 bf16;
typedef __attribute__((ext_vector_type(8))) short bf16x8;
typedef __attribute__((ext_vector_type(4))) float f32x4;
typedef __attribute__((ext_vector_type(2))) float f32x2;
typedef __attribute__((ext_vector_type(4))) unsigned u32x4;
typedef __attribute__((ext_vector_type(2))) unsigned u32x2;
typedef __attribute__((ext_vector_type(2))) __bf16 bf16x2_t;
typedef __amdgpu_buffer_rsrc_t rsrc_t;

#define DI __device__ __forceinline__
constexpr int CODEPF = 68;

constexpr int S_LEN = 2048;
constexpr int D_DIM = 1024;
constexpr int F_DIM = 3584;
constexpr int QBAR = 2;
constexpr int NP1 = 5;
constexpr int PSLP = 4;
constexpr int NDLY = 20, TDLY = 15;
constexpr int TS = 12, TP = 6;
constexpr int WOS = 6;
constexpr int GDEP = 12, GS0 = 28;
constexpr int TPOS = 6, WTD = 6, CT3 = 24, ST3 = 16;

template <int V> struct ic { static constexpr int value = V; };
template <int N, int I = 0, class F>
DI void static_for(F&& f) {
  if constexpr (I < N) { f(ic<I>{}); static_for<N, I + 1>(f); }
}

DI float bf_lo(unsigned u) { return __uint_as_float(u << 16); }
DI float bf_hi(unsigned u) { return __uint_as_float(u & 0xffff0000u); }
DI unsigned pk_bf16(float lo, float hi) {
  const f32x2 v = {lo, hi};
  return __builtin_bit_cast(unsigned, __builtin_convertvector(v, bf16x2_t));
}
DI rsrc_t mk_rsrc(const void* p, unsigned bytes) { return __builtin_amdgcn_make_buffer_rsrc((void*)p, (short)0, (int)bytes, 0x00020000); }

template <int N>
DI void wait_vm() { asm volatile("s_waitcnt vmcnt(%0)" ::"n"(N > 63 ? 63 : N) : "memory"); }
#define WAIT_LGKM0() do { asm volatile("s_waitcnt lgkmcnt(0)" ::: "memory"); __builtin_amdgcn_sched_barrier(0); } while (0)
#define SCHED0() __builtin_amdgcn_sched_barrier(0)

#define MFMA_AV(C, B, A) asm volatile("v_mfma_f32_16x16x32_bf16 %0, %1, %2, %0" : "+v"(C) : "v"(B), "v"(A))
template <int OFF>
DI void bload_v(u32x4& d, unsigned voff, rsrc_t rs, unsigned soff) {
  asm volatile("buffer_load_dwordx4 %0, %1, %2, %3 offen offset:%4" : "+v"(d) : "v"(voff), "s"(rs), "s"(soff), "i"(OFF) : "memory");
}
template <int OFF>
DI void bload_lds(unsigned m0v, unsigned voff, rsrc_t rs, unsigned soff) {
  asm volatile("s_mov_b32 m0, %0\n\ts_nop 0\n\tbuffer_load_dwordx4 %1, %2, %3 offen offset:%4 lds" ::"s"(m0v), "v"(voff), "s"(rs), "s"(soff), "i"(OFF) : "memory");
}
DI void touch_lds(unsigned m0v, unsigned voff, rsrc_t rs, unsigned soff) {
  asm volatile("s_mov_b32 m0, %0\n\ts_nop 0\n\tbuffer_load_dword %1, %2, %3 offen lds" ::"s"(m0v), "v"(voff), "s"(rs), "s"(soff) : "memory");
}
template <int OFF>
DI void bload_lds_sc1(unsigned m0v, unsigned voff, rsrc_t rs, unsigned soff) {
  asm volatile("s_mov_b32 m0, %0\n\ts_nop 0\n\tbuffer_load_dwordx4 %1, %2, %3 offen offset:%4 sc1 lds" ::"s"(m0v), "v"(voff), "s"(rs), "s"(soff), "i"(OFF) : "memory");
}
template <int OFF>
DI void dsr128(u32x4& d, unsigned addr) { asm volatile("ds_read_b128 %0, %1 offset:%2" : "=v"(d) : "v"(addr), "i"(OFF) : "memory"); }
template <int OFF>
DI void dsr32(unsigned& d, unsigned addr) { asm volatile("ds_read_b32 %0, %1 offset:%2" : "=v"(d) : "v"(addr), "i"(OFF) : "memory"); }
DI void bload_nt(u32x4& d, unsigned voff, rsrc_t rs, unsigned soff) {
  asm volatile("buffer_load_dwordx4 %0, %1, %2, %3 offen nt" : "=v"(d) : "v"(voff), "s"(rs), "s"(soff) : "memory");
}
template <int OFF>
DI void dsr64(u32x2& d, unsigned addr) { asm volatile("ds_read_b64 %0, %1 offset:%2" : "=v"(d) : "v"(addr), "i"(OFF) : "memory"); }
template <int OFF>
DI void dsr_tr(u32x2& d, unsigned addr) { asm volatile("ds_read_b64_tr_b16 %0, %1 offset:%2" : "=v"(d) : "v"(addr), "i"(OFF) : "memory"); }
template <int OFF>
DI void dsw128(unsigned addr, u32x4 d) { asm volatile("ds_write_b128 %0, %1 offset:%2" ::"v"(addr), "v"(d), "i"(OFF) : "memory"); }
template <int OFF>
DI void dsw32(unsigned addr, unsigned d) { asm volatile("ds_write_b32 %0, %1 offset:%2" ::"v"(addr), "v"(d), "i"(OFF) : "memory"); }

DI void gst128(void* p, u32x4 v) {
  asm volatile("global_store_dwordx4 %0, %1, off sc1\n\ts_nop 1" ::"v"(p), "v"(v) : "memory");
}
DI int fresh_lane() {
  int l;
  asm volatile("v_mbcnt_lo_u32_b32 %0, -1, 0\n\tv_mbcnt_hi_u32_b32 %0, -1, %0" : "=v"(l));
  return l;
}
DI float hadd(f32x2 v) {
  float r;
  asm("v_add_f32 %0, %1, %2" : "=v"(r) : "v"(v[0]), "v"(v[1]));
  return r;
}
DI f32x2 lo2(f32x4 v) { return __builtin_shufflevector(v, v, 0, 1); }
DI f32x2 hi2(f32x4 v) { return __builtin_shufflevector(v, v, 2, 3); }
DI float rowsum4(float v) {
  const unsigned u = __float_as_uint(v);
  const auto a = __builtin_amdgcn_permlane16_swap(u, u, false, false);
  const unsigned t = __float_as_uint(__uint_as_float(a[0]) + __uint_as_float(a[1]));
  const auto b = __builtin_amdgcn_permlane32_swap(t, t, false, false);
  return __uint_as_float(b[0]) + __uint_as_float(b[1]);
}
DI float silu_f(float v) { return v * __builtin_amdgcn_rcpf(1.0f + __builtin_amdgcn_exp2f(v * -1.4426950408889634f)); }
DI float rbf(float v) { return bf_lo(pk_bf16(v, v)); }
DI float gain_nz(float m) {
  const float g = rbf(1.0f + rbf(m));
  return __builtin_fabsf(g) < 8.67361738e-19f ? 8.67361738e-19f : g;
}
template <int OFF>
DI void bload_sc1(u32x4& d, unsigned voff, rsrc_t rs, unsigned soff) {
  asm volatile("buffer_load_dwordx4 %0, %1, %2, %3 offen offset:%4 sc1" : "+v"(d) : "v"(voff), "s"(rs), "s"(soff), "i"(OFF) : "memory");
}
template <int OFF>
DI void bstore_sc1(u32x4 d, unsigned voff, rsrc_t rs, unsigned soff) {
  asm volatile("buffer_store_dwordx4 %0, %1, %2, %3 offen offset:%4 sc1\n\ts_nop 1" ::"v"(d), "v"(voff), "s"(rs), "s"(soff), "i"(OFF) : "memory");
}
DI void bload1_sc1(unsigned& d, unsigned voff, rsrc_t rs) {
  asm volatile("buffer_load_dword %0, %1, %2, 0 offen sc1" : "+v"(d) : "v"(voff), "s"(rs) : "memory");
}

constexpr int Q_SLOT = 21504, Q_NSL = 7;
constexpr int QX_WH = 4 * Q_SLOT;
constexpr int QX_HX = 65536;
constexpr int QX_GT = QX_WH + 65536;
constexpr int QX_CNT = QX_GT + 512;
constexpr int QX_SINK = QX_CNT + 512;
constexpr int G3_CT = QX_SINK + 1024;
constexpr int X_CN = G3_CT + 4096;
constexpr int Q_LDS = X_CN + 2048;
constexpr int P_TAB = 4 * Q_SLOT, P_RED = 5 * Q_SLOT, P_ST = P_RED + 4096;

DI unsigned lds_add(unsigned addr, unsigned v0) {
  unsigned o;
  asm volatile("ds_add_rtn_u32 %0, %1, %2\n\ts_waitcnt lgkmcnt(0)" : "=v"(o) : "v"(addr), "v"(v0) : "memory");
  return __builtin_amdgcn_readfirstlane(o);
}
template <int SLP>
DI void lds_wait(unsigned addr, unsigned n) {
  for (;;) {
    unsigned v;
    asm volatile("ds_read_b32 %0, %1\n\ts_waitcnt lgkmcnt(0)" : "=v"(v) : "v"(addr) : "memory");
    if (__builtin_amdgcn_readfirstlane(v) >= n) break;
    __builtin_amdgcn_s_sleep(SLP);
  }
}
struct poll_t { unsigned a, b; };
struct sig_t { rsrc_t rs; unsigned vo; };
DI sig_t drain(unsigned* out, int lane, int no, int so) {
  sig_t g = {mk_rsrc(out, (no - 1) * so + 4), (unsigned)(lane * so)};
  asm volatile("s_waitcnt vmcnt(0)" : "+v"(g.vo) : "s"(g.rs) : "memory");
  return g;
}
DI void signal(const sig_t& g) { asm volatile("buffer_atomic_add %0, %1, %2, 0 offen sc1" ::"v"(1u), "v"(g.vo), "s"(g.rs) : "memory"); }
#define POLL_RD(r) "v_mov_b32 " r ", 0\n\tbuffer_atomic_add " r ", %3, %4, 0 offen sc0 sc1\n\t"
#define POLL_CK(r) "s_waitcnt vmcnt(1)\n\tv_cmp_lt_u32 vcc, " r ", %5\n\ts_cbranch_vccz 2f\n\t"
DI poll_t poll(unsigned skip, unsigned* in, int lane, int nl, unsigned n) {
  poll_t r;
  asm volatile("s_cmp_lg_u32 %2, 0\n\ts_cbranch_scc1 2f\n\t" POLL_RD("%0") "s_sleep %6\n1:\n\t" POLL_RD("%1") POLL_CK("%0") POLL_RD("%0") POLL_CK("%1")
               "s_add_u32 %2, %2, 1\n\ts_cmp_lt_u32 %2, 0x80000\n\ts_cbranch_scc1 1b\n2:"
               : "=&v"(r.a), "=&v"(r.b), "+s"(skip) : "v"(lane * 128), "s"(mk_rsrc(in, nl * 128)), "v"(lane < nl ? n : 0u), "n"(PSLP) : "memory", "vcc", "scc");
  return r;
}
DI void poll_end(const poll_t& r) { asm volatile("" ::"v"(r.a), "v"(r.b)); }
template <int NW, int NO, int SO>
DI unsigned arrive(unsigned lcnt, int lane, unsigned* out) {
  asm volatile("" ::"v"(lcnt + lane * 4));
  const sig_t g = drain(out, lane, NO, SO);
  const unsigned o = lds_add(lcnt + lane * 4, 1u);
  if (o == NW - 1) {
    signal(g);
  }
  return o;
}
DI poll_t await(unsigned o, int lane, unsigned* in, int nl, unsigned n) {
  const poll_t r = poll(o, in, lane, nl, n);
  __builtin_amdgcn_s_barrier();
  return r;
}
constexpr int FL_M = 0, FL_H = 544, FL_O = 608, FL_P = 736, FL_Y = 992, FL_A = 1248, FL_B = 1376, FL_S = 1504;

DI f32x4 mfma0(u32x4 a, u32x4 b) {
  return __builtin_amdgcn_mfma_f32_16x16x32_bf16(__builtin_bit_cast(bf16x8, a), __builtin_bit_cast(bf16x8, b), f32x4{0, 0, 0, 0}, 0, 0, 0);
}
DI u32x4 pack8(const f32x4 a, const f32x4 b) { return u32x4{pk_bf16(a[0], a[1]), pk_bf16(a[2], a[3]), pk_bf16(b[0], b[1]), pk_bf16(b[2], b[3])}; }
template <class OP>
DI float rows4(const float (&v)[4], OP op) {
  const auto a = __builtin_amdgcn_permlane16_swap(__float_as_uint(v[0]), __float_as_uint(v[1]), false, false);
  const auto b = __builtin_amdgcn_permlane16_swap(__float_as_uint(v[2]), __float_as_uint(v[3]), false, false);
  const float t0 = op(__uint_as_float(a[0]), __uint_as_float(a[1])), t1 = op(__uint_as_float(b[0]), __uint_as_float(b[1]));
  const auto c = __builtin_amdgcn_permlane32_swap(__float_as_uint(t0), __float_as_uint(t1), false, false);
  return op(__uint_as_float(c[0]), __uint_as_float(c[1]));
}
DI void bcast4(float t, float (&v)[4]) {
  const auto c = __builtin_amdgcn_permlane32_swap(__float_as_uint(t), __float_as_uint(t), false, false);
  const auto a = __builtin_amdgcn_permlane16_swap(c[0], c[0], false, false);
  const auto b = __builtin_amdgcn_permlane16_swap(c[1], c[1], false, false);
  v[0] = __uint_as_float(a[0]); v[1] = __uint_as_float(a[1]); v[2] = __uint_as_float(b[0]); v[3] = __uint_as_float(b[1]);
}
DI float fmax2(float a, float b) { return __builtin_amdgcn_fmed3f(a, b, __builtin_inff()); }

DI float sum16(float v) {
  v += __builtin_bit_cast(float, __builtin_amdgcn_update_dpp(0, __builtin_bit_cast(int, v), 0x128, 0xf, 0xf, false));
  v += __builtin_bit_cast(float, __builtin_amdgcn_update_dpp(0, __builtin_bit_cast(int, v), 0x124, 0xf, 0xf, false));
  v += __builtin_bit_cast(float, __builtin_amdgcn_update_dpp(0, __builtin_bit_cast(int, v), 0x122, 0xf, 0xf, false));
  v += __builtin_bit_cast(float, __builtin_amdgcn_update_dpp(0, __builtin_bit_cast(int, v), 0x121, 0xf, 0xf, false));
  return v;
}
DI f32x4 sum4(const u32x4* p) {
  return (__builtin_bit_cast(f32x4, p[0]) + __builtin_bit_cast(f32x4, p[1])) + (__builtin_bit_cast(f32x4, p[2]) + __builtin_bit_cast(f32x4, p[3]));
}
template <int NB, int NMID, class MID>
DI void mod_gemv(unsigned red, int lane, int col0, int k0, const bf16* __restrict__ c, const bf16* __restrict__ wmod, MID mid) {
  const int fr = lane & 15, fq = lane >> 4;
  const rsrc_t rsW = mk_rsrc(wmod, 1024 * 6144 * 2);
  const unsigned vw = (unsigned)(((k0 + (fr >> 3) * 32 + fq * 8) * 6144 + col0 + (fr & 7) * 8) * 2);
  u32x4 raw[NB][8], cv[NB];
  static_for<NB>([&](auto B) {
    asm volatile("" : "=v"(cv[B.value]));
    bload_v<B.value * 128>(cv[B.value], (unsigned)((k0 + (fr & 1) * 32 + fq * 8) * 2), mk_rsrc(c, 2048), 0);
  });
  static_for<NB * 8>([&](auto N) {
    constexpr int b = N.value >> 3, j = N.value & 7;
    bload_nt(raw[b][j], vw, rsW, (b * 64 + j) * 12288);
  });
  __builtin_amdgcn_s_barrier();
  f32x4 acc[8];
#pragma unroll
  for (int n = 0; n < 8; ++n) acc[n] = f32x4{0, 0, 0, 0};
  u32x4 cf[NB];
  static_for<NB>([&](auto B) {
    constexpr int b = B.value;
    asm volatile("s_waitcnt vmcnt(%1)" : "+v"(cv[b]) : "n"(NB * 8) : "memory");
#pragma unroll
    for (int e = 0; e < 4; ++e) cf[b][e] = pk_bf16(silu_f(bf_lo(cv[b][e])), silu_f(bf_hi(cv[b][e])));
    asm volatile("" : "+v"(cf[b]));
  });
  static_for<NB>([&](auto B) {
    constexpr int b = B.value;
    if constexpr (b == 1) {
#pragma unroll
      for (int n = 0; n < 8; ++n) asm volatile("" : "+v"(acc[n]));
      mid();
    }
    asm volatile("s_waitcnt vmcnt(%8)" : "+v"(raw[b][0]), "+v"(raw[b][1]), "+v"(raw[b][2]), "+v"(raw[b][3]), "+v"(raw[b][4]), "+v"(raw[b][5]), "+v"(raw[b][6]), "+v"(raw[b][7])
                 : "n"(b ? NMID : (NB - 1) * 8) : "memory");
#pragma unroll
    for (int p = 0; p < 4; ++p) {
      u32x4 lo, hi;
#pragma unroll
      for (int t = 0; t < 4; ++t) {
        lo[t] = __builtin_amdgcn_perm(raw[b][2 * t + 1][p], raw[b][2 * t][p], 0x05040100u);
        hi[t] = __builtin_amdgcn_perm(raw[b][2 * t + 1][p], raw[b][2 * t][p], 0x07060302u);
      }
      acc[2 * p] = __builtin_amdgcn_mfma_f32_16x16x32_bf16(__builtin_bit_cast(bf16x8, lo), __builtin_bit_cast(bf16x8, cf[b]), acc[2 * p], 0, 0, 0);
      acc[2 * p + 1] = __builtin_amdgcn_mfma_f32_16x16x32_bf16(__builtin_bit_cast(bf16x8, hi), __builtin_bit_cast(bf16x8, cf[b]), acc[2 * p + 1], 0, 0, 0);
    }
  });
  if (fr == (fq >> 1)) {
    static_for<8>([&](auto R) {
      constexpr int r = R.value >> 1, h = (R.value & 1) * 4;
      dsw128<R.value * 16>(red + fq * 128, __builtin_bit_cast(u32x4, f32x4{acc[h][r], acc[h + 1][r], acc[h + 2][r], acc[h + 3][r]}));
    });
  }
}
template <int NR>
DI float red_sum(unsigned red, int lane) {
  unsigned v[NR];
  static_for<NR>([&](auto R) { dsr32<R.value * 256>(v[R.value], red + lane * 4); });
  asm volatile("s_waitcnt lgkmcnt(0)" ::: "memory");
  float s = 0;
#pragma unroll
  for (int r = 0; r < NR; r += 2) {
    asm volatile("" : "+v"(v[r]), "+v"(v[r + 1]));
    s += __uint_as_float(v[r]) + __uint_as_float(v[r + 1]);
  }
  return s;
}
DI unsigned cnt_read(unsigned* in, int lane, int nl) {
  unsigned t = 0;
  asm volatile("buffer_atomic_add %0, %1, %2, 0 offen sc0 sc1\n\ts_waitcnt vmcnt(0)" : "+v"(t) : "v"(lane * 128), "s"(mk_rsrc(in, nl * 128)) : "memory");
  return t;
}
DI bool words_set(unsigned* word, int lane) {
  unsigned t = 0;
  asm volatile("buffer_atomic_add %0, %1, %2, 0 offen sc0 sc1\n\ts_waitcnt vmcnt(0)" : "+v"(t) : "v"(lane * 8192), "s"(mk_rsrc(word, 7 * 8192 + 4)) : "memory");
  return (__builtin_amdgcn_ballot_w64(t == 0u) & 255u) == 0u;
}
DI void confirm(unsigned* word, int lane) {
#pragma unroll 1
  for (unsigned spins = 0; !words_set(word, lane) && ++spins < (1u << 20);) __builtin_amdgcn_s_sleep(2);
}
DI void set_words(unsigned* flags, int lane) {
  bstore_sc1<0>(u32x4{1u, 1u, 1u, 1u}, (unsigned)(lane * 128), mk_rsrc(flags + (FL_S + (blockIdx.x & 7) * 64) * 32, 64 * 128), 0);
}

template <class PRE>
DI void first_phase(unsigned smem_base, int wid, int lane, const bf16* __restrict__ x, const bf16* __restrict__ c, const bf16* __restrict__ wmod,
                    const bf16* __restrict__ W, float* modp, bf16* H, unsigned* flags, PRE prefill) {
  const int xcd = blockIdx.x & 7, idx = blockIdx.x >> 3;
  const int fr = lane & 15, fq = lane >> 4;
  const int u = xcd * 16 + (idx & 15), v = xcd * 8 + idx;
  unsigned* word = flags + __builtin_amdgcn_readfirstlane((FL_S + (blockIdx.x >> 2)) * 32 + (blockIdx.x & 3));
  const bool master = idx == 15;
  if (wid == (idx >= 16 ? 0 : 7)) {
    asm volatile("buffer_store_dword %0, %1, %2, 0 offen sc1" ::"v"(0u), "v"(lane * 8192), "s"(mk_rsrc(word, 7 * 8192 + 4)) : "memory");
  }
  if (master) {
    bstore_sc1<0>(u32x4{0u, 0u, 0u, 0u}, (unsigned)(wid * 2048 + lane * 16), mk_rsrc(flags + xcd * (FL_O / 8) * 32, FL_O / 8 * 128), 0);
    bstore_sc1<1024>(u32x4{0u, 0u, 0u, 0u}, (unsigned)(wid * 2048 + lane * 16), mk_rsrc(flags + xcd * (FL_O / 8) * 32, FL_O / 8 * 128), 0);
  }
  if (wid == 7) {
    dsw32<0>(smem_base + QX_CNT + lane * 4, 0u); dsw32<256>(smem_base + QX_CNT + lane * 4, 0u);
    dsw128<0>(smem_base + X_CN + lane * 16, u32x4{0u, 0u, 0u, 0u}); dsw128<1024>(smem_base + X_CN + lane * 16, u32x4{0u, 0u, 0u, 0u});
    asm volatile("s_waitcnt lgkmcnt(0)" ::: "memory");
  }
  if (__builtin_expect(idx >= 16, 1)) {
    unsigned o = 0;
    if (wid < 4) {
      const int col0 = (u >> 3) * 128, kp = u & 7;
      mod_gemv<1, 0>(smem_base + P_RED + wid * 512, lane, col0 + (wid & 1) * 64, kp * 128 + (wid >> 1) * 64, c, wmod, [] {});
      const sig_t g = {mk_rsrc(flags + (FL_M + (u & 15)) * 32, 16 * 4096 + 4), (unsigned)(lane * 4096)};
      asm volatile("" ::"s"(g.rs), "v"(g.vo));
      o = lds_add(smem_base + X_CN + 1280 + lane * 4, 1u);
      if (o == 3) {
        u32x2 r[4];
        static_for<4>([&](auto R) { dsr64<(R.value >> 1) * 1024 + (R.value & 1) * 256>(r[R.value], smem_base + P_RED + (lane >> 5) * 512 + (lane & 31) * 8); });
        asm volatile("s_waitcnt lgkmcnt(0)" : "+v"(r[0]), "+v"(r[1]), "+v"(r[2]), "+v"(r[3])::"memory");
        const f32x2 sum = (__builtin_bit_cast(f32x2, r[0]) + __builtin_bit_cast(f32x2, r[1])) + (__builtin_bit_cast(f32x2, r[2]) + __builtin_bit_cast(f32x2, r[3]));
        asm volatile("buffer_store_dwordx2 %0, %1, %2, 0 offen sc1" ::"v"(sum), "v"(lane * 8), "s"(mk_rsrc(modp + kp * 2048 + col0, 512)) : "memory");
        confirm(word, lane);
        signal(g);
      }
    } else {
      const rsrc_t rsX = mk_rsrc(x, S_LEN * D_DIM * 2);
      const int r0 = (wid - 4) * 4;
      u32x4 a[8];
      static_for<8>([&](auto N) {
        asm volatile("" : "=v"(a[N.value]));
        bload_v<(N.value & 1) * 1024>(a[N.value], (unsigned)((u * 16 + r0) * 2048 + lane * 16), rsX, (N.value >> 1) * 2048);
      });
      __builtin_amdgcn_s_barrier();
      asm volatile("s_waitcnt vmcnt(0)" : "+v"(a[0]), "+v"(a[1]), "+v"(a[2]), "+v"(a[3]), "+v"(a[4]), "+v"(a[5]), "+v"(a[6]), "+v"(a[7])::"memory");
      float sm[4], q[4];
      f32x2 xf[4][8];
#pragma unroll
      for (int i = 0; i < 4; ++i) {
        const unsigned ad = smem_base + (lane >> 5) * Q_SLOT + (lane & 31) * 256 + (((r0 + i) ^ (lane & 7)) * 16);
        dsw128<0>(ad, a[2 * i]); dsw128<2 * Q_SLOT>(ad, a[2 * i + 1]);
#pragma unroll
        for (int j = 0; j < 8; ++j) xf[i][j] = f32x2{bf_lo(a[2 * i + (j >> 2)][j & 3]), bf_hi(a[2 * i + (j >> 2)][j & 3])};
        const f32x2 t = ((xf[i][0] + xf[i][1]) + (xf[i][2] + xf[i][3])) + ((xf[i][4] + xf[i][5]) + (xf[i][6] + xf[i][7]));
        sm[i] = hadd(t);
      }
      const float mrow = sum16(rows4(sm, [](float p, float r) { return p + r; })) * (1.0f / D_DIM);
      bcast4(mrow, sm);
#pragma unroll
      for (int i = 0; i < 4; ++i) {
        f32x2 t0 = xf[i][0] - sm[i], t1 = xf[i][1] - sm[i];
        t0 *= t0; t1 *= t1;
#pragma unroll
        for (int j = 2; j < 8; j += 2) {
          const f32x2 d0 = xf[i][j] - sm[i], d1 = xf[i][j + 1] - sm[i];
          t0 += d0 * d0; t1 += d1 * d1;
        }
        t0 += t1;
        q[i] = hadd(t0);
      }
      const float rstd = __builtin_amdgcn_rsqf(sum16(rows4(q, [](float p, float r) { return p + r; })) * (1.0f / D_DIM) + 1e-5f);
      asm volatile("ds_write_b64 %0, %1\n\ts_waitcnt lgkmcnt(0)" ::"v"(smem_base + P_ST + (r0 + fq) * 8), "v"(f32x2{rstd, -mrow * rstd}) : "memory");
      prefill(wid - 4); prefill(wid);
    }
    u32x4 mp[8], xv[4], tb[4][4];
    u32x2 rr;
    const rsrc_t rsM = mk_rsrc(modp, 8 * 2048 * 4);
    const unsigned vm = (unsigned)((lane >> 5) * 4096 + wid * 512 + (lane & 31) * 16);
    asm volatile("" ::"s"(rsM), "v"(vm));
    const poll_t pm = poll(3 - o, flags + (FL_M + (1 + (u >> 3)) * 32) * 32, lane, 16, 8u);
    __builtin_amdgcn_s_barrier();
#pragma unroll
    for (int i = 0; i < 8; ++i) { asm volatile("" : "=v"(mp[i])); bload_sc1<0>(mp[i], vm, rsM, i * 8192); }
    const unsigned xa = smem_base + (wid >> 1) * Q_SLOT + (wid & 1) * 4096 + fq * 256 + ((fr ^ fq) * 16);
    dsr128<0>(xv[0], xa); dsr128<1024>(xv[1], xa ^ 64u); dsr128<2048>(xv[2], xa); dsr128<3072>(xv[3], xa ^ 64u);
    dsr64<0>(rr, smem_base + P_ST + fr * 8);
    asm volatile("s_waitcnt lgkmcnt(0)" : "+v"(xv[0]), "+v"(xv[1]), "+v"(xv[2]), "+v"(xv[3]), "+v"(rr)::"memory");
    f32x2 xn[4][4];
#pragma unroll
    for (int i = 0; i < 4; ++i)
#pragma unroll
      for (int d = 0; d < 4; ++d) {
        xn[i][d] = f32x2{bf_lo(xv[i][d]), bf_hi(xv[i][d])} * __uint_as_float(rr[0]) + __uint_as_float(rr[1]);
        asm volatile("" : "+v"(xn[i][d]));
      }
    char* hp = (char*)H + ((size_t)u * 32 + wid * 4) * 1024 + lane * 16;
    asm volatile("s_waitcnt vmcnt(0)" : "+v"(mp[0]), "+v"(mp[1]), "+v"(mp[2]), "+v"(mp[3]), "+v"(mp[4]), "+v"(mp[5]), "+v"(mp[6]), "+v"(mp[7]), "+v"(hp)::"memory");
    poll_end(pm);
    f32x4 mv = sum4(mp) + sum4(mp + 4);
#pragma unroll
    for (int e = 0; e < 4; ++e) {
      mv[e] = rbf(mv[e]);
      if (lane & 32) mv[e] = rbf(1.0f + mv[e]);
    }
    dsw128<0>(smem_base + P_TAB + wid * 1024 + lane * 16, __builtin_bit_cast(u32x4, mv));
    static_for<16>([&](auto N) {
      constexpr int i = N.value >> 2, j = N.value & 3;
      dsr128<i * 128 + (j >> 1) * 512 + (j & 1) * 16>(tb[i][j], smem_base + P_TAB + wid * 1024 + fq * 32);
    });
    static_for<4>([&](auto I) {
      constexpr int i = I.value;
      asm volatile("s_waitcnt lgkmcnt(%4)" : "+v"(tb[i][0]), "+v"(tb[i][1]), "+v"(tb[i][2]), "+v"(tb[i][3]) : "n"(12 - 4 * i) : "memory");
      u32x4 ho;
#pragma unroll
      for (int d = 0; d < 4; ++d) {
        const f32x4 gg = __builtin_bit_cast(f32x4, tb[i][2 + (d >> 1)]), ss = __builtin_bit_cast(f32x4, tb[i][d >> 1]);
        const f32x2 hv = xn[i][d] * ((d & 1) ? hi2(gg) : lo2(gg)) + ((d & 1) ? hi2(ss) : lo2(ss));
        ho[d] = pk_bf16(hv[0], hv[1]);
      }
      gst128(hp + i * 1024, ho);
    });
    const sig_t g = drain(flags + (FL_H + (u >> 3) * 4) * 32, lane, ((u & 7) == 7 && u != 127) ? 8 : 4, 128);
    if (lds_add(smem_base + X_CN + 1024 + lane * 4, 1u) == 7) {
      signal(g);
    }
  } else {
    const rsrc_t rsS = mk_rsrc(word, wid == 7 ? 7 * 8192 + 4 : 0);
    unsigned seen = 0;
    auto mid = [&] { asm volatile("buffer_atomic_add %0, %1, %2, 0 offen sc0 sc1" : "+v"(seen) : "v"(lane * 8192), "s"(rsS) : "memory"); };
    if (!master) {
      const int ql = (wid * 7 + idx - 8) * 64 + lane;
      if (idx >= 8 && wid < 3) touch_lds(smem_base + QX_SINK, (unsigned)((ql >> 1) * 2048 + (xcd & 3) * 512 + 256 + (ql & 1) * 128), mk_rsrc(W, D_DIM * 3 * D_DIM * 2), 0);
      prefill(wid);
    }
    if (idx < 8) {
      if (wid == 0)
        bstore_sc1<0>(u32x4{0u, 0u, 0u, 0u}, (unsigned)(lane * 128), mk_rsrc(flags + (FL_O + v * ((FL_S - FL_O) / 64)) * 32, (FL_S - FL_O) / 64 * 128), 0);
      __builtin_amdgcn_s_sleep(NDLY);
      mod_gemv<2, 1>(smem_base + P_RED + wid * 512, lane, 2048 + (xcd * 8 + wid) * 64, idx * 128, c, wmod, mid);
      const float sum = red_sum<2>(smem_base + P_RED + wid * 512, lane);
      asm volatile("buffer_store_dword %1, %2, %3, 0 offen sc1\n\ts_waitcnt vmcnt(1)" : "+v"(seen) : "v"(sum), "v"(lane * 4), "s"(mk_rsrc(modp + 16384 + ((xcd * 8 + wid) * 8 + idx) * 64, 256)) : "memory");
    } else if (!master) {
      __builtin_amdgcn_s_sleep(TDLY);
#pragma unroll
      for (int q = 0; q < 3; ++q) {
        const int ql = ((q * 8 + wid) * 7 + idx - 8) * 64 + lane + 224 * 12;
        touch_lds(smem_base + QX_SINK, (unsigned)((ql >> 2) * 2048 + (xcd & 3) * 512 + (ql & 3) * 128), mk_rsrc(W, D_DIM * 3 * D_DIM * 2), 0);
      }
      __builtin_amdgcn_s_barrier();
      wait_vm<3>();
      mid();
      asm volatile("s_waitcnt vmcnt(0)" : "+v"(seen)::"memory");
    } else {
      wait_vm<0>();
      __builtin_amdgcn_s_barrier();
      bstore_sc1<0>(u32x4{1u, 1u, 1u, 1u}, (unsigned)(wid * 1024 + lane * 16), mk_rsrc(flags + (FL_S + xcd * 64) * 32, 64 * 128), 0);
      unsigned spins = 0;
      if (wid == 7) {
#pragma unroll 1
        do set_words(flags, lane); while (!words_set(word, lane) && ++spins < (1u << 20));
      }
      __builtin_amdgcn_s_barrier();
      prefill(wid);
      if (wid == 7) {
#pragma unroll 1
        do set_words(flags, lane); while (__builtin_amdgcn_ballot_w64(cnt_read(flags + FL_M * 32, lane, 16) < (lane < 16 ? 8u : 0u)) != 0ull && ++spins < (1u << 20));
      }
      seen = 1;
    }
    if (wid != 7) wait_vm<0>();
    else if ((__builtin_amdgcn_ballot_w64(seen == 0u) & 255u) != 0u) confirm(word, lane);
  }
}
DI void wo_piece(unsigned smem_base, unsigned p, unsigned rot, unsigned voff, rsrc_t rs) {
  const unsigned pb = p >> 2, kb = (pb & 24u) | ((pb + rot) & 7u);
  bload_lds<0>(smem_base + (p < 64u ? QX_WH + p * 1024u : (p - 64u) * 1024u), voff, rs, kb * 65536u + (p & 3u) * 16384u);
}

DI void qkv_ldw(unsigned smem_base, int lane, int sw, int n0, rsrc_t rsW, unsigned so, unsigned u) {
#pragma unroll
  for (int i = 0; i < 4; ++i) {
    const int r = (i & 1) * 8 + (lane >> 3), ch = lane & 7;
    const int g = ((r >> 1) & 1) | (((r >> 3) & 1) << 1);
    bload_lds<0>(smem_base + 9216 + sw * 4096 + so + i * 1024, (unsigned)(r * 6144 + sw * 2048 + (n0 + (((ch >> 1) ^ g) * 16) + (ch & 1) * 8) * 2), rsW,
                 u * 196608u + (i >> 1) * 98304u);
  }
}
DI void qkv_loader(unsigned smem_base, int lane, int sw, int m0, int n0, const bf16* H, rsrc_t rsW,
                   unsigned rot, unsigned voffO, rsrc_t rsO) {
  const rsrc_t rsH = mk_rsrc(H, S_LEN * D_DIM * 2);
  int fb0 = (m0 >> 4) - 1 + 3 * sw; fb0 = fb0 < 0 ? 0 : fb0;
  const unsigned voffH0 = (unsigned)(fb0 * 32768 + lane * 16);
  const unsigned voffH1 = (unsigned)(((m0 >> 4) + 3 * sw) * 32768 + lane * 16);
  const unsigned m0a = smem_base + sw * 3072;
  auto ldh = [&](unsigned so, unsigned u) {
    bload_lds_sc1<0>(m0a + so, voffH0, rsH, u * 1024u);
    bload_lds_sc1<0>(m0a + so + 1024, voffH1, rsH, u * 1024u);
    bload_lds_sc1<0>(m0a + so + 2048, voffH1, rsH, u * 1024u + 32768u);
  };
  auto ldw = [&](unsigned so, unsigned u) { qkv_ldw(smem_base, lane, sw, n0, rsW, so, u); };
  asm volatile("" ::"v"(voffH0), "v"(voffH1), "s"(rsH), "s"(m0a));
  __builtin_amdgcn_s_barrier();
#pragma unroll
  for (int u = 1; u <= NP1; ++u) ldh(u * Q_SLOT, u);
  wait_vm<3 * (NP1 - 1)>();
  __builtin_amdgcn_s_barrier();
#pragma unroll
  for (int u = NP1 + 1; u < Q_NSL; ++u) ldh(u * Q_SLOT, u);
  static_for<Q_NSL - 2, 1>([&](auto U) {
    constexpr int u = U.value;
    ldh((u - 1) * Q_SLOT, u + Q_NSL - 1); ldw((u - 1) * Q_SLOT, u + Q_NSL - 1);
    wait_vm<3 * (Q_NSL - 2 - u) + 7 * u>();
    __builtin_amdgcn_s_barrier();
  });
  unsigned sb = (Q_NSL - 3) * Q_SLOT;
  for (int u = Q_NSL - 2; u < 32 - (Q_NSL - 1); ++u) {
    ldh(sb, u + Q_NSL - 1); ldw(sb, u + Q_NSL - 1);
    sb = (sb == (Q_NSL - 1) * Q_SLOT) ? 0u : sb + Q_SLOT;
    wait_vm<(Q_NSL - 2) * 7>();
    __builtin_amdgcn_s_barrier();
  }
  for (int t = 0; t < 6; ++t) {
    const unsigned p0 = (unsigned)(t * 21 + (t >= 3 ? 1 : 0) + sw * 7);
#pragma unroll
    for (int i = 0; i < 7; ++i) wo_piece(smem_base, p0 + i, rot, voffO, rsO);
    if (t < 5) wait_vm<(Q_NSL - 2) * 7>();
    __builtin_amdgcn_s_barrier();
  }
}

template <int WM>
DI void qkv_mfma(unsigned smem_base, int lane, int wn, int bm, int bn, const bf16* H, bf16* __restrict__ O) {
  const int fr = lane & 15, fq = lane >> 4;
  unsigned bb0, bb1;
  const unsigned ab = smem_base + lane * 16;
  {
    const int q = (lane >> 2) & 3, p = lane & 3;
    const int r = 8 * fq + q;
    const int g = ((r >> 1) & 1) | (((r >> 3) & 1) << 1);
    bb0 = smem_base + 9216 + r * 128 + (((wn * 2) ^ g) * 32) + 8 * p;
    bb1 = bb0 ^ 32u;
  }
  const bool par = fr & 1, c0 = 4 * fq + (fr & 1) <= fr, c1 = 4 * fq + 2 + (fr & 1) <= fr;
  const unsigned s0 = par ? 0x01000c0cu : 0x0c0c0100u, s1 = par ? 0x03020c0cu : 0x0c0c0302u;
  u32x4 sel = {c0 ? s0 : 0x0c0c0c0cu, c1 ? s1 : 0x0c0c0c0cu, c0 ? 0x0c0c0c0cu : s0, c1 ? 0x0c0c0c0cu : s1};
  unsigned hx = ab + QX_HX + wn * 3072;
  asm volatile("" : "+v"(sel), "+v"(hx));
  const rsrc_t rsO = mk_rsrc((char*)O + (size_t)((bm * 2 + WM) * 32 + bn * 2 + wn) * 4096, 4096);
  f32x4 acc[4][6], acch[2];
#pragma unroll
  for (int i = 0; i < 4; ++i)
#pragma unroll
    for (int j = 0; j < 6; ++j) { acc[i][j] = f32x4{0, 0, 0, 0}; asm volatile("" : "+v"(acc[i][j])); }
#pragma unroll
  for (int j = 0; j < 2; ++j) { acch[j] = f32x4{0, 0, 0, 0}; asm volatile("" : "+v"(acch[j])); }
  __builtin_amdgcn_s_setprio(2);
  u32x4 aX[5], aY[5];
  u32x2 bXl[6], bXh[6], bYl[6], bYh[6];
  unsigned ra = ab, rb0 = bb0, rb1 = bb1, so = 0;
#define Q_READ(AR, BL, BH, R)                                                                                    \
  do {                                                                                                           \
    if ((R) < 12) {                                                                                              \
      constexpr int jb = ((R) < 12 ? (R) : 0) >> 1;                                                              \
      if ((R) & 1) dsr_tr<(jb >> 1) * 4096 + 512>(BH[jb], (jb & 1) ? rb1 : rb0);                                 \
      else dsr_tr<(jb >> 1) * 4096>(BL[jb], (jb & 1) ? rb1 : rb0);                                               \
    } else if ((R) < 16) {                                                                                       \
      constexpr int ia = ((R) >= 12 && (R) < 16) ? (R) - 12 : 0;                                                 \
      dsr128<1024 + WM * 4096 + ia * 1024>(AR[ia], ra);                                                          \
    } else {                                                                                                     \
      dsr128<0>(AR[4], ra);                                                                                      \
    }                                                                                                            \
  } while (0)
#define Q_MFMA(AR, BB, N)                                                                                        \
  do {                                                                                                           \
    if ((N) < 16) MFMA_AV(acc[(N) & 3][((N) < 24 ? (N) : 0) >> 2], BB[((N) < 24 ? (N) : 0) >> 2], AR[(N) & 3]);  \
    else if ((N) < 24) MFMA_AV(acc[(N) & 3][((N) < 24 ? (N) : 0) >> 2], AR[(N) & 3], BB[((N) < 24 ? (N) : 0) >> 2]); \
    else if (WM == 0) MFMA_AV(acch[(N) & 1], BB[2 + ((N) & 1)], AR[4]);                                          \
    else MFMA_AV(acch[(N) & 1], AR[4], BB[4 + ((N) & 1)]);                                                       \
  } while (0)
#define Q_ADV(K)                                                                                                 \
  do {                                                                                                           \
    if ((K) == 0) asm volatile("s_add_u32 %0, %0, %1\n\ts_cmp_eq_u32 %0, %2\n\ts_cselect_b32 %0, 0, %0" : "+s"(so) : "n"(Q_SLOT), "n"(Q_NSL * Q_SLOT) : "scc"); \
    else if ((K) == 1) asm volatile("v_add_u32 %0, %1, %2" : "=v"(rb0) : "s"(so), "v"(bb0));                     \
    else if ((K) == 2) asm volatile("v_add_u32 %0, %1, %2" : "=v"(rb1) : "s"(so), "v"(bb1));                     \
    else asm volatile("v_add_u32 %0, %1, %2" : "=v"(ra) : "s"(so), "v"(ab));                                     \
  } while (0)
#define Q_UNIT(AR, BL, BH, NAR, NBL, NBH)                                                                        \
  do {                                                                                                           \
    WAIT_LGKM0();                                                                                                \
    u32x4 bb[6];                                                                                                 \
    _Pragma("unroll") for (int j = 0; j < 6; ++j) bb[j] = u32x4{BL[j][0], BL[j][1], BH[j][0], BH[j][1]};         \
    SCHED0();                                                                                                    \
    static_for<26>([&](auto N) {                                                                                 \
      constexpr int n = N.value;                                                                                 \
      if constexpr (n == QBAR) __builtin_amdgcn_s_barrier();                                                     \
      Q_MFMA(AR, bb, n);                                                                                         \
      if constexpr (n >= QBAR && n < QBAR + 17) Q_READ(NAR, NBL, NBH, n - QBAR);                                 \
      if constexpr (n >= QBAR + 18 && n < QBAR + 22) Q_ADV(n - QBAR - 18);                                       \
    });                                                                                                          \
    SCHED0();                                                                                                    \
  } while (0)
  {
    const rsrc_t rsH = mk_rsrc(H, S_LEN * D_DIM * 2);
    unsigned vh = lane * 16 + (bm * 8 + WM * 4) * 32768, vl = lane * 16 + (bm ? bm * 8 - 1 : 0) * 32768;
    asm volatile("" : "+v"(vh), "+v"(vl), "+v"(rb0), "+v"(rb1) : "s"(rsH));
    __builtin_amdgcn_s_barrier();
    static_for<5>([&](auto I) { asm volatile("" : "=v"(aX[I.value])); bload_sc1<0>(aX[I.value], I.value < 4 ? vh : vl, rsH, (I.value & 3) * 32768); });
    static_for<12>([&](auto R) { Q_READ(aX, bXl, bXh, R.value); });
    static_for<4>([&](auto K) { Q_ADV(K.value); });
    asm volatile("s_waitcnt vmcnt(0)" : "+v"(aX[0]), "+v"(aX[1]), "+v"(aX[2]), "+v"(aX[3]), "+v"(aX[4])::"memory");
  }
  for (int u = 0; u < 32; u += 2) {
    Q_UNIT(aX, bXl, bXh, aY, bYl, bYh);
    Q_UNIT(aY, bYl, bYh, aX, bXl, bXh);
  }
  asm volatile("s_waitcnt lgkmcnt(0)\n\ts_nop 7" ::: "memory");
#pragma unroll
  for (int i = 0; i < 5; ++i) asm volatile("" : "+v"(aX[i]));
#pragma unroll
  for (int j = 0; j < 6; ++j) asm volatile("" : "+v"(bXl[j]), "+v"(bXh[j]));
#pragma unroll
  for (int i = 0; i < 4; ++i)
#pragma unroll
    for (int j = 0; j < 6; ++j) asm volatile("" : "+v"(acc[i][j]));
  asm volatile("" : "+v"(acch[0]), "+v"(acch[1]));
  SCHED0();
  u32x4 kk[5], vv[5];
  if (WM == 0) {
    kk[4] = pack8(acc[3][2], acc[3][3]); vv[4] = pack8(acc[3][4], acc[3][5]);
    dsw128<0>(hx, kk[4]); dsw128<1024>(hx, vv[4]);
  } else {
    dsw128<2048>(hx, pack8(acch[0], acch[1]));
  }
  asm volatile("s_waitcnt lgkmcnt(0)" ::: "memory");
  __builtin_amdgcn_s_barrier();
  if (WM == 0) dsr128<2048>(vv[0], hx);
  else { dsr128<0>(kk[0], hx); dsr128<1024>(vv[0], hx); }
  SCHED0();
  if (WM == 0) kk[0] = pack8(acch[0], acch[1]);
#pragma unroll
  for (int i = 0; i < (WM == 0 ? 3 : 4); ++i) kk[i + 1] = pack8(acc[i][2], acc[i][3]);
  const float cs = 0.17677669529663687f * 1.4426950408889634f;
  float l0[4], l1[4], tm[4];
#pragma unroll
  for (int ii = 1; ii < 5; ++ii) {
    const int i = ii & 3;
    const u32x4 q = pack8(acc[i][0], acc[i][1]);
    if (WM == 1 && i == 0) asm volatile("s_waitcnt lgkmcnt(0)" : "+v"(kk[0]), "+v"(vv[0])::"memory");
    const f32x4 sc = mfma0(kk[i + 1], q), sp = mfma0(kk[i], q);
    float p0 = par ? sp[1] : sp[0], p1 = par ? sp[3] : sp[2];
    if (WM == 0 && i == 0 && bm == 0) { p0 = -5.656854249e9f; p1 = -5.656854249e9f; }
    l0[i] = c0 ? (par ? sc[1] : sc[0]) : p0;
    l1[i] = c1 ? (par ? sc[3] : sc[2]) : p1;
    tm[i] = fmax2(l0[i], l1[i]);
  }
#pragma unroll
  for (int i = 0; i < (WM == 0 ? 3 : 4); ++i) vv[i + 1] = pack8(acc[i][4], acc[i][5]);
  bcast4(rows4(tm, [](float a, float b) { return fmax2(a, b); }), tm);
#pragma unroll
  for (int i = 0; i < 4; ++i) {
    l0[i] = __builtin_amdgcn_exp2f((l0[i] - tm[i]) * cs);
    l1[i] = __builtin_amdgcn_exp2f((l1[i] - tm[i]) * cs);
    tm[i] = l0[i] + l1[i];
  }
  bcast4(__builtin_amdgcn_rcpf(rows4(tm, [](float a, float b) { return a + b; })), tm);
  if (WM == 0) asm volatile("s_waitcnt lgkmcnt(0)" : "+v"(vv[0])::"memory");
#pragma unroll
  for (int i = 0; i < 4; ++i) {
    const unsigned pp = pk_bf16(l0[i] * tm[i], l1[i] * tm[i]);
    const u32x4 pr = {__builtin_amdgcn_perm(pp, pp, sel[0]), __builtin_amdgcn_perm(pp, pp, sel[1]),
                      __builtin_amdgcn_perm(pp, pp, sel[2]), __builtin_amdgcn_perm(pp, pp, sel[3])};
    const u32x4 ov = pack8(mfma0(u32x4{vv[i + 1][0], vv[i + 1][1], vv[i][0], vv[i][1]}, pr), mfma0(u32x4{vv[i + 1][2], vv[i + 1][3], vv[i][2], vv[i][3]}, pr));
    asm volatile("buffer_store_dwordx4 %0, %1, %2, 0 offen offset:%3 sc1\n\ts_nop 1" ::"v"(ov), "v"(lane * 16), "s"(rsO), "i"(i * 1024) : "memory");
  }
}

template <class GST>
DI void attn_phase(unsigned smem_base, int wid, bf16* H, const bf16* __restrict__ W, bf16* O,
                   const bf16* __restrict__ wo, const bf16* __restrict__ X, float* modp, bf16* Y, float* stat,
                   unsigned* flags, const bf16* __restrict__ wg, const bf16* __restrict__ wu,
                   unsigned* ctab, const bf16* __restrict__ cnd, const bf16* __restrict__ wmod, GST gst) {
  const int tid = threadIdx.x, lane = tid & 63;
  const int xcd = blockIdx.x & 7, idx = blockIdx.x >> 3;
  const int bm = (xcd >> 2) * 8 + (idx & 7), bn = (xcd & 3) * 4 + (idx >> 3);
  const int rb = xcd * 2 + (idx & 1), cg = idx >> 1;
  const unsigned rot = __builtin_amdgcn_readfirstlane((cg >> 2) * 2);
  const unsigned cnt = smem_base + QX_CNT, cntl = cnt + lane * 4;
  const rsrc_t rsW = mk_rsrc(W, D_DIM * 3 * D_DIM * 2);
  first_phase(smem_base, wid, lane, X, cnd, wmod, W, modp, H, flags, [&](int w) {
#pragma unroll
    for (int i = 0; i < 3; ++i) {
      const int p = w + 8 * i < 21 ? w + 8 * i : 20, u = (p * 11) >> 5;
      qkv_ldw(smem_base, lane, p - 3 * u, bn * 64, rsW, u * Q_SLOT, u);
    }
  });
  if (wid < 4) {
    if (wid & 1) qkv_mfma<1>(smem_base, lane, wid >> 1, bm, bn, H, O);
    else qkv_mfma<0>(smem_base, lane, wid >> 1, bm, bn, H, O);
  } else {
    const rsrc_t rsO = mk_rsrc(wo, D_DIM * D_DIM * 2);
    const unsigned voffO = (unsigned)((lane >> 3) * 2048 + cg * 128 + ((((lane & 7) >> 1) ^ (lane >> 4)) * 32) + (lane & 1) * 16);
    if (wid < 7) qkv_loader(smem_base, lane, wid - 4, bm * 128, bn * 64, H, rsW, rot, voffO, rsO);
    else {
      const unsigned vgu = idx < 16 ? 0x40000000u : (unsigned)((((idx - 16) * 64 + lane) / 7) * (F_DIM * 2) + xcd * 896 + (((idx - 16) * 64 + lane) % 7) * 128);
      const poll_t ph = poll(0u, flags + (FL_H + bm * 4 + (xcd & 3)) * 32, lane, 1, bm ? 9u : 8u);
      asm volatile("s_barrier" ::: "memory");
      SCHED0();
      unsigned* r1 = flags + (FL_M + (1 + xcd) * 32 + 24) * 32;
      const sig_t gi = drain(idx < 8 ? r1 : flags + (FL_M + 24 + xcd) * 32, lane, idx < 16 ? 1 : 0, 4);
      poll_end(ph);
      signal(gi);
      if (idx < 8) {
        const int v = xcd * 8 + idx;
        unsigned t = 0;
        for (int i = 0; i < 4; ++i) __builtin_amdgcn_s_barrier();
        asm volatile("buffer_atomic_add %0, %1, %2, 0 offen sc0 sc1\n\ts_barrier\n\ts_barrier\n\ts_barrier\n\ts_barrier\n\ts_waitcnt vmcnt(0)" : "+v"(t) : "v"(lane * 128), "s"(mk_rsrc(r1, 128)) : "memory");
#pragma unroll 1
        for (unsigned spins = 0; __builtin_amdgcn_readfirstlane(t) < 8u && spins < (1u << 20); ++spins) t = cnt_read(r1, lane, 1);
        static_for<2>([&](auto I) { bload_lds_sc1<I.value * 1024>(smem_base + G3_CT, lane * 16, mk_rsrc(modp + 16384 + v * 512, 2048), 0); });
        for (int i = 0; i < 4; ++i) __builtin_amdgcn_s_barrier();
        wait_vm<0>();
        float sum = red_sum<8>(smem_base + G3_CT, lane);
        sum = (v >> 4) == 2 ? gain_nz(sum) : rbf(sum);
        asm volatile("buffer_store_dword %0, %1, %2, 0 offen sc1" ::"v"(sum), "v"(lane * 4), "s"(mk_rsrc(modp + 49152 + v * 64, 256)) : "memory");
        asm volatile("buffer_store_short_d16_hi %0, %1, %2, 0 offen sc1" ::"v"(sum), "v"((lane >> 5) * 128 + (lane & 31) * 2),
                     "s"(mk_rsrc(ctab + (v & 15) * 64 + (v & 16), (v >> 4) == 1 || (v >> 4) == 2 ? 256 : 0)) : "memory");
        for (int i = 0; i < 4; ++i) __builtin_amdgcn_s_barrier();
        signal(drain(flags + (FL_M + 16 + idx) * 32, lane, 9, 4096));
      }
      {
        int nxt = TS + idx % TP, ev = 0;
#pragma unroll 1
        for (int i = idx < 8 ? 16 : 0; i < 27; ++i) {
          if (i == WOS && idx >= 16) {
            touch_lds(smem_base + QX_SINK, (blockIdx.x - 128) * 16384 + lane * 128, rsO, 0);
            touch_lds(smem_base + QX_SINK, (blockIdx.x - 128) * 16384 + lane * 128, rsO, 8192);
          }
          if (i == nxt && ev < 2) {
            touch_lds(smem_base + QX_SINK, vgu, mk_rsrc(ev ? wu : wg, D_DIM * F_DIM * 2), 0);
            nxt += TP; ++ev;
          }
          __builtin_amdgcn_s_barrier();
        }
      }
      unsigned* cr = flags + (FL_M + (idx == 15 ? 0 : 1 + xcd) * 32 + 16) * 32;
      const int nl = idx == 15 ? 16 : 8;
      unsigned t = 0;
      asm volatile("buffer_atomic_add %0, %1, %2, 0 offen sc0 sc1" : "+v"(t) : "v"(lane * 128), "s"(mk_rsrc(cr, nl * 128)) : "memory");
      wo_piece(smem_base, 63u, rot, voffO, rsO);
      __builtin_amdgcn_s_barrier();
      __builtin_amdgcn_s_barrier();
      asm volatile("s_waitcnt vmcnt(1)" : "+v"(t)::"memory");
      for (unsigned spins = 0; __builtin_amdgcn_ballot_w64(t < (lane < nl ? 8u : 0u)) != 0ull && spins < (1u << 20); ++spins) {
        if (idx == 15) set_words(flags, lane);
        t = cnt_read(cr, lane, nl);
      }
      static_for<4>([&](auto I) { bload_lds_sc1<I.value * 1024>(smem_base + G3_CT, lane * 16, mk_rsrc(ctab, 4096), 0); });
#pragma unroll
      for (int i = 0; i < 2; ++i)
        asm volatile("s_mov_b32 m0, %0\n\ts_nop 0\n\tbuffer_load_dword %1, %2, 0 offen sc1 lds" ::"s"(smem_base + QX_GT + i * 256), "v"(lane * 4),
                     "s"(mk_rsrc(modp + 49152 + i * 2048 + cg * 64, 256)) : "memory");
      for (int i = 30; i < 33; ++i) __builtin_amdgcn_s_barrier();
      wo_piece(smem_base, 127u, rot, voffO, rsO);
    }
    __builtin_amdgcn_s_barrier();
    wait_vm<0>();
    lds_add(cntl, 1u);
  }
  const int fr = lane & 15, fq = lane >> 4;
  const int kq = wid & 3, rg = rb * 2 + (wid >> 2);
  const int ko = (kq + cg) & 3;
  lds_wait<2>(cnt, 4);
  u32x2 wl[8][4], wh[8][4];
  {
    const int q = (lane >> 2) & 3, p = lane & 3, r = 4 * fq + q;
    const unsigned wb = smem_base + (kq < 2 ? QX_WH + kq * 32768 : (kq - 2) * 32768) + r * 128 + (((r >> 1) & 3) * 32) + 8 * p;
    unsigned wa[4];
#pragma unroll
    for (int f = 0; f < 4; ++f) wa[f] = wb ^ (f * 32);
    static_for<32>([&](auto E) {
      constexpr int s = E.value >> 2, f = E.value & 3;
      dsr_tr<s * 4096>(wl[s][f], wa[f]);
      dsr_tr<s * 4096 + 2048>(wh[s][f], wa[f]);
    });
  }
  const rsrc_t rsA = mk_rsrc((const char*)O + (size_t)rg * 131072, 131072);
  unsigned voffA[4], hs[8];
#pragma unroll
  for (int i = 0; i < 4; ++i) { voffA[i] = lane * 16 + ((ko + 1 + i) & 3) * 1024; asm volatile("" : "+v"(voffA[i])); }
#pragma unroll
  for (int i = 0; i < 8; ++i) { hs[i] = __builtin_amdgcn_readfirstlane((kq * 8 + ((i + rot) & 7)) * 4096); asm volatile("" : "+s"(hs[i])); }
  asm volatile("" ::"s"(rsA));
  if (wid < 4) {
    const sig_t g = drain(flags + (FL_O + bm * 8 + (bn & 1)) * 32, lane, 4, 256);
    if (lds_add(cntl + 256, 1u) == 3u) signal(g);
  }
  const poll_t pl = poll(wid - 7, flags + (FL_O + rb * 8 + (cg & 3) * 2) * 32, lane, 2, 8u);
  asm volatile("s_waitcnt lgkmcnt(0)" ::: "memory");
  __builtin_amdgcn_s_barrier();
  u32x4 wf[8][4];
#pragma unroll
  for (int s = 0; s < 8; ++s)
#pragma unroll
    for (int f = 0; f < 4; ++f) {
      asm volatile("" : "+v"(wl[s][f]), "+v"(wh[s][f]));
      wf[s][f] = u32x4{wl[s][f][0], wl[s][f][1], wh[s][f][0], wh[s][f][1]};
    }
  const unsigned voffL = lane * 16;
  u32x4 af[32], xv[2], pr[3][4], gt[4], ct[4];
  f32x4 acc[4][4];
  auto aload = [&](auto N) {
    constexpr int n = N.value;
    asm volatile("" : "=v"(af[n]));
    bload_sc1<0>(af[n], voffA[n >> 3], rsA, hs[n & 7]);
  };
  static_for<GDEP>([&](auto N) { aload(N); });
  const unsigned sl = smem_base + 49152 + voffL + (wid >> 2) * 49152;
  const int sw = ((fq & 1) << 1) | (fq >> 1);
  SCHED0();
  static_for<32>([&](auto N) {
    constexpr int n = N.value, ps = n >> 3, s = n & 7, XG = 32 - GDEP;
    constexpr int last = n - 1 + GDEP < 31 ? n - 1 + GDEP : 31;
    asm volatile("s_waitcnt vmcnt(%1)" : "+v"(af[n]) : "n"(last - n + (n > XG ? 2 : 0) + (n > GS0 + 3 ? 6 : (n > GS0 ? 2 * (n - GS0) : 0))) : "memory");
    if constexpr (n == 0) poll_end(pl);
    if constexpr (n == 28) { WAIT_LGKM0(); __builtin_amdgcn_s_barrier(); }
    static_for<4>([&](auto F) {
      constexpr int f = F.value;
      if constexpr (s == 0) asm volatile("v_mfma_f32_16x16x32_bf16 %0, %1, %2, 0" : "=&v"(acc[ps][f]) : "v"(wf[s][f]), "v"(af[n]));
      else MFMA_AV(acc[ps][f], wf[s][f], af[n]);
      if constexpr (f == 0 && n + GDEP < 32) aload(ic<(n + GDEP < 32 ? n + GDEP : 0)>{});
      if constexpr (f == 0 && n == XG) {
        const rsrc_t rsX = mk_rsrc(X, S_LEN * D_DIM * 2);
        const unsigned vx = (unsigned)(((rg * 64 + ko * 16 + fr) * D_DIM + cg * 64 + sw * 8) * 2);
        asm volatile("" : "=v"(xv[0]), "=v"(xv[1]));
        bload_v<0>(xv[0], vx, rsX, 0); bload_v<64>(xv[1], vx, rsX, 0);
      }
      if constexpr ((f & 1) && n >= GS0 && n < GS0 + 3) gst(ic<(n >= GS0 ? n - GS0 : 0)>{}, ic<(f >> 1)>{});
      if constexpr (ps > 0 && (s == 2 || s == 3) && (f & 1)) {
        constexpr int e = (s - 2) * 2 + (f >> 1);
        dsw128<(ps - 1) * 4096 + e * 1024>(sl + ((kq + ps) & 3) * 12288, __builtin_bit_cast(u32x4, acc[ps - 1][e]));
      }
      if constexpr (n >= 28 && n < 31) dsr128<(n - 28) * 4096 + f * 1024>(pr[n - 28][f], sl + kq * 12288);
      if constexpr (n == 31) { dsr128<f * 64>(gt[f], smem_base + QX_GT + fq * 16); dsr128<256 + f * 64>(ct[f], smem_base + QX_GT + fq * 16); }
    });
  });
  asm volatile("s_waitcnt vmcnt(6)" : "+v"(xv[0]), "+v"(xv[1])::"memory");
  asm volatile("s_waitcnt lgkmcnt(0)\n\ts_nop 4" ::: "memory");
  f32x4 fin[4], yv[4];
#pragma unroll
  for (int f = 0; f < 4; ++f) asm volatile("" : "+v"(acc[3][f]), "+v"(pr[0][f]), "+v"(pr[1][f]), "+v"(pr[2][f]), "+v"(gt[f]), "+v"(ct[f]));
#pragma unroll
  for (int c = 0; c < 2; ++c) {
    const auto x0 = __builtin_amdgcn_permlane16_swap(xv[c][0], xv[c][2], false, false);
    const auto x1 = __builtin_amdgcn_permlane16_swap(xv[c][1], xv[c][3], false, false);
#pragma unroll
    for (int h = 0; h < 2; ++h) {
      const int f = 2 * c + h;
      const f32x4 a = (acc[3][f] + __builtin_bit_cast(f32x4, pr[0][f])) + (__builtin_bit_cast(f32x4, pr[1][f]) + __builtin_bit_cast(f32x4, pr[2][f]));
      const f32x4 xf = {bf_lo(x0[h]), bf_hi(x0[h]), bf_lo(x1[h]), bf_hi(x1[h])};
      fin[f] = xf + __builtin_bit_cast(f32x4, gt[f]) * a;
      yv[f] = fin[f] * __builtin_bit_cast(f32x4, ct[f]);
    }
    const auto y0 = __builtin_amdgcn_permlane16_swap(pk_bf16(yv[2 * c][0], yv[2 * c][1]), pk_bf16(yv[2 * c + 1][0], yv[2 * c + 1][1]), false, false);
    const auto y1 = __builtin_amdgcn_permlane16_swap(pk_bf16(yv[2 * c][2], yv[2 * c][3]), pk_bf16(yv[2 * c + 1][2], yv[2 * c + 1][3]), false, false);
    gst128((char*)Y + ((size_t)(rg >> 2) * 32 + cg * 2 + c) * 16384 + ((rg & 3) * 4 + ko) * 1024 + (sw * 16 + fr) * 16,
           u32x4{y0[0], y1[0], y0[1], y1[1]});
  }
  const f32x4 s4 = (fin[0] + fin[1]) + (fin[2] + fin[3]);
  const float s = rowsum4(hadd(lo2(s4) + hi2(s4)));
  const float mb = s * (1.0f / 64.0f);
  f32x4 q4 = {0, 0, 0, 0};
#pragma unroll
  for (int f = 0; f < 4; ++f) { const f32x4 d = fin[f] - mb; q4 += d * d; }
  const float m2 = rowsum4(hadd(lo2(q4) + hi2(q4)));
  asm volatile("buffer_store_dwordx2 %0, %1, %2, 0 offen sc1" ::"v"(f32x2{s, m2}), "v"(lane * 8),
               "s"(mk_rsrc(stat + ((size_t)cg * S_LEN + rg * 64 + ko * 16) * 2, 128)) : "memory");
}

#define G3_STAGE(SLOT, P, U)                                                                               \
  do {                                                                                                     \
    if ((P) == 0) bload_lds_sc1<0>(m0w + 65536 + (SLOT) * 16384, voffA, rsA, (unsigned)(U) * 16384u);      \
    else if ((P) == 1) bload_lds_sc1<1024>(m0w + 65536 + (SLOT) * 16384, voffA, rsA, (unsigned)(U) * 16384u);  \
    else bload_lds<0>(m0w + (SLOT) * 16384 + ((P) - 2) * 1024, (P) == 2 ? voffB0 : voffB1, rsB, (unsigned)(U) * 229376u);  \
  } while (0)

DI constexpr int g3_line(int gx, int p) {
  const int t = p + ((3 + gx) & 7);
  return 7 * gx + (t >= 7 ? t - 7 : t);
}
constexpr unsigned act_frag(int kh, int p) {
  const int tl = p < 48 ? p / 3 : (p - 48) >> 2, jv = (tl & 3) * 7 + (p < 48 ? 4 + p % 3 : p & 3);
  return g3_line(kh * 4 + (tl >> 2), jv >> 2) * 4 + (jv & 3);
}
template <int FN, class WST, class HOOK, class PRE>
DI void g3_body(unsigned smem_base, int wm, int wn, int wid, int lane, int bm, int bn, rsrc_t rsA, rsrc_t rsB,
                rsrc_t rsE, const char* ta, unsigned voffA, unsigned voffB0, unsigned voffB1, bf16* act, WST wst, HOOK hook, PRE pre) {
  constexpr int NB = 2 * FN, NM = 4 * NB, RD = 5 + 2 * NB;
  const int fr = lane & 15, fq = lane >> 4;
  f32x4 acc[4][NB], accC[2];
  accC[0] = f32x4{0, 0, 0, 0}; accC[1] = f32x4{0, 0, 0, 0};
  asm volatile("" : "+v"(accC[0]), "+v"(accC[1]));
  const int rot = wm % FN;
  unsigned cad = fr >= 2 || wm >= FN ? smem_base + X_CN + 1536 : smem_base + G3_CT + (fr & 1) * 64 + fq * 16;
  unsigned cin = fr >= 2 || wm >= FN ? 0u : 512u;
  asm volatile("" : "+v"(cin));
#pragma unroll
  for (int i = 0; i < 4; ++i)
#pragma unroll
    for (int j = 0; j < NB; ++j) { acc[i][j] = f32x4{0, 0, 0, 0}; asm volatile("" : "+v"(acc[i][j])); }
  unsigned aad = smem_base + 65536 + wm * 4096 + lane * 16;
  unsigned bad[FN];
  {
    const int q = (lane >> 2) & 3, p = lane & 3;
    const int r = 8 * fq + q;
    const int sw = (r & 3) | (((r >> 3) & 1) << 2);
#pragma unroll
    for (int j = 0; j < FN; ++j) {
      const int gi = wn * 4 + (j + rot) % FN + (bn & 1);
      bad[j] = smem_base + r * 256 + ((gi ^ sw) * 32) + 8 * p;
    }
  }
  u32x4 aX[5], aY[5];
  u32x2 bXl[NB], bXh[NB], bYl[NB], bYh[NB];
  const unsigned m0w = smem_base + wid * 2048;
  const int ut = wid == 7 ? CT3 : wid == 6 ? ST3 : -1;
  const unsigned ve = wid == 7 ? lane * 32 + (blockIdx.x >> 3) * 2048u + 8192u : ((bn & 3) * 4 + (lane >> 4)) * 16384 + bm * 2048 + (lane & 15) * 128;
  unsigned tinc = lane < 14 ? 8 * 229376 : 8192;
  asm volatile("" : "+v"(ta), "+v"(tinc));
#define G3_READ(AR, BL, BH, SLOT, R)                                                                       \
  do {                                                                                                     \
    if ((R) < 2 * NB) {                                                                                    \
      constexpr int jb = ((R) < 2 * NB ? (R) : 0) >> 1;                                                    \
      constexpr int off = (SLOT) * 16384 + (jb / FN) * 8192 + ((R) & 1) * 1024;                            \
      if ((R) & 1) dsr_tr<off>(BH[jb], bad[jb % FN]);                                                       \
      else dsr_tr<off>(BL[jb], bad[jb % FN]);                                                               \
    } else if ((R) < 2 * NB + 4) {                                                                         \
      constexpr int ia = ((R) >= 2 * NB && (R) < 2 * NB + 4) ? (R) - 2 * NB : 0;                           \
      dsr128<(SLOT) * 16384 + ia * 1024>(AR[ia], aad);                                                      \
    } else {                                                                                               \
      if ((SLOT) == 0) cad += cin;                                       \
      dsr128<(SLOT) * 128>(AR[4], cad);                                                                    \
    }                                                                                                      \
  } while (0)
  auto rd = [&](auto SL, auto RR, u32x4 (&AR)[5], u32x2 (&BL)[NB], u32x2 (&BH)[NB]) { G3_READ(AR, BL, BH, SL.value, RR.value); };
#define G3_UNIT(AR, BL, BH, NAR, NBL, NBH, SLOT, U, LI)                                                    \
  do {                                                                                                     \
    WAIT_LGKM0();                                                                                          \
    u32x4 bb[NB];                                                                                          \
    _Pragma("unroll") for (int j = 0; j < NB; ++j) bb[j] = u32x4{BL[j][0], BL[j][1], BH[j][0], BH[j][1]};  \
    SCHED0();                                                                                              \
    asm volatile("s_nop 1");                                                                               \
    static_for<NM>([&](auto N) {                                                                           \
      constexpr int n = N.value;                                                                           \
      if constexpr (n == NM / 2) {                                                                         \
        wait_vm<(LI && (SLOT) < 2 && FN == 3) ? 13 + 4 * (SLOT) : 8>(); __builtin_amdgcn_s_barrier();      \
      }                                                                                                    \
      if constexpr (n == NM / 2 + TPOS && !LI) {                                                           \
          \
        if (((U) & 7) == wid) {                                                                            \
          asm volatile("s_mov_b32 m0, %2\n\ts_mov_b64 exec, %1\n\tglobal_load_lds_dword %0, off\n\ts_mov_b64 exec, -1"      \
                       ::"v"(ta), "s"((U) + WTD < 32 ? 0x3fffffull : 0x3fc000ull), "s"(smem_base + 131072 + 4096) : "memory"); \
          ta += tinc;                                                                                      \
        }                                                                                                  \
        if (FN == 3 && (SLOT) == 0 && (U) == ut) touch_lds(smem_base + 131072 + 4096, ve, rsE, 0);         \
      }                                                                                                    \
      MFMA_AV(acc[n & 3][n >> 2], bb[n >> 2], AR[n & 3]);                                                  \
      if constexpr (LI && (SLOT) == 0 && n < 12 && n % 3 == 0) hook(ic<-n / 3>{});                         \
      if constexpr (LI && (SLOT) == 0 && (n == 15 || n == 21)) hook(ic<-(n + 9) / 6>{});                   \
      if constexpr (LI && (SLOT) == 1 && (n == 0 || n == 3)) hook(ic<-6 - n / 3>{});                       \
      if constexpr (LI && (SLOT) == 2 && n >= 21 && n < 24) hook(ic<n - 8>{});                             \
      if constexpr (n < NM / 2) {                                                                          \
        if constexpr (LI && (SLOT) == 2 && n < 12) hook(ic<n + 1>{});                                      \
        if constexpr (n % 3 == 1 && n / 3 < 4) {                                                           \
          if constexpr (LI && (SLOT) > 0) wst(ic<((SLOT) + 3) & 3>{}, ic<(n / 3) & 3>{});                  \
          else G3_STAGE(((SLOT) + 3) & 3, n / 3, (U) + 3);                                                 \
        }                                                                                                  \
      } else {                                                                                             \
        constexpr int r0 = (n - NM / 2) * 2;                                                               \
        if constexpr (r0 < RD) rd(ic<((SLOT) + 1) & 3>{}, ic<(r0 < RD ? r0 : 0)>{}, NAR, NBL, NBH);        \
        if constexpr (r0 + 1 < RD) rd(ic<((SLOT) + 1) & 3>{}, ic<(r0 + 1 < RD ? r0 + 1 : 0)>{}, NAR, NBL, NBH); \
      }                                                                                                    \
    });                                                                                                    \
    MFMA_AV(accC[0], bb[0], AR[4]);                                                                        \
    MFMA_AV(accC[1], bb[FN], AR[4]);                                                                       \
    SCHED0();                                                                                              \
  } while (0)

  unsigned v0 = wm * 4096 + lane * 16;
#pragma unroll
  for (int j = 0; j < FN; ++j) asm volatile("" : "+v"(bad[j]));
  asm volatile("" : "+v"(aad), "+v"(cad), "+v"(voffA), "+v"(v0) : "s"(rsA), "s"(m0w));
  const poll_t pl = pre();
  static_for<4>([&](auto I) { asm volatile("" : "=v"(aX[I.value])); bload_sc1<I.value * 1024>(aX[I.value], v0, rsA, 0); });
  static_for<2 * NB>([&](auto RR) { rd(ic<0>{}, RR, aX, bXl, bXh); });
  dsr128<0>(aX[4], cad);
  G3_STAGE(1, 0, 1); G3_STAGE(1, 1, 1); G3_STAGE(2, 0, 2); G3_STAGE(2, 1, 2);
#pragma unroll
  for (int i = 0; i < 2; ++i) bload_lds<0>(smem_base + 131072 + 4096, voffA, rsA, 0x40000000u);
  asm volatile("s_waitcnt vmcnt(6)" : "+v"(aX[0]), "+v"(aX[1]), "+v"(aX[2]), "+v"(aX[3])::"memory");
  poll_end(pl);
  for (int u = 0; u < 28; u += 4) {
    G3_UNIT(aX, bXl, bXh, aY, bYl, bYh, 0, u, false);
    G3_UNIT(aY, bYl, bYh, aX, bXl, bXh, 1, u + 1, false);
    G3_UNIT(aX, bXl, bXh, aY, bYl, bYh, 2, u + 2, false);
    G3_UNIT(aY, bYl, bYh, aX, bXl, bXh, 3, u + 3, false);
  }
  G3_UNIT(aX, bXl, bXh, aY, bYl, bYh, 0, 28, true);
  G3_UNIT(aY, bYl, bYh, aX, bXl, bXh, 1, 29, true);
  if (FN == 3) __builtin_amdgcn_s_setprio(1);
  G3_UNIT(aX, bXl, bXh, aY, bYl, bYh, 2, 30, true);
  constexpr float KG = -1.4426950408889634f, KU = -0.6931471805599453f;
  constexpr int EB = 7, RS = 131072, CC = RS + 2048;
  u32x2 rs[4];
  u32x4 cc[2][4];
  unsigned ca[FN], pa, ra, sa;
  int l2;
  auto rdc = [&](auto J, auto E) { dsr128<E.value * 512>(cc[J.value & 1][E.value], ca[J.value]); };
  {
    WAIT_LGKM0();
    u32x4 bb[NB];
#pragma unroll
    for (int j = 0; j < NB; ++j) bb[j] = u32x4{bYl[j][0], bYl[j][1], bYh[j][0], bYh[j][1]};
    SCHED0();
    asm volatile("s_nop 1");
    static_for<NM>([&](auto N) {
      constexpr int n = N.value, k = n - EB;
      if constexpr (n == EB) {
        asm volatile("s_waitcnt lgkmcnt(0)" ::: "memory"); __builtin_amdgcn_s_barrier();
      }
      if constexpr (n == 0) { MFMA_AV(accC[0], bb[0], aY[4]); MFMA_AV(accC[1], bb[FN], aY[4]); }
      MFMA_AV(acc[n & 3][n >> 2], bb[n >> 2], aY[n & 3]);
      if constexpr (n == 0) {
        l2 = fresh_lane();
#pragma unroll
        for (int j = 0; j < FN; ++j) { ca[j] = smem_base + CC + (wn * 4 + (j + rot) % FN) * 64 + (l2 & 48); asm volatile("" : "+v"(ca[j])); }
      }
      if constexpr (n == 1) {
        pa = ca[0] + (l2 & 15) * 512;
        sa = smem_base + QX_SINK + l2 * 16;
        asm volatile("" : "+v"(pa), "+v"(sa), "+v"(l2));
      }
      if constexpr (n == 2) {
        pa = (l2 & 15) < (wm < FN ? 2 : 0) ? pa : sa;
        ra = smem_base + RS + (wm * 64 + (l2 & 15)) * 8;
        asm volatile("" : "+v"(pa), "+v"(ra));
      }
      if constexpr (n == 5) {
        asm volatile("" : "+v"(l2));
        sa = (l2 & 16) * 64 + (l2 & 32) * 8 + (l2 & 15) * 16;
        asm volatile("" : "+v"(sa));
      }
      if constexpr (n % 3 == 1 && n / 3 < 4) wst(ic<2>{}, ic<(n / 3) & 3>{});
      if constexpr (n == 3) {
        asm volatile("" : "+v"(accC[0]), "+v"(accC[1]));
        dsw128<0>(pa, __builtin_bit_cast(u32x4, accC[0] * KG));
        dsw128<1024>(pa, __builtin_bit_cast(u32x4, accC[1] * KU));
      }
      if constexpr (k == 0 || k == 1) { dsr64<k * 256>(rs[2 * k], ra); dsr64<k * 256 + 128>(rs[2 * k + 1], ra); }
      if constexpr (k >= 2 && k < 6) { rdc(ic<(k - 2) / 2>{}, ic<(k & 1) * 2>{}); rdc(ic<(k - 2) / 2>{}, ic<(k & 1) * 2 + 1>{}); }
      if constexpr (k >= 6 && k < 10) wst(ic<3>{}, ic<(k - 6) & 3>{});
    });
    asm volatile("s_nop 7" ::: "memory");
    SCHED0();
  }
  const rsrc_t rsO = mk_rsrc((char*)act + (size_t)(bm * 4 + wm) * (F_DIM / 32) * 4096, F_DIM / 32 * 4096);
  asm volatile("s_waitcnt lgkmcnt(4)" : "+v"(rs[0]), "+v"(rs[1]), "+v"(rs[2]), "+v"(rs[3]), "+v"(cc[0][0]), "+v"(cc[0][1]), "+v"(cc[0][2]), "+v"(cc[0][3])::"memory");
  float rg[4], ru[4];
#pragma unroll
  for (int i = 0; i < 4; ++i) { rg[i] = __uint_as_float(rs[i][1]) * KG; ru[i] = __uint_as_float(rs[i][1]) * KU; }
  auto frag = [&](auto JJ) {
    constexpr int j = JJ.value;
    auto half = [](auto v, int n) { const f32x4 f = __builtin_bit_cast(f32x4, v); return (n & 1) ? hi2(f) : lo2(f); };
    f32x2 g[8], uu[8], e[8];
    u32x4(&c)[4] = cc[j & 1];
#define STAGE(X) do { SCHED0(); _Pragma("unroll") for (int n = 0; n < 8; ++n) { X; } } while (0)
    STAGE(g[n] = __uint_as_float(rs[n >> 1][0]) * half(c[0], n) + half(c[1], n));
    STAGE(g[n] = rg[n >> 1] * half(acc[n >> 1][j], n) + g[n]);
    STAGE(e[n] = (f32x2{__builtin_amdgcn_exp2f(g[n][0]), __builtin_amdgcn_exp2f(g[n][1])}));
    STAGE(uu[n] = __uint_as_float(rs[n >> 1][0]) * half(c[2], n) + half(c[3], n));
    SCHED0();
    if constexpr (j + 2 < FN) static_for<4>([&](auto E) { rdc(ic<j + 2>{}, E); });
    STAGE(e[n] = e[n] + 1.0f);
    STAGE(e[n] = (f32x2{__builtin_amdgcn_rcpf(e[n][0]), __builtin_amdgcn_rcpf(e[n][1])}));
    STAGE(uu[n] = ru[n >> 1] * half(acc[n >> 1][FN + j], n) + uu[n]);
    STAGE(g[n] = g[n] * uu[n]);
    STAGE(g[n] = g[n] * e[n]);
    unsigned pk[8];
    STAGE(pk[n] = pk_bf16(g[n][0], g[n][1]));
    SCHED0();
    const int J = (bn >> 4) * 112 + (FN == 4 ? 48 : 0) + (bn & 15) * FN + (j + rot) % FN;
    static_for<2>([&](auto I) {
      constexpr int i = I.value * 4;
      const auto s0 = __builtin_amdgcn_permlane16_swap(pk[i], pk[i + 2], false, false);
      const auto s1 = __builtin_amdgcn_permlane16_swap(pk[i + 1], pk[i + 3], false, false);
      bstore_sc1<I.value * 2048>(u32x4{s0[0], s1[0], s0[1], s1[1]}, sa, rsO, (unsigned)((J >> 1) * 4096 + (J & 1) * 512));
    });
    if constexpr (j + 1 < FN) {
      u32x4(&d)[4] = cc[(j + 1) & 1];
      asm volatile("s_waitcnt lgkmcnt(%4)" : "+v"(d[0]), "+v"(d[1]), "+v"(d[2]), "+v"(d[3]) : "n"(j + 2 < FN ? 4 : 0) : "memory");
    }
    SCHED0();
  };
  static_for<FN>(frag);
}

namespace g4 {
constexpr int NU = 14, SL = 4, AD = 3;
constexpr int CN = X_CN;
constexpr int GT = 131072 + 5120;
constexpr int na(int t) { return t + AD - 1 < NU ? 4 : 0; }
constexpr int CU = 1;
constexpr int nb(int mh, int t) { return t < 0 ? 0 : (t + SL < NU ? 4 + (t == (mh ? CU : 0)) : (t == NU - 3 && mh ? 4 : 0)); }
constexpr int behind(int mh, int t0, int u, int b) {
  int s = b ? na(u) : 0;
  for (int t = t0 + 1; t < u; ++t) s += na(t) + nb(mh, t);
  return s;
}
constexpr int vm(int mh, int u, int b) {
  const int wa = nb(mh, u - AD + 1) + behind(mh, u - AD + 1, u, b), wb = behind(mh, u == NU - 2 ? NU - 1 - SL : u + 1 - SL, u, b);
  return b == 0 ? wa + 2 : (u + 1 < NU && u + 1 >= SL && wb < wa ? wb : wa);
}
}

template <int MH, class WST, class PRE, class EXTRA>
DI void g4_loop(f32x4 (&acc)[2][8], unsigned smem_base, int lane, int wid, int kb0, rsrc_t rsA, unsigned* fb, WST ldb, PRE pre, EXTRA extra) {
  using namespace g4;
  const int fq = lane >> 4, k2 = wid & 1;
  unsigned voffA[2], bad[8];
#pragma unroll
  for (int i = 0; i < 2; ++i) voffA[i] = lane * 16 + ((wid & 3) ^ i) * 1024;
  {
    const int q = (lane >> 2) & 3, r = 8 * fq + q, sw = q | ((fq & 1) << 2);
#pragma unroll
    for (int f = 0; f < 8; ++f) bad[f] = smem_base + k2 * 65536 + r * 256 + ((f ^ sw) * 32) + 8 * (lane & 3);
  }
  const unsigned sA = (unsigned)(kb0 + 2 * k2) * 4096u;
  u32x4 af[AD][4];
  u32x2 bl[2][8], bh[2][8];
  auto lda = [&](auto V, auto J) {
    constexpr int v = V.value, j = J.value;
    asm volatile("" : "=v"(af[v % AD][j]));
    bload_sc1<0>(af[v % AD][j], voffA[j & 1], rsA, sA + (4 * v + (j >> 1)) * 4096u);
  };
  auto rdb = [&](auto V, auto B, auto E) {
    constexpr int o = (V.value & 3) * 16384 + B.value * 8192, f = E.value >> 1;
    if constexpr (E.value & 1) dsr_tr<o + 1024>(bh[B.value][f], bad[f]);
    else dsr_tr<o>(bl[B.value][f], bad[f]);
  };
  unsigned wp = smem_base + k2 * 65536 + 32768 + (wid >> 1) * 8192 + lane * 16;
#pragma unroll
  for (int f = 0; f < 8; ++f) asm volatile("" : "+v"(bad[f]));
  asm volatile("" : "+v"(voffA[0]), "+v"(voffA[1]), "+v"(wp) : "s"(rsA), "s"(sA));
  unsigned cv = 0;
  const poll_t pl = pre();
  if (MH == 1) __builtin_amdgcn_s_setprio(0);
  static_for<4 * (AD - 1)>([&](auto J) { lda(ic<J.value / 4>{}, ic<J.value % 4>{}); });
  static_for<16>([&](auto E) { rdb(ic<0>{}, ic<0>{}, E); });
  static_for<NU>([&](auto U) {
    constexpr int u = U.value;
    constexpr bool last = u == NU - 1;
    static_for<32>([&](auto N) {
      constexpr int n = N.value, b = n >> 4, m = n & 15;
      constexpr int f = (last && b) ? (m & 7) : (m >> 1), i = (last && b) ? 1 - (m >> 3) : (m & 1);
      if constexpr (m == 0) {
        asm volatile("s_waitcnt vmcnt(%2) lgkmcnt(0)" : "+v"(af[u % AD][2 * b]), "+v"(af[u % AD][2 * b + 1])
                     : "n"(vm(MH, u, b)) : "memory");
        if constexpr (u == 0 && b == 0 && MH == 1) poll_end(pl);
#pragma unroll
        for (int g = 0; g < 8; ++g) asm volatile("" : "+v"(bl[b][g]), "+v"(bh[b][g]));
        if constexpr (b == 1 && u <= NU - 2) __builtin_amdgcn_s_barrier();
        if constexpr (b == 1 && u == NU - 2 && MH == 0) __builtin_amdgcn_s_setprio(2);
      }
      const u32x4 bb = {bl[b][f][0], bl[b][f][1], bh[b][f][0], bh[b][f][1]};
      if constexpr (u == 0 && b == 0) asm volatile("v_mfma_f32_16x16x32_bf16 %0, %1, %2, 0" : "=&v"(acc[i][f]) : "v"(bb), "v"(af[u % AD][i]));
      else MFMA_AV(acc[i][f], bb, af[u % AD][2 * b + i]);
      if constexpr (m < 8 && !(last && b)) {
        rdb(ic<u + b>{}, ic<1 - b>{}, ic<2 * m>{});
        rdb(ic<u + b>{}, ic<1 - b>{}, ic<2 * m + 1>{});
      }
      if constexpr ((m & 3) == 2) {
        if constexpr (b == 0 && na(u)) lda(ic<u + AD - 1>{}, ic<(m >> 2)>{});
        if constexpr (b == 1 && u + SL < NU) ldb(ic<u + SL>{}, ic<(m >> 2)>{});
        if constexpr (b == 1 && u + 3 == NU) extra(ic<(m >> 2)>{});
      }
      if constexpr (MH == 0 && u == 0 && n == 0)
        asm volatile("ds_add_rtn_u32 %0, %1, %2" : "=v"(cv) : "v"(smem_base + CN + 256 + lane * 4), "v"(1u) : "memory");
      if constexpr (MH == 0 && u == 0 && n == 20) {
        asm volatile("" : "+v"(cv));
        signal(sig_t{mk_rsrc(fb, __builtin_amdgcn_readfirstlane(cv) == 3u ? 3 * 256 + 4 : 0), (unsigned)(lane * 256)});
      }
      if constexpr (MH == 1 && u == CU && n == 31)
        asm volatile("buffer_atomic_add %0, %1, %2, 0 offen sc0 sc1" : "+v"(cv) : "v"(lane * 128), "s"(mk_rsrc(fb, wid == 4 ? 256 : 0)) : "memory");
      if constexpr (MH == 1 && u == 6 - AD && n == 8) {
        asm volatile("s_waitcnt vmcnt(%1)" : "+v"(cv) : "n"(behind(1, CU, u, 0) + 2) : "memory");
        if (__builtin_expect(((unsigned)__builtin_amdgcn_ballot_w64(cv < 8u) & (wid == 4 ? 3u : 0u)) != 0u, 0)) {
          const poll_t p = poll(0u, fb, lane, 2, 8u);
          wait_vm<0>();
          poll_end(p);
        }
      }
      if constexpr (last && n >= 24) dsw128<(m & 7) * 1024>(wp, __builtin_bit_cast(u32x4, acc[1][m & 7]));
    });
  });
  dsw32<0>(smem_base + CN + wid * 4, 1u);
  const unsigned rp = wp + 65536 - k2 * 131072;
  u32x4 pq[8];
  for (;;) {
    unsigned fl;
    asm volatile("ds_read_b32 %0, %1" : "=v"(fl) : "v"(smem_base + CN + (wid ^ 1) * 4) : "memory");
    static_for<8>([&](auto F) { dsr128<F.value * 1024>(pq[F.value], rp); });
    asm volatile("s_waitcnt lgkmcnt(0)" : "+v"(fl), "+v"(pq[0]), "+v"(pq[1]), "+v"(pq[2]), "+v"(pq[3]), "+v"(pq[4]), "+v"(pq[5]), "+v"(pq[6]), "+v"(pq[7])::"memory");
    if (__builtin_amdgcn_readfirstlane(fl) != 0u) break;
  }
#pragma unroll
  for (int f = 0; f < 8; ++f) {
    asm volatile("" : "+v"(acc[0][f]));
    acc[0][f] += __builtin_bit_cast(f32x4, pq[f]);
  }
}

template <int MH, class WST>
DI void g4_phase(unsigned smem_base, int wid, const bf16* At, WST wst, const bf16* Yt,
                 bf16* __restrict__ C, float* xbuf, unsigned* flags) {
  using namespace g4;
  constexpr int KB = F_DIM / 32;
  const int lane = fresh_lane();
  constexpr int mh = MH;
  const int kq = wid & 3;
  const int xcd = blockIdx.x & 7, idx = blockIdx.x >> 3;
  const int kh = xcd & 1, bm = ((xcd >> 1) & 1) * 8 + (idx & 7), bn = (xcd >> 2) * 4 + (idx >> 3);
  const unsigned partner = blockIdx.x ^ 1;
  const int n0 = bn * 128, bm64 = bm * 2 + kh;
  const int fr = lane & 15, fq = lane >> 4;
  const unsigned voffL = lane * 16;
  const rsrc_t rsA = mk_rsrc((const char*)At + (size_t)(bm64 ^ mh ^ 1) * KB * 4096, KB * 4096);
  const rsrc_t rsEx = mk_rsrc(xbuf, 256 * 32768);
  const int kb0 = kh * (KB / 2);
  f32x4 acc[2][8];
  const int fo = ((idx & 7) * 2 + (xcd >> 2)) * 8 + (xcd & 1), fi = ((bm >> 1) * 2 + kh) * 8 + (idx >> 3) * 2;
  if constexpr (mh == 0) {
    g4_loop<0>(acc, smem_base, lane, wid, kb0, rsA, flags + (FL_B + fo) * 32, wst,
               [&] { wait_vm<8>(); __builtin_amdgcn_s_barrier(); return poll_t{0u, 0u}; }, [](auto) {});
    static_for<4>([&](auto F) {
      constexpr int f = F.value;
      bstore_sc1<f * 1024>(pack8(acc[0][2 * f], acc[0][2 * f + 1]), voffL, rsEx, blockIdx.x * 32768u + kq * 8192u);
    });
    signal(drain(flags + (FL_P + blockIdx.x) * 32 + kq, lane, 1, 4));
  } else {
  u32x4 yv[4];
  const rsrc_t rsX = mk_rsrc(Yt, S_LEN * D_DIM * 2);
  unsigned vx = (unsigned)(((bm64 >> 2) * 32 + bn * 4) * 16384 + ((bm64 & 3) * 4 + kq) * 1024 + ((((fq & 1) << 1) | (fq >> 1)) * 16 + fr) * 16);
  const unsigned oa = arrive<4, 4, 256>(smem_base + CN + 512, lane, flags + (FL_A + fo) * 32);
  asm volatile("" : "+v"(vx));
  g4_loop<1>(acc, smem_base, lane, wid, kb0, rsA, flags + (FL_B + fi) * 32, wst, [&] { return await(oa, lane, flags + (FL_A + fi) * 32, 2, 8u); },
             [&](auto J) { asm volatile("" : "=v"(yv[J.value])); bload_sc1<0>(yv[J.value], vx, rsX, J.value * 16384); });
  u32x4 pv[4], gt[8], ct[8];
  static_for<8>([&](auto F) { dsr128<F.value * 64>(gt[F.value], smem_base + GT + fq * 16); dsr128<512 + F.value * 64>(ct[F.value], smem_base + GT + fq * 16); });
  asm volatile("s_waitcnt vmcnt(0) lgkmcnt(0)" : "+v"(yv[0]), "+v"(yv[1]), "+v"(yv[2]), "+v"(yv[3])::"memory");
#pragma unroll
  for (int c = 0; c < 4; ++c) {
    const auto x0 = __builtin_amdgcn_permlane16_swap(yv[c][0], yv[c][2], false, false);
    const auto x1 = __builtin_amdgcn_permlane16_swap(yv[c][1], yv[c][3], false, false);
#pragma unroll
    for (int h = 0; h < 2; ++h) {
      const int f = 2 * c + h;
      asm volatile("" : "+v"(gt[f]), "+v"(ct[f]));
      acc[0][f] = f32x4{bf_lo(x0[h]), bf_hi(x0[h]), bf_lo(x1[h]), bf_hi(x1[h])} * __builtin_bit_cast(f32x4, ct[f]) + __builtin_bit_cast(f32x4, gt[f]) * acc[0][f];
      asm volatile("" : "+v"(acc[0][f]));
    }
  }
  char* cp = (char*)C + (size_t)(bm64 * 64 + kq * 16 + (fr & 7)) * (D_DIM * 2) + n0 * 2 + (((fq & 1) << 1) | (fq >> 1)) * 16 + (fr >> 3) * 64;
  asm volatile("" : "+v"(cp));
  const poll_t pl = poll(0u, flags + (FL_P + partner) * 32 + kq, lane, 1, 1u);
  static_for<4>([&](auto F) {
    asm volatile("" : "=v"(pv[F.value]));
    bload_sc1<F.value * 1024>(pv[F.value], voffL, rsEx, partner * 32768u + kq * 8192u);
  });
  asm volatile("s_waitcnt vmcnt(0)" : "+v"(pv[0]), "+v"(pv[1]), "+v"(pv[2]), "+v"(pv[3])::"memory");
  poll_end(pl);
  static_for<2>([&](auto L) {
    constexpr int l = L.value;
    u32x4 ov[2];
#pragma unroll
    for (int c = 0; c < 2; ++c) {
      const int f = 4 * l + 2 * c;
      const u32x4 pb = pv[f >> 1];
      const f32x4 p0 = {bf_lo(pb[0]), bf_hi(pb[0]), bf_lo(pb[1]), bf_hi(pb[1])};
      const f32x4 p1 = {bf_lo(pb[2]), bf_hi(pb[2]), bf_lo(pb[3]), bf_hi(pb[3])};
      const f32x4 o0 = acc[0][f] + __builtin_bit_cast(f32x4, gt[f]) * p0;
      const f32x4 o1 = acc[0][f + 1] + __builtin_bit_cast(f32x4, gt[f + 1]) * p1;
      const auto s0 = __builtin_amdgcn_permlane16_swap(pk_bf16(o0[0], o0[1]), pk_bf16(o1[0], o1[1]), false, false);
      const auto s1 = __builtin_amdgcn_permlane16_swap(pk_bf16(o0[2], o0[3]), pk_bf16(o1[2], o1[3]), false, false);
      ov[c] = u32x4{s0[0], s1[0], s0[1], s1[1]};
    }
    u32x4 lo, hi;
#pragma unroll
    for (int e = 0; e < 4; ++e) {
      lo[e] = __builtin_amdgcn_update_dpp(ov[0][e], ov[1][e], 0x128, 0xf, 0xc, false);
      hi[e] = __builtin_amdgcn_update_dpp(ov[1][e], ov[0][e], 0x128, 0xf, 0x3, false);
    }
    gst128(cp + l * 128, lo);
    gst128(cp + 8 * (D_DIM * 2) + l * 128, hi);
  });
}
}

__global__ __launch_bounds__(512, 2) void block_kernel(char* ws, const bf16* X, const bf16* cnd, const bf16* wmod, const bf16* Wq,
                                                       const bf16* Wo, const bf16* Wg, const bf16* Wu, const bf16* Wd, bf16* C) {
  __shared__ __attribute__((aligned(1024))) char smem[Q_LDS];
  unsigned long long pc;
  asm volatile("s_getpc_b64 %0" : "=s"(pc));
  asm volatile("s_cmp_ge_u32 %0, 9\n\ts_cbranch_scc1 1f\n\ts_mov_b32 m0, %1\n\ts_nop 0\n\tbuffer_load_dword %2, %3, 0 offen lds\n1:"
               ::"s"((blockIdx.x >> 3) + __builtin_amdgcn_readfirstlane(threadIdx.x ^ 64)), "s"((unsigned)(uintptr_t)smem + QX_SINK), "v"((threadIdx.x & 63) * 128 + (blockIdx.x >> 3) * 8192),
                 "s"(mk_rsrc((const void*)(uintptr_t)(pc & ~255ull), CODEPF * 1024)) : "memory", "scc");
  asm volatile("" : "+s"(ws) : "s"(X), "s"(cnd), "s"(wmod), "s"(Wq), "s"(Wo), "s"(Wg), "s"(Wu), "s"(Wd), "s"(C));
  float* modp = (float*)ws;
  unsigned* ctab = (unsigned*)(ws + 212992);
  unsigned* flags = (unsigned*)(ws + 262144);
  float* stat = (float*)(ws + 524288);
  bf16* O = (bf16*)(ws + (1 << 20));
  bf16* Y = (bf16*)(ws + (9 << 20));
  bf16* H = (bf16*)(ws + (13 << 20));
  bf16* act = (bf16*)(ws + (17 << 20));
  float* xbuf = (float*)(ws + (32 << 20));
  const int wid = __builtin_amdgcn_readfirstlane(threadIdx.x >> 6);
  const int wm = wid & 3, wn = wid >> 2;
  const int xcd = blockIdx.x & 7, idx = blockIdx.x >> 3;
  const int bm = idx & 7, bn = xcd * 4 + (idx >> 3);
  const unsigned smem_base = (unsigned)(uintptr_t)smem;
  const rsrc_t rsA = mk_rsrc((const char*)Y + (size_t)bm * 32 * 16384, 32 * 16384);
  const rsrc_t rsB = mk_rsrc(wn ? Wu : Wg, D_DIM * F_DIM * 2);
  const rsrc_t rsE = wid == 7 ? mk_rsrc((const void*)(uintptr_t)(pc & ~255ull), CODEPF * 1024) : mk_rsrc(stat, S_LEN * 128);
  const unsigned m0w = smem_base + wid * 2048;
  unsigned voffB0 = 0, voffB1 = 0;
  attn_phase(smem_base, wid, H, Wq, O, Wo, X, modp, Y, stat, flags, Wg, Wu, ctab, cnd, wmod, [&](auto S, auto I) {
    if constexpr (S.value == 0) {
      const int l2 = fresh_lane();
      const int r = (wm * 2 + I.value) * 4 + (l2 >> 4);
      const int sw = (r & 3) | (((r >> 3) & 1) << 2);
      const int c16 = l2 & 15;
      const int vb = (((bn & 3) * 224) & ~63) + ((c16 >> 1) ^ sw) * 32 + (c16 & 1) * 16;
      (I.value ? voffB1 : voffB0) = (unsigned)(r * (F_DIM * 2) + g3_line(bn >> 2, vb >> 7) * 128 + (vb & 127));
    }
    bload_lds<0>(m0w + S.value * 16384 + I.value * 1024, I.value ? voffB1 : voffB0, rsB, S.value * 229376u);
  });
  __builtin_amdgcn_s_setprio(0);
  const int lane = fresh_lane();
  const unsigned voffA = wid * 2048 + lane * 16;
  const unsigned oy = arrive<8, 8, 512>(smem_base + X_CN + 768, lane, flags + (FL_Y + xcd * 32 + (idx & 3)) * 32);
  auto pl = [&] { return await(oy, lane, flags + (FL_Y + bm * 32 + xcd * 4) * 32, 4, 8u); };
  const int n0 = ((xcd >> 2) * 4 + (idx >> 3)) * 128;
  const char* ta = lane >= 14 ? (const char*)Wd + blockIdx.x * 28672 + wid * 1024 + ((lane - 14) & 7) * 128
                              : (const char*)(lane < 7 ? Wg : Wu) + ((wid + WTD) * 32 + idx) * (F_DIM * 2) + xcd * 896 + (lane < 7 ? lane : lane - 7) * 128;
  unsigned vo = 0;
  const unsigned fsel = 0x80000u | (((xcd & 1) * 2 + (wm >> 1)) * 8);
  auto wsn = [&](auto WN, auto V, auto J) {
    constexpr int v = V.value, j = J.value, p = (4 * v + 2 * (j >> 1) + WN.value) * 2;
    if constexpr (v == 0 && j == 0) {
      const int l2 = fresh_lane();
      vo = (wid & 1) * 16384 + (l2 >> 4) * 2048 + ((((l2 & 15) >> 1) ^ ((l2 >> 4) | ((wid & 1) << 2))) * 32) + (l2 & 1) * 16;
    }
    unsigned so;
    asm volatile("s_bfe_u32 %0, %1, %2\n\ts_lshl_b32 %0, %0, 15\n\ts_or_b32 %0, %0, %3" : "=s"(so)
                 : "n"(act_frag(0, p) | act_frag(0, p + 1) << 8 | act_frag(1, p) << 16 | act_frag(1, p + 1) << 24), "s"(fsel), "n"((j & 1) * 8192) : "scc");
    bload_lds<0>(m0w + (j >> 1) * 65536 + (v & 3) * 16384 + (j & 1) * 1024, vo, mk_rsrc((const char*)Wd + n0 * 2, F_DIM * D_DIM * 2 - n0 * 2), so);
  };
  if (wn == 0) {
    auto wst = [&](auto V, auto J) { wsn(ic<0>{}, V, J); };
    g3_body<4>(smem_base, wm, wn, wid, lane, bm, bn, rsA, rsB, rsE, ta, voffA, voffB0, voffB1, act, wst, [](auto) {}, pl);
    g4_phase<0>(smem_base, wid, act, wst, Y, C, xbuf, flags);
  } else {
    auto wst = [&](auto V, auto J) { wsn(ic<1>{}, V, J); };
    u32x4 tv[8];
    f32x4 ps;
    float q0 = 0, q1 = 0;
    unsigned vs, gv;
    g3_body<3>(smem_base, wm, wn, wid, lane, bm, bn, rsA, rsB, rsE, ta, voffA, voffB0, voffB1, act, wst, [&](auto K) {
      constexpr int k = K.value;
      if constexpr (k <= 0) {
        if constexpr (k == 0) {
          const int l2 = fresh_lane();
          asm volatile("" : "=v"(gv));
          bload1_sc1(gv, (unsigned)((n0 + (wm & 1) * 64 + l2) * 4), mk_rsrc(modp + 49152 + (wm >= 2 ? 2048 : 3072), 4096));
          vs = (l2 >> 5) * 131072 + (bm * 256 + wm * 64) * 8 + (l2 & 31) * 16;
        }
        asm volatile("" : "=v"(tv[-k]));
        bload_sc1<0>(tv[-k], vs, mk_rsrc(stat, S_LEN * 128), -k * 16384);
      } else if constexpr (k <= 4) {
        if constexpr (k == 1) wait_vm<3>();
        asm volatile("" : "+v"(tv[2 * k - 2]), "+v"(tv[2 * k - 1]));
        const f32x4 t = __builtin_bit_cast(f32x4, tv[2 * k - 2]) + __builtin_bit_cast(f32x4, tv[2 * k - 1]);
        ps = k == 1 ? t : ps + t;
        asm volatile("" : "+v"(ps));
      } else if constexpr (k == 5) {
        const auto s0 = __builtin_amdgcn_permlane32_swap(__float_as_uint(ps[0]), __float_as_uint(ps[2]), false, false);
        const auto s1 = __builtin_amdgcn_permlane32_swap(__float_as_uint(ps[1]), __float_as_uint(ps[3]), false, false);
        ps[0] = (__uint_as_float(s0[0]) + __uint_as_float(s0[1])) * (1.0f / 16.0f);
        ps[1] = __uint_as_float(s1[0]) + __uint_as_float(s1[1]);
        const auto m = __builtin_amdgcn_permlane32_swap(__float_as_uint(ps[0]), __float_as_uint(ps[0]), false, false);
        ps[2] = __uint_as_float(m[0]); ps[3] = __uint_as_float(m[1]);
        asm volatile("" : "+v"(ps), "+v"(q0), "+v"(q1));
      } else if constexpr (k <= 13) {
        float d0, d1;
        asm volatile("v_sub_f32 %0, %4, %6\n\tv_sub_f32 %1, %5, %7\n\tv_fmac_f32 %2, %0, %0\n\tv_fmac_f32 %3, %1, %1"
                     : "=&v"(d0), "=&v"(d1), "+v"(q0), "+v"(q1) : "v"(tv[k - 6][0]), "v"(tv[k - 6][2]), "v"(ps[2]), "v"(ps[3]));
        if constexpr (k == 13) asm volatile("s_nop 1" : "+v"(q0), "+v"(q1));
      } else if constexpr (k == 14) {
        const auto q = __builtin_amdgcn_permlane32_swap(__float_as_uint(q0), __float_as_uint(q1), false, false);
        const float rstd = __builtin_amdgcn_rsqf((ps[1] + (__uint_as_float(q[0]) + __uint_as_float(q[1])) * (1.0f / 64.0f)) * (1.0f / D_DIM) + 1e-5f);
        const int l2 = fresh_lane();
        asm volatile("ds_write_b64 %0, %1" ::"v"(smem_base + 131072 + wm * 512 + (l2 & 31) * 16 + (l2 >> 5) * 8), "v"(f32x2{ps[0] * (-1.0f / 64.0f) * rstd, rstd}) : "memory");
      } else {
        asm volatile("" : "+v"(gv));
        dsw32<0>(smem_base + g4::GT + (wm * 64 + fresh_lane()) * 4, wm >= 2 ? __float_as_uint(__builtin_amdgcn_rcpf(__uint_as_float(gv))) : gv);
      }
    }, pl);
    g4_phase<1>(smem_base, wid, act, wst, Y, C, xbuf, flags);
  }
}

extern "C" void kernel_launch(void* const* d_in, const int* in_sizes, int n_in, void* d_out, int out_size,
                              void* d_ws, size_t ws_size, hipStream_t stream) {
  const bf16* x = (const bf16*)d_in[0];
  const bf16* wqkv = (const bf16*)d_in[1];
  const bf16* wo = (const bf16*)d_in[2];
  const bf16* wg = (const bf16*)d_in[4];
  const bf16* wu = (const bf16*)d_in[5];
  const bf16* wd = (const bf16*)d_in[6];
  const bf16* c = (const bf16*)d_in[7];
  const bf16* wmod = (const bf16*)d_in[8];
  bf16* out = (bf16*)d_out;

  block_kernel<<<256, 512, 0, stream>>>((char*)d_ws, x, c, wmod, wqkv, wo, wg, wu, wd, out);
}
```

```cpp
#include <hip/hip_runtime.h>
#include <hip/hip_bf16.h>
#include <stdint.h>

typedef __hip_bfloat16 bf16;
typedef __attribute__((ext_vector_type(8))) short bf16x8;
typedef __attribute__((ext_vector_type(4))) float f32x4;
typedef __attribute__((ext_vector_type(2))) float f32x2;
typedef __attribute__((ext_vector_type(4))) unsigned u32x4;
typedef __attribute__((ext_vector_type(2))) unsigned u32x2;
typedef __attribute__((ext_vector_type(2))) __bf16 bf16x2_t;
typedef __amdgpu_buffer_rsrc_t rsrc_t;

#define DI __device__ __forceinline__
constexpr int CODEPF = 68;

constexpr int S_LEN = 2048;
constexpr int D_DIM = 1024;
constexpr int F_DIM = 3584;
constexpr int QBAR = 2;
constexpr int NP1 = 5;
constexpr int PSLP = 4;
constexpr int NDLY = 20, TDLY = 15;
constexpr int TS = 12, TP = 6;
constexpr int WOS = 6;
constexpr int GDEP = 12, GS0 = 28;
constexpr int TPOS = 6, WTD = 6, CT3 = 24, ST3 = 16;

template <int V> struct ic { static constexpr int value = V; };
template <int N, int I = 0, class F>
DI void static_for(F&& f) {
  if constexpr (I < N) { f(ic<I>{}); static_for<N, I + 1>(f); }
}

DI float bf_lo(unsigned u) { return __uint_as_float(u << 16); }
DI float bf_hi(unsigned u) { return __uint_as_float(u & 0xffff0000u); }
DI unsigned pk_bf16(float lo, float hi) {
  const f32x2 v = {lo, hi};
  return __builtin_bit_cast(unsigned, __builtin_convertvector(v, bf16x2_t));
}
DI rsrc_t mk_rsrc(const void* p, unsigned bytes) { return __builtin_amdgcn_make_buffer_rsrc((void*)p, (short)0, (int)bytes, 0x00020000); }

template <int N>
DI void wait_vm() { asm volatile("s_waitcnt vmcnt(%0)" ::"n"(N > 63 ? 63 : N) : "memory"); }
#define WAIT_LGKM0() do { asm volatile("s_waitcnt lgkmcnt(0)" ::: "memory"); __builtin_amdgcn_sched_barrier(0); } while (0)
#define SCHED0() __builtin_amdgcn_sched_barrier(0)

#define MFMA_AV(C, B, A) asm volatile("v_mfma_f32_16x16x32_bf16 %0, %1, %2, %0" : "+v"(C) : "v"(B), "v"(A))
template <int OFF>
DI void bload_v(u32x4& d, unsigned voff, rsrc_t rs, unsigned soff) {
  asm volatile("buffer_load_dwordx4 %0, %1, %2, %3 offen offset:%4" : "+v"(d) : "v"(voff), "s"(rs), "s"(soff), "i"(OFF) : "memory");
}
template <int OFF>
DI void bload_lds(unsigned m0v, unsigned voff, rsrc_t rs, unsigned soff) {
  asm volatile("s_mov_b32 m0, %0\n\ts_nop 0\n\tbuffer_load_dwordx4 %1, %2, %3 offen offset:%4 lds" ::"s"(m0v), "v"(voff), "s"(rs), "s"(soff), "i"(OFF) : "memory");
}
DI void touch_lds(unsigned m0v, unsigned voff, rsrc_t rs, unsigned soff) {
  asm volatile("s_mov_b32 m0, %0\n\ts_nop 0\n\tbuffer_load_dword %1, %2, %3 offen lds" ::"s"(m0v), "v"(voff), "s"(rs), "s"(soff) : "memory");
}
template <int OFF>
DI void bload_lds_sc1(unsigned m0v, unsigned voff, rsrc_t rs, unsigned soff) {
  asm volatile("s_mov_b32 m0, %0\n\ts_nop 0\n\tbuffer_load_dwordx4 %1, %2, %3 offen offset:%4 sc1 lds" ::"s"(m0v), "v"(voff), "s"(rs), "s"(soff), "i"(OFF) : "memory");
}
template <int OFF>
DI void dsr128(u32x4& d, unsigned addr) { asm volatile("ds_read_b128 %0, %1 offset:%2" : "=v"(d) : "v"(addr), "i"(OFF) : "memory"); }
template <int OFF>
DI void dsr32(unsigned& d, unsigned addr) { asm volatile("ds_read_b32 %0, %1 offset:%2" : "=v"(d) : "v"(addr), "i"(OFF) : "memory"); }
DI void bload_nt(u32x4& d, unsigned voff, rsrc_t rs, unsigned soff) {
  asm volatile("buffer_load_dwordx4 %0, %1, %2, %3 offen nt" : "=v"(d) : "v"(voff), "s"(rs), "s"(soff) : "memory");
}
template <int OFF>
DI void dsr64(u32x2& d, unsigned addr) { asm volatile("ds_read_b64 %0, %1 offset:%2" : "=v"(d) : "v"(addr), "i"(OFF) : "memory"); }
template <int OFF>
DI void dsr_tr(u32x2& d, unsigned addr) { asm volatile("ds_read_b64_tr_b16 %0, %1 offset:%2" : "=v"(d) : "v"(addr), "i"(OFF) : "memory"); }
template <int OFF>
DI void dsw128(unsigned addr, u32x4 d) { asm volatile("ds_write_b128 %0, %1 offset:%2" ::"v"(addr), "v"(d), "i"(OFF) : "memory"); }
template <int OFF>
DI void dsw32(unsigned addr, unsigned d) { asm volatile("ds_write_b32 %0, %1 offset:%2" ::"v"(addr), "v"(d), "i"(OFF) : "memory"); }

DI void gst128(void* p, u32x4 v) {
  asm volatile("global_store_dwordx4 %0, %1, off sc1\n\ts_nop 1" ::"v"(p), "v"(v) : "memory");
}
DI int fresh_lane() {
  int l;
  asm volatile("v_mbcnt_lo_u32_b32 %0, -1, 0\n\tv_mbcnt_hi_u32_b32 %0, -1, %0" : "=v"(l));
  return l;
}
DI float hadd(f32x2 v) {
  float r;
  asm("v_add_f32 %0, %1, %2" : "=v"(r) : "v"(v[0]), "v"(v[1]));
  return r;
}
DI f32x2 lo2(f32x4 v) { return __builtin_shufflevector(v, v, 0, 1); }
DI f32x2 hi2(f32x4 v) { return __builtin_shufflevector(v, v, 2, 3); }
DI float rowsum4(float v) {
  const unsigned u = __float_as_uint(v);
  const auto a = __builtin_amdgcn_permlane16_swap(u, u, false, false);
  const unsigned t = __float_as_uint(__uint_as_float(a[0]) + __uint_as_float(a[1]));
  const auto b = __builtin_amdgcn_permlane32_swap(t, t, false, false);
  return __uint_as_float(b[0]) + __uint_as_float(b[1]);
}
DI float silu_f(float v) { return v * __builtin_amdgcn_rcpf(1.0f + __builtin_amdgcn_exp2f(v * -1.4426950408889634f)); }
DI float rbf(float v) { return bf_lo(pk_bf16(v, v)); }
DI float gain_nz(float m) {
  const float g = rbf(1.0f + rbf(m));
  return __builtin_fabsf(g) < 8.67361738e-19f ? 8.67361738e-19f : g;
}
template <int OFF>
DI void bload_sc1(u32x4& d, unsigned voff, rsrc_t rs, unsigned soff) {
  asm volatile("buffer_load_dwordx4 %0, %1, %2, %3 offen offset:%4 sc1" : "+v"(d) : "v"(voff), "s"(rs), "s"(soff), "i"(OFF) : "memory");
}
template <int OFF>
DI void bstore_sc1(u32x4 d, unsigned voff, rsrc_t rs, unsigned soff) {
  asm volatile("buffer_store_dwordx4 %0, %1, %2, %3 offen offset:%4 sc1\n\ts_nop 1" ::"v"(d), "v"(voff), "s"(rs), "s"(soff), "i"(OFF) : "memory");
}
DI void bload1_sc1(unsigned& d, unsigned voff, rsrc_t rs) {
  asm volatile("buffer_load_dword %0, %1, %2, 0 offen sc1" : "+v"(d) : "v"(voff), "s"(rs) : "memory");
}

constexpr int Q_SLOT = 21504, Q_NSL = 7;
constexpr int QX_WH = 4 * Q_SLOT;
constexpr int QX_HX = 65536;
constexpr int QX_GT = QX_WH + 65536;
constexpr int QX_CNT = QX_GT + 512;
constexpr int QX_SINK = QX_CNT + 512;
constexpr int G3_CT = QX_SINK + 1024;
constexpr int X_CN = G3_CT + 4096;
constexpr int Q_LDS = X_CN + 2048;
constexpr int P_TAB = 4 * Q_SLOT, P_RED = 5 * Q_SLOT, P_ST = P_RED + 4096;

DI unsigned lds_add(unsigned addr, unsigned v0) {
  unsigned o;
  asm volatile("ds_add_rtn_u32 %0, %1, %2\n\ts_waitcnt lgkmcnt(0)" : "=v"(o) : "v"(addr), "v"(v0) : "memory");
  return __builtin_amdgcn_readfirstlane(o);
}
template <int SLP>
DI void lds_wait(unsigned addr, unsigned n) {
  for (;;) {
    unsigned v;
    asm volatile("ds_read_b32 %0, %1\n\ts_waitcnt lgkmcnt(0)" : "=v"(v) : "v"(addr) : "memory");
    if (__builtin_amdgcn_readfirstlane(v) >= n) break;
    __builtin_amdgcn_s_sleep(SLP);
  }
}
struct poll_t { unsigned a, b; };
struct sig_t { rsrc_t rs; unsigned vo; };
DI sig_t drain(unsigned* out, int lane, int no, int so) {
  sig_t g = {mk_rsrc(out, (no - 1) * so + 4), (unsigned)(lane * so)};
  asm volatile("s_waitcnt vmcnt(0)" : "+v"(g.vo) : "s"(g.rs) : "memory");
  return g;
}
DI void signal(const sig_t& g) { asm volatile("buffer_atomic_add %0, %1, %2, 0 offen sc1" ::"v"(1u), "v"(g.vo), "s"(g.rs) : "memory"); }
#define POLL_RD(r) "v_mov_b32 " r ", 0\n\tbuffer_atomic_add " r ", %3, %4, 0 offen sc0 sc1\n\t"
#define POLL_CK(r) "s_waitcnt vmcnt(1)\n\tv_cmp_lt_u32 vcc, " r ", %5\n\ts_cbranch_vccz 2f\n\t"
DI poll_t poll(unsigned skip, unsigned* in, int lane, int nl, unsigned n) {
  poll_t r;
  asm volatile("s_cmp_lg_u32 %2, 0\n\ts_cbranch_scc1 2f\n\t" POLL_RD("%0") "s_sleep %6\n1:\n\t" POLL_RD("%1") POLL_CK("%0") POLL_RD("%0") POLL_CK("%1")
               "s_add_u32 %2, %2, 1\n\ts_cmp_lt_u32 %2, 0x80000\n\ts_cbranch_scc1 1b\n2:"
               : "=&v"(r.a), "=&v"(r.b), "+s"(skip) : "v"(lane * 128), "s"(mk_rsrc(in, nl * 128)), "v"(lane < nl ? n : 0u), "n"(PSLP) : "memory", "vcc", "scc");
  return r;
}
DI void poll_end(const poll_t& r) { asm volatile("" ::"v"(r.a), "v"(r.b)); }
template <int NW, int NO, int SO>
DI unsigned arrive(unsigned lcnt, int lane, unsigned* out) {
  asm volatile("" ::"v"(lcnt + lane * 4));
  const sig_t g = drain(out, lane, NO, SO);
  const unsigned o = lds_add(lcnt + lane * 4, 1u);
  if (o == NW - 1) {
    signal(g);
  }
  return o;
}
DI poll_t await(unsigned o, int lane, unsigned* in, int nl, unsigned n) {
  const poll_t r = poll(o, in, lane, nl, n);
  __builtin_amdgcn_s_barrier();
  return r;
}
constexpr int FL_M = 0, FL_H = 544, FL_O = 608, FL_P = 736, FL_Y = 992, FL_A = 1248, FL_B = 1376, FL_S = 1504;

DI f32x4 mfma0(u32x4 a, u32x4 b) {
  return __builtin_amdgcn_mfma_f32_16x16x32_bf16(__builtin_bit_cast(bf16x8, a), __builtin_bit_cast(bf16x8, b), f32x4{0, 0, 0, 0}, 0, 0, 0);
}
DI u32x4 pack8(const f32x4 a, const f32x4 b) { return u32x4{pk_bf16(a[0], a[1]), pk_bf16(a[2], a[3]), pk_bf16(b[0], b[1]), pk_bf16(b[2], b[3])}; }
template <class OP>
DI float rows4(const float (&v)[4], OP op) {
  const auto a = __builtin_amdgcn_permlane16_swap(__float_as_uint(v[0]), __float_as_uint(v[1]), false, false);
  const auto b = __builtin_amdgcn_permlane16_swap(__float_as_uint(v[2]), __float_as_uint(v[3]), false, false);
  const float t0 = op(__uint_as_float(a[0]), __uint_as_float(a[1])), t1 = op(__uint_as_float(b[0]), __uint_as_float(b[1]));
  const auto c = __builtin_amdgcn_permlane32_swap(__float_as_uint(t0), __float_as_uint(t1), false, false);
  return op(__uint_as_float(c[0]), __uint_as_float(c[1]));
}
DI void bcast4(float t, float (&v)[4]) {
  const auto c = __builtin_amdgcn_permlane32_swap(__float_as_uint(t), __float_as_uint(t), false, false);
  const auto a = __builtin_amdgcn_permlane16_swap(c[0], c[0], false, false);
  const auto b = __builtin_amdgcn_permlane16_swap(c[1], c[1], false, false);
  v[0] = __uint_as_float(a[0]); v[1] = __uint_as_float(a[1]); v[2] = __uint_as_float(b[0]); v[3] = __uint_as_float(b[1]);
}
DI float fmax2(float a, float b) { return __builtin_amdgcn_fmed3f(a, b, __builtin_inff()); }

DI float sum16(float v) {
  v += __builtin_bit_cast(float, __builtin_amdgcn_update_dpp(0, __builtin_bit_cast(int, v), 0x128, 0xf, 0xf, false));
  v += __builtin_bit_cast(float, __builtin_amdgcn_update_dpp(0, __builtin_bit_cast(int, v), 0x124, 0xf, 0xf, false));
  v += __builtin_bit_cast(float, __builtin_amdgcn_update_dpp(0, __builtin_bit_cast(int, v), 0x122, 0xf, 0xf, false));
  v += __builtin_bit_cast(float, __builtin_amdgcn_update_dpp(0, __builtin_bit_cast(int, v), 0x121, 0xf, 0xf, false));
  return v;
}
DI f32x4 sum4(const u32x4* p) {
  return (__builtin_bit_cast(f32x4, p[0]) + __builtin_bit_cast(f32x4, p[1])) + (__builtin_bit_cast(f32x4, p[2]) + __builtin_bit_cast(f32x4, p[3]));
}
template <int NB, int NMID, class MID>
DI void mod_gemv(unsigned red, int lane, int col0, int k0, const bf16* __restrict__ c, const bf16* __restrict__ wmod, MID mid) {
  const int fr = lane & 15, fq = lane >> 4;
  const rsrc_t rsW = mk_rsrc(wmod, 1024 * 6144 * 2);
  const unsigned vw = (unsigned)(((k0 + (fr >> 3) * 32 + fq * 8) * 6144 + col0 + (fr & 7) * 8) * 2);
  u32x4 raw[NB][8], cv[NB];
  static_for<NB>([&](auto B) {
    asm volatile("" : "=v"(cv[B.value]));
    bload_v<B.value * 128>(cv[B.value], (unsigned)((k0 + (fr & 1) * 32 + fq * 8) * 2), mk_rsrc(c, 2048), 0);
  });
  static_for<NB * 8>([&](auto N) {
    constexpr int b = N.value >> 3, j = N.value & 7;
    bload_nt(raw[b][j], vw, rsW, (b * 64 + j) * 12288);
  });
  __builtin_amdgcn_s_barrier();
  f32x4 acc[8];
#pragma unroll
  for (int n = 0; n < 8; ++n) acc[n] = f32x4{0, 0, 0, 0};
  u32x4 cf[NB];
  static_for<NB>([&](auto B) {
    constexpr int b = B.value;
    asm volatile("s_waitcnt vmcnt(%1)" : "+v"(cv[b]) : "n"(NB * 8) : "memory");
#pragma unroll
    for (int e = 0; e < 4; ++e) cf[b][e] = pk_bf16(silu_f(bf_lo(cv[b][e])), silu_f(bf_hi(cv[b][e])));
    asm volatile("" : "+v"(cf[b]));
  });
  static_for<NB>([&](auto B) {
    constexpr int b = B.value;
    if constexpr (b == 1) {
#pragma unroll
      for (int n = 0; n < 8; ++n) asm volatile("" : "+v"(acc[n]));
      mid();
    }
    asm volatile("s_waitcnt vmcnt(%8)" : "+v"(raw[b][0]), "+v"(raw[b][1]), "+v"(raw[b][2]), "+v"(raw[b][3]), "+v"(raw[b][4]), "+v"(raw[b][5]), "+v"(raw[b][6]), "+v"(raw[b][7])
                 : "n"(b ? NMID : (NB - 1) * 8) : "memory");
#pragma unroll
    for (int p = 0; p < 4; ++p) {
      u32x4 lo, hi;
#pragma unroll
      for (int t = 0; t < 4; ++t) {
        lo[t] = __builtin_amdgcn_perm(raw[b][2 * t + 1][p], raw[b][2 * t][p], 0x05040100u);
        hi[t] = __builtin_amdgcn_perm(raw[b][2 * t + 1][p], raw[b][2 * t][p], 0x07060302u);
      }
      acc[2 * p] = __builtin_amdgcn_mfma_f32_16x16x32_bf16(__builtin_bit_cast(bf16x8, lo), __builtin_bit_cast(bf16x8, cf[b]), acc[2 * p], 0, 0, 0);
      acc[2 * p + 1] = __builtin_amdgcn_mfma_f32_16x16x32_bf16(__builtin_bit_cast(bf16x8, hi), __builtin_bit_cast(bf16x8, cf[b]), acc[2 * p + 1], 0, 0, 0);
    }
  });
  if (fr == (fq >> 1)) {
    static_for<8>([&](auto R) {
      constexpr int r = R.value >> 1, h = (R.value & 1) * 4;
      dsw128<R.value * 16>(red + fq * 128, __builtin_bit_cast(u32x4, f32x4{acc[h][r], acc[h + 1][r], acc[h + 2][r], acc[h + 3][r]}));
    });
  }
}
template <int NR>
DI float red_sum(unsigned red, int lane) {
  unsigned v[NR];
  static_for<NR>([&](auto R) { dsr32<R.value * 256>(v[R.value], red + lane * 4); });
  asm volatile("s_waitcnt lgkmcnt(0)" ::: "memory");
  float s = 0;
#pragma unroll
  for (int r = 0; r < NR; r += 2) {
    asm volatile("" : "+v"(v[r]), "+v"(v[r + 1]));
    s += __uint_as_float(v[r]) + __uint_as_float(v[r + 1]);
  }
  return s;
}
DI unsigned cnt_read(unsigned* in, int lane, int nl) {
  unsigned t = 0;
  asm volatile("buffer_atomic_add %0, %1, %2, 0 offen sc0 sc1\n\ts_waitcnt vmcnt(0)" : "+v"(t) : "v"(lane * 128), "s"(mk_rsrc(in, nl * 128)) : "memory");
  return t;
}
DI bool words_set(unsigned* word, int lane) {
  unsigned t = 0;
  asm volatile("buffer_atomic_add %0, %1, %2, 0 offen sc0 sc1\n\ts_waitcnt vmcnt(0)" : "+v"(t) : "v"(lane * 8192), "s"(mk_rsrc(word, 7 * 8192 + 4)) : "memory");
  return (__builtin_amdgcn_ballot_w64(t == 0u) & 255u) == 0u;
}
DI void confirm(unsigned* word, int lane) {
#pragma unroll 1
  for (unsigned spins = 0; !words_set(word, lane) && ++spins < (1u << 20);) __builtin_amdgcn_s_sleep(2);
}
DI void set_words(unsigned* flags, int lane) {
  bstore_sc1<0>(u32x4{1u, 1u, 1u, 1u}, (unsigned)(lane * 128), mk_rsrc(flags + (FL_S + (blockIdx.x & 7) * 64) * 32, 64 * 128), 0);
}

template <class PRE>
DI void first_phase(unsigned smem_base, int wid, int lane, const bf16* __restrict__ x, const bf16* __restrict__ c, const bf16* __restrict__ wmod,
                    const bf16* __restrict__ W, float* modp, bf16* H, unsigned* flags, PRE prefill) {
  const int xcd = blockIdx.x & 7, idx = blockIdx.x >> 3;
  const int fr = lane & 15, fq = lane >> 4;
  const int u = xcd * 16 + (idx & 15), v = xcd * 8 + idx;
  unsigned* word = flags + __builtin_amdgcn_readfirstlane((FL_S + (blockIdx.x >> 2)) * 32 + (blockIdx.x & 3));
  const bool master = idx == 15;
  if (wid == (idx >= 16 ? 0 : 7)) {
    asm volatile("buffer_store_dword %0, %1, %2, 0 offen sc1" ::"v"(0u), "v"(lane * 8192), "s"(mk_rsrc(word, 7 * 8192 + 4)) : "memory");
  }
  if (master) {
    bstore_sc1<0>(u32x4{0u, 0u, 0u, 0u}, (unsigned)(wid * 2048 + lane * 16), mk_rsrc(flags + xcd * (FL_O / 8) * 32, FL_O / 8 * 128), 0);
    bstore_sc1<1024>(u32x4{0u, 0u, 0u, 0u}, (unsigned)(wid * 2048 + lane * 16), mk_rsrc(flags + xcd * (FL_O / 8) * 32, FL_O / 8 * 128), 0);
  }
  if (wid == 7) {
    dsw32<0>(smem_base + QX_CNT + lane * 4, 0u); dsw32<256>(smem_base + QX_CNT + lane * 4, 0u);
    dsw128<0>(smem_base + X_CN + lane * 16, u32x4{0u, 0u, 0u, 0u}); dsw128<1024>(smem_base + X_CN + lane * 16, u32x4{0u, 0u, 0u, 0u});
    asm volatile("s_waitcnt lgkmcnt(0)" ::: "memory");
  }
  if (__builtin_expect(idx >= 16, 1)) {
    unsigned o = 0;
    if (wid < 4) {
      const int col0 = (u >> 3) * 128, kp = u & 7;
      mod_gemv<1, 0>(smem_base + P_RED + wid * 512, lane, col0 + (wid & 1) * 64, kp * 128 + (wid >> 1) * 64, c, wmod, [] {});
      const sig_t g = {mk_rsrc(flags + (FL_M + (u & 15)) * 32, 16 * 4096 + 4), (unsigned)(lane * 4096)};
      asm volatile("" ::"s"(g.rs), "v"(g.vo));
      o = lds_add(smem_base + X_CN + 1280 + lane * 4, 1u);
      if (o == 3) {
        u32x2 r[4];
        static_for<4>([&](auto R) { dsr64<(R.value >> 1) * 1024 + (R.value & 1) * 256>(r[R.value], smem_base + P_RED + (lane >> 5) * 512 + (lane & 31) * 8); });
        asm volatile("s_waitcnt lgkmcnt(0)" : "+v"(r[0]), "+v"(r[1]), "+v"(r[2]), "+v"(r[3])::"memory");
        const f32x2 sum = (__builtin_bit_cast(f32x2, r[0]) + __builtin_bit_cast(f32x2, r[1])) + (__builtin_bit_cast(f32x2, r[2]) + __builtin_bit_cast(f32x2, r[3]));
        asm volatile("buffer_store_dwordx2 %0, %1, %2, 0 offen sc1" ::"v"(sum), "v"(lane * 8), "s"(mk_rsrc(modp + kp * 2048 + col0, 512)) : "memory");
        confirm(word, lane);
        signal(g);
      }
    } else {
      const rsrc_t rsX = mk_rsrc(x, S_LEN * D_DIM * 2);
      const int r0 = (wid - 4) * 4;
      u32x4 a[8];
      static_for<8>([&](auto N) {
        asm volatile("" : "=v"(a[N.value]));
        bload_v<(N.value & 1) * 1024>(a[N.value], (unsigned)((u * 16 + r0) * 2048 + lane * 16), rsX, (N.value >> 1) * 2048);
      });
      __builtin_amdgcn_s_barrier();
      asm volatile("s_waitcnt vmcnt(0)" : "+v"(a[0]), "+v"(a[1]), "+v"(a[2]), "+v"(a[3]), "+v"(a[4]), "+v"(a[5]), "+v"(a[6]), "+v"(a[7])::"memory");
      float sm[4], q[4];
      f32x2 xf[4][8];
#pragma unroll
      for (int i = 0; i < 4; ++i) {
        const unsigned ad = smem_base + (lane >> 5) * Q_SLOT + (lane & 31) * 256 + (((r0 + i) ^ (lane & 7)) * 16);
        dsw128<0>(ad, a[2 * i]); dsw128<2 * Q_SLOT>(ad, a[2 * i + 1]);
#pragma unroll
        for (int j = 0; j < 8; ++j) xf[i][j] = f32x2{bf_lo(a[2 * i + (j >> 2)][j & 3]), bf_hi(a[2 * i + (j >> 2)][j & 3])};
        const f32x2 t = ((xf[i][0] + xf[i][1]) + (xf[i][2] + xf[i][3])) + ((xf[i][4] + xf[i][5]) + (xf[i][6] + xf[i][7]));
        sm[i] = hadd(t);
      }
      const float mrow = sum16(rows4(sm, [](float p, float r) { return p + r; })) * (1.0f / D_DIM);
      bcast4(mrow, sm);
#pragma unroll
      for (int i = 0; i < 4; ++i) {
        f32x2 t0 = xf[i][0] - sm[i], t1 = xf[i][1] - sm[i];
        t0 *= t0; t1 *= t1;
#pragma unroll
        for (int j = 2; j < 8; j += 2) {
          const f32x2 d0 = xf[i][j] - sm[i], d1 = xf[i][j + 1] - sm[i];
          t0 += d0 * d0; t1 += d1 * d1;
        }
        t0 += t1;
        q[i] = hadd(t0);
      }
      const float rstd = __builtin_amdgcn_rsqf(sum16(rows4(q, [](float p, float r) { return p + r; })) * (1.0f / D_DIM) + 1e-5f);
      asm volatile("ds_write_b64 %0, %1\n\ts_waitcnt lgkmcnt(0)" ::"v"(smem_base + P_ST + (r0 + fq) * 8), "v"(f32x2{rstd, -mrow * rstd}) : "memory");
      prefill(wid - 4); prefill(wid);
    }
    u32x4 mp[8], xv[4], tb[4][4];
    u32x2 rr;
    const rsrc_t rsM = mk_rsrc(modp, 8 * 2048 * 4);
    const unsigned vm = (unsigned)((lane >> 5) * 4096 + wid * 512 + (lane & 31) * 16);
    asm volatile("" ::"s"(rsM), "v"(vm));
    const poll_t pm = poll(3 - o, flags + (FL_M + (1 + (u >> 3)) * 32) * 32, lane, 16, 8u);
    __builtin_amdgcn_s_barrier();
#pragma unroll
    for (int i = 0; i < 8; ++i) { asm volatile("" : "=v"(mp[i])); bload_sc1<0>(mp[i], vm, rsM, i * 8192); }
    const unsigned xa = smem_base + (wid >> 1) * Q_SLOT + (wid & 1) * 4096 + fq * 256 + ((fr ^ fq) * 16);
    dsr128<0>(xv[0], xa); dsr128<1024>(xv[1], xa ^ 64u); dsr128<2048>(xv[2], xa); dsr128<3072>(xv[3], xa ^ 64u);
    dsr64<0>(rr, smem_base + P_ST + fr * 8);
    asm volatile("s_waitcnt lgkmcnt(0)" : "+v"(xv[0]), "+v"(xv[1]), "+v"(xv[2]), "+v"(xv[3]), "+v"(rr)::"memory");
    f32x2 xn[4][4];
#pragma unroll
    for (int i = 0; i < 4; ++i)
#pragma unroll
      for (int d = 0; d < 4; ++d) {
        xn[i][d] = f32x2{bf_lo(xv[i][d]), bf_hi(xv[i][d])} * __uint_as_float(rr[0]) + __uint_as_float(rr[1]);
        asm volatile("" : "+v"(xn[i][d]));
      }
    char* hp = (char*)H + ((size_t)u * 32 + wid * 4) * 1024 + lane * 16;
    asm volatile("s_waitcnt vmcnt(0)" : "+v"(mp[0]), "+v"(mp[1]), "+v"(mp[2]), "+v"(mp[3]), "+v"(mp[4]), "+v"(mp[5]), "+v"(mp[6]), "+v"(mp[7]), "+v"(hp)::"memory");
    poll_end(pm);
    f32x4 mv = sum4(mp) + sum4(mp + 4);
#pragma unroll
    for (int e = 0; e < 4; ++e) {
      mv[e] = rbf(mv[e]);
      if (lane & 32) mv[e] = rbf(1.0f + mv[e]);
    }
    dsw128<0>(smem_base + P_TAB + wid * 1024 + lane * 16, __builtin_bit_cast(u32x4, mv));
    static_for<16>([&](auto N) {
      constexpr int i = N.value >> 2, j = N.value & 3;
      dsr128<i * 128 + (j >> 1) * 512 + (j & 1) * 16>(tb[i][j], smem_base + P_TAB + wid * 1024 + fq * 32);
    });
    static_for<4>([&](auto I) {
      constexpr int i = I.value;
      asm volatile("s_waitcnt lgkmcnt(%4)" : "+v"(tb[i][0]), "+v"(tb[i][1]), "+v"(tb[i][2]), "+v"(tb[i][3]) : "n"(12 - 4 * i) : "memory");
      u32x4 ho;
#pragma unroll
      for (int d = 0; d < 4; ++d) {
        const f32x4 gg = __builtin_bit_cast(f32x4, tb[i][2 + (d >> 1)]), ss = __builtin_bit_cast(f32x4, tb[i][d >> 1]);
        const f32x2 hv = xn[i][d] * ((d & 1) ? hi2(gg) : lo2(gg)) + ((d & 1) ? hi2(ss) : lo2(ss));
        ho[d] = pk_bf16(hv[0], hv[1]);
      }
      gst128(hp + i * 1024, ho);
    });
    const sig_t g = drain(flags + (FL_H + (u >> 3) * 4) * 32, lane, ((u & 7) == 7 && u != 127) ? 8 : 4, 128);
    if (lds_add(smem_base + X_CN + 1024 + lane * 4, 1u) == 7) {
      signal(g);
    }
  } else {
    const rsrc_t rsS = mk_rsrc(word, wid == 7 ? 7 * 8192 + 4 : 0);
    unsigned seen = 0;
    auto mid = [&] { asm volatile("buffer_atomic_add %0, %1, %2, 0 offen sc0 sc1" : "+v"(seen) : "v"(lane * 8192), "s"(rsS) : "memory"); };
    if (!master) {
      const int ql = (wid * 7 + idx - 8) * 64 + lane;
      if (idx >= 8 && wid < 3) touch_lds(smem_base + QX_SINK, (unsigned)((ql >> 1) * 2048 + (xcd & 3) * 512 + 256 + (ql & 1) * 128), mk_rsrc(W, D_DIM * 3 * D_DIM * 2), 0);
      prefill(wid);
    }
    if (idx < 8) {
      if (wid == 0)
        bstore_sc1<0>(u32x4{0u, 0u, 0u, 0u}, (unsigned)(lane * 128), mk_rsrc(flags + (FL_O + v * ((FL_S - FL_O) / 64)) * 32, (FL_S - FL_O) / 64 * 128), 0);
      __builtin_amdgcn_s_sleep(NDLY);
      mod_gemv<2, 1>(smem_base + P_RED + wid * 512, lane, 2048 + (xcd * 8 + wid) * 64, idx * 128, c, wmod, mid);
      const float sum = red_sum<2>(smem_base + P_RED + wid * 512, lane);
      asm volatile("buffer_store_dword %1, %2, %3, 0 offen sc1\n\ts_waitcnt vmcnt(1)" : "+v"(seen) : "v"(sum), "v"(lane * 4), "s"(mk_rsrc(modp + 16384 + ((xcd * 8 + wid) * 8 + idx) * 64, 256)) : "memory");
    } else if (!master) {
      __builtin_amdgcn_s_sleep(TDLY);
#pragma unroll
      for (int q = 0; q < 3; ++q) {
        const int ql = ((q * 8 + wid) * 7 + idx - 8) * 64 + lane + 224 * 12;
        touch_lds(smem_base + QX_SINK, (unsigned)((ql >> 2) * 2048 + (xcd & 3) * 512 + (ql & 3) * 128), mk_rsrc(W, D_DIM * 3 * D_DIM * 2), 0);
      }
      __builtin_amdgcn_s_barrier();
      wait_vm<3>();
      mid();
      asm volatile("s_waitcnt vmcnt(0)" : "+v"(seen)::"memory");
    } else {
      wait_vm<0>();
      __builtin_amdgcn_s_barrier();
      bstore_sc1<0>(u32x4{1u, 1u, 1u, 1u}, (unsigned)(wid * 1024 + lane * 16), mk_rsrc(flags + (FL_S + xcd * 64) * 32, 64 * 128), 0);
      unsigned spins = 0;
      if (wid == 7) {
#pragma unroll 1
        do set_words(flags, lane); while (!words_set(word, lane) && ++spins < (1u << 20));
      }
      __builtin_amdgcn_s_barrier();
      prefill(wid);
      if (wid == 7) {
#pragma unroll 1
        do set_words(flags, lane); while (__builtin_amdgcn_ballot_w64(cnt_read(flags + FL_M * 32, lane, 16) < (lane < 16 ? 8u : 0u)) != 0ull && ++spins < (1u << 20));
      }
      seen = 1;
    }
    if (wid != 7) wait_vm<0>();
    else if ((__builtin_amdgcn_ballot_w64(seen == 0u) & 255u) != 0u) confirm(word, lane);
  }
}
DI void wo_piece(unsigned smem_base, unsigned p, unsigned rot, unsigned voff, rsrc_t rs) {
  const unsigned pb = p >> 2, kb = (pb & 24u) | ((pb + rot) & 7u);
  bload_lds<0>(smem_base + (p < 64u ? QX_WH + p * 1024u : (p - 64u) * 1024u), voff, rs, kb * 65536u + (p & 3u) * 16384u);
}

DI void qkv_ldw(unsigned smem_base, int lane, int sw, int n0, rsrc_t rsW, unsigned so, unsigned u) {
#pragma unroll
  for (int i = 0; i < 4; ++i) {
    const int r = (i & 1) * 8 + (lane >> 3), ch = lane & 7;
    const int g = ((r >> 1) & 1) | (((r >> 3) & 1) << 1);
    bload_lds<0>(smem_base + 9216 + sw * 4096 + so + i * 1024, (unsigned)(r * 6144 + sw * 2048 + (n0 + (((ch >> 1) ^ g) * 16) + (ch & 1) * 8) * 2), rsW,
                 u * 196608u + (i >> 1) * 98304u);
  }
}
DI void qkv_loader(unsigned smem_base, int lane, int sw, int m0, int n0, const bf16* H, rsrc_t rsW,
                   unsigned rot, unsigned voffO, rsrc_t rsO) {
  const rsrc_t rsH = mk_rsrc(H, S_LEN * D_DIM * 2);
  int fb0 = (m0 >> 4) - 1 + 3 * sw; fb0 = fb0 < 0 ? 0 : fb0;
  const unsigned voffH0 = (unsigned)(fb0 * 32768 + lane * 16);
  const unsigned voffH1 = (unsigned)(((m0 >> 4) + 3 * sw) * 32768 + lane * 16);
  const unsigned m0a = smem_base + sw * 3072;
  auto ldh = [&](unsigned so, unsigned u) {
    bload_lds_sc1<0>(m0a + so, voffH0, rsH, u * 1024u);
    bload_lds_sc1<0>(m0a + so + 1024, voffH1, rsH, u * 1024u);
    bload_lds_sc1<0>(m0a + so + 2048, voffH1, rsH, u * 1024u + 32768u);
  };
  auto ldw = [&](unsigned so, unsigned u) { qkv_ldw(smem_base, lane, sw, n0, rsW, so, u); };
  asm volatile("" ::"v"(voffH0), "v"(voffH1), "s"(rsH), "s"(m0a));
  __builtin_amdgcn_s_barrier();
#pragma unroll
  for (int u = 1; u <= NP1; ++u) ldh(u * Q_SLOT, u);
  wait_vm<3 * (NP1 - 1)>();
  __builtin_amdgcn_s_barrier();
#pragma unroll
  for (int u = NP1 + 1; u < Q_NSL; ++u) ldh(u * Q_SLOT, u);
  static_for<Q_NSL - 2, 1>([&](auto U) {
    constexpr int u = U.value;
    ldh((u - 1) * Q_SLOT, u + Q_NSL - 1); ldw((u - 1) * Q_SLOT, u + Q_NSL - 1);
    wait_vm<3 * (Q_NSL - 2 - u) + 7 * u>();
    __builtin_amdgcn_s_barrier();
  });
  unsigned sb = (Q_NSL - 3) * Q_SLOT;
  for (int u = Q_NSL - 2; u < 32 - (Q_NSL - 1); ++u) {
    ldh(sb, u + Q_NSL - 1); ldw(sb, u + Q_NSL - 1);
    sb = (sb == (Q_NSL - 1) * Q_SLOT) ? 0u : sb + Q_SLOT;
    wait_vm<(Q_NSL - 2) * 7>();
    __builtin_amdgcn_s_barrier();
  }
  for (int t = 0; t < 6; ++t) {
    const unsigned p0 = (unsigned)(t * 21 + (t >= 3 ? 1 : 0) + sw * 7);
#pragma unroll
    for (int i = 0; i < 7; ++i) wo_piece(smem_base, p0 + i, rot, voffO, rsO);
    if (t < 5) wait_vm<(Q_NSL - 2) * 7>();
    __builtin_amdgcn_s_barrier();
  }
}

template <int WM>
DI void qkv_mfma(unsigned smem_base, int lane, int wn, int bm, int bn, const bf16* H, bf16* __restrict__ O) {
  const int fr = lane & 15, fq = lane >> 4;
  unsigned bb0, bb1;
  const unsigned ab = smem_base + lane * 16;
  {
    const int q = (lane >> 2) & 3, p = lane & 3;
    const int r = 8 * fq + q;
    const int g = ((r >> 1) & 1) | (((r >> 3) & 1) << 1);
    bb0 = smem_base + 9216 + r * 128 + (((wn * 2) ^ g) * 32) + 8 * p;
    bb1 = bb0 ^ 32u;
  }
  const bool par = fr & 1, c0 = 4 * fq + (fr & 1) <= fr, c1 = 4 * fq + 2 + (fr & 1) <= fr;
  const unsigned s0 = par ? 0x01000c0cu : 0x0c0c0100u, s1 = par ? 0x03020c0cu : 0x0c0c0302u;
  u32x4 sel = {c0 ? s0 : 0x0c0c0c0cu, c1 ? s1 : 0x0c0c0c0cu, c0 ? 0x0c0c0c0cu : s0, c1 ? 0x0c0c0c0cu : s1};
  unsigned hx = ab + QX_HX + wn * 3072;
  asm volatile("" : "+v"(sel), "+v"(hx));
  const rsrc_t rsO = mk_rsrc((char*)O + (size_t)((bm * 2 + WM) * 32 + bn * 2 + wn) * 4096, 4096);
  f32x4 acc[4][6], acch[2];
#pragma unroll
  for (int i = 0; i < 4; ++i)
#pragma unroll
    for (int j = 0; j < 6; ++j) { acc[i][j] = f32x4{0, 0, 0, 0}; asm volatile("" : "+v"(acc[i][j])); }
#pragma unroll
  for (int j = 0; j < 2; ++j) { acch[j] = f32x4{0, 0, 0, 0}; asm volatile("" : "+v"(acch[j])); }
  __builtin_amdgcn_s_setprio(2);
  u32x4 aX[5], aY[5];
  u32x2 bXl[6], bXh[6], bYl[6], bYh[6];
  unsigned ra = ab, rb0 = bb0, rb1 = bb1, so = 0;
#define Q_READ(AR, BL, BH, R)                                                                                    \
  do {                                                                                                           \
    if ((R) < 12) {                                                                                              \
      constexpr int jb = ((R) < 12 ? (R) : 0) >> 1;                                                              \
      if ((R) & 1) dsr_tr<(jb >> 1) * 4096 + 512>(BH[jb], (jb & 1) ? rb1 : rb0);                                 \
      else dsr_tr<(jb >> 1) * 4096>(BL[jb], (jb & 1) ? rb1 : rb0);                                               \
    } else if ((R) < 16) {                                                                                       \
      constexpr int ia = ((R) >= 12 && (R) < 16) ? (R) - 12 : 0;                                                 \
      dsr128<1024 + WM * 4096 + ia * 1024>(AR[ia], ra);                                                          \
    } else {                                                                                                     \
      dsr128<0>(AR[4], ra);                                                                                      \
    }                                                                                                            \
  } while (0)
#define Q_MFMA(AR, BB, N)                                                                                        \
  do {                                                                                                           \
    if ((N) < 16) MFMA_AV(acc[(N) & 3][((N) < 24 ? (N) : 0) >> 2], BB[((N) < 24 ? (N) : 0) >> 2], AR[(N) & 3]);  \
    else if ((N) < 24) MFMA_AV(acc[(N) & 3][((N) < 24 ? (N) : 0) >> 2], AR[(N) & 3], BB[((N) < 24 ? (N) : 0) >> 2]); \
    else if (WM == 0) MFMA_AV(acch[(N) & 1], BB[2 + ((N) & 1)], AR[4]);                                          \
    else MFMA_AV(acch[(N) & 1], AR[4], BB[4 + ((N) & 1)]);                                                       \
  } while (0)
#define Q_ADV(K)                                                                                                 \
  do {                                                                                                           \
    if ((K) == 0) asm volatile("s_add_u32 %0, %0, %1\n\ts_cmp_eq_u32 %0, %2\n\ts_cselect_b32 %0, 0, %0" : "+s"(so) : "n"(Q_SLOT), "n"(Q_NSL * Q_SLOT) : "scc"); \
    else if ((K) == 1) asm volatile("v_add_u32 %0, %1, %2" : "=v"(rb0) : "s"(so), "v"(bb0));                     \
    else if ((K) == 2) asm volatile("v_add_u32 %0, %1, %2" : "=v"(rb1) : "s"(so), "v"(bb1));                     \
    else asm volatile("v_add_u32 %0, %1, %2" : "=v"(ra) : "s"(so), "v"(ab));                                     \
  } while (0)
#define Q_UNIT(AR, BL, BH, NAR, NBL, NBH)                                                                        \
  do {                                                                                                           \
    WAIT_LGKM0();                                                                                                \
    u32x4 bb[6];                                                                                                 \
    _Pragma("unroll") for (int j = 0; j < 6; ++j) bb[j] = u32x4{BL[j][0], BL[j][1], BH[j][0], BH[j][1]};         \
    SCHED0();                                                                                                    \
    static_for<26>([&](auto N) {                                                                                 \
      constexpr int n = N.value;                                                                                 \
      if constexpr (n == QBAR) __builtin_amdgcn_s_barrier();                                                     \
      Q_MFMA(AR, bb, n);                                                                                         \
      if constexpr (n >= QBAR && n < QBAR + 17) Q_READ(NAR, NBL, NBH, n - QBAR);                                 \
      if constexpr (n >= QBAR + 18 && n < QBAR + 22) Q_ADV(n - QBAR - 18);                                       \
    });                                                                                                          \
    SCHED0();                                                                                                    \
  } while (0)
  {
    const rsrc_t rsH = mk_rsrc(H, S_LEN * D_DIM * 2);
    unsigned vh = lane * 16 + (bm * 8 + WM * 4) * 32768, vl = lane * 16 + (bm ? bm * 8 - 1 : 0) * 32768;
    asm volatile("" : "+v"(vh), "+v"(vl), "+v"(rb0), "+v"(rb1) : "s"(rsH));
    __builtin_amdgcn_s_barrier();
    static_for<5>([&](auto I) { asm volatile("" : "=v"(aX[I.value])); bload_sc1<0>(aX[I.value], I.value < 4 ? vh : vl, rsH, (I.value & 3) * 32768); });
    static_for<12>([&](auto R) { Q_READ(aX, bXl, bXh, R.value); });
    static_for<4>([&](auto K) { Q_ADV(K.value); });
    asm volatile("s_waitcnt vmcnt(0)" : "+v"(aX[0]), "+v"(aX[1]), "+v"(aX[2]), "+v"(aX[3]), "+v"(aX[4])::"memory");
  }
  for (int u = 0; u < 32; u += 2) {
    Q_UNIT(aX, bXl, bXh, aY, bYl, bYh);
    Q_UNIT(aY, bYl, bYh, aX, bXl, bXh);
  }
  asm volatile("s_waitcnt lgkmcnt(0)\n\ts_nop 7" ::: "memory");
#pragma unroll
  for (int i = 0; i < 5; ++i) asm volatile("" : "+v"(aX[i]));
#pragma unroll
  for (int j = 0; j < 6; ++j) asm volatile("" : "+v"(bXl[j]), "+v"(bXh[j]));
#pragma unroll
  for (int i = 0; i < 4; ++i)
#pragma unroll
    for (int j = 0; j < 6; ++j) asm volatile("" : "+v"(acc[i][j]));
  asm volatile("" : "+v"(acch[0]), "+v"(acch[1]));
  SCHED0();
  u32x4 kk[5], vv[5];
  if (WM == 0) {
    kk[4] = pack8(acc[3][2], acc[3][3]); vv[4] = pack8(acc[3][4], acc[3][5]);
    dsw128<0>(hx, kk[4]); dsw128<1024>(hx, vv[4]);
  } else {
    dsw128<2048>(hx, pack8(acch[0], acch[1]));
  }
  asm volatile("s_waitcnt lgkmcnt(0)" ::: "memory");
  __builtin_amdgcn_s_barrier();
  if (WM == 0) dsr128<2048>(vv[0], hx);
  else { dsr128<0>(kk[0], hx); dsr128<1024>(vv[0], hx); }
  SCHED0();
  if (WM == 0) kk[0] = pack8(acch[0], acch[1]);
#pragma unroll
  for (int i = 0; i < (WM == 0 ? 3 : 4); ++i) kk[i + 1] = pack8(acc[i][2], acc[i][3]);
  const float cs = 0.17677669529663687f * 1.4426950408889634f;
  float l0[4], l1[4], tm[4];
#pragma unroll
  for (int ii = 1; ii < 5; ++ii) {
    const int i = ii & 3;
    const u32x4 q = pack8(acc[i][0], acc[i][1]);
    if (WM == 1 && i == 0) asm volatile("s_waitcnt lgkmcnt(0)" : "+v"(kk[0]), "+v"(vv[0])::"memory");
    const f32x4 sc = mfma0(kk[i + 1], q), sp = mfma0(kk[i], q);
    float p0 = par ? sp[1] : sp[0], p1 = par ? sp[3] : sp[2];
    if (WM == 0 && i == 0 && bm == 0) { p0 = -5.656854249e9f; p1 = -5.656854249e9f; }
    l0[i] = c0 ? (par ? sc[1] : sc[0]) : p0;
    l1[i] = c1 ? (par ? sc[3] : sc[2]) : p1;
    tm[i] = fmax2(l0[i], l1[i]);
  }
#pragma unroll
  for (int i = 0; i < (WM == 0 ? 3 : 4); ++i) vv[i + 1] = pack8(acc[i][4], acc[i][5]);
  bcast4(rows4(tm, [](float a, float b) { return fmax2(a, b); }), tm);
#pragma unroll
  for (int i = 0; i < 4; ++i) {
    l0[i] = __builtin_amdgcn_exp2f((l0[i] - tm[i]) * cs);
    l1[i] = __builtin_amdgcn_exp2f((l1[i] - tm[i]) * cs);
    tm[i] = l0[i] + l1[i];
  }
  bcast4(__builtin_amdgcn_rcpf(rows4(tm, [](float a, float b) { return a + b; })), tm);
  if (WM == 0) asm volatile("s_waitcnt lgkmcnt(0)" : "+v"(vv[0])::"memory");
#pragma unroll
  for (int i = 0; i < 4; ++i) {
    const unsigned pp = pk_bf16(l0[i] * tm[i], l1[i] * tm[i]);
    const u32x4 pr = {__builtin_amdgcn_perm(pp, pp, sel[0]), __builtin_amdgcn_perm(pp, pp, sel[1]),
                      __builtin_amdgcn_perm(pp, pp, sel[2]), __builtin_amdgcn_perm(pp, pp, sel[3])};
    const u32x4 ov = pack8(mfma0(u32x4{vv[i + 1][0], vv[i + 1][1], vv[i][0], vv[i][1]}, pr), mfma0(u32x4{vv[i + 1][2], vv[i + 1][3], vv[i][2], vv[i][3]}, pr));
    asm volatile("buffer_store_dwordx4 %0, %1, %2, 0 offen offset:%3 sc1\n\ts_nop 1" ::"v"(ov), "v"(lane * 16), "s"(rsO), "i"(i * 1024) : "memory");
  }
}

template <class GST>
DI void attn_phase(unsigned smem_base, int wid, bf16* H, const bf16* __restrict__ W, bf16* O,
                   const bf16* __restrict__ wo, const bf16* __restrict__ X, float* modp, bf16* Y, float* stat,
                   unsigned* flags, const bf16* __restrict__ wg, const bf16* __restrict__ wu,
                   unsigned* ctab, const bf16* __restrict__ cnd, const bf16* __restrict__ wmod, GST gst) {
  const int tid = threadIdx.x, lane = tid & 63;
  const int xcd = blockIdx.x & 7, idx = blockIdx.x >> 3;
  const int bm = (xcd >> 2) * 8 + (idx & 7), bn = (xcd & 3) * 4 + (idx >> 3);
  const int rb = xcd * 2 + (idx & 1), cg = idx >> 1;
  const unsigned rot = __builtin_amdgcn_readfirstlane((cg >> 2) * 2);
  const unsigned cnt = smem_base + QX_CNT, cntl = cnt + lane * 4;
  const rsrc_t rsW = mk_rsrc(W, D_DIM * 3 * D_DIM * 2);
  first_phase(smem_base, wid, lane, X, cnd, wmod, W, modp, H, flags, [&](int w) {
#pragma unroll
    for (int i = 0; i < 3; ++i) {
      const int p = w + 8 * i < 21 ? w + 8 * i : 20, u = (p * 11) >> 5;
      qkv_ldw(smem_base, lane, p - 3 * u, bn * 64, rsW, u * Q_SLOT, u);
    }
  });
  if (wid < 4) {
    if (wid & 1) qkv_mfma<1>(smem_base, lane, wid >> 1, bm, bn, H, O);
    else qkv_mfma<0>(smem_base, lane, wid >> 1, bm, bn, H, O);
  } else {
    const rsrc_t rsO = mk_rsrc(wo, D_DIM * D_DIM * 2);
    const unsigned voffO = (unsigned)((lane >> 3) * 2048 + cg * 128 + ((((lane & 7) >> 1) ^ (lane >> 4)) * 32) + (lane & 1) * 16);
    if (wid < 7) qkv_loader(smem_base, lane, wid - 4, bm * 128, bn * 64, H, rsW, rot, voffO, rsO);
    else {
      const unsigned vgu = idx < 16 ? 0x40000000u : (unsigned)((((idx - 16) * 64 + lane) / 7) * (F_DIM * 2) + xcd * 896 + (((idx - 16) * 64 + lane) % 7) * 128);
      const poll_t ph = poll(0u, flags + (FL_H + bm * 4 + (xcd & 3)) * 32, lane, 1, bm ? 9u : 8u);
      asm volatile("s_barrier" ::: "memory");
      SCHED0();
      unsigned* r1 = flags + (FL_M + (1 + xcd) * 32 + 24) * 32;
      const sig_t gi = drain(idx < 8 ? r1 : flags + (FL_M + 24 + xcd) * 32, lane, idx < 16 ? 1 : 0, 4);
      poll_end(ph);
      signal(gi);
      if (idx < 8) {
        const int v = xcd * 8 + idx;
        unsigned t = 0;
        for (int i = 0; i < 4; ++i) __builtin_amdgcn_s_barrier();
        asm volatile("buffer_atomic_add %0, %1, %2, 0 offen sc0 sc1\n\ts_barrier\n\ts_barrier\n\ts_barrier\n\ts_barrier\n\ts_waitcnt vmcnt(0)" : "+v"(t) : "v"(lane * 128), "s"(mk_rsrc(r1, 128)) : "memory");
#pragma unroll 1
        for (unsigned spins = 0; __builtin_amdgcn_readfirstlane(t) < 8u && spins < (1u << 20); ++spins) t = cnt_read(r1, lane, 1);
        static_for<2>([&](auto I) { bload_lds_sc1<I.value * 1024>(smem_base + G3_CT, lane * 16, mk_rsrc(modp + 16384 + v * 512, 2048), 0); });
        for (int i = 0; i < 4; ++i) __builtin_amdgcn_s_barrier();
        wait_vm<0>();
        float sum = red_sum<8>(smem_base + G3_CT, lane);
        sum = (v >> 4) == 2 ? gain_nz(sum) : rbf(sum);
        asm volatile("buffer_store_dword %0, %1, %2, 0 offen sc1" ::"v"(sum), "v"(lane * 4), "s"(mk_rsrc(modp + 49152 + v * 64, 256)) : "memory");
        asm volatile("buffer_store_short_d16_hi %0, %1, %2, 0 offen sc1" ::"v"(sum), "v"((lane >> 5) * 128 + (lane & 31) * 2),
                     "s"(mk_rsrc(ctab + (v & 15) * 64 + (v & 16), (v >> 4) == 1 || (v >> 4) == 2 ? 256 : 0)) : "memory");
        for (int i = 0; i < 4; ++i) __builtin_amdgcn_s_barrier();
        signal(drain(flags + (FL_M + 16 + idx) * 32, lane, 9, 4096));
      }
      {
        int nxt = TS + idx % TP, ev = 0;
#pragma unroll 1
        for (int i = idx < 8 ? 16 : 0; i < 27; ++i) {
          if (i == WOS && idx >= 16) {
            touch_lds(smem_base + QX_SINK, (blockIdx.x - 128) * 16384 + lane * 128, rsO, 0);
            touch_lds(smem_base + QX_SINK, (blockIdx.x - 128) * 16384 + lane * 128, rsO, 8192);
          }
          if (i == nxt && ev < 2) {
            touch_lds(smem_base + QX_SINK, vgu, mk_rsrc(ev ? wu : wg, D_DIM * F_DIM * 2), 0);
            nxt += TP; ++ev;
          }
          __builtin_amdgcn_s_barrier();
        }
      }
      unsigned* cr = flags + (FL_M + (idx == 15 ? 0 : 1 + xcd) * 32 + 16) * 32;
      const int nl = idx == 15 ? 16 : 8;
      unsigned t = 0;
      asm volatile("buffer_atomic_add %0, %1, %2, 0 offen sc0 sc1" : "+v"(t) : "v"(lane * 128), "s"(mk_rsrc(cr, nl * 128)) : "memory");
      wo_piece(smem_base, 63u, rot, voffO, rsO);
      __builtin_amdgcn_s_barrier();
      __builtin_amdgcn_s_barrier();
      asm volatile("s_waitcnt vmcnt(1)" : "+v"(t)::"memory");
      for (unsigned spins = 0; __builtin_amdgcn_ballot_w64(t < (lane < nl ? 8u : 0u)) != 0ull && spins < (1u << 20); ++spins) {
        if (idx == 15) set_words(flags, lane);
        t = cnt_read(cr, lane, nl);
      }
      static_for<4>([&](auto I) { bload_lds_sc1<I.value * 1024>(smem_base + G3_CT, lane * 16, mk_rsrc(ctab, 4096), 0); });
#pragma unroll
      for (int i = 0; i < 2; ++i)
        asm volatile("s_mov_b32 m0, %0\n\ts_nop 0\n\tbuffer_load_dword %1, %2, 0 offen sc1 lds" ::"s"(smem_base + QX_GT + i * 256), "v"(lane * 4),
                     "s"(mk_rsrc(modp + 49152 + i * 2048 + cg * 64, 256)) : "memory");
      for (int i = 30; i < 33; ++i) __builtin_amdgcn_s_barrier();
      wo_piece(smem_base, 127u, rot, voffO, rsO);
    }
    __builtin_amdgcn_s_barrier();
    wait_vm<0>();
    lds_add(cntl, 1u);
  }
  const int fr = lane & 15, fq = lane >> 4;
  const int kq = wid & 3, rg = rb * 2 + (wid >> 2);
  const int ko = (kq + cg) & 3;
  lds_wait<2>(cnt, 4);
  u32x2 wl[8][4], wh[8][4];
  {
    const int q = (lane >> 2) & 3, p = lane & 3, r = 4 * fq + q;
    const unsigned wb = smem_base + (kq < 2 ? QX_WH + kq * 32768 : (kq - 2) * 32768) + r * 128 + (((r >> 1) & 3) * 32) + 8 * p;
    unsigned wa[4];
#pragma unroll
    for (int f = 0; f < 4; ++f) wa[f] = wb ^ (f * 32);
    static_for<32>([&](auto E) {
      constexpr int s = E.value >> 2, f = E.value & 3;
      dsr_tr<s * 4096>(wl[s][f], wa[f]);
      dsr_tr<s * 4096 + 2048>(wh[s][f], wa[f]);
    });
  }
  const rsrc_t rsA = mk_rsrc((const char*)O + (size_t)rg * 131072, 131072);
  unsigned voffA[4], hs[8];
#pragma unroll
  for (int i = 0; i < 4; ++i) { voffA[i] = lane * 16 + ((ko + 1 + i) & 3) * 1024; asm volatile("" : "+v"(voffA[i])); }
#pragma unroll
  for (int i = 0; i < 8; ++i) { hs[i] = __builtin_amdgcn_readfirstlane((kq * 8 + ((i + rot) & 7)) * 4096); asm volatile("" : "+s"(hs[i])); }
  asm volatile("" ::"s"(rsA));
  if (wid < 4) {
    const sig_t g = drain(flags + (FL_O + bm * 8 + (bn & 1)) * 32, lane, 4, 256);
    if (lds_add(cntl + 256, 1u) == 3u) signal(g);
  }
  const poll_t pl = poll(wid - 7, flags + (FL_O + rb * 8 + (cg & 3) * 2) * 32, lane, 2, 8u);
  asm volatile("s_waitcnt lgkmcnt(0)" ::: "memory");
  __builtin_amdgcn_s_barrier();
  u32x4 wf[8][4];
#pragma unroll
  for (int s = 0; s < 8; ++s)
#pragma unroll
    for (int f = 0; f < 4; ++f) {
      asm volatile("" : "+v"(wl[s][f]), "+v"(wh[s][f]));
      wf[s][f] = u32x4{wl[s][f][0], wl[s][f][1], wh[s][f][0], wh[s][f][1]};
    }
  const unsigned voffL = lane * 16;
  u32x4 af[32], xv[2], pr[3][4], gt[4], ct[4];
  f32x4 acc[4][4];
  auto aload = [&](auto N) {
    constexpr int n = N.value;
    asm volatile("" : "=v"(af[n]));
    bload_sc1<0>(af[n], voffA[n >> 3], rsA, hs[n & 7]);
  };
  static_for<GDEP>([&](auto N) { aload(N); });
  const unsigned sl = smem_base + 49152 + voffL + (wid >> 2) * 49152;
  const int sw = ((fq & 1) << 1) | (fq >> 1);
  SCHED0();
  static_for<32>([&](auto N) {
    constexpr int n = N.value, ps = n >> 3, s = n & 7, XG = 32 - GDEP;
    constexpr int last = n - 1 + GDEP < 31 ? n - 1 + GDEP : 31;
    asm volatile("s_waitcnt vmcnt(%1)" : "+v"(af[n]) : "n"(last - n + (n > XG ? 2 : 0) + (n > GS0 + 3 ? 6 : (n > GS0 ? 2 * (n - GS0) : 0))) : "memory");
    if constexpr (n == 0) poll_end(pl);
    if constexpr (n == 28) { WAIT_LGKM0(); __builtin_amdgcn_s_barrier(); }
    static_for<4>([&](auto F) {
      constexpr int f = F.value;
      if constexpr (s == 0) asm volatile("v_mfma_f32_16x16x32_bf16 %0, %1, %2, 0" : "=&v"(acc[ps][f]) : "v"(wf[s][f]), "v"(af[n]));
      else MFMA_AV(acc[ps][f], wf[s][f], af[n]);
      if constexpr (f == 0 && n + GDEP < 32) aload(ic<(n + GDEP < 32 ? n + GDEP : 0)>{});
      if constexpr (f == 0 && n == XG) {
        const rsrc_t rsX = mk_rsrc(X, S_LEN * D_DIM * 2);
        const unsigned vx = (unsigned)(((rg * 64 + ko * 16 + fr) * D_DIM + cg * 64 + sw * 8) * 2);
        asm volatile("" : "=v"(xv[0]), "=v"(xv[1]));
        bload_v<0>(xv[0], vx, rsX, 0); bload_v<64>(xv[1], vx, rsX, 0);
      }
      if constexpr ((f & 1) && n >= GS0 && n < GS0 + 3) gst(ic<(n >= GS0 ? n - GS0 : 0)>{}, ic<(f >> 1)>{});
      if constexpr (ps > 0 && (s == 2 || s == 3) && (f & 1)) {
        constexpr int e = (s - 2) * 2 + (f >> 1);
        dsw128<(ps - 1) * 4096 + e * 1024>(sl + ((kq + ps) & 3) * 12288, __builtin_bit_cast(u32x4, acc[ps - 1][e]));
      }
      if constexpr (n >= 28 && n < 31) dsr128<(n - 28) * 4096 + f * 1024>(pr[n - 28][f], sl + kq * 12288);
      if constexpr (n == 31) { dsr128<f * 64>(gt[f], smem_base + QX_GT + fq * 16); dsr128<256 + f * 64>(ct[f], smem_base + QX_GT + fq * 16); }
    });
  });
  asm volatile("s_waitcnt vmcnt(6)" : "+v"(xv[0]), "+v"(xv[1])::"memory");
  asm volatile("s_waitcnt lgkmcnt(0)\n\ts_nop 4" ::: "memory");
  f32x4 fin[4], yv[4];
#pragma unroll
  for (int f = 0; f < 4; ++f) asm volatile("" : "+v"(acc[3][f]), "+v"(pr[0][f]), "+v"(pr[1][f]), "+v"(pr[2][f]), "+v"(gt[f]), "+v"(ct[f]));
#pragma unroll
  for (int c = 0; c < 2; ++c) {
    const auto x0 = __builtin_amdgcn_permlane16_swap(xv[c][0], xv[c][2], false, false);
    const auto x1 = __builtin_amdgcn_permlane16_swap(xv[c][1], xv[c][3], false, false);
#pragma unroll
    for (int h = 0; h < 2; ++h) {
      const int f = 2 * c + h;
      const f32x4 a = (acc[3][f] + __builtin_bit_cast(f32x4, pr[0][f])) + (__builtin_bit_cast(f32x4, pr[1][f]) + __builtin_bit_cast(f32x4, pr[2][f]));
      const f32x4 xf = {bf_lo(x0[h]), bf_hi(x0[h]), bf_lo(x1[h]), bf_hi(x1[h])};
      fin[f] = xf + __builtin_bit_cast(f32x4, gt[f]) * a;
      yv[f] = fin[f] * __builtin_bit_cast(f32x4, ct[f]);
    }
    const auto y0 = __builtin_amdgcn_permlane16_swap(pk_bf16(yv[2 * c][0], yv[2 * c][1]), pk_bf16(yv[2 * c + 1][0], yv[2 * c + 1][1]), false, false);
    const auto y1 = __builtin_amdgcn_permlane16_swap(pk_bf16(yv[2 * c][2], yv[2 * c][3]), pk_bf16(yv[2 * c + 1][2], yv[2 * c + 1][3]), false, false);
    gst128((char*)Y + ((size_t)(rg >> 2) * 32 + cg * 2 + c) * 16384 + ((rg & 3) * 4 + ko) * 1024 + (sw * 16 + fr) * 16,
           u32x4{y0[0], y1[0], y0[1], y1[1]});
  }
  const f32x4 s4 = (fin[0] + fin[1]) + (fin[2] + fin[3]);
  const float s = rowsum4(hadd(lo2(s4) + hi2(s4)));
  const float mb = s * (1.0f / 64.0f);
  f32x4 q4 = {0, 0, 0, 0};
#pragma unroll
  for (int f = 0; f < 4; ++f) { const f32x4 d = fin[f] - mb; q4 += d * d; }
  const float m2 = rowsum4(hadd(lo2(q4) + hi2(q4)));
  asm volatile("buffer_store_dwordx2 %0, %1, %2, 0 offen sc1" ::"v"(f32x2{s, m2}), "v"(lane * 8),
               "s"(mk_rsrc(stat + ((size_t)cg * S_LEN + rg * 64 + ko * 16) * 2, 128)) : "memory");
}

#define G3_STAGE(SLOT, P, U)                                                                               \
  do {                                                                                                     \
    if ((P) == 0) bload_lds_sc1<0>(m0w + 65536 + (SLOT) * 16384, voffA, rsA, (unsigned)(U) * 16384u);      \
    else if ((P) == 1) bload_lds_sc1<1024>(m0w + 65536 + (SLOT) * 16384, voffA, rsA, (unsigned)(U) * 16384u);  \
    else bload_lds<0>(m0w + (SLOT) * 16384 + ((P) - 2) * 1024, (P) == 2 ? voffB0 : voffB1, rsB, (unsigned)(U) * 229376u);  \
  } while (0)

DI constexpr int g3_line(int gx, int p) {
  const int t = p + ((3 + gx) & 7);
  return 7 * gx + (t >= 7 ? t - 7 : t);
}
constexpr unsigned act_frag(int kh, int p) {
  const int tl = p < 48 ? p / 3 : (p - 48) >> 2, jv = (tl & 3) * 7 + (p < 48 ? 4 + p % 3 : p & 3);
  return g3_line(kh * 4 + (tl >> 2), jv >> 2) * 4 + (jv & 3);
}
template <int FN, class WST, class HOOK, class PRE>
DI void g3_body(unsigned smem_base, int wm, int wn, int wid, int lane, int bm, int bn, rsrc_t rsA, rsrc_t rsB,
                rsrc_t rsE, const char* ta, unsigned voffA, unsigned voffB0, unsigned voffB1, bf16* act, WST wst, HOOK hook, PRE pre) {
  constexpr int NB = 2 * FN, NM = 4 * NB, RD = 5 + 2 * NB;
  const int fr = lane & 15, fq = lane >> 4;
  f32x4 acc[4][NB], accC[2];
  accC[0] = f32x4{0, 0, 0, 0}; accC[1] = f32x4{0, 0, 0, 0};
  asm volatile("" : "+v"(accC[0]), "+v"(accC[1]));
  const int rot = wm % FN;
  unsigned cad = fr >= 2 || wm >= FN ? smem_base + X_CN + 1536 : smem_base + G3_CT + (fr & 1) * 64 + fq * 16;
  unsigned cin = fr >= 2 || wm >= FN ? 0u : 512u;
  asm volatile("" : "+v"(cin));
#pragma unroll
  for (int i = 0; i < 4; ++i)
#pragma unroll
    for (int j = 0; j < NB; ++j) { acc[i][j] = f32x4{0, 0, 0, 0}; asm volatile("" : "+v"(acc[i][j])); }
  unsigned aad = smem_base + 65536 + wm * 4096 + lane * 16;
  unsigned bad[FN];
  {
    const int q = (lane >> 2) & 3, p = lane & 3;
    const int r = 8 * fq + q;
    const int sw = (r & 3) | (((r >> 3) & 1) << 2);
#pragma unroll
    for (int j = 0; j < FN; ++j) {
      const int gi = wn * 4 + (j + rot) % FN + (bn & 1);
      bad[j] = smem_base + r * 256 + ((gi ^ sw) * 32) + 8 * p;
    }
  }
  u32x4 aX[5], aY[5];
  u32x2 bXl[NB], bXh[NB], bYl[NB], bYh[NB];
  const unsigned m0w = smem_base + wid * 2048;
  const int ut = wid == 7 ? CT3 : wid == 6 ? ST3 : -1;
  const unsigned ve = wid == 7 ? lane * 32 + (blockIdx.x >> 3) * 2048u + 8192u : ((bn & 3) * 4 + (lane >> 4)) * 16384 + bm * 2048 + (lane & 15) * 128;
  unsigned tinc = lane < 14 ? 8 * 229376 : 8192;
  asm volatile("" : "+v"(ta), "+v"(tinc));
#define G3_READ(AR, BL, BH, SLOT, R)                                                                       \
  do {                                                                                                     \
    if ((R) < 2 * NB) {                                                                                    \
      constexpr int jb = ((R) < 2 * NB ? (R) : 0) >> 1;                                                    \
      constexpr int off = (SLOT) * 16384 + (jb / FN) * 8192 + ((R) & 1) * 1024;                            \
      if ((R) & 1) dsr_tr<off>(BH[jb], bad[jb % FN]);                                                       \
      else dsr_tr<off>(BL[jb], bad[jb % FN]);                                                               \
    } else if ((R) < 2 * NB + 4) {                                                                         \
      constexpr int ia = ((R) >= 2 * NB && (R) < 2 * NB + 4) ? (R) - 2 * NB : 0;                           \
      dsr128<(SLOT) * 16384 + ia * 1024>(AR[ia], aad);                                                      \
    } else {                                                                                               \
      if ((SLOT) == 0) cad += cin;                                       \
      dsr128<(SLOT) * 128>(AR[4], cad);                                                                    \
    }                                                                                                      \
  } while (0)
  auto rd = [&](auto SL, auto RR, u32x4 (&AR)[5], u32x2 (&BL)[NB], u32x2 (&BH)[NB]) { G3_READ(AR, BL, BH, SL.value, RR.value); };
#define G3_UNIT(AR, BL, BH, NAR, NBL, NBH, SLOT, U, LI)                                                    \
  do {                                                                                                     \
    WAIT_LGKM0();                                                                                          \
    u32x4 bb[NB];                                                                                          \
    _Pragma("unroll") for (int j = 0; j < NB; ++j) bb[j] = u32x4{BL[j][0], BL[j][1], BH[j][0], BH[j][1]};  \
    SCHED0();                                                                                              \
    asm volatile("s_nop 1");                                                                               \
    static_for<NM>([&](auto N) {                                                                           \
      constexpr int n = N.value;                                                                           \
      if constexpr (n == NM / 2) {                                                                         \
        wait_vm<(LI && (SLOT) < 2 && FN == 3) ? 13 + 4 * (SLOT) : 8>(); __builtin_amdgcn_s_barrier();      \
      }                                                                                                    \
      if constexpr (n == NM / 2 + TPOS && !LI) {                                                           \
          \
        if (((U) & 7) == wid) {                                                                            \
          asm volatile("s_mov_b32 m0, %2\n\ts_mov_b64 exec, %1\n\tglobal_load_lds_dword %0, off\n\ts_mov_b64 exec, -1"      \
                       ::"v"(ta), "s"((U) + WTD < 32 ? 0x3fffffull : 0x3fc000ull), "s"(smem_base + 131072 + 4096) : "memory"); \
          ta += tinc;                                                                                      \
        }                                                                                                  \
        if (FN == 3 && (SLOT) == 0 && (U) == ut) touch_lds(smem_base + 131072 + 4096, ve, rsE, 0);         \
      }                                                                                                    \
      MFMA_AV(acc[n & 3][n >> 2], bb[n >> 2], AR[n & 3]);                                                  \
      if constexpr (LI && (SLOT) == 0 && n < 12 && n % 3 == 0) hook(ic<-n / 3>{});                         \
      if constexpr (LI && (SLOT) == 0 && (n == 15 || n == 21)) hook(ic<-(n + 9) / 6>{});                   \
      if constexpr (LI && (SLOT) == 1 && (n == 0 || n == 3)) hook(ic<-6 - n / 3>{});                       \
      if constexpr (LI && (SLOT) == 2 && n >= 21 && n < 24) hook(ic<n - 8>{});                             \
      if constexpr (n < NM / 2) {                                                                          \
        if constexpr (LI && (SLOT) == 2 && n < 12) hook(ic<n + 1>{});                                      \
        if constexpr (n % 3 == 1 && n / 3 < 4) {                                                           \
          if constexpr (LI && (SLOT) > 0) wst(ic<((SLOT) + 3) & 3>{}, ic<(n / 3) & 3>{});                  \
          else G3_STAGE(((SLOT) + 3) & 3, n / 3, (U) + 3);                                                 \
        }                                                                                                  \
      } else {                                                                                             \
        constexpr int r0 = (n - NM / 2) * 2;                                                               \
        if constexpr (r0 < RD) rd(ic<((SLOT) + 1) & 3>{}, ic<(r0 < RD ? r0 : 0)>{}, NAR, NBL, NBH);        \
        if constexpr (r0 + 1 < RD) rd(ic<((SLOT) + 1) & 3>{}, ic<(r0 + 1 < RD ? r0 + 1 : 0)>{}, NAR, NBL, NBH); \
      }                                                                                                    \
    });                                                                                                    \
    MFMA_AV(accC[0], bb[0], AR[4]);                                                                        \
    MFMA_AV(accC[1], bb[FN], AR[4]);                                                                       \
    SCHED0();                                                                                              \
  } while (0)

  unsigned v0 = wm * 4096 + lane * 16;
#pragma unroll
  for (int j = 0; j < FN; ++j) asm volatile("" : "+v"(bad[j]));
  asm volatile("" : "+v"(aad), "+v"(cad), "+v"(voffA), "+v"(v0) : "s"(rsA), "s"(m0w));
  const poll_t pl = pre();
  static_for<4>([&](auto I) { asm volatile("" : "=v"(aX[I.value])); bload_sc1<I.value * 1024>(aX[I.value], v0, rsA, 0); });
  static_for<2 * NB>([&](auto RR) { rd(ic<0>{}, RR, aX, bXl, bXh); });
  dsr128<0>(aX[4], cad);
  G3_STAGE(1, 0, 1); G3_STAGE(1, 1, 1); G3_STAGE(2, 0, 2); G3_STAGE(2, 1, 2);
#pragma unroll
  for (int i = 0; i < 2; ++i) bload_lds<0>(smem_base + 131072 + 4096, voffA, rsA, 0x40000000u);
  asm volatile("s_waitcnt vmcnt(6)" : "+v"(aX[0]), "+v"(aX[1]), "+v"(aX[2]), "+v"(aX[3])::"memory");
  poll_end(pl);
  for (int u = 0; u < 28; u += 4) {
    G3_UNIT(aX, bXl, bXh, aY, bYl, bYh, 0, u, false);
    G3_UNIT(aY, bYl, bYh, aX, bXl, bXh, 1, u + 1, false);
    G3_UNIT(aX, bXl, bXh, aY, bYl, bYh, 2, u + 2, false);
    G3_UNIT(aY, bYl, bYh, aX, bXl, bXh, 3, u + 3, false);
  }
  G3_UNIT(aX, bXl, bXh, aY, bYl, bYh, 0, 28, true);
  G3_UNIT(aY, bYl, bYh, aX, bXl, bXh, 1, 29, true);
  if (FN == 3) __builtin_amdgcn_s_setprio(1);
  G3_UNIT(aX, bXl, bXh, aY, bYl, bYh, 2, 30, true);
  constexpr float KG = -1.4426950408889634f, KU = -0.6931471805599453f;
  constexpr int EB = 7, RS = 131072, CC = RS + 2048;
  u32x2 rs[4];
  u32x4 cc[2][4];
  unsigned ca[FN], pa, ra, sa;
  int l2;
  auto rdc = [&](auto J, auto E) { dsr128<E.value * 512>(cc[J.value & 1][E.value], ca[J.value]); };
  {
    WAIT_LGKM0();
    u32x4 bb[NB];
#pragma unroll
    for (int j = 0; j < NB; ++j) bb[j] = u32x4{bYl[j][0], bYl[j][1], bYh[j][0], bYh[j][1]};
    SCHED0();
    asm volatile("s_nop 1");
    static_for<NM>([&](auto N) {
      constexpr int n = N.value, k = n - EB;
      if constexpr (n == EB) {
        asm volatile("s_waitcnt lgkmcnt(0)" ::: "memory"); __builtin_amdgcn_s_barrier();
      }
      if constexpr (n == 0) { MFMA_AV(accC[0], bb[0], aY[4]); MFMA_AV(accC[1], bb[FN], aY[4]); }
      MFMA_AV(acc[n & 3][n >> 2], bb[n >> 2], aY[n & 3]);
      if constexpr (n == 0) {
        l2 = fresh_lane();
#pragma unroll
        for (int j = 0; j < FN; ++j) { ca[j] = smem_base + CC + (wn * 4 + (j + rot) % FN) * 64 + (l2 & 48); asm volatile("" : "+v"(ca[j])); }
      }
      if constexpr (n == 1) {
        pa = ca[0] + (l2 & 15) * 512;
        sa = smem_base + QX_SINK + l2 * 16;
        asm volatile("" : "+v"(pa), "+v"(sa), "+v"(l2));
      }
      if constexpr (n == 2) {
        pa = (l2 & 15) < (wm < FN ? 2 : 0) ? pa : sa;
        ra = smem_base + RS + (wm * 64 + (l2 & 15)) * 8;
        asm volatile("" : "+v"(pa), "+v"(ra));
      }
      if constexpr (n == 5) {
        asm volatile("" : "+v"(l2));
        sa = (l2 & 16) * 64 + (l2 & 32) * 8 + (l2 & 15) * 16;
        asm volatile("" : "+v"(sa));
      }
      if constexpr (n % 3 == 1 && n / 3 < 4) wst(ic<2>{}, ic<(n / 3) & 3>{});
      if constexpr (n == 3) {
        asm volatile("" : "+v"(accC[0]), "+v"(accC[1]));
        dsw128<0>(pa, __builtin_bit_cast(u32x4, accC[0] * KG));
        dsw128<1024>(pa, __builtin_bit_cast(u32x4, accC[1] * KU));
      }
      if constexpr (k == 0 || k == 1) { dsr64<k * 256>(rs[2 * k], ra); dsr64<k * 256 + 128>(rs[2 * k + 1], ra); }
      if constexpr (k >= 2 && k < 6) { rdc(ic<(k - 2) / 2>{}, ic<(k & 1) * 2>{}); rdc(ic<(k - 2) / 2>{}, ic<(k & 1) * 2 + 1>{}); }
      if constexpr (k >= 6 && k < 10) wst(ic<3>{}, ic<(k - 6) & 3>{});
    });
    asm volatile("s_nop 7" ::: "memory");
    SCHED0();
  }
  const rsrc_t rsO = mk_rsrc((char*)act + (size_t)(bm * 4 + wm) * (F_DIM / 32) * 4096, F_DIM / 32 * 4096);
  asm volatile("s_waitcnt lgkmcnt(4)" : "+v"(rs[0]), "+v"(rs[1]), "+v"(rs[2]), "+v"(rs[3]), "+v"(cc[0][0]), "+v"(cc[0][1]), "+v"(cc[0][2]), "+v"(cc[0][3])::"memory");
  float rg[4], ru[4];
#pragma unroll
  for (int i = 0; i < 4; ++i) { rg[i] = __uint_as_float(rs[i][1]) * KG; ru[i] = __uint_as_float(rs[i][1]) * KU; }
  auto frag = [&](auto JJ) {
    constexpr int j = JJ.value;
    auto half = [](auto v, int n) { const f32x4 f = __builtin_bit_cast(f32x4, v); return (n & 1) ? hi2(f) : lo2(f); };
    f32x2 g[8], uu[8], e[8];
    u32x4(&c)[4] = cc[j & 1];
#define STAGE(X) do { SCHED0(); _Pragma("unroll") for (int n = 0; n < 8; ++n) { X; } } while (0)
    STAGE(g[n] = __uint_as_float(rs[n >> 1][0]) * half(c[0], n) + half(c[1], n));
    STAGE(g[n] = rg[n >> 1] * half(acc[n >> 1][j], n) + g[n]);
    STAGE(e[n] = (f32x2{__builtin_amdgcn_exp2f(g[n][0]), __builtin_amdgcn_exp2f(g[n][1])}));
    STAGE(uu[n] = __uint_as_float(rs[n >> 1][0]) * half(c[2], n) + half(c[3], n));
    SCHED0();
    if constexpr (j + 2 < FN) static_for<4>([&](auto E) { rdc(ic<j + 2>{}, E); });
    STAGE(e[n] = e[n] + 1.0f);
    STAGE(e[n] = (f32x2{__builtin_amdgcn_rcpf(e[n][0]), __builtin_amdgcn_rcpf(e[n][1])}));
    STAGE(uu[n] = ru[n >> 1] * half(acc[n >> 1][FN + j], n) + uu[n]);
    STAGE(g[n] = g[n] * uu[n]);
    STAGE(g[n] = g[n] * e[n]);
    unsigned pk[8];
    STAGE(pk[n] = pk_bf16(g[n][0], g[n][1]));
    SCHED0();
    const int J = (bn >> 4) * 112 + (FN == 4 ? 48 : 0) + (bn & 15) * FN + (j + rot) % FN;
    static_for<2>([&](auto I) {
      constexpr int i = I.value * 4;
      const auto s0 = __builtin_amdgcn_permlane16_swap(pk[i], pk[i + 2], false, false);
      const auto s1 = __builtin_amdgcn_permlane16_swap(pk[i + 1], pk[i + 3], false, false);
      bstore_sc1<I.value * 2048>(u32x4{s0[0], s1[0], s0[1], s1[1]}, sa, rsO, (unsigned)((J >> 1) * 4096 + (J & 1) * 512));
    });
    if constexpr (j + 1 < FN) {
      u32x4(&d)[4] = cc[(j + 1) & 1];
      asm volatile("s_waitcnt lgkmcnt(%4)" : "+v"(d[0]), "+v"(d[1]), "+v"(d[2]), "+v"(d[3]) : "n"(j + 2 < FN ? 4 : 0) : "memory");
    }
    SCHED0();
  };
  static_for<FN>(frag);
}

namespace g4 {
constexpr int NU = 14, SL = 4, AD = 3;
constexpr int CN = X_CN;
constexpr int GT = 131072 + 5120;
constexpr int CU = 1;
constexpr int TU = NU - SL;
constexpr int PX = 137216;
constexpr int XP = 30, X3 = 16;
constexpr int na(int mh, int t) { return t < (mh ? TU - 2 : TU + 1) ? 4 : 0; }
constexpr int nb(int mh, int t) { return t < 0 || t >= TU ? 0 : t == TU - 1 && !mh ? 8 : 4 + (t == (mh ? CU : 0)); }
constexpr int behind(int mh, int t0, int u, int b) {
  int s = b ? na(mh, u) : 0;
  for (int t = t0 + 1; t < u; ++t) s += na(mh, t) + nb(mh, t);
  return s;
}
constexpr int vm(int mh, int u, int b) {
  if (mh && u >= TU) return u == TU ? 18 - 2 * b : u == TU + 1 ? 8 : 4 * (NU - 1 - u) + 2 - 2 * b;
  const int wa = u < NU - 2 ? nb(mh, u - AD + 1) + behind(mh, u - AD + 1, u, b) : behind(mh, u + 1 - SL, u, b);
  const int wb = u + 1 < SL ? 63 : u < TU - 1 ? behind(mh, u + 1 - SL, u, b) : u == TU - 1 ? behind(mh, u + 2 - SL, u, b) : u == TU + 1 ? behind(mh, TU - 1, u, b) : 63;
  return b == 0 ? wa + 2 : (wb < wa ? wb : wa);
}
constexpr int as(int mh, int v) { return v < NU - 2 ? v % AD : (v == NU - 2) == !mh ? AD : (TU - 1) % AD; }
}

template <int MH, class WST, class PRE, class EXTRA>
DI void g4_loop(f32x4 (&acc)[2][8], unsigned smem_base, int lane, int wid, int kb0, rsrc_t rsA, unsigned* fb, WST ldb, PRE pre, EXTRA extra) {
  using namespace g4;
  const int fq = lane >> 4, k2 = wid & 1;
  unsigned voffA[2], bad[8];
#pragma unroll
  for (int i = 0; i < 2; ++i) voffA[i] = lane * 16 + ((wid & 3) ^ i) * 1024;
  {
    const int q = (lane >> 2) & 3, r = 8 * fq + q, sw = q | ((fq & 1) << 2);
#pragma unroll
    for (int f = 0; f < 8; ++f) bad[f] = smem_base + k2 * 65536 + r * 256 + ((f ^ sw) * 32) + 8 * (lane & 3);
  }
  const unsigned sA = (unsigned)(kb0 + 2 * k2) * 4096u;
  u32x4 af[AD + 1][4];
  u32x2 bl[2][8], bh[2][8];
  auto lda = [&](auto V, auto J) {
    constexpr int v = V.value, j = J.value;
    asm volatile("" : "=v"(af[as(MH, v)][j]));
    bload_sc1<0>(af[as(MH, v)][j], voffA[j & 1], rsA, sA + (4 * v + (j >> 1)) * 4096u);
  };
  auto rdb = [&](auto V, auto B, auto E) {
    constexpr int o = (V.value & 3) * 16384 + B.value * 8192, f = E.value >> 1;
    if constexpr (E.value & 1) dsr_tr<o + 1024>(bh[B.value][f], bad[f]);
    else dsr_tr<o>(bl[B.value][f], bad[f]);
  };
  const unsigned wo = wid >> 1 == 1 ? 8192u : 65536u;
  unsigned wp = smem_base + (wid >> 1 == 1 ? PX : 32768 + (wid >> 1) * 8192) + k2 * wo + lane * 16;
#pragma unroll
  for (int f = 0; f < 8; ++f) asm volatile("" : "+v"(bad[f]));
  asm volatile("" : "+v"(voffA[0]), "+v"(voffA[1]), "+v"(wp) : "s"(rsA), "s"(sA));
  unsigned cv = 0, rp, fa, fb2, one = 1;
  const unsigned wf = smem_base + CN + wid * 4;
  const poll_t pl = pre();
  if (MH == 1) __builtin_amdgcn_s_setprio(0);
  static_for<4 * (AD - 1)>([&](auto J) { lda(ic<J.value / 4>{}, ic<J.value % 4>{}); });
  static_for<16>([&](auto E) { rdb(ic<0>{}, ic<0>{}, E); });
  static_for<NU>([&](auto U) {
    constexpr int u = U.value;
    constexpr bool last = u == NU - 1;
    static_for<32>([&](auto N) {
      constexpr int n = N.value, b = last ? n >= 24 || (n >= 12 && n < 20) : n >> 4, m = last ? (n == 12 ? 0 : n) : n & 15;
      constexpr int f = last ? (n >= 12 && n < 20 ? n - 12 : n & 7) : m >> 1, i = last ? n < 8 || (n >= 12 && n < 20) : m & 1;
      if constexpr (MH == 1 && u == TU && n == 0) {
        asm volatile("s_waitcnt vmcnt(0) lgkmcnt(0)\n\ts_barrier" ::: "memory");
        static_for<16>([&](auto J) {
          if constexpr (J.value == 8) extra(ic<-1>{});
          lda(ic<TU + J.value / 4>{}, ic<J.value % 4>{});
        });
        __builtin_amdgcn_s_barrier();
      }
      if constexpr (MH ? u == TU && n == X3 : last && n == XP) __builtin_amdgcn_s_barrier();
      if constexpr (m == 0) {
        asm volatile("s_waitcnt vmcnt(%2) lgkmcnt(0)" : "+v"(af[as(MH, u)][2 * b]), "+v"(af[as(MH, u)][2 * b + 1])
                     : "n"(vm(MH, u, b)) : "memory");
        if constexpr (u == 0 && b == 0 && MH == 1) poll_end(pl);
#pragma unroll
        for (int g = 0; g < 8; ++g) asm volatile("" : "+v"(bl[b][g]), "+v"(bh[b][g]));
        if constexpr (b == 1 && (u < TU || (u == TU + 1 && !MH))) __builtin_amdgcn_s_barrier();
        if constexpr (b == 1 && u == TU - 1 && MH == 0) __builtin_amdgcn_s_setprio(2);
      }
      const u32x4 bb = {bl[b][f][0], bl[b][f][1], bh[b][f][0], bh[b][f][1]};
      if constexpr (u == 0 && b == 0) asm volatile("v_mfma_f32_16x16x32_bf16 %0, %1, %2, 0" : "=&v"(acc[i][f]) : "v"(bb), "v"(af[0][i]));
      else MFMA_AV(acc[i][f], bb, af[as(MH, u)][2 * b + i]);
      if constexpr (m < 8 && !(last && b)) {
        rdb(ic<u + b>{}, ic<1 - b>{}, ic<2 * m>{});
        rdb(ic<u + b>{}, ic<1 - b>{}, ic<2 * m + 1>{});
      }
      if constexpr ((m & 3) == 2) {
        if constexpr (b == 0 && na(MH, u) == 4) lda(ic<(u < TU ? u + AD - 1 : NU - 1)>{}, ic<(m >> 2)>{});
        if constexpr (b == 1 && u + SL < NU) ldb(ic<u + SL>{}, ic<(m >> 2)>{});
      }
      if constexpr (MH == 0 && u == TU - 1 && b == 1 && (m & 3) == 0) lda(ic<NU - 2>{}, ic<(m >> 2)>{});
      if constexpr (MH == 0 && u == 0 && n == 0)
        asm volatile("ds_add_rtn_u32 %0, %1, %2" : "=v"(cv) : "v"(smem_base + CN + 256 + lane * 4), "v"(1u) : "memory");
      if constexpr (MH == 0 && u == 0 && n == 20) {
        asm volatile("" : "+v"(cv));
        signal(sig_t{mk_rsrc(fb, __builtin_amdgcn_readfirstlane(cv) == 3u ? 3 * 256 + 4 : 0), (unsigned)(lane * 256)});
      }
      if constexpr (MH == 1 && u == CU && n == 31)
        asm volatile("buffer_atomic_add %0, %1, %2, 0 offen sc0 sc1" : "+v"(cv) : "v"(lane * 128), "s"(mk_rsrc(fb, wid == 4 ? 256 : 0)) : "memory");
      if constexpr (MH == 1 && u == 6 - AD && n == 8) {
        asm volatile("s_waitcnt vmcnt(%1)" : "+v"(cv) : "n"(behind(1, CU, u, 0) + 2) : "memory");
        if (__builtin_expect(((unsigned)__builtin_amdgcn_ballot_w64(cv < 8u) & (wid == 4 ? 3u : 0u)) != 0u, 0)) {
          const poll_t p = poll(0u, fb, lane, 2, 8u);
          wait_vm<0>();
          poll_end(p);
        }
      }
      if constexpr (u >= TU) extra(ic<(u - TU) * 32 + n>{});
      if constexpr (MH && u == TU + 1 && n == 24) dsw32<64>(wf, 1u);
      if constexpr (MH && u == NU - 2 && n == 24) dsr32<64>(cv, wf ^ 8u);
      if constexpr (MH && last && n == 9) {
        asm volatile("" : "+v"(cv));
        if (__builtin_expect(__builtin_amdgcn_readfirstlane(cv) == 0u, 0)) lds_wait<1>((wf ^ 8u) + 64, 1u);
      }
      if constexpr (last && n == 10) {
        rp = wp + wo - k2 * 2 * wo; fa = wf; fb2 = wf ^ 4u;
        asm volatile("" : "+v"(rp), "+v"(fa), "+v"(fb2), "+v"(one));
      }
      if constexpr (last && n >= 15 && n < 31 && (n & 1)) dsw128<(n - 15) / 2 * 1024>(wp, __builtin_bit_cast(u32x4, acc[1][(n - 15) / 2 & 7]));
      if constexpr (last && n == 29) dsw32<0>(fa, one);
    });
  });
  u32x4 pq[8];
  unsigned fl;
  auto rdq = [&] {
    dsr32<0>(fl, fb2);
    static_for<8>([&](auto F) { dsr128<F.value * 1024>(pq[F.value], rp); });
  };
  auto there = [&] {
    asm volatile("s_waitcnt lgkmcnt(0)" : "+v"(fl), "+v"(pq[0]), "+v"(pq[1]), "+v"(pq[2]), "+v"(pq[3]), "+v"(pq[4]), "+v"(pq[5]), "+v"(pq[6]), "+v"(pq[7])::"memory");
    return __builtin_amdgcn_readfirstlane(fl) != 0u;
  };
  rdq();
  extra(ic<128>{});
  if (!there()) do rdq(); while (!there());
#pragma unroll
  for (int f = 0; f < 8; ++f) {
    asm volatile("" : "+v"(acc[0][f]));
    acc[0][f] += __builtin_bit_cast(f32x4, pq[f]);
  }
}

template <int MH, class WST>
DI void g4_phase(unsigned smem_base, int wid, const bf16* At, WST wst, const bf16* Yt,
                 bf16* __restrict__ C, float* xbuf, unsigned* flags) {
  using namespace g4;
  constexpr int KB = F_DIM / 32;
  const int lane = fresh_lane();
  constexpr int mh = MH;
  const int kq = wid & 3;
  const int xcd = blockIdx.x & 7, idx = blockIdx.x >> 3;
  const int kh = xcd & 1, bm = ((xcd >> 1) & 1) * 8 + (idx & 7), bn = (xcd >> 2) * 4 + (idx >> 3);
  const unsigned partner = blockIdx.x ^ 1;
  const int n0 = bn * 128, bm64 = bm * 2 + kh;
  const int fr = lane & 15, fq = lane >> 4;
  const unsigned voffL = lane * 16;
  const rsrc_t rsA = mk_rsrc((const char*)At + (size_t)(bm64 ^ mh ^ 1) * KB * 4096, KB * 4096);
  const rsrc_t rsEx = mk_rsrc(xbuf, 256 * 32768);
  const int kb0 = kh * (KB / 2);
  f32x4 acc[2][8];
  const int fo = ((idx & 7) * 2 + (xcd >> 2)) * 8 + (xcd & 1), fi = ((bm >> 1) * 2 + kh) * 8 + (idx >> 3) * 2;
  const unsigned pb = smem_base + (kq & 2 ? PX + (~kq & 1) * 8192 : 32768 + (~kq & 1) * 65536);
  unsigned pf = smem_base + CN + kq * 4;
  if constexpr (mh == 0) {
    g4_loop<0>(acc, smem_base, lane, wid, kb0, rsA, flags + (FL_B + fo) * 32, wst,
               [&] { wait_vm<8>(); __builtin_amdgcn_s_barrier(); return poll_t{0u, 0u}; }, [](auto) {});
    unsigned one = 1;
    poll_t pl = {0u, 0u};
    asm volatile("" : "+v"(pf), "+v"(one), "+v"(pl.a), "+v"(pl.b));
    static_for<4>([&](auto F) {
      constexpr int f = F.value;
      bstore_sc1<f * 1024>(pack8(acc[0][2 * f], acc[0][2 * f + 1]), voffL, rsEx, blockIdx.x * 32768u + kq * 8192u);
    });
    __builtin_amdgcn_s_barrier();
    __builtin_amdgcn_s_setprio(0);
    asm volatile("s_waitcnt vmcnt(0)\n\tbuffer_atomic_add %0, %1, %2, 0 offen sc1" ::"v"(one), "v"(voffL), "s"(mk_rsrc(flags + (FL_P + blockIdx.x) * 32 + kq, 4)) : "memory");
    unsigned spins = 0, t;
    asm volatile("buffer_atomic_add %0, %4, %5, 0 offen sc0 sc1\n\ts_sleep %6\n1:\n\t"
                 "buffer_atomic_add %1, %4, %5, 0 offen sc0 sc1\n\ts_waitcnt vmcnt(1)\n\tv_readfirstlane_b32 %3, %0\n\ts_nop 0\n\ts_cmp_lg_u32 %3, 0\n\ts_cbranch_scc1 2f\n\t"
                 "buffer_atomic_add %0, %4, %5, 0 offen sc0 sc1\n\ts_waitcnt vmcnt(1)\n\tv_readfirstlane_b32 %3, %1\n\ts_nop 0\n\ts_cmp_lg_u32 %3, 0\n\ts_cbranch_scc1 2f\n\t"
                 "s_add_u32 %2, %2, 1\n\ts_cmp_lt_u32 %2, 0x80000\n\ts_cbranch_scc1 1b\n2:"
                 : "+v"(pl.a), "+v"(pl.b), "+s"(spins), "=&s"(t) : "v"(voffL), "s"(mk_rsrc(flags + (FL_P + partner) * 32 + kq, 4)), "n"(PSLP) : "memory", "scc");
    static_for<4>([&](auto F) { bload_lds_sc1<F.value * 1024>(pb, voffL, rsEx, partner * 32768u + kq * 8192u); });
    wait_vm<0>();
    dsw32<128>(pf, one);
    poll_end(pl);
  } else {
  u32x4 yv[4];
  const rsrc_t rsX = mk_rsrc(Yt, S_LEN * D_DIM * 2);
  unsigned vx = (unsigned)(((bm64 >> 2) * 32 + bn * 4) * 16384 + ((bm64 & 3) * 4 + kq) * 1024 + ((((fq & 1) << 1) | (fq >> 1)) * 16 + fr) * 16);
  const unsigned oa = arrive<4, 4, 256>(smem_base + CN + 512, lane, flags + (FL_A + fo) * 32);
  asm volatile("" : "+v"(vx));
  u32x4 pv[4], gt[8], ct[2][2];
  f32x4 xr[8];
  unsigned ga = smem_base + GT + fq * 16, pa = pb + voffL;
  char* cp;
  unsigned fl;
  auto rdp = [&] {
    dsr32<128>(fl, pf);
    static_for<4>([&](auto F) { dsr128<F.value * 1024>(pv[F.value], pa); });
  };
  g4_loop<1>(acc, smem_base, lane, wid, kb0, rsA, flags + (FL_B + fi) * 32, wst, [&] { return await(oa, lane, flags + (FL_A + fi) * 32, 2, 8u); },
             [&](auto T) {
    constexpr int t = T.value, h = t >> 4, m = t & 15, c = h - 2;
    if constexpr (t < 0) {
      static_for<4>([&](auto J) { asm volatile("" : "=v"(yv[J.value])); bload_sc1<0>(yv[J.value], vx, rsX, J.value * 16384); });
      asm volatile("" : "+v"(ga), "+v"(pa), "+v"(pf));
    } else if constexpr (t == 128) rdp();
    else {
      if constexpr (t == X3) __builtin_amdgcn_s_setprio(1);
      if constexpr (c >= 0 && c < 4 && m == 7) {
        asm volatile("" : "+v"(yv[c]), "+v"(ct[c & 1][0]), "+v"(ct[c & 1][1]));
        const auto x0 = __builtin_amdgcn_permlane16_swap(yv[c][0], yv[c][2], false, false);
        const auto x1 = __builtin_amdgcn_permlane16_swap(yv[c][1], yv[c][3], false, false);
        yv[c] = u32x4{x0[0], x1[0], x0[1], x1[1]};
        asm volatile("" : "+v"(yv[c]));
      }
      if constexpr (c >= 0 && c < 4 && m >= 8) {
        constexpr int f = (m >> 1) & 1, e = (m & 1) * 2;
        if constexpr (m < 12) asm volatile("v_lshlrev_b32 %0, 16, %2\n\tv_and_b32 %1, 0xffff0000, %2" : "=&v"(xr[2 * c + f][e]), "=v"(xr[2 * c + f][e + 1]) : "v"(yv[c][m & 3]));
        else asm volatile("v_mul_f32 %0, %0, %2\n\tv_mul_f32 %1, %1, %3" : "+v"(xr[2 * c + f][e]), "+v"(xr[2 * c + f][e + 1]) : "v"(ct[c & 1][f][e]), "v"(ct[c & 1][f][e + 1]));
      }
      if constexpr (h >= 1 && h < 5 && (m == 8 || m == 9)) dsr128<512 + (2 * h + m - 10) * 64>(ct[(h - 1) & 1][m - 8], ga);
      if constexpr (t >= 108 && t < 124 && !(t & 1)) dsr128<(t - 108) * 32>(gt[(t - 108) / 2], ga);
      if constexpr (t == 20) {
        cp = (char*)C + (size_t)(bm64 * 64 + kq * 16 + (fr & 7)) * (D_DIM * 2) + n0 * 2 + (((fq & 1) << 1) | (fq >> 1)) * 16 + (fr >> 3) * 64;
        asm volatile("" : "+v"(cp));
      }
    }
  });
  asm volatile("" : "+v"(fl), "+v"(pv[0]), "+v"(pv[1]), "+v"(pv[2]), "+v"(pv[3]));
#pragma unroll
  for (int f = 0; f < 8; ++f) {
    asm volatile("" : "+v"(gt[f]));
    xr[f] += __builtin_bit_cast(f32x4, gt[f]) * acc[0][f];
    asm volatile("" : "+v"(xr[f]));
  }
  if (__builtin_amdgcn_readfirstlane(fl) == 0u) do {
    rdp();
    asm volatile("s_waitcnt lgkmcnt(0)" : "+v"(fl), "+v"(pv[0]), "+v"(pv[1]), "+v"(pv[2]), "+v"(pv[3])::"memory");
  } while (__builtin_amdgcn_readfirstlane(fl) == 0u);
  static_for<2>([&](auto L) {
    constexpr int l = L.value;
    u32x4 ov[2];
#pragma unroll
    for (int c = 0; c < 2; ++c) {
      const int f = 4 * l + 2 * c;
      const u32x4 p = pv[f >> 1];
      const f32x4 o0 = xr[f] + __builtin_bit_cast(f32x4, gt[f]) * f32x4{bf_lo(p[0]), bf_hi(p[0]), bf_lo(p[1]), bf_hi(p[1])};
      const f32x4 o1 = xr[f + 1] + __builtin_bit_cast(f32x4, gt[f + 1]) * f32x4{bf_lo(p[2]), bf_hi(p[2]), bf_lo(p[3]), bf_hi(p[3])};
      const auto s0 = __builtin_amdgcn_permlane16_swap(pk_bf16(o0[0], o0[1]), pk_bf16(o1[0], o1[1]), false, false);
      const auto s1 = __builtin_amdgcn_permlane16_swap(pk_bf16(o0[2], o0[3]), pk_bf16(o1[2], o1[3]), false, false);
      ov[c] = u32x4{s0[0], s1[0], s0[1], s1[1]};
    }
    u32x4 lo, hi;
#pragma unroll
    for (int e = 0; e < 4; ++e) {
      lo[e] = __builtin_amdgcn_update_dpp(ov[0][e], ov[1][e], 0x128, 0xf, 0xc, false);
      hi[e] = __builtin_amdgcn_update_dpp(ov[1][e], ov[0][e], 0x128, 0xf, 0x3, false);
    }
    gst128(cp + l * 128, lo);
    gst128(cp + 8 * (D_DIM * 2) + l * 128, hi);
  });
}
}

__global__ __launch_bounds__(512, 2) void block_kernel(char* ws, const bf16* X, const bf16* cnd, const bf16* wmod, const bf16* Wq,
                                                       const bf16* Wo, const bf16* Wg, const bf16* Wu, const bf16* Wd, bf16* C) {
  __shared__ __attribute__((aligned(1024))) char smem[Q_LDS];
  unsigned long long pc;
  asm volatile("s_getpc_b64 %0" : "=s"(pc));
  asm volatile("s_cmp_ge_u32 %0, 9\n\ts_cbranch_scc1 1f\n\ts_mov_b32 m0, %1\n\ts_nop 0\n\tbuffer_load_dword %2, %3, 0 offen lds\n1:"
               ::"s"((blockIdx.x >> 3) + __builtin_amdgcn_readfirstlane(threadIdx.x ^ 64)), "s"((unsigned)(uintptr_t)smem + QX_SINK), "v"((threadIdx.x & 63) * 128 + (blockIdx.x >> 3) * 8192),
                 "s"(mk_rsrc((const void*)(uintptr_t)(pc & ~255ull), CODEPF * 1024)) : "memory", "scc");
  asm volatile("" : "+s"(ws) : "s"(X), "s"(cnd), "s"(wmod), "s"(Wq), "s"(Wo), "s"(Wg), "s"(Wu), "s"(Wd), "s"(C));
  float* modp = (float*)ws;
  unsigned* ctab = (unsigned*)(ws + 212992);
  unsigned* flags = (unsigned*)(ws + 262144);
  float* stat = (float*)(ws + 524288);
  bf16* O = (bf16*)(ws + (1 << 20));
  bf16* Y = (bf16*)(ws + (9 << 20));
  bf16* H = (bf16*)(ws + (13 << 20));
  bf16* act = (bf16*)(ws + (17 << 20));
  float* xbuf = (float*)(ws + (32 << 20));
  const int wid = __builtin_amdgcn_readfirstlane(threadIdx.x >> 6);
  const int wm = wid & 3, wn = wid >> 2;
  const int xcd = blockIdx.x & 7, idx = blockIdx.x >> 3;
  const int bm = idx & 7, bn = xcd * 4 + (idx >> 3);
  const unsigned smem_base = (unsigned)(uintptr_t)smem;
  const rsrc_t rsA = mk_rsrc((const char*)Y + (size_t)bm * 32 * 16384, 32 * 16384);
  const rsrc_t rsB = mk_rsrc(wn ? Wu : Wg, D_DIM * F_DIM * 2);
  const rsrc_t rsE = wid == 7 ? mk_rsrc((const void*)(uintptr_t)(pc & ~255ull), CODEPF * 1024) : mk_rsrc(stat, S_LEN * 128);
  const unsigned m0w = smem_base + wid * 2048;
  unsigned voffB0 = 0, voffB1 = 0;
  attn_phase(smem_base, wid, H, Wq, O, Wo, X, modp, Y, stat, flags, Wg, Wu, ctab, cnd, wmod, [&](auto S, auto I) {
    if constexpr (S.value == 0) {
      const int l2 = fresh_lane();
      const int r = (wm * 2 + I.value) * 4 + (l2 >> 4);
      const int sw = (r & 3) | (((r >> 3) & 1) << 2);
      const int c16 = l2 & 15;
      const int vb = (((bn & 3) * 224) & ~63) + ((c16 >> 1) ^ sw) * 32 + (c16 & 1) * 16;
      (I.value ? voffB1 : voffB0) = (unsigned)(r * (F_DIM * 2) + g3_line(bn >> 2, vb >> 7) * 128 + (vb & 127));
    }
    bload_lds<0>(m0w + S.value * 16384 + I.value * 1024, I.value ? voffB1 : voffB0, rsB, S.value * 229376u);
  });
  __builtin_amdgcn_s_setprio(0);
  const int lane = fresh_lane();
  const unsigned voffA = wid * 2048 + lane * 16;
  const unsigned oy = arrive<8, 8, 512>(smem_base + X_CN + 768, lane, flags + (FL_Y + xcd * 32 + (idx & 3)) * 32);
  auto pl = [&] { return await(oy, lane, flags + (FL_Y + bm * 32 + xcd * 4) * 32, 4, 8u); };
  const int n0 = ((xcd >> 2) * 4 + (idx >> 3)) * 128;
  const char* ta = lane >= 14 ? (const char*)Wd + blockIdx.x * 28672 + wid * 1024 + ((lane - 14) & 7) * 128
                              : (const char*)(lane < 7 ? Wg : Wu) + ((wid + WTD) * 32 + idx) * (F_DIM * 2) + xcd * 896 + (lane < 7 ? lane : lane - 7) * 128;
  unsigned vo = 0;
  const unsigned fsel = 0x80000u | (((xcd & 1) * 2 + (wm >> 1)) * 8);
  auto wsn = [&](auto WN, auto V, auto J) {
    constexpr int v = V.value, j = J.value, p = (4 * v + 2 * (j >> 1) + WN.value) * 2;
    if constexpr (v == 0 && j == 0) {
      const int l2 = fresh_lane();
      vo = (wid & 1) * 16384 + (l2 >> 4) * 2048 + ((((l2 & 15) >> 1) ^ ((l2 >> 4) | ((wid & 1) << 2))) * 32) + (l2 & 1) * 16;
    }
    unsigned so;
    asm volatile("s_bfe_u32 %0, %1, %2\n\ts_lshl_b32 %0, %0, 15\n\ts_or_b32 %0, %0, %3" : "=s"(so)
                 : "n"(act_frag(0, p) | act_frag(0, p + 1) << 8 | act_frag(1, p) << 16 | act_frag(1, p + 1) << 24), "s"(fsel), "n"((j & 1) * 8192) : "scc");
    bload_lds<0>(m0w + (j >> 1) * 65536 + (v & 3) * 16384 + (j & 1) * 1024, vo, mk_rsrc((const char*)Wd + n0 * 2, F_DIM * D_DIM * 2 - n0 * 2), so);
  };
  if (wn == 0) {
    auto wst = [&](auto V, auto J) { wsn(ic<0>{}, V, J); };
    g3_body<4>(smem_base, wm, wn, wid, lane, bm, bn, rsA, rsB, rsE, ta, voffA, voffB0, voffB1, act, wst, [](auto) {}, pl);
    g4_phase<0>(smem_base, wid, act, wst, Y, C, xbuf, flags);
  } else {
    auto wst = [&](auto V, auto J) { wsn(ic<1>{}, V, J); };
    u32x4 tv[8];
    f32x4 ps;
    float q0 = 0, q1 = 0;
    unsigned vs, gv;
    g3_body<3>(smem_base, wm, wn, wid, lane, bm, bn, rsA, rsB, rsE, ta, voffA, voffB0, voffB1, act, wst, [&](auto K) {
      constexpr int k = K.value;
      if constexpr (k <= 0) {
        if constexpr (k == 0) {
          const int l2 = fresh_lane();
          asm volatile("" : "=v"(gv));
          bload1_sc1(gv, (unsigned)((n0 + (wm & 1) * 64 + l2) * 4), mk_rsrc(modp + 49152 + (wm >= 2 ? 2048 : 3072), 4096));
          vs = (l2 >> 5) * 131072 + (bm * 256 + wm * 64) * 8 + (l2 & 31) * 16;
        }
        asm volatile("" : "=v"(tv[-k]));
        bload_sc1<0>(tv[-k], vs, mk_rsrc(stat, S_LEN * 128), -k * 16384);
      } else if constexpr (k <= 4) {
        if constexpr (k == 1) wait_vm<3>();
        asm volatile("" : "+v"(tv[2 * k - 2]), "+v"(tv[2 * k - 1]));
        const f32x4 t = __builtin_bit_cast(f32x4, tv[2 * k - 2]) + __builtin_bit_cast(f32x4, tv[2 * k - 1]);
        ps = k == 1 ? t : ps + t;
        asm volatile("" : "+v"(ps));
      } else if constexpr (k == 5) {
        const auto s0 = __builtin_amdgcn_permlane32_swap(__float_as_uint(ps[0]), __float_as_uint(ps[2]), false, false);
        const auto s1 = __builtin_amdgcn_permlane32_swap(__float_as_uint(ps[1]), __float_as_uint(ps[3]), false, false);
        ps[0] = (__uint_as_float(s0[0]) + __uint_as_float(s0[1])) * (1.0f / 16.0f);
        ps[1] = __uint_as_float(s1[0]) + __uint_as_float(s1[1]);
        const auto m = __builtin_amdgcn_permlane32_swap(__float_as_uint(ps[0]), __float_as_uint(ps[0]), false, false);
        ps[2] = __uint_as_float(m[0]); ps[3] = __uint_as_float(m[1]);
        asm volatile("" : "+v"(ps), "+v"(q0), "+v"(q1));
      } else if constexpr (k <= 13) {
        float d0, d1;
        asm volatile("v_sub_f32 %0, %4, %6\n\tv_sub_f32 %1, %5, %7\n\tv_fmac_f32 %2, %0, %0\n\tv_fmac_f32 %3, %1, %1"
                     : "=&v"(d0), "=&v"(d1), "+v"(q0), "+v"(q1) : "v"(tv[k - 6][0]), "v"(tv[k - 6][2]), "v"(ps[2]), "v"(ps[3]));
        if constexpr (k == 13) asm volatile("s_nop 1" : "+v"(q0), "+v"(q1));
      } else if constexpr (k == 14) {
        const auto q = __builtin_amdgcn_permlane32_swap(__float_as_uint(q0), __float_as_uint(q1), false, false);
        const float rstd = __builtin_amdgcn_rsqf((ps[1] + (__uint_as_float(q[0]) + __uint_as_float(q[1])) * (1.0f / 64.0f)) * (1.0f / D_DIM) + 1e-5f);
        const int l2 = fresh_lane();
        asm volatile("ds_write_b64 %0, %1" ::"v"(smem_base + 131072 + wm * 512 + (l2 & 31) * 16 + (l2 >> 5) * 8), "v"(f32x2{ps[0] * (-1.0f / 64.0f) * rstd, rstd}) : "memory");
      } else {
        asm volatile("" : "+v"(gv));
        dsw32<0>(smem_base + g4::GT + (wm * 64 + fresh_lane()) * 4, wm >= 2 ? __float_as_uint(__builtin_amdgcn_rcpf(__uint_as_float(gv))) : gv);
      }
    }, pl);
    g4_phase<1>(smem_base, wid, act, wst, Y, C, xbuf, flags);
  }
}

extern "C" void kernel_launch(void* const* d_in, const int* in_sizes, int n_in, void* d_out, int out_size,
                              void* d_ws, size_t ws_size, hipStream_t stream) {
  const bf16* x = (const bf16*)d_in[0];
  const bf16* wqkv = (const bf16*)d_in[1];
  const bf16* wo = (const bf16*)d_in[2];
  const bf16* wg = (const bf16*)d_in[4];
  const bf16* wu = (const bf16*)d_in[5];
  const bf16* wd = (const bf16*)d_in[6];
  const bf16* c = (const bf16*)d_in[7];
  const bf16* wmod = (const bf16*)d_in[8];
  bf16* out = (bf16*)d_out;

  block_kernel<<<256, 512, 0, stream>>>((char*)d_ws, x, c, wmod, wqkv, wo, wg, wu, wd, out);
}
```

```cpp
#define TSH 20
#include <hip/hip_runtime.h>
#include <hip/hip_bf16.h>
#include <stdint.h>

typedef __hip_bfloat16 bf16;
typedef __attribute__((ext_vector_type(8))) short bf16x8;
typedef __attribute__((ext_vector_type(4))) float f32x4;
typedef __attribute__((ext_vector_type(2))) float f32x2;
typedef __attribute__((ext_vector_type(4))) unsigned u32x4;
typedef __attribute__((ext_vector_type(2))) unsigned u32x2;
typedef __attribute__((ext_vector_type(2))) __bf16 bf16x2_t;
typedef __amdgpu_buffer_rsrc_t rsrc_t;

#define DI __device__ __forceinline__
constexpr int CODEPF = 68;

constexpr int S_LEN = 2048;
constexpr int D_DIM = 1024;
constexpr int F_DIM = 3584;
constexpr int QBAR = 2;
constexpr int NP1 = 5;
constexpr int PSLP = 4;
constexpr int NDLY = 20, TDLY = 15;
constexpr int TS = 12, TP = 6;
constexpr int WOS = 6;
constexpr int GDEP = 12, GS0 = 28;
constexpr int TPOS = 6, WTD = 6, CT3 = 24, ST3 = 16;

template <int V> struct ic { static constexpr int value = V; };
template <int N, int I = 0, class F>
DI void static_for(F&& f) {
  if constexpr (I < N) { f(ic<I>{}); static_for<N, I + 1>(f); }
}

DI float bf_lo(unsigned u) { return __uint_as_float(u << 16); }
DI float bf_hi(unsigned u) { return __uint_as_float(u & 0xffff0000u); }
DI unsigned pk_bf16(float lo, float hi) {
  const f32x2 v = {lo, hi};
  return __builtin_bit_cast(unsigned, __builtin_convertvector(v, bf16x2_t));
}
DI rsrc_t mk_rsrc(const void* p, unsigned bytes) { return __builtin_amdgcn_make_buffer_rsrc((void*)p, (short)0, (int)bytes, 0x00020000); }

template <int N>
DI void wait_vm() { asm volatile("s_waitcnt vmcnt(%0)" ::"n"(N > 63 ? 63 : N) : "memory"); }
#define WAIT_LGKM0() do { asm volatile("s_waitcnt lgkmcnt(0)" ::: "memory"); __builtin_amdgcn_sched_barrier(0); } while (0)
#define SCHED0() __builtin_amdgcn_sched_barrier(0)

#define MFMA_AV(C, B, A) asm volatile("v_mfma_f32_16x16x32_bf16 %0, %1, %2, %0" : "+v"(C) : "v"(B), "v"(A))
template <int OFF>
DI void bload_v(u32x4& d, unsigned voff, rsrc_t rs, unsigned soff) {
  asm volatile("buffer_load_dwordx4 %0, %1, %2, %3 offen offset:%4" : "+v"(d) : "v"(voff), "s"(rs), "s"(soff), "i"(OFF) : "memory");
}
template <int OFF>
DI void bload_lds(unsigned m0v, unsigned voff, rsrc_t rs, unsigned soff) {
  asm volatile("s_mov_b32 m0, %0\n\ts_nop 0\n\tbuffer_load_dwordx4 %1, %2, %3 offen offset:%4 lds" ::"s"(m0v), "v"(voff), "s"(rs), "s"(soff), "i"(OFF) : "memory");
}
DI void touch_lds(unsigned m0v, unsigned voff, rsrc_t rs, unsigned soff) {
  asm volatile("s_mov_b32 m0, %0\n\ts_nop 0\n\tbuffer_load_dword %1, %2, %3 offen lds" ::"s"(m0v), "v"(voff), "s"(rs), "s"(soff) : "memory");
}
template <int OFF>
DI void bload_lds_sc1(unsigned m0v, unsigned voff, rsrc_t rs, unsigned soff) {
  asm volatile("s_mov_b32 m0, %0\n\ts_nop 0\n\tbuffer_load_dwordx4 %1, %2, %3 offen offset:%4 sc1 lds" ::"s"(m0v), "v"(voff), "s"(rs), "s"(soff), "i"(OFF) : "memory");
}
template <int OFF>
DI void dsr128(u32x4& d, unsigned addr) { asm volatile("ds_read_b128 %0, %1 offset:%2" : "=v"(d) : "v"(addr), "i"(OFF) : "memory"); }
template <int OFF>
DI void dsr32(unsigned& d, unsigned addr) { asm volatile("ds_read_b32 %0, %1 offset:%2" : "=v"(d) : "v"(addr), "i"(OFF) : "memory"); }
DI void bload_nt(u32x4& d, unsigned voff, rsrc_t rs, unsigned soff) {
  asm volatile("buffer_load_dwordx4 %0, %1, %2, %3 offen nt" : "=v"(d) : "v"(voff), "s"(rs), "s"(soff) : "memory");
}
template <int OFF>
DI void dsr64(u32x2& d, unsigned addr) { asm volatile("ds_read_b64 %0, %1 offset:%2" : "=v"(d) : "v"(addr), "i"(OFF) : "memory"); }
template <int OFF>
DI void dsr_tr(u32x2& d, unsigned addr) { asm volatile("ds_read_b64_tr_b16 %0, %1 offset:%2" : "=v"(d) : "v"(addr), "i"(OFF) : "memory"); }
template <int OFF>
DI void dsw128(unsigned addr, u32x4 d) { asm volatile("ds_write_b128 %0, %1 offset:%2" ::"v"(addr), "v"(d), "i"(OFF) : "memory"); }
template <int OFF>
DI void dsw32(unsigned addr, unsigned d) { asm volatile("ds_write_b32 %0, %1 offset:%2" ::"v"(addr), "v"(d), "i"(OFF) : "memory"); }

DI void gst128(void* p, u32x4 v) {
  asm volatile("global_store_dwordx4 %0, %1, off sc1\n\ts_nop 1" ::"v"(p), "v"(v) : "memory");
}
DI int fresh_lane() {
  int l;
  asm volatile("v_mbcnt_lo_u32_b32 %0, -1, 0\n\tv_mbcnt_hi_u32_b32 %0, -1, %0" : "=v"(l));
  return l;
}
DI float hadd(f32x2 v) {
  float r;
  asm("v_add_f32 %0, %1, %2" : "=v"(r) : "v"(v[0]), "v"(v[1]));
  return r;
}
DI f32x2 lo2(f32x4 v) { return __builtin_shufflevector(v, v, 0, 1); }
DI f32x2 hi2(f32x4 v) { return __builtin_shufflevector(v, v, 2, 3); }
DI float rowsum4(float v) {
  const unsigned u = __float_as_uint(v);
  const auto a = __builtin_amdgcn_permlane16_swap(u, u, false, false);
  const unsigned t = __float_as_uint(__uint_as_float(a[0]) + __uint_as_float(a[1]));
  const auto b = __builtin_amdgcn_permlane32_swap(t, t, false, false);
  return __uint_as_float(b[0]) + __uint_as_float(b[1]);
}
DI float silu_f(float v) { return v * __builtin_amdgcn_rcpf(1.0f + __builtin_amdgcn_exp2f(v * -1.4426950408889634f)); }
DI float rbf(float v) { return bf_lo(pk_bf16(v, v)); }
DI float gain_nz(float m) {
  const float g = rbf(1.0f + rbf(m));
  return __builtin_fabsf(g) < 8.67361738e-19f ? 8.67361738e-19f : g;
}
template <int OFF>
DI void bload_sc1(u32x4& d, unsigned voff, rsrc_t rs, unsigned soff) {
  asm volatile("buffer_load_dwordx4 %0, %1, %2, %3 offen offset:%4 sc1" : "+v"(d) : "v"(voff), "s"(rs), "s"(soff), "i"(OFF) : "memory");
}
template <int OFF>
DI void bstore_sc1(u32x4 d, unsigned voff, rsrc_t rs, unsigned soff) {
  asm volatile("buffer_store_dwordx4 %0, %1, %2, %3 offen offset:%4 sc1\n\ts_nop 1" ::"v"(d), "v"(voff), "s"(rs), "s"(soff), "i"(OFF) : "memory");
}
DI void bload1_sc1(unsigned& d, unsigned voff, rsrc_t rs) {
  asm volatile("buffer_load_dword %0, %1, %2, 0 offen sc1" : "+v"(d) : "v"(voff), "s"(rs) : "memory");
}

constexpr int Q_SLOT = 21504, Q_NSL = 7;
constexpr int QX_WH = 4 * Q_SLOT;
constexpr int QX_HX = 65536;
constexpr int QX_GT = QX_WH + 65536;
constexpr int QX_CNT = QX_GT + 512;
constexpr int QX_SINK = QX_CNT + 512;
constexpr int G3_CT = QX_SINK + 1024;
constexpr int X_CN = G3_CT + 4096;
constexpr int Q_LDS = X_CN + 2048;
constexpr int P_TAB = 4 * Q_SLOT, P_RED = 5 * Q_SLOT, P_ST = P_RED + 4096;

DI unsigned lds_add(unsigned addr, unsigned v0) {
  unsigned o;
  asm volatile("ds_add_rtn_u32 %0, %1, %2\n\ts_waitcnt lgkmcnt(0)" : "=v"(o) : "v"(addr), "v"(v0) : "memory");
  return __builtin_amdgcn_readfirstlane(o);
}
template <int SLP>
DI void lds_wait(unsigned addr, unsigned n) {
  for (;;) {
    unsigned v;
    asm volatile("ds_read_b32 %0, %1\n\ts_waitcnt lgkmcnt(0)" : "=v"(v) : "v"(addr) : "memory");
    if (__builtin_amdgcn_readfirstlane(v) >= n) break;
    __builtin_amdgcn_s_sleep(SLP);
  }
}
struct poll_t { unsigned a, b; };
struct sig_t { rsrc_t rs; unsigned vo; };
DI sig_t drain(unsigned* out, int lane, int no, int so) {
  sig_t g = {mk_rsrc(out, (no - 1) * so + 4), (unsigned)(lane * so)};
  asm volatile("s_waitcnt vmcnt(0)" : "+v"(g.vo) : "s"(g.rs) : "memory");
  return g;
}
DI void signal(const sig_t& g) { asm volatile("buffer_atomic_add %0, %1, %2, 0 offen sc1" ::"v"(1u), "v"(g.vo), "s"(g.rs) : "memory"); }
#define POLL_RD(r) "v_mov_b32 " r ", 0\n\tbuffer_atomic_add " r ", %3, %4, 0 offen sc0 sc1\n\t"
#define POLL_CK(r) "s_waitcnt vmcnt(1)\n\tv_cmp_lt_u32 vcc, " r ", %5\n\ts_cbranch_vccz 2f\n\t"
DI poll_t poll(unsigned skip, unsigned* in, int lane, int nl, unsigned n) {
  poll_t r;
  asm volatile("s_cmp_lg_u32 %2, 0\n\ts_cbranch_scc1 2f\n\t" POLL_RD("%0") "s_sleep %6\n1:\n\t" POLL_RD("%1") POLL_CK("%0") POLL_RD("%0") POLL_CK("%1")
               "s_add_u32 %2, %2, 1\n\ts_cmp_lt_u32 %2, 0x80000\n\ts_cbranch_scc1 1b\n2:"
               : "=&v"(r.a), "=&v"(r.b), "+s"(skip) : "v"(lane * 128), "s"(mk_rsrc(in, nl * 128)), "v"(lane < nl ? n : 0u), "n"(PSLP) : "memory", "vcc", "scc");
  return r;
}
DI void poll_end(const poll_t& r) { asm volatile("" ::"v"(r.a), "v"(r.b)); }
template <int NW, int NO, int SO>
DI unsigned arrive(unsigned lcnt, int lane, unsigned* out) {
  asm volatile("" ::"v"(lcnt + lane * 4));
  const sig_t g = drain(out, lane, NO, SO);
  const unsigned o = lds_add(lcnt + lane * 4, 1u);
  if (o == NW - 1) {
    signal(g);
  }
  return o;
}
DI poll_t await(unsigned o, int lane, unsigned* in, int nl, unsigned n) {
  const poll_t r = poll(o, in, lane, nl, n);
  __builtin_amdgcn_s_barrier();
  return r;
}
constexpr int FL_M = 0, FL_H = 544, FL_O = 608, FL_P = 736, FL_Y = 992, FL_A = 1248, FL_B = 1376, FL_S = 1504;

DI f32x4 mfma0(u32x4 a, u32x4 b) {
  return __builtin_amdgcn_mfma_f32_16x16x32_bf16(__builtin_bit_cast(bf16x8, a), __builtin_bit_cast(bf16x8, b), f32x4{0, 0, 0, 0}, 0, 0, 0);
}
DI u32x4 pack8(const f32x4 a, const f32x4 b) { return u32x4{pk_bf16(a[0], a[1]), pk_bf16(a[2], a[3]), pk_bf16(b[0], b[1]), pk_bf16(b[2], b[3])}; }
template <class OP>
DI float rows4(const float (&v)[4], OP op) {
  const auto a = __builtin_amdgcn_permlane16_swap(__float_as_uint(v[0]), __float_as_uint(v[1]), false, false);
  const auto b = __builtin_amdgcn_permlane16_swap(__float_as_uint(v[2]), __float_as_uint(v[3]), false, false);
  const float t0 = op(__uint_as_float(a[0]), __uint_as_float(a[1])), t1 = op(__uint_as_float(b[0]), __uint_as_float(b[1]));
  const auto c = __builtin_amdgcn_permlane32_swap(__float_as_uint(t0), __float_as_uint(t1), false, false);
  return op(__uint_as_float(c[0]), __uint_as_float(c[1]));
}
DI void bcast4(float t, float (&v)[4]) {
  const auto c = __builtin_amdgcn_permlane32_swap(__float_as_uint(t), __float_as_uint(t), false, false);
  const auto a = __builtin_amdgcn_permlane16_swap(c[0], c[0], false, false);
  const auto b = __builtin_amdgcn_permlane16_swap(c[1], c[1], false, false);
  v[0] = __uint_as_float(a[0]); v[1] = __uint_as_float(a[1]); v[2] = __uint_as_float(b[0]); v[3] = __uint_as_float(b[1]);
}
DI float fmax2(float a, float b) { return __builtin_amdgcn_fmed3f(a, b, __builtin_inff()); }

DI float sum16(float v) {
  v += __builtin_bit_cast(float, __builtin_amdgcn_update_dpp(0, __builtin_bit_cast(int, v), 0x128, 0xf, 0xf, false));
  v += __builtin_bit_cast(float, __builtin_amdgcn_update_dpp(0, __builtin_bit_cast(int, v), 0x124, 0xf, 0xf, false));
  v += __builtin_bit_cast(float, __builtin_amdgcn_update_dpp(0, __builtin_bit_cast(int, v), 0x122, 0xf, 0xf, false));
  v += __builtin_bit_cast(float, __builtin_amdgcn_update_dpp(0, __builtin_bit_cast(int, v), 0x121, 0xf, 0xf, false));
  return v;
}
DI f32x4 sum4(const u32x4* p) {
  return (__builtin_bit_cast(f32x4, p[0]) + __builtin_bit_cast(f32x4, p[1])) + (__builtin_bit_cast(f32x4, p[2]) + __builtin_bit_cast(f32x4, p[3]));
}
template <int NB, int NMID, class MID>
DI void mod_gemv(unsigned red, int lane, int col0, int k0, const bf16* __restrict__ c, const bf16* __restrict__ wmod, MID mid) {
  const int fr = lane & 15, fq = lane >> 4;
  const rsrc_t rsW = mk_rsrc(wmod, 1024 * 6144 * 2);
  const unsigned vw = (unsigned)(((k0 + (fr >> 3) * 32 + fq * 8) * 6144 + col0 + (fr & 7) * 8) * 2);
  u32x4 raw[NB][8], cv[NB];
  static_for<NB>([&](auto B) {
    asm volatile("" : "=v"(cv[B.value]));
    bload_v<B.value * 128>(cv[B.value], (unsigned)((k0 + (fr & 1) * 32 + fq * 8) * 2), mk_rsrc(c, 2048), 0);
  });
  static_for<NB * 8>([&](auto N) {
    constexpr int b = N.value >> 3, j = N.value & 7;
    bload_nt(raw[b][j], vw, rsW, (b * 64 + j) * 12288);
  });
  __builtin_amdgcn_s_barrier();
  f32x4 acc[8];
#pragma unroll
  for (int n = 0; n < 8; ++n) acc[n] = f32x4{0, 0, 0, 0};
  u32x4 cf[NB];
  static_for<NB>([&](auto B) {
    constexpr int b = B.value;
    asm volatile("s_waitcnt vmcnt(%1)" : "+v"(cv[b]) : "n"(NB * 8) : "memory");
#pragma unroll
    for (int e = 0; e < 4; ++e) cf[b][e] = pk_bf16(silu_f(bf_lo(cv[b][e])), silu_f(bf_hi(cv[b][e])));
    asm volatile("" : "+v"(cf[b]));
  });
  static_for<NB>([&](auto B) {
    constexpr int b = B.value;
    if constexpr (b == 1) {
#pragma unroll
      for (int n = 0; n < 8; ++n) asm volatile("" : "+v"(acc[n]));
      mid();
    }
    asm volatile("s_waitcnt vmcnt(%8)" : "+v"(raw[b][0]), "+v"(raw[b][1]), "+v"(raw[b][2]), "+v"(raw[b][3]), "+v"(raw[b][4]), "+v"(raw[b][5]), "+v"(raw[b][6]), "+v"(raw[b][7])
                 : "n"(b ? NMID : (NB - 1) * 8) : "memory");
#pragma unroll
    for (int p = 0; p < 4; ++p) {
      u32x4 lo, hi;
#pragma unroll
      for (int t = 0; t < 4; ++t) {
        lo[t] = __builtin_amdgcn_perm(raw[b][2 * t + 1][p], raw[b][2 * t][p], 0x05040100u);
        hi[t] = __builtin_amdgcn_perm(raw[b][2 * t + 1][p], raw[b][2 * t][p], 0x07060302u);
      }
      acc[2 * p] = __builtin_amdgcn_mfma_f32_16x16x32_bf16(__builtin_bit_cast(bf16x8, lo), __builtin_bit_cast(bf16x8, cf[b]), acc[2 * p], 0, 0, 0);
      acc[2 * p + 1] = __builtin_amdgcn_mfma_f32_16x16x32_bf16(__builtin_bit_cast(bf16x8, hi), __builtin_bit_cast(bf16x8, cf[b]), acc[2 * p + 1], 0, 0, 0);
    }
  });
  if (fr == (fq >> 1)) {
    static_for<8>([&](auto R) {
      constexpr int r = R.value >> 1, h = (R.value & 1) * 4;
      dsw128<R.value * 16>(red + fq * 128, __builtin_bit_cast(u32x4, f32x4{acc[h][r], acc[h + 1][r], acc[h + 2][r], acc[h + 3][r]}));
    });
  }
}
template <int NR>
DI float red_sum(unsigned red, int lane) {
  unsigned v[NR];
  static_for<NR>([&](auto R) { dsr32<R.value * 256>(v[R.value], red + lane * 4); });
  asm volatile("s_waitcnt lgkmcnt(0)" ::: "memory");
  float s = 0;
#pragma unroll
  for (int r = 0; r < NR; r += 2) {
    asm volatile("" : "+v"(v[r]), "+v"(v[r + 1]));
    s += __uint_as_float(v[r]) + __uint_as_float(v[r + 1]);
  }
  return s;
}
DI unsigned cnt_read(unsigned* in, int lane, int nl) {
  unsigned t = 0;
  asm volatile("buffer_atomic_add %0, %1, %2, 0 offen sc0 sc1\n\ts_waitcnt vmcnt(0)" : "+v"(t) : "v"(lane * 128), "s"(mk_rsrc(in, nl * 128)) : "memory");
  return t;
}
DI bool words_set(unsigned* word, int lane) {
  unsigned t = 0;
  asm volatile("buffer_atomic_add %0, %1, %2, 0 offen sc0 sc1\n\ts_waitcnt vmcnt(0)" : "+v"(t) : "v"(lane * 8192), "s"(mk_rsrc(word, 7 * 8192 + 4)) : "memory");
  return (__builtin_amdgcn_ballot_w64(t == 0u) & 255u) == 0u;
}
DI void confirm(unsigned* word, int lane) {
#pragma unroll 1
  for (unsigned spins = 0; !words_set(word, lane) && ++spins < (1u << 20);) __builtin_amdgcn_s_sleep(2);
}
DI void set_words(unsigned* flags, int lane) {
  bstore_sc1<0>(u32x4{1u, 1u, 1u, 1u}, (unsigned)(lane * 128), mk_rsrc(flags + (FL_S + (blockIdx.x & 7) * 64) * 32, 64 * 128), 0);
}

template <class PRE>
DI void first_phase(unsigned smem_base, int wid, int lane, const bf16* __restrict__ x, const bf16* __restrict__ c, const bf16* __restrict__ wmod,
                    const bf16* __restrict__ W, float* modp, bf16* H, unsigned* flags, PRE prefill) {
  const int xcd = blockIdx.x & 7, idx = blockIdx.x >> 3;
  const int fr = lane & 15, fq = lane >> 4;
  const int u = xcd * 16 + (idx & 15), v = xcd * 8 + idx;
  unsigned* word = flags + __builtin_amdgcn_readfirstlane((FL_S + (blockIdx.x >> 2)) * 32 + (blockIdx.x & 3));
  const bool master = idx == 15;
  if (wid == (idx >= 16 ? 0 : 7)) {
    asm volatile("buffer_store_dword %0, %1, %2, 0 offen sc1" ::"v"(0u), "v"(lane * 8192), "s"(mk_rsrc(word, 7 * 8192 + 4)) : "memory");
  }
  if (master) {
    bstore_sc1<0>(u32x4{0u, 0u, 0u, 0u}, (unsigned)(wid * 2048 + lane * 16), mk_rsrc(flags + xcd * (FL_O / 8) * 32, FL_O / 8 * 128), 0);
    bstore_sc1<1024>(u32x4{0u, 0u, 0u, 0u}, (unsigned)(wid * 2048 + lane * 16), mk_rsrc(flags + xcd * (FL_O / 8) * 32, FL_O / 8 * 128), 0);
  }
  if (wid == 7) {
    dsw32<0>(smem_base + QX_CNT + lane * 4, 0u); dsw32<256>(smem_base + QX_CNT + lane * 4, 0u);
    dsw128<0>(smem_base + X_CN + lane * 16, u32x4{0u, 0u, 0u, 0u}); dsw128<1024>(smem_base + X_CN + lane * 16, u32x4{0u, 0u, 0u, 0u});
    asm volatile("s_waitcnt lgkmcnt(0)" ::: "memory");
  }
  if (__builtin_expect(idx >= 16, 1)) {
    unsigned o = 0;
    if (wid < 4) {
      const int col0 = (u >> 3) * 128, kp = u & 7;
      mod_gemv<1, 0>(smem_base + P_RED + wid * 512, lane, col0 + (wid & 1) * 64, kp * 128 + (wid >> 1) * 64, c, wmod, [] {});
      const sig_t g = {mk_rsrc(flags + (FL_M + (u & 15)) * 32, 16 * 4096 + 4), (unsigned)(lane * 4096)};
      asm volatile("" ::"s"(g.rs), "v"(g.vo));
      o = lds_add(smem_base + X_CN + 1280 + lane * 4, 1u);
      if (o == 3) {
        u32x2 r[4];
        static_for<4>([&](auto R) { dsr64<(R.value >> 1) * 1024 + (R.value & 1) * 256>(r[R.value], smem_base + P_RED + (lane >> 5) * 512 + (lane & 31) * 8); });
        asm volatile("s_waitcnt lgkmcnt(0)" : "+v"(r[0]), "+v"(r[1]), "+v"(r[2]), "+v"(r[3])::"memory");
        const f32x2 sum = (__builtin_bit_cast(f32x2, r[0]) + __builtin_bit_cast(f32x2, r[1])) + (__builtin_bit_cast(f32x2, r[2]) + __builtin_bit_cast(f32x2, r[3]));
        asm volatile("buffer_store_dwordx2 %0, %1, %2, 0 offen sc1" ::"v"(sum), "v"(lane * 8), "s"(mk_rsrc(modp + kp * 2048 + col0, 512)) : "memory");
        confirm(word, lane);
        signal(g);
      }
    } else {
      const rsrc_t rsX = mk_rsrc(x, S_LEN * D_DIM * 2);
      const int r0 = (wid - 4) * 4;
      u32x4 a[8];
      static_for<8>([&](auto N) {
        asm volatile("" : "=v"(a[N.value]));
        bload_v<(N.value & 1) * 1024>(a[N.value], (unsigned)((u * 16 + r0) * 2048 + lane * 16), rsX, (N.value >> 1) * 2048);
      });
      __builtin_amdgcn_s_barrier();
      asm volatile("s_waitcnt vmcnt(0)" : "+v"(a[0]), "+v"(a[1]), "+v"(a[2]), "+v"(a[3]), "+v"(a[4]), "+v"(a[5]), "+v"(a[6]), "+v"(a[7])::"memory");
      float sm[4], q[4];
      f32x2 xf[4][8];
#pragma unroll
      for (int i = 0; i < 4; ++i) {
        const unsigned ad = smem_base + (lane >> 5) * Q_SLOT + (lane & 31) * 256 + (((r0 + i) ^ (lane & 7)) * 16);
        dsw128<0>(ad, a[2 * i]); dsw128<2 * Q_SLOT>(ad, a[2 * i + 1]);
#pragma unroll
        for (int j = 0; j < 8; ++j) xf[i][j] = f32x2{bf_lo(a[2 * i + (j >> 2)][j & 3]), bf_hi(a[2 * i + (j >> 2)][j & 3])};
        const f32x2 t = ((xf[i][0] + xf[i][1]) + (xf[i][2] + xf[i][3])) + ((xf[i][4] + xf[i][5]) + (xf[i][6] + xf[i][7]));
        sm[i] = hadd(t);
      }
      const float mrow = sum16(rows4(sm, [](float p, float r) { return p + r; })) * (1.0f / D_DIM);
      bcast4(mrow, sm);
#pragma unroll
      for (int i = 0; i < 4; ++i) {
        f32x2 t0 = xf[i][0] - sm[i], t1 = xf[i][1] - sm[i];
        t0 *= t0; t1 *= t1;
#pragma unroll
        for (int j = 2; j < 8; j += 2) {
          const f32x2 d0 = xf[i][j] - sm[i], d1 = xf[i][j + 1] - sm[i];
          t0 += d0 * d0; t1 += d1 * d1;
        }
        t0 += t1;
        q[i] = hadd(t0);
      }
      const float rstd = __builtin_amdgcn_rsqf(sum16(rows4(q, [](float p, float r) { return p + r; })) * (1.0f / D_DIM) + 1e-5f);
      asm volatile("ds_write_b64 %0, %1\n\ts_waitcnt lgkmcnt(0)" ::"v"(smem_base + P_ST + (r0 + fq) * 8), "v"(f32x2{rstd, -mrow * rstd}) : "memory");
      prefill(wid - 4); prefill(wid);
    }
    u32x4 mp[8], xv[4], tb[4][4];
    u32x2 rr;
    const rsrc_t rsM = mk_rsrc(modp, 8 * 2048 * 4);
    const unsigned vm = (unsigned)((lane >> 5) * 4096 + wid * 512 + (lane & 31) * 16);
    asm volatile("" ::"s"(rsM), "v"(vm));
    const poll_t pm = poll(3 - o, flags + (FL_M + (1 + (u >> 3)) * 32) * 32, lane, 16, 8u);
    __builtin_amdgcn_s_barrier();
#pragma unroll
    for (int i = 0; i < 8; ++i) { asm volatile("" : "=v"(mp[i])); bload_sc1<0>(mp[i], vm, rsM, i * 8192); }
    const unsigned xa = smem_base + (wid >> 1) * Q_SLOT + (wid & 1) * 4096 + fq * 256 + ((fr ^ fq) * 16);
    dsr128<0>(xv[0], xa); dsr128<1024>(xv[1], xa ^ 64u); dsr128<2048>(xv[2], xa); dsr128<3072>(xv[3], xa ^ 64u);
    dsr64<0>(rr, smem_base + P_ST + fr * 8);
    asm volatile("s_waitcnt lgkmcnt(0)" : "+v"(xv[0]), "+v"(xv[1]), "+v"(xv[2]), "+v"(xv[3]), "+v"(rr)::"memory");
    f32x2 xn[4][4];
#pragma unroll
    for (int i = 0; i < 4; ++i)
#pragma unroll
      for (int d = 0; d < 4; ++d) {
        xn[i][d] = f32x2{bf_lo(xv[i][d]), bf_hi(xv[i][d])} * __uint_as_float(rr[0]) + __uint_as_float(rr[1]);
        asm volatile("" : "+v"(xn[i][d]));
      }
    char* hp = (char*)H + ((size_t)u * 32 + wid * 4) * 1024 + lane * 16;
    asm volatile("s_waitcnt vmcnt(0)" : "+v"(mp[0]), "+v"(mp[1]), "+v"(mp[2]), "+v"(mp[3]), "+v"(mp[4]), "+v"(mp[5]), "+v"(mp[6]), "+v"(mp[7]), "+v"(hp)::"memory");
    poll_end(pm);
    f32x4 mv = sum4(mp) + sum4(mp + 4);
#pragma unroll
    for (int e = 0; e < 4; ++e) {
      mv[e] = rbf(mv[e]);
      if (lane & 32) mv[e] = rbf(1.0f + mv[e]);
    }
    dsw128<0>(smem_base + P_TAB + wid * 1024 + lane * 16, __builtin_bit_cast(u32x4, mv));
    static_for<16>([&](auto N) {
      constexpr int i = N.value >> 2, j = N.value & 3;
      dsr128<i * 128 + (j >> 1) * 512 + (j & 1) * 16>(tb[i][j], smem_base + P_TAB + wid * 1024 + fq * 32);
    });
    static_for<4>([&](auto I) {
      constexpr int i = I.value;
      asm volatile("s_waitcnt lgkmcnt(%4)" : "+v"(tb[i][0]), "+v"(tb[i][1]), "+v"(tb[i][2]), "+v"(tb[i][3]) : "n"(12 - 4 * i) : "memory");
      u32x4 ho;
#pragma unroll
      for (int d = 0; d < 4; ++d) {
        const f32x4 gg = __builtin_bit_cast(f32x4, tb[i][2 + (d >> 1)]), ss = __builtin_bit_cast(f32x4, tb[i][d >> 1]);
        const f32x2 hv = xn[i][d] * ((d & 1) ? hi2(gg) : lo2(gg)) + ((d & 1) ? hi2(ss) : lo2(ss));
        ho[d] = pk_bf16(hv[0], hv[1]);
      }
      gst128(hp + i * 1024, ho);
    });
    const sig_t g = drain(flags + (FL_H + (u >> 3) * 4) * 32, lane, ((u & 7) == 7 && u != 127) ? 8 : 4, 128);
    if (lds_add(smem_base + X_CN + 1024 + lane * 4, 1u) == 7) {
      signal(g);
    }
  } else {
    const rsrc_t rsS = mk_rsrc(word, wid == 7 ? 7 * 8192 + 4 : 0);
    unsigned seen = 0;
    auto mid = [&] { asm volatile("buffer_atomic_add %0, %1, %2, 0 offen sc0 sc1" : "+v"(seen) : "v"(lane * 8192), "s"(rsS) : "memory"); };
    if (!master) {
      const int ql = (wid * 7 + idx - 8) * 64 + lane;
      if (idx >= 8 && wid < 3) touch_lds(smem_base + QX_SINK, (unsigned)((ql >> 1) * 2048 + (xcd & 3) * 512 + 256 + (ql & 1) * 128), mk_rsrc(W, D_DIM * 3 * D_DIM * 2), 0);
      prefill(wid);
    }
    if (idx < 8) {
      if (wid == 0)
        bstore_sc1<0>(u32x4{0u, 0u, 0u, 0u}, (unsigned)(lane * 128), mk_rsrc(flags + (FL_O + v * ((FL_S - FL_O) / 64)) * 32, (FL_S - FL_O) / 64 * 128), 0);
      __builtin_amdgcn_s_sleep(NDLY);
      mod_gemv<2, 1>(smem_base + P_RED + wid * 512, lane, 2048 + (xcd * 8 + wid) * 64, idx * 128, c, wmod, mid);
      const float sum = red_sum<2>(smem_base + P_RED + wid * 512, lane);
      asm volatile("buffer_store_dword %1, %2, %3, 0 offen sc1\n\ts_waitcnt vmcnt(1)" : "+v"(seen) : "v"(sum), "v"(lane * 4), "s"(mk_rsrc(modp + 16384 + ((xcd * 8 + wid) * 8 + idx) * 64, 256)) : "memory");
    } else if (!master) {
      __builtin_amdgcn_s_sleep(TDLY);
#pragma unroll
      for (int q = 0; q < 3; ++q) {
        const int ql = ((q * 8 + wid) * 7 + idx - 8) * 64 + lane + 224 * 12;
        touch_lds(smem_base + QX_SINK, (unsigned)((ql >> 2) * 2048 + (xcd & 3) * 512 + (ql & 3) * 128), mk_rsrc(W, D_DIM * 3 * D_DIM * 2), 0);
      }
      __builtin_amdgcn_s_barrier();
      wait_vm<3>();
      mid();
      asm volatile("s_waitcnt vmcnt(0)" : "+v"(seen)::"memory");
    } else {
      wait_vm<0>();
      __builtin_amdgcn_s_barrier();
      bstore_sc1<0>(u32x4{1u, 1u, 1u, 1u}, (unsigned)(wid * 1024 + lane * 16), mk_rsrc(flags + (FL_S + xcd * 64) * 32, 64 * 128), 0);
      unsigned spins = 0;
      if (wid == 7) {
#pragma unroll 1
        do set_words(flags, lane); while (!words_set(word, lane) && ++spins < (1u << 20));
      }
      __builtin_amdgcn_s_barrier();
      prefill(wid);
      if (wid == 7) {
#pragma unroll 1
        do set_words(flags, lane); while (__builtin_amdgcn_ballot_w64(cnt_read(flags + FL_M * 32, lane, 16) < (lane < 16 ? 8u : 0u)) != 0ull && ++spins < (1u << 20));
      }
      seen = 1;
    }
    if (wid != 7) wait_vm<0>();
    else if ((__builtin_amdgcn_ballot_w64(seen == 0u) & 255u) != 0u) confirm(word, lane);
  }
}
DI void wo_piece(unsigned smem_base, unsigned p, unsigned rot, unsigned voff, rsrc_t rs) {
  const unsigned pb = p >> 2, kb = (pb & 24u) | ((pb + rot) & 7u);
  bload_lds<0>(smem_base + (p < 64u ? QX_WH + p * 1024u : (p - 64u) * 1024u), voff, rs, kb * 65536u + (p & 3u) * 16384u);
}

DI void qkv_ldw(unsigned smem_base, int lane, int sw, int n0, rsrc_t rsW, unsigned so, unsigned u) {
#pragma unroll
  for (int i = 0; i < 4; ++i) {
    const int r = (i & 1) * 8 + (lane >> 3), ch = lane & 7;
    const int g = ((r >> 1) & 1) | (((r >> 3) & 1) << 1);
    bload_lds<0>(smem_base + 9216 + sw * 4096 + so + i * 1024, (unsigned)(r * 6144 + sw * 2048 + (n0 + (((ch >> 1) ^ g) * 16) + (ch & 1) * 8) * 2), rsW,
                 u * 196608u + (i >> 1) * 98304u);
  }
}
DI void qkv_loader(unsigned smem_base, int lane, int sw, int m0, int n0, const bf16* H, rsrc_t rsW,
                   unsigned rot, unsigned voffO, rsrc_t rsO) {
  const rsrc_t rsH = mk_rsrc(H, S_LEN * D_DIM * 2);
  int fb0 = (m0 >> 4) - 1 + 3 * sw; fb0 = fb0 < 0 ? 0 : fb0;
  const unsigned voffH0 = (unsigned)(fb0 * 32768 + lane * 16);
  const unsigned voffH1 = (unsigned)(((m0 >> 4) + 3 * sw) * 32768 + lane * 16);
  const unsigned m0a = smem_base + sw * 3072;
  auto ldh = [&](unsigned so, unsigned u) {
    bload_lds_sc1<0>(m0a + so, voffH0, rsH, u * 1024u);
    bload_lds_sc1<0>(m0a + so + 1024, voffH1, rsH, u * 1024u);
    bload_lds_sc1<0>(m0a + so + 2048, voffH1, rsH, u * 1024u + 32768u);
  };
  auto ldw = [&](unsigned so, unsigned u) { qkv_ldw(smem_base, lane, sw, n0, rsW, so, u); };
  asm volatile("" ::"v"(voffH0), "v"(voffH1), "s"(rsH), "s"(m0a));
  __builtin_amdgcn_s_barrier();
#pragma unroll
  for (int u = 1; u <= NP1; ++u) ldh(u * Q_SLOT, u);
  wait_vm<3 * (NP1 - 1)>();
  __builtin_amdgcn_s_barrier();
#pragma unroll
  for (int u = NP1 + 1; u < Q_NSL; ++u) ldh(u * Q_SLOT, u);
  static_for<Q_NSL - 2, 1>([&](auto U) {
    constexpr int u = U.value;
    ldh((u - 1) * Q_SLOT, u + Q_NSL - 1); ldw((u - 1) * Q_SLOT, u + Q_NSL - 1);
    wait_vm<3 * (Q_NSL - 2 - u) + 7 * u>();
    __builtin_amdgcn_s_barrier();
  });
  unsigned sb = (Q_NSL - 3) * Q_SLOT;
  for (int u = Q_NSL - 2; u < 32 - (Q_NSL - 1); ++u) {
    ldh(sb, u + Q_NSL - 1); ldw(sb, u + Q_NSL - 1);
    sb = (sb == (Q_NSL - 1) * Q_SLOT) ? 0u : sb + Q_SLOT;
    wait_vm<(Q_NSL - 2) * 7>();
    __builtin_amdgcn_s_barrier();
  }
  for (int t = 0; t < 6; ++t) {
    const unsigned p0 = (unsigned)(t * 21 + (t >= 3 ? 1 : 0) + sw * 7);
#pragma unroll
    for (int i = 0; i < 7; ++i) wo_piece(smem_base, p0 + i, rot, voffO, rsO);
    if (t < 5) wait_vm<(Q_NSL - 2) * 7>();
    __builtin_amdgcn_s_barrier();
  }
}

template <int WM>
DI void qkv_mfma(unsigned smem_base, int lane, int wn, int bm, int bn, const bf16* H, bf16* __restrict__ O) {
  const int fr = lane & 15, fq = lane >> 4;
  unsigned bb0, bb1;
  const unsigned ab = smem_base + lane * 16;
  {
    const int q = (lane >> 2) & 3, p = lane & 3;
    const int r = 8 * fq + q;
    const int g = ((r >> 1) & 1) | (((r >> 3) & 1) << 1);
    bb0 = smem_base + 9216 + r * 128 + (((wn * 2) ^ g) * 32) + 8 * p;
    bb1 = bb0 ^ 32u;
  }
  const bool par = fr & 1, c0 = 4 * fq + (fr & 1) <= fr, c1 = 4 * fq + 2 + (fr & 1) <= fr;
  const unsigned s0 = par ? 0x01000c0cu : 0x0c0c0100u, s1 = par ? 0x03020c0cu : 0x0c0c0302u;
  u32x4 sel = {c0 ? s0 : 0x0c0c0c0cu, c1 ? s1 : 0x0c0c0c0cu, c0 ? 0x0c0c0c0cu : s0, c1 ? 0x0c0c0c0cu : s1};
  unsigned hx = ab + QX_HX + wn * 3072;
  asm volatile("" : "+v"(sel), "+v"(hx));
  const rsrc_t rsO = mk_rsrc((char*)O + (size_t)((bm * 2 + WM) * 32 + bn * 2 + wn) * 4096, 4096);
  f32x4 acc[4][6], acch[2];
#pragma unroll
  for (int i = 0; i < 4; ++i)
#pragma unroll
    for (int j = 0; j < 6; ++j) { acc[i][j] = f32x4{0, 0, 0, 0}; asm volatile("" : "+v"(acc[i][j])); }
#pragma unroll
  for (int j = 0; j < 2; ++j) { acch[j] = f32x4{0, 0, 0, 0}; asm volatile("" : "+v"(acch[j])); }
  __builtin_amdgcn_s_setprio(2);
  u32x4 aX[5], aY[5];
  u32x2 bXl[6], bXh[6], bYl[6], bYh[6];
  unsigned ra = ab, rb0 = bb0, rb1 = bb1, so = 0;
#define Q_READ(AR, BL, BH, R)                                                                                    \
  do {                                                                                                           \
    if ((R) < 12) {                                                                                              \
      constexpr int jb = ((R) < 12 ? (R) : 0) >> 1;                                                              \
      if ((R) & 1) dsr_tr<(jb >> 1) * 4096 + 512>(BH[jb], (jb & 1) ? rb1 : rb0);                                 \
      else dsr_tr<(jb >> 1) * 4096>(BL[jb], (jb & 1) ? rb1 : rb0);                                               \
    } else if ((R) < 16) {                                                                                       \
      constexpr int ia = ((R) >= 12 && (R) < 16) ? (R) - 12 : 0;                                                 \
      dsr128<1024 + WM * 4096 + ia * 1024>(AR[ia], ra);                                                          \
    } else {                                                                                                     \
      dsr128<0>(AR[4], ra);                                                                                      \
    }                                                                                                            \
  } while (0)
#define Q_MFMA(AR, BB, N)                                                                                        \
  do {                                                                                                           \
    if ((N) < 16) MFMA_AV(acc[(N) & 3][((N) < 24 ? (N) : 0) >> 2], BB[((N) < 24 ? (N) : 0) >> 2], AR[(N) & 3]);  \
    else if ((N) < 24) MFMA_AV(acc[(N) & 3][((N) < 24 ? (N) : 0) >> 2], AR[(N) & 3], BB[((N) < 24 ? (N) : 0) >> 2]); \
    else if (WM == 0) MFMA_AV(acch[(N) & 1], BB[2 + ((N) & 1)], AR[4]);                                          \
    else MFMA_AV(acch[(N) & 1], AR[4], BB[4 + ((N) & 1)]);                                                       \
  } while (0)
#define Q_ADV(K)                                                                                                 \
  do {                                                                                                           \
    if ((K) == 0) asm volatile("s_add_u32 %0, %0, %1\n\ts_cmp_eq_u32 %0, %2\n\ts_cselect_b32 %0, 0, %0" : "+s"(so) : "n"(Q_SLOT), "n"(Q_NSL * Q_SLOT) : "scc"); \
    else if ((K) == 1) asm volatile("v_add_u32 %0, %1, %2" : "=v"(rb0) : "s"(so), "v"(bb0));                     \
    else if ((K) == 2) asm volatile("v_add_u32 %0, %1, %2" : "=v"(rb1) : "s"(so), "v"(bb1));                     \
    else asm volatile("v_add_u32 %0, %1, %2" : "=v"(ra) : "s"(so), "v"(ab));                                     \
  } while (0)
#define Q_UNIT(AR, BL, BH, NAR, NBL, NBH)                                                                        \
  do {                                                                                                           \
    WAIT_LGKM0();                                                                                                \
    u32x4 bb[6];                                                                                                 \
    _Pragma("unroll") for (int j = 0; j < 6; ++j) bb[j] = u32x4{BL[j][0], BL[j][1], BH[j][0], BH[j][1]};         \
    SCHED0();                                                                                                    \
    static_for<26>([&](auto N) {                                                                                 \
      constexpr int n = N.value;                                                                                 \
      if constexpr (n == QBAR) __builtin_amdgcn_s_barrier();                                                     \
      Q_MFMA(AR, bb, n);                                                                                         \
      if constexpr (n >= QBAR && n < QBAR + 17) Q_READ(NAR, NBL, NBH, n - QBAR);                                 \
      if constexpr (n >= QBAR + 18 && n < QBAR + 22) Q_ADV(n - QBAR - 18);                                       \
    });                                                                                                          \
    SCHED0();                                                                                                    \
  } while (0)
  {
    const rsrc_t rsH = mk_rsrc(H, S_LEN * D_DIM * 2);
    unsigned vh = lane * 16 + (bm * 8 + WM * 4) * 32768, vl = lane * 16 + (bm ? bm * 8 - 1 : 0) * 32768;
    asm volatile("" : "+v"(vh), "+v"(vl), "+v"(rb0), "+v"(rb1) : "s"(rsH));
    __builtin_amdgcn_s_barrier();
    static_for<5>([&](auto I) { asm volatile("" : "=v"(aX[I.value])); bload_sc1<0>(aX[I.value], I.value < 4 ? vh : vl, rsH, (I.value & 3) * 32768); });
    static_for<12>([&](auto R) { Q_READ(aX, bXl, bXh, R.value); });
    static_for<4>([&](auto K) { Q_ADV(K.value); });
    asm volatile("s_waitcnt vmcnt(0)" : "+v"(aX[0]), "+v"(aX[1]), "+v"(aX[2]), "+v"(aX[3]), "+v"(aX[4])::"memory");
  }
  for (int u = 0; u < 32; u += 2) {
    Q_UNIT(aX, bXl, bXh, aY, bYl, bYh);
    Q_UNIT(aY, bYl, bYh, aX, bXl, bXh);
  }
  asm volatile("s_waitcnt lgkmcnt(0)\n\ts_nop 7" ::: "memory");
#pragma unroll
  for (int i = 0; i < 5; ++i) asm volatile("" : "+v"(aX[i]));
#pragma unroll
  for (int j = 0; j < 6; ++j) asm volatile("" : "+v"(bXl[j]), "+v"(bXh[j]));
#pragma unroll
  for (int i = 0; i < 4; ++i)
#pragma unroll
    for (int j = 0; j < 6; ++j) asm volatile("" : "+v"(acc[i][j]));
  asm volatile("" : "+v"(acch[0]), "+v"(acch[1]));
  SCHED0();
  u32x4 kk[5], vv[5];
  if (WM == 0) {
    kk[4] = pack8(acc[3][2], acc[3][3]); vv[4] = pack8(acc[3][4], acc[3][5]);
    dsw128<0>(hx, kk[4]); dsw128<1024>(hx, vv[4]);
  } else {
    dsw128<2048>(hx, pack8(acch[0], acch[1]));
  }
  asm volatile("s_waitcnt lgkmcnt(0)" ::: "memory");
  __builtin_amdgcn_s_barrier();
  if (WM == 0) dsr128<2048>(vv[0], hx);
  else { dsr128<0>(kk[0], hx); dsr128<1024>(vv[0], hx); }
  SCHED0();
  if (WM == 0) kk[0] = pack8(acch[0], acch[1]);
#pragma unroll
  for (int i = 0; i < (WM == 0 ? 3 : 4); ++i) kk[i + 1] = pack8(acc[i][2], acc[i][3]);
  const float cs = 0.17677669529663687f * 1.4426950408889634f;
  float l0[4], l1[4], tm[4];
#pragma unroll
  for (int ii = 1; ii < 5; ++ii) {
    const int i = ii & 3;
    const u32x4 q = pack8(acc[i][0], acc[i][1]);
    if (WM == 1 && i == 0) asm volatile("s_waitcnt lgkmcnt(0)" : "+v"(kk[0]), "+v"(vv[0])::"memory");
    const f32x4 sc = mfma0(kk[i + 1], q), sp = mfma0(kk[i], q);
    float p0 = par ? sp[1] : sp[0], p1 = par ? sp[3] : sp[2];
    if (WM == 0 && i == 0 && bm == 0) { p0 = -5.656854249e9f; p1 = -5.656854249e9f; }
    l0[i] = c0 ? (par ? sc[1] : sc[0]) : p0;
    l1[i] = c1 ? (par ? sc[3] : sc[2]) : p1;
    tm[i] = fmax2(l0[i], l1[i]);
  }
#pragma unroll
  for (int i = 0; i < (WM == 0 ? 3 : 4); ++i) vv[i + 1] = pack8(acc[i][4], acc[i][5]);
  bcast4(rows4(tm, [](float a, float b) { return fmax2(a, b); }), tm);
#pragma unroll
  for (int i = 0; i < 4; ++i) {
    l0[i] = __builtin_amdgcn_exp2f((l0[i] - tm[i]) * cs);
    l1[i] = __builtin_amdgcn_exp2f((l1[i] - tm[i]) * cs);
    tm[i] = l0[i] + l1[i];
  }
  bcast4(__builtin_amdgcn_rcpf(rows4(tm, [](float a, float b) { return a + b; })), tm);
  if (WM == 0) asm volatile("s_waitcnt lgkmcnt(0)" : "+v"(vv[0])::"memory");
#pragma unroll
  for (int i = 0; i < 4; ++i) {
    const unsigned pp = pk_bf16(l0[i] * tm[i], l1[i] * tm[i]);
    const u32x4 pr = {__builtin_amdgcn_perm(pp, pp, sel[0]), __builtin_amdgcn_perm(pp, pp, sel[1]),
                      __builtin_amdgcn_perm(pp, pp, sel[2]), __builtin_amdgcn_perm(pp, pp, sel[3])};
    const u32x4 ov = pack8(mfma0(u32x4{vv[i + 1][0], vv[i + 1][1], vv[i][0], vv[i][1]}, pr), mfma0(u32x4{vv[i + 1][2], vv[i + 1][3], vv[i][2], vv[i][3]}, pr));
    asm volatile("buffer_store_dwordx4 %0, %1, %2, 0 offen offset:%3 sc1\n\ts_nop 1" ::"v"(ov), "v"(lane * 16), "s"(rsO), "i"(i * 1024) : "memory");
  }
}

template <class GST>
DI void attn_phase(unsigned smem_base, int wid, bf16* H, const bf16* __restrict__ W, bf16* O,
                   const bf16* __restrict__ wo, const bf16* __restrict__ X, float* modp, bf16* Y, float* stat,
                   unsigned* flags, const bf16* __restrict__ wg, const bf16* __restrict__ wu,
                   unsigned* ctab, const bf16* __restrict__ cnd, const bf16* __restrict__ wmod, GST gst) {
  const int tid = threadIdx.x, lane = tid & 63;
  const int xcd = blockIdx.x & 7, idx = blockIdx.x >> 3;
  const int bm = (xcd >> 2) * 8 + (idx & 7), bn = (xcd & 3) * 4 + (idx >> 3);
  const int rb = xcd * 2 + (idx & 1), cg = idx >> 1;
  const unsigned rot = __builtin_amdgcn_readfirstlane((cg >> 2) * 2);
  const unsigned cnt = smem_base + QX_CNT, cntl = cnt + lane * 4;
  const rsrc_t rsW = mk_rsrc(W, D_DIM * 3 * D_DIM * 2);
  first_phase(smem_base, wid, lane, X, cnd, wmod, W, modp, H, flags, [&](int w) {
#pragma unroll
    for (int i = 0; i < 3; ++i) {
      const int p = w + 8 * i < 21 ? w + 8 * i : 20, u = (p * 11) >> 5;
      qkv_ldw(smem_base, lane, p - 3 * u, bn * 64, rsW, u * Q_SLOT, u);
    }
  });
  if (wid < 4) {
    if (wid & 1) qkv_mfma<1>(smem_base, lane, wid >> 1, bm, bn, H, O);
    else qkv_mfma<0>(smem_base, lane, wid >> 1, bm, bn, H, O);
  } else {
    const rsrc_t rsO = mk_rsrc(wo, D_DIM * D_DIM * 2);
    const unsigned voffO = (unsigned)((lane >> 3) * 2048 + cg * 128 + ((((lane & 7) >> 1) ^ (lane >> 4)) * 32) + (lane & 1) * 16);
    if (wid < 7) qkv_loader(smem_base, lane, wid - 4, bm * 128, bn * 64, H, rsW, rot, voffO, rsO);
    else {
      const unsigned vgu = idx < 16 ? 0x40000000u : (unsigned)((((idx - 16) * 64 + lane) / 7) * (F_DIM * 2) + xcd * 896 + (((idx - 16) * 64 + lane) % 7) * 128);
      const poll_t ph = poll(0u, flags + (FL_H + bm * 4 + (xcd & 3)) * 32, lane, 1, bm ? 9u : 8u);
      asm volatile("s_barrier" ::: "memory");
      SCHED0();
      unsigned* r1 = flags + (FL_M + (1 + xcd) * 32 + 24) * 32;
      const sig_t gi = drain(idx < 8 ? r1 : flags + (FL_M + 24 + xcd) * 32, lane, idx < 16 ? 1 : 0, 4);
      poll_end(ph);
      signal(gi);
      if (idx < 8) {
        const int v = xcd * 8 + idx;
        unsigned t = 0;
        for (int i = 0; i < 4; ++i) __builtin_amdgcn_s_barrier();
        asm volatile("buffer_atomic_add %0, %1, %2, 0 offen sc0 sc1\n\ts_barrier\n\ts_barrier\n\ts_barrier\n\ts_barrier\n\ts_waitcnt vmcnt(0)" : "+v"(t) : "v"(lane * 128), "s"(mk_rsrc(r1, 128)) : "memory");
#pragma unroll 1
        for (unsigned spins = 0; __builtin_amdgcn_readfirstlane(t) < 8u && spins < (1u << 20); ++spins) t = cnt_read(r1, lane, 1);
        static_for<2>([&](auto I) { bload_lds_sc1<I.value * 1024>(smem_base + G3_CT, lane * 16, mk_rsrc(modp + 16384 + v * 512, 2048), 0); });
        for (int i = 0; i < 4; ++i) __builtin_amdgcn_s_barrier();
        wait_vm<0>();
        float sum = red_sum<8>(smem_base + G3_CT, lane);
        sum = (v >> 4) == 2 ? gain_nz(sum) : rbf(sum);
        asm volatile("buffer_store_dword %0, %1, %2, 0 offen sc1" ::"v"(sum), "v"(lane * 4), "s"(mk_rsrc(modp + 49152 + v * 64, 256)) : "memory");
        asm volatile("buffer_store_short_d16_hi %0, %1, %2, 0 offen sc1" ::"v"(sum), "v"((lane >> 5) * 128 + (lane & 31) * 2),
                     "s"(mk_rsrc(ctab + (v & 15) * 64 + (v & 16), (v >> 4) == 1 || (v >> 4) == 2 ? 256 : 0)) : "memory");
        for (int i = 0; i < 4; ++i) __builtin_amdgcn_s_barrier();
        signal(drain(flags + (FL_M + 16 + idx) * 32, lane, 9, 4096));
      }
      {
        int nxt = TS + idx % TP, ev = 0;
#pragma unroll 1
        for (int i = idx < 8 ? 16 : 0; i < 27; ++i) {
          if (i == WOS && idx >= 16) {
            touch_lds(smem_base + QX_SINK, (blockIdx.x - 128) * 16384 + lane * 128, rsO, 0);
            touch_lds(smem_base + QX_SINK, (blockIdx.x - 128) * 16384 + lane * 128, rsO, 8192);
          }
          if (i == nxt && ev < 2) {
            touch_lds(smem_base + QX_SINK, vgu, mk_rsrc(ev ? wu : wg, D_DIM * F_DIM * 2), 0);
            nxt += TP; ++ev;
          }
          __builtin_amdgcn_s_barrier();
        }
      }
      unsigned* cr = flags + (FL_M + (idx == 15 ? 0 : 1 + xcd) * 32 + 16) * 32;
      const int nl = idx == 15 ? 16 : 8;
      unsigned t = 0;
      asm volatile("buffer_atomic_add %0, %1, %2, 0 offen sc0 sc1" : "+v"(t) : "v"(lane * 128), "s"(mk_rsrc(cr, nl * 128)) : "memory");
      wo_piece(smem_base, 63u, rot, voffO, rsO);
      __builtin_amdgcn_s_barrier();
      __builtin_amdgcn_s_barrier();
      asm volatile("s_waitcnt vmcnt(1)" : "+v"(t)::"memory");
      for (unsigned spins = 0; __builtin_amdgcn_ballot_w64(t < (lane < nl ? 8u : 0u)) != 0ull && spins < (1u << 20); ++spins) {
        if (idx == 15) set_words(flags, lane);
        t = cnt_read(cr, lane, nl);
      }
      static_for<4>([&](auto I) { bload_lds_sc1<I.value * 1024>(smem_base + G3_CT, lane * 16, mk_rsrc(ctab, 4096), 0); });
#pragma unroll
      for (int i = 0; i < 2; ++i)
        asm volatile("s_mov_b32 m0, %0\n\ts_nop 0\n\tbuffer_load_dword %1, %2, 0 offen sc1 lds" ::"s"(smem_base + QX_GT + i * 256), "v"(lane * 4),
                     "s"(mk_rsrc(modp + 49152 + i * 2048 + cg * 64, 256)) : "memory");
      for (int i = 30; i < 33; ++i) __builtin_amdgcn_s_barrier();
      wo_piece(smem_base, 127u, rot, voffO, rsO);
    }
    __builtin_amdgcn_s_barrier();
    wait_vm<0>();
    lds_add(cntl, 1u);
  }
  const int fr = lane & 15, fq = lane >> 4;
  const int kq = wid & 3, rg = rb * 2 + (wid >> 2);
  const int ko = (kq + cg) & 3;
  lds_wait<2>(cnt, 4);
  u32x2 wl[8][4], wh[8][4];
  {
    const int q = (lane >> 2) & 3, p = lane & 3, r = 4 * fq + q;
    const unsigned wb = smem_base + (kq < 2 ? QX_WH + kq * 32768 : (kq - 2) * 32768) + r * 128 + (((r >> 1) & 3) * 32) + 8 * p;
    unsigned wa[4];
#pragma unroll
    for (int f = 0; f < 4; ++f) wa[f] = wb ^ (f * 32);
    static_for<32>([&](auto E) {
      constexpr int s = E.value >> 2, f = E.value & 3;
      dsr_tr<s * 4096>(wl[s][f], wa[f]);
      dsr_tr<s * 4096 + 2048>(wh[s][f], wa[f]);
    });
  }
  const rsrc_t rsA = mk_rsrc((const char*)O + (size_t)rg * 131072, 131072);
  unsigned voffA[4], hs[8];
#pragma unroll
  for (int i = 0; i < 4; ++i) { voffA[i] = lane * 16 + ((ko + 1 + i) & 3) * 1024; asm volatile("" : "+v"(voffA[i])); }
#pragma unroll
  for (int i = 0; i < 8; ++i) { hs[i] = __builtin_amdgcn_readfirstlane((kq * 8 + ((i + rot) & 7)) * 4096); asm volatile("" : "+s"(hs[i])); }
  asm volatile("" ::"s"(rsA));
  if (wid < 4) {
    const sig_t g = drain(flags + (FL_O + bm * 8 + (bn & 1)) * 32, lane, 4, 256);
    if (lds_add(cntl + 256, 1u) == 3u) signal(g);
  }
  const poll_t pl = poll(wid - 7, flags + (FL_O + rb * 8 + (cg & 3) * 2) * 32, lane, 2, 8u);
  asm volatile("s_waitcnt lgkmcnt(0)" ::: "memory");
  __builtin_amdgcn_s_barrier();
  u32x4 wf[8][4];
#pragma unroll
  for (int s = 0; s < 8; ++s)
#pragma unroll
    for (int f = 0; f < 4; ++f) {
      asm volatile("" : "+v"(wl[s][f]), "+v"(wh[s][f]));
      wf[s][f] = u32x4{wl[s][f][0], wl[s][f][1], wh[s][f][0], wh[s][f][1]};
    }
  const unsigned voffL = lane * 16;
  u32x4 af[32], xv[2], pr[3][4], gt[4], ct[4];
  f32x4 acc[4][4];
  auto aload = [&](auto N) {
    constexpr int n = N.value;
    asm volatile("" : "=v"(af[n]));
    bload_sc1<0>(af[n], voffA[n >> 3], rsA, hs[n & 7]);
  };
  static_for<GDEP>([&](auto N) { aload(N); });
  const unsigned sl = smem_base + 49152 + voffL + (wid >> 2) * 49152;
  const int sw = ((fq & 1) << 1) | (fq >> 1);
  SCHED0();
  static_for<32>([&](auto N) {
    constexpr int n = N.value, ps = n >> 3, s = n & 7, XG = 32 - GDEP;
    constexpr int last = n - 1 + GDEP < 31 ? n - 1 + GDEP : 31;
    asm volatile("s_waitcnt vmcnt(%1)" : "+v"(af[n]) : "n"(last - n + (n > XG ? 2 : 0) + (n > GS0 + 3 ? 6 : (n > GS0 ? 2 * (n - GS0) : 0))) : "memory");
    if constexpr (n == 0) poll_end(pl);
    if constexpr (n == 28) { WAIT_LGKM0(); __builtin_amdgcn_s_barrier(); }
    static_for<4>([&](auto F) {
      constexpr int f = F.value;
      if constexpr (s == 0) asm volatile("v_mfma_f32_16x16x32_bf16 %0, %1, %2, 0" : "=&v"(acc[ps][f]) : "v"(wf[s][f]), "v"(af[n]));
      else MFMA_AV(acc[ps][f], wf[s][f], af[n]);
      if constexpr (f == 0 && n + GDEP < 32) aload(ic<(n + GDEP < 32 ? n + GDEP : 0)>{});
      if constexpr (f == 0 && n == XG) {
        const rsrc_t rsX = mk_rsrc(X, S_LEN * D_DIM * 2);
        const unsigned vx = (unsigned)(((rg * 64 + ko * 16 + fr) * D_DIM + cg * 64 + sw * 8) * 2);
        asm volatile("" : "=v"(xv[0]), "=v"(xv[1]));
        bload_v<0>(xv[0], vx, rsX, 0); bload_v<64>(xv[1], vx, rsX, 0);
      }
      if constexpr ((f & 1) && n >= GS0 && n < GS0 + 3) gst(ic<(n >= GS0 ? n - GS0 : 0)>{}, ic<(f >> 1)>{});
      if constexpr (ps > 0 && (s == 2 || s == 3) && (f & 1)) {
        constexpr int e = (s - 2) * 2 + (f >> 1);
        dsw128<(ps - 1) * 4096 + e * 1024>(sl + ((kq + ps) & 3) * 12288, __builtin_bit_cast(u32x4, acc[ps - 1][e]));
      }
      if constexpr (n >= 28 && n < 31) dsr128<(n - 28) * 4096 + f * 1024>(pr[n - 28][f], sl + kq * 12288);
      if constexpr (n == 31) { dsr128<f * 64>(gt[f], smem_base + QX_GT + fq * 16); dsr128<256 + f * 64>(ct[f], smem_base + QX_GT + fq * 16); }
    });
  });
  asm volatile("s_waitcnt vmcnt(6)" : "+v"(xv[0]), "+v"(xv[1])::"memory");
  asm volatile("s_waitcnt lgkmcnt(0)\n\ts_nop 4" ::: "memory");
  f32x4 fin[4], yv[4];
#pragma unroll
  for (int f = 0; f < 4; ++f) asm volatile("" : "+v"(acc[3][f]), "+v"(pr[0][f]), "+v"(pr[1][f]), "+v"(pr[2][f]), "+v"(gt[f]), "+v"(ct[f]));
#pragma unroll
  for (int c = 0; c < 2; ++c) {
    const auto x0 = __builtin_amdgcn_permlane16_swap(xv[c][0], xv[c][2], false, false);
    const auto x1 = __builtin_amdgcn_permlane16_swap(xv[c][1], xv[c][3], false, false);
#pragma unroll
    for (int h = 0; h < 2; ++h) {
      const int f = 2 * c + h;
      const f32x4 a = (acc[3][f] + __builtin_bit_cast(f32x4, pr[0][f])) + (__builtin_bit_cast(f32x4, pr[1][f]) + __builtin_bit_cast(f32x4, pr[2][f]));
      const f32x4 xf = {bf_lo(x0[h]), bf_hi(x0[h]), bf_lo(x1[h]), bf_hi(x1[h])};
      fin[f] = xf + __builtin_bit_cast(f32x4, gt[f]) * a;
      yv[f] = fin[f] * __builtin_bit_cast(f32x4, ct[f]);
    }
    const auto y0 = __builtin_amdgcn_permlane16_swap(pk_bf16(yv[2 * c][0], yv[2 * c][1]), pk_bf16(yv[2 * c + 1][0], yv[2 * c + 1][1]), false, false);
    const auto y1 = __builtin_amdgcn_permlane16_swap(pk_bf16(yv[2 * c][2], yv[2 * c][3]), pk_bf16(yv[2 * c + 1][2], yv[2 * c + 1][3]), false, false);
    gst128((char*)Y + ((size_t)(rg >> 2) * 32 + cg * 2 + c) * 16384 + ((rg & 3) * 4 + ko) * 1024 + (sw * 16 + fr) * 16,
           u32x4{y0[0], y1[0], y0[1], y1[1]});
  }
  const f32x4 s4 = (fin[0] + fin[1]) + (fin[2] + fin[3]);
  const float s = rowsum4(hadd(lo2(s4) + hi2(s4)));
  const float mb = s * (1.0f / 64.0f);
  f32x4 q4 = {0, 0, 0, 0};
#pragma unroll
  for (int f = 0; f < 4; ++f) { const f32x4 d = fin[f] - mb; q4 += d * d; }
  const float m2 = rowsum4(hadd(lo2(q4) + hi2(q4)));
  asm volatile("buffer_store_dwordx2 %0, %1, %2, 0 offen sc1" ::"v"(f32x2{s, m2}), "v"(lane * 8),
               "s"(mk_rsrc(stat + ((size_t)cg * S_LEN + rg * 64 + ko * 16) * 2, 128)) : "memory");
}

#define G3_STAGE(SLOT, P, U)                                                                               \
  do {                                                                                                     \
    if ((P) == 0) bload_lds_sc1<0>(m0w + 65536 + (SLOT) * 16384, voffA, rsA, (unsigned)(U) * 16384u);      \
    else if ((P) == 1) bload_lds_sc1<1024>(m0w + 65536 + (SLOT) * 16384, voffA, rsA, (unsigned)(U) * 16384u);  \
    else bload_lds<0>(m0w + (SLOT) * 16384 + ((P) - 2) * 1024, (P) == 2 ? voffB0 : voffB1, rsB, (unsigned)(U) * 229376u);  \
  } while (0)

DI constexpr int g3_line(int gx, int p) {
  const int t = p + ((3 + gx) & 7);
  return 7 * gx + (t >= 7 ? t - 7 : t);
}
constexpr unsigned act_frag(int kh, int p) {
  const int tl = p < 48 ? p / 3 : (p - 48) >> 2, jv = (tl & 3) * 7 + (p < 48 ? 4 + p % 3 : p & 3);
  return g3_line(kh * 4 + (tl >> 2), jv >> 2) * 4 + (jv & 3);
}
template <int FN, class WST, class HOOK, class PRE>
DI void g3_body(unsigned smem_base, int wm, int wn, int wid, int lane, int bm, int bn, rsrc_t rsA, rsrc_t rsB,
                rsrc_t rsE, const char* ta, unsigned voffA, unsigned voffB0, unsigned voffB1, bf16* act, WST wst, HOOK hook, PRE pre) {
  constexpr int NB = 2 * FN, NM = 4 * NB, RD = 5 + 2 * NB;
  const int fr = lane & 15, fq = lane >> 4;
  f32x4 acc[4][NB], accC[2];
  accC[0] = f32x4{0, 0, 0, 0}; accC[1] = f32x4{0, 0, 0, 0};
  asm volatile("" : "+v"(accC[0]), "+v"(accC[1]));
  const int rot = wm % FN;
  unsigned cad = fr >= 2 || wm >= FN ? smem_base + X_CN + 1536 : smem_base + G3_CT + (fr & 1) * 64 + fq * 16;
  unsigned cin = fr >= 2 || wm >= FN ? 0u : 512u;
  asm volatile("" : "+v"(cin));
#pragma unroll
  for (int i = 0; i < 4; ++i)
#pragma unroll
    for (int j = 0; j < NB; ++j) { acc[i][j] = f32x4{0, 0, 0, 0}; asm volatile("" : "+v"(acc[i][j])); }
  unsigned aad = smem_base + 65536 + wm * 4096 + lane * 16;
  unsigned bad[FN];
  {
    const int q = (lane >> 2) & 3, p = lane & 3;
    const int r = 8 * fq + q;
    const int sw = (r & 3) | (((r >> 3) & 1) << 2);
#pragma unroll
    for (int j = 0; j < FN; ++j) {
      const int gi = wn * 4 + (j + rot) % FN + (bn & 1);
      bad[j] = smem_base + r * 256 + ((gi ^ sw) * 32) + 8 * p;
    }
  }
  u32x4 aX[5], aY[5];
  u32x2 bXl[NB], bXh[NB], bYl[NB], bYh[NB];
  const unsigned m0w = smem_base + wid * 2048;
  const int ut = wid == 7 ? CT3 : wid == 6 ? ST3 : -1;
  const unsigned ve = wid == 7 ? lane * 32 + (blockIdx.x >> 3) * 2048u + 8192u : ((bn & 3) * 4 + (lane >> 4)) * 16384 + bm * 2048 + (lane & 15) * 128;
  unsigned tinc = lane < 14 ? 8 * 229376 : 8192;
  asm volatile("" : "+v"(ta), "+v"(tinc));
#define G3_READ(AR, BL, BH, SLOT, R)                                                                       \
  do {                                                                                                     \
    if ((R) < 2 * NB) {                                                                                    \
      constexpr int jb = ((R) < 2 * NB ? (R) : 0) >> 1;                                                    \
      constexpr int off = (SLOT) * 16384 + (jb / FN) * 8192 + ((R) & 1) * 1024;                            \
      if ((R) & 1) dsr_tr<off>(BH[jb], bad[jb % FN]);                                                       \
      else dsr_tr<off>(BL[jb], bad[jb % FN]);                                                               \
    } else if ((R) < 2 * NB + 4) {                                                                         \
      constexpr int ia = ((R) >= 2 * NB && (R) < 2 * NB + 4) ? (R) - 2 * NB : 0;                           \
      dsr128<(SLOT) * 16384 + ia * 1024>(AR[ia], aad);                                                      \
    } else {                                                                                               \
      if ((SLOT) == 0) cad += cin;                                       \
      dsr128<(SLOT) * 128>(AR[4], cad);                                                                    \
    }                                                                                                      \
  } while (0)
  auto rd = [&](auto SL, auto RR, u32x4 (&AR)[5], u32x2 (&BL)[NB], u32x2 (&BH)[NB]) { G3_READ(AR, BL, BH, SL.value, RR.value); };
#define G3_UNIT(AR, BL, BH, NAR, NBL, NBH, SLOT, U, LI)                                                    \
  do {                                                                                                     \
    WAIT_LGKM0();                                                                                          \
    u32x4 bb[NB];                                                                                          \
    _Pragma("unroll") for (int j = 0; j < NB; ++j) bb[j] = u32x4{BL[j][0], BL[j][1], BH[j][0], BH[j][1]};  \
    SCHED0();                                                                                              \
    asm volatile("s_nop 1");                                                                               \
    static_for<NM>([&](auto N) {                                                                           \
      constexpr int n = N.value;                                                                           \
      if constexpr (n == NM / 2) {                                                                         \
        wait_vm<(LI && (SLOT) < 2 && FN == 3) ? 13 + 4 * (SLOT) : 8>(); __builtin_amdgcn_s_barrier();      \
      }                                                                                                    \
      if constexpr (n == NM / 2 + TPOS && !LI) {                                                           \
          \
        if (((U) & 7) == wid) {                                                                            \
          asm volatile("s_mov_b32 m0, %2\n\ts_mov_b64 exec, %1\n\tglobal_load_lds_dword %0, off\n\ts_mov_b64 exec, -1"      \
                       ::"v"(ta), "s"((U) + WTD < 32 ? 0x3fffffull : 0x3fc000ull), "s"(smem_base + 131072 + 4096) : "memory"); \
          ta += tinc;                                                                                      \
        }                                                                                                  \
        if (FN == 3 && (SLOT) == 0 && (U) == ut) touch_lds(smem_base + 131072 + 4096, ve, rsE, 0);         \
      }                                                                                                    \
      MFMA_AV(acc[n & 3][n >> 2], bb[n >> 2], AR[n & 3]);                                                  \
      if constexpr (LI && (SLOT) == 0 && n < 12 && n % 3 == 0) hook(ic<-n / 3>{});                         \
      if constexpr (LI && (SLOT) == 0 && (n == 15 || n == 21)) hook(ic<-(n + 9) / 6>{});                   \
      if constexpr (LI && (SLOT) == 1 && (n == 0 || n == 3)) hook(ic<-6 - n / 3>{});                       \
      if constexpr (LI && (SLOT) == 2 && n >= 21 && n < 24) hook(ic<n - 8>{});                             \
      if constexpr (n < NM / 2) {                                                                          \
        if constexpr (LI && (SLOT) == 2 && n < 12) hook(ic<n + 1>{});                                      \
        if constexpr (n % 3 == 1 && n / 3 < 4) {                                                           \
          if constexpr (LI && (SLOT) > 0) wst(ic<((SLOT) + 3) & 3>{}, ic<(n / 3) & 3>{});                  \
          else G3_STAGE(((SLOT) + 3) & 3, n / 3, (U) + 3);                                                 \
        }                                                                                                  \
      } else {                                                                                             \
        constexpr int r0 = (n - NM / 2) * 2;                                                               \
        if constexpr (r0 < RD) rd(ic<((SLOT) + 1) & 3>{}, ic<(r0 < RD ? r0 : 0)>{}, NAR, NBL, NBH);        \
        if constexpr (r0 + 1 < RD) rd(ic<((SLOT) + 1) & 3>{}, ic<(r0 + 1 < RD ? r0 + 1 : 0)>{}, NAR, NBL, NBH); \
      }                                                                                                    \
    });                                                                                                    \
    MFMA_AV(accC[0], bb[0], AR[4]);                                                                        \
    MFMA_AV(accC[1], bb[FN], AR[4]);                                                                       \
    SCHED0();                                                                                              \
  } while (0)

  unsigned v0 = wm * 4096 + lane * 16;
#pragma unroll
  for (int j = 0; j < FN; ++j) asm volatile("" : "+v"(bad[j]));
  asm volatile("" : "+v"(aad), "+v"(cad), "+v"(voffA), "+v"(v0) : "s"(rsA), "s"(m0w));
  const poll_t pl = pre();
  static_for<4>([&](auto I) { asm volatile("" : "=v"(aX[I.value])); bload_sc1<I.value * 1024>(aX[I.value], v0, rsA, 0); });
  static_for<2 * NB>([&](auto RR) { rd(ic<0>{}, RR, aX, bXl, bXh); });
  dsr128<0>(aX[4], cad);
  G3_STAGE(1, 0, 1); G3_STAGE(1, 1, 1); G3_STAGE(2, 0, 2); G3_STAGE(2, 1, 2);
#pragma unroll
  for (int i = 0; i < 2; ++i) bload_lds<0>(smem_base + 131072 + 4096, voffA, rsA, 0x40000000u);
  asm volatile("s_waitcnt vmcnt(6)" : "+v"(aX[0]), "+v"(aX[1]), "+v"(aX[2]), "+v"(aX[3])::"memory");
  poll_end(pl);
  for (int u = 0; u < 28; u += 4) {
    G3_UNIT(aX, bXl, bXh, aY, bYl, bYh, 0, u, false);
    G3_UNIT(aY, bYl, bYh, aX, bXl, bXh, 1, u + 1, false);
    G3_UNIT(aX, bXl, bXh, aY, bYl, bYh, 2, u + 2, false);
    G3_UNIT(aY, bYl, bYh, aX, bXl, bXh, 3, u + 3, false);
  }
  G3_UNIT(aX, bXl, bXh, aY, bYl, bYh, 0, 28, true);
  G3_UNIT(aY, bYl, bYh, aX, bXl, bXh, 1, 29, true);
  if (FN == 3) __builtin_amdgcn_s_setprio(1);
  G3_UNIT(aX, bXl, bXh, aY, bYl, bYh, 2, 30, true);
  constexpr float KG = -1.4426950408889634f, KU = -0.6931471805599453f;
  constexpr int EB = 7, RS = 131072, CC = RS + 2048;
  u32x2 rs[4];
  u32x4 cc[2][4];
  unsigned ca[FN], pa, ra, sa;
  int l2;
  auto rdc = [&](auto J, auto E) { dsr128<E.value * 512>(cc[J.value & 1][E.value], ca[J.value]); };
  {
    WAIT_LGKM0();
    u32x4 bb[NB];
#pragma unroll
    for (int j = 0; j < NB; ++j) bb[j] = u32x4{bYl[j][0], bYl[j][1], bYh[j][0], bYh[j][1]};
    SCHED0();
    asm volatile("s_nop 1");
    static_for<NM>([&](auto N) {
      constexpr int n = N.value, k = n - EB;
      if constexpr (n == EB) {
        asm volatile("s_waitcnt lgkmcnt(0)" ::: "memory"); __builtin_amdgcn_s_barrier();
      }
      if constexpr (n == 0) { MFMA_AV(accC[0], bb[0], aY[4]); MFMA_AV(accC[1], bb[FN], aY[4]); }
      MFMA_AV(acc[n & 3][n >> 2], bb[n >> 2], aY[n & 3]);
      if constexpr (n == 0) {
        l2 = fresh_lane();
#pragma unroll
        for (int j = 0; j < FN; ++j) { ca[j] = smem_base + CC + (wn * 4 + (j + rot) % FN) * 64 + (l2 & 48); asm volatile("" : "+v"(ca[j])); }
      }
      if constexpr (n == 1) {
        pa = ca[0] + (l2 & 15) * 512;
        sa = smem_base + QX_SINK + l2 * 16;
        asm volatile("" : "+v"(pa), "+v"(sa), "+v"(l2));
      }
      if constexpr (n == 2) {
        pa = (l2 & 15) < (wm < FN ? 2 : 0) ? pa : sa;
        ra = smem_base + RS + (wm * 64 + (l2 & 15)) * 8;
        asm volatile("" : "+v"(pa), "+v"(ra));
      }
      if constexpr (n == 5) {
        asm volatile("" : "+v"(l2));
        sa = (l2 & 16) * 64 + (l2 & 32) * 8 + (l2 & 15) * 16;
        asm volatile("" : "+v"(sa));
      }
      if constexpr (n % 3 == 1 && n / 3 < 4) wst(ic<2>{}, ic<(n / 3) & 3>{});
      if constexpr (n == 3) {
        asm volatile("" : "+v"(accC[0]), "+v"(accC[1]));
        dsw128<0>(pa, __builtin_bit_cast(u32x4, accC[0] * KG));
        dsw128<1024>(pa, __builtin_bit_cast(u32x4, accC[1] * KU));
      }
      if constexpr (k == 0 || k == 1) { dsr64<k * 256>(rs[2 * k], ra); dsr64<k * 256 + 128>(rs[2 * k + 1], ra); }
      if constexpr (k >= 2 && k < 6) { rdc(ic<(k - 2) / 2>{}, ic<(k & 1) * 2>{}); rdc(ic<(k - 2) / 2>{}, ic<(k & 1) * 2 + 1>{}); }
      if constexpr (k >= 6 && k < 10) wst(ic<3>{}, ic<(k - 6) & 3>{});
    });
    asm volatile("s_nop 7" ::: "memory");
    SCHED0();
  }
  const rsrc_t rsO = mk_rsrc((char*)act + (size_t)(bm * 4 + wm) * (F_DIM / 32) * 4096, F_DIM / 32 * 4096);
  asm volatile("s_waitcnt lgkmcnt(4)" : "+v"(rs[0]), "+v"(rs[1]), "+v"(rs[2]), "+v"(rs[3]), "+v"(cc[0][0]), "+v"(cc[0][1]), "+v"(cc[0][2]), "+v"(cc[0][3])::"memory");
  float rg[4], ru[4];
#pragma unroll
  for (int i = 0; i < 4; ++i) { rg[i] = __uint_as_float(rs[i][1]) * KG; ru[i] = __uint_as_float(rs[i][1]) * KU; }
  auto frag = [&](auto JJ) {
    constexpr int j = JJ.value;
    auto half = [](auto v, int n) { const f32x4 f = __builtin_bit_cast(f32x4, v); return (n & 1) ? hi2(f) : lo2(f); };
    f32x2 g[8], uu[8], e[8];
    u32x4(&c)[4] = cc[j & 1];
#define STAGE(X) do { SCHED0(); _Pragma("unroll") for (int n = 0; n < 8; ++n) { X; } } while (0)
    STAGE(g[n] = __uint_as_float(rs[n >> 1][0]) * half(c[0], n) + half(c[1], n));
    STAGE(g[n] = rg[n >> 1] * half(acc[n >> 1][j], n) + g[n]);
    STAGE(e[n] = (f32x2{__builtin_amdgcn_exp2f(g[n][0]), __builtin_amdgcn_exp2f(g[n][1])}));
    STAGE(uu[n] = __uint_as_float(rs[n >> 1][0]) * half(c[2], n) + half(c[3], n));
    SCHED0();
    if constexpr (j + 2 < FN) static_for<4>([&](auto E) { rdc(ic<j + 2>{}, E); });
    STAGE(e[n] = e[n] + 1.0f);
    STAGE(e[n] = (f32x2{__builtin_amdgcn_rcpf(e[n][0]), __builtin_amdgcn_rcpf(e[n][1])}));
    STAGE(uu[n] = ru[n >> 1] * half(acc[n >> 1][FN + j], n) + uu[n]);
    STAGE(g[n] = g[n] * uu[n]);
    STAGE(g[n] = g[n] * e[n]);
    unsigned pk[8];
    STAGE(pk[n] = pk_bf16(g[n][0], g[n][1]));
    SCHED0();
    const int J = (bn >> 4) * 112 + (FN == 4 ? 48 : 0) + (bn & 15) * FN + (j + rot) % FN;
    static_for<2>([&](auto I) {
      constexpr int i = I.value * 4;
      const auto s0 = __builtin_amdgcn_permlane16_swap(pk[i], pk[i + 2], false, false);
      const auto s1 = __builtin_amdgcn_permlane16_swap(pk[i + 1], pk[i + 3], false, false);
      bstore_sc1<I.value * 2048>(u32x4{s0[0], s1[0], s0[1], s1[1]}, sa, rsO, (unsigned)((J >> 1) * 4096 + (J & 1) * 512));
    });
    if constexpr (j + 1 < FN) {
      u32x4(&d)[4] = cc[(j + 1) & 1];
      asm volatile("s_waitcnt lgkmcnt(%4)" : "+v"(d[0]), "+v"(d[1]), "+v"(d[2]), "+v"(d[3]) : "n"(j + 2 < FN ? 4 : 0) : "memory");
    }
    SCHED0();
  };
  static_for<FN>(frag);
}

namespace g4 {
constexpr int NU = 14, SL = 4, AD = 3;
constexpr int CN = X_CN;
constexpr int GT = 131072 + 5120;
constexpr int CU = 1;
constexpr int TU = NU - SL;
constexpr int PX = 137216;
constexpr int XP = 30, X3 = 16;
constexpr int na(int mh, int t) { return t < (mh ? TU - 2 : TU + 1) ? 4 : 0; }
constexpr int nb(int mh, int t) { return t < 0 || t >= TU ? 0 : t == TU - 1 && !mh ? 8 : 4 + (t == (mh ? CU : 0)); }
constexpr int behind(int mh, int t0, int u, int b) {
  int s = b ? na(mh, u) : 0;
  for (int t = t0 + 1; t < u; ++t) s += na(mh, t) + nb(mh, t);
  return s;
}
constexpr int vm(int mh, int u, int b) {
  if (mh && u >= TU) return u == TU ? 18 - 2 * b : u == TU + 1 ? 8 : 4 * (NU - 1 - u) + 2 - 2 * b;
  const int wa = u < NU - 2 ? nb(mh, u - AD + 1) + behind(mh, u - AD + 1, u, b) : behind(mh, u + 1 - SL, u, b);
  const int wb = u + 1 < SL ? 63 : u < TU - 1 ? behind(mh, u + 1 - SL, u, b) : u == TU - 1 ? behind(mh, u + 2 - SL, u, b) : u == TU + 1 ? behind(mh, TU - 1, u, b) : 63;
  return b == 0 ? wa + 2 : (wb < wa ? wb : wa);
}
constexpr int as(int mh, int v) { return v < NU - 2 ? v % AD : (v == NU - 2) == !mh ? AD : (TU - 1) % AD; }
}

template <int MH, class WST, class PRE, class EXTRA>
DI void g4_loop(f32x4 (&acc)[2][8], unsigned smem_base, int lane, int wid, int kb0, rsrc_t rsA, unsigned* fb, WST ldb, PRE pre, EXTRA extra) {
  using namespace g4;
  const int fq = lane >> 4, k2 = wid & 1;
  unsigned voffA[2], bad[8];
#pragma unroll
  for (int i = 0; i < 2; ++i) voffA[i] = lane * 16 + ((wid & 3) ^ i) * 1024;
  {
    const int q = (lane >> 2) & 3, r = 8 * fq + q, sw = q | ((fq & 1) << 2);
#pragma unroll
    for (int f = 0; f < 8; ++f) bad[f] = smem_base + k2 * 65536 + r * 256 + ((f ^ sw) * 32) + 8 * (lane & 3);
  }
  const unsigned sA = (unsigned)(kb0 + 2 * k2) * 4096u;
  u32x4 af[AD + 1][4];
  u32x2 bl[2][8], bh[2][8];
  auto lda = [&](auto V, auto J) {
    constexpr int v = V.value, j = J.value;
    asm volatile("" : "=v"(af[as(MH, v)][j]));
    bload_sc1<0>(af[as(MH, v)][j], voffA[j & 1], rsA, sA + (4 * v + (j >> 1)) * 4096u);
  };
  auto rdb = [&](auto V, auto B, auto E) {
    constexpr int o = (V.value & 3) * 16384 + B.value * 8192, f = E.value >> 1;
    if constexpr (E.value & 1) dsr_tr<o + 1024>(bh[B.value][f], bad[f]);
    else dsr_tr<o>(bl[B.value][f], bad[f]);
  };
  const unsigned wo = wid >> 1 == 1 ? 8192u : 65536u;
  unsigned wp = smem_base + (wid >> 1 == 1 ? PX : 32768 + (wid >> 1) * 8192) + k2 * wo + lane * 16;
#pragma unroll
  for (int f = 0; f < 8; ++f) asm volatile("" : "+v"(bad[f]));
  asm volatile("" : "+v"(voffA[0]), "+v"(voffA[1]), "+v"(wp) : "s"(rsA), "s"(sA));
  unsigned cv = 0, rp, fa, fb2, one = 1;
  const unsigned wf = smem_base + CN + wid * 4;
  const poll_t pl = pre();
  if (MH == 1) __builtin_amdgcn_s_setprio(0);
  static_for<4 * (AD - 1)>([&](auto J) { lda(ic<J.value / 4>{}, ic<J.value % 4>{}); });
  static_for<16>([&](auto E) { rdb(ic<0>{}, ic<0>{}, E); });
  static_for<NU>([&](auto U) {
    constexpr int u = U.value;
    constexpr bool last = u == NU - 1;
    static_for<32>([&](auto N) {
      constexpr int n = N.value, b = last ? n >= 24 || (n >= 12 && n < 20) : n >> 4, m = last ? (n == 12 ? 0 : n) : n & 15;
      constexpr int f = last ? (n >= 12 && n < 20 ? n - 12 : n & 7) : m >> 1, i = last ? n < 8 || (n >= 12 && n < 20) : m & 1;
      if constexpr (MH == 1 && u == TU && n == 0) {
        asm volatile("s_waitcnt vmcnt(0) lgkmcnt(0)\n\ts_barrier" ::: "memory");
        static_for<16>([&](auto J) {
          if constexpr (J.value == 8) extra(ic<-1>{});
          lda(ic<TU + J.value / 4>{}, ic<J.value % 4>{});
        });
        __builtin_amdgcn_s_barrier();
      }
      if constexpr (MH ? u == TU && n == X3 : last && n == XP) __builtin_amdgcn_s_barrier();
      if constexpr (m == 0) {
        asm volatile("s_waitcnt vmcnt(%2) lgkmcnt(0)" : "+v"(af[as(MH, u)][2 * b]), "+v"(af[as(MH, u)][2 * b + 1])
                     : "n"(vm(MH, u, b)) : "memory");
        if constexpr (u == 0 && b == 0 && MH == 1) poll_end(pl);
#pragma unroll
        for (int g = 0; g < 8; ++g) asm volatile("" : "+v"(bl[b][g]), "+v"(bh[b][g]));
        if constexpr (b == 1 && (u < TU || (u == TU + 1 && !MH))) __builtin_amdgcn_s_barrier();
        if constexpr (b == 1 && u == TU - 1 && MH == 0) __builtin_amdgcn_s_setprio(2);
      }
      const u32x4 bb = {bl[b][f][0], bl[b][f][1], bh[b][f][0], bh[b][f][1]};
      if constexpr (u == 0 && b == 0) asm volatile("v_mfma_f32_16x16x32_bf16 %0, %1, %2, 0" : "=&v"(acc[i][f]) : "v"(bb), "v"(af[0][i]));
      else MFMA_AV(acc[i][f], bb, af[as(MH, u)][2 * b + i]);
      if constexpr (m < 8 && !(last && b)) {
        rdb(ic<u + b>{}, ic<1 - b>{}, ic<2 * m>{});
        rdb(ic<u + b>{}, ic<1 - b>{}, ic<2 * m + 1>{});
      }
      if constexpr ((m & 3) == 2) {
        if constexpr (b == 0 && na(MH, u) == 4) lda(ic<(u < TU ? u + AD - 1 : NU - 1)>{}, ic<(m >> 2)>{});
        if constexpr (b == 1 && u + SL < NU) ldb(ic<u + SL>{}, ic<(m >> 2)>{});
      }
      if constexpr (MH == 0 && u == TU - 1 && b == 1 && (m & 3) == 0) lda(ic<NU - 2>{}, ic<(m >> 2)>{});
      if constexpr (MH == 0 && u == 0 && n == 0)
        asm volatile("ds_add_rtn_u32 %0, %1, %2" : "=v"(cv) : "v"(smem_base + CN + 256 + lane * 4), "v"(1u) : "memory");
      if constexpr (MH == 0 && u == 0 && n == 20) {
        asm volatile("" : "+v"(cv));
        signal(sig_t{mk_rsrc(fb, __builtin_amdgcn_readfirstlane(cv) == 3u ? 3 * 256 + 4 : 0), (unsigned)(lane * 256)});
      }
      if constexpr (MH == 1 && u == CU && n == 31)
        asm volatile("buffer_atomic_add %0, %1, %2, 0 offen sc0 sc1" : "+v"(cv) : "v"(lane * 128), "s"(mk_rsrc(fb, wid == 4 ? 256 : 0)) : "memory");
      if constexpr (MH == 1 && u == 6 - AD && n == 8) {
        asm volatile("s_waitcnt vmcnt(%1)" : "+v"(cv) : "n"(behind(1, CU, u, 0) + 2) : "memory");
        if (__builtin_expect(((unsigned)__builtin_amdgcn_ballot_w64(cv < 8u) & (wid == 4 ? 3u : 0u)) != 0u, 0)) {
          const poll_t p = poll(0u, fb, lane, 2, 8u);
          wait_vm<0>();
          poll_end(p);
        }
      }
      if constexpr (u >= TU) extra(ic<(u - TU) * 32 + n>{});
      if constexpr (MH && u == TU + 1 && n == 24) dsw32<64>(wf, 1u);
      if constexpr (MH && u == NU - 2 && n == 24) dsr32<64>(cv, wf ^ 8u);
      if constexpr (MH && last && n == 9) {
        asm volatile("" : "+v"(cv));
        if (__builtin_expect(__builtin_amdgcn_readfirstlane(cv) == 0u, 0)) lds_wait<1>((wf ^ 8u) + 64, 1u);
      }
      if constexpr (last && n == 10) {
        rp = wp + wo - k2 * 2 * wo; fa = wf; fb2 = wf ^ 4u;
        asm volatile("" : "+v"(rp), "+v"(fa), "+v"(fb2), "+v"(one));
      }
      if constexpr (last && n >= 15 && n < 31 && (n & 1)) dsw128<(n - 15) / 2 * 1024>(wp, __builtin_bit_cast(u32x4, acc[1][(n - 15) / 2 & 7]));
      if constexpr (last && n == 29) dsw32<0>(fa, one);
    });
  });
  u32x4 pq[8];
  unsigned fl;
  auto rdq = [&] {
    dsr32<0>(fl, fb2);
    static_for<8>([&](auto F) { dsr128<F.value * 1024>(pq[F.value], rp); });
  };
  auto there = [&] {
    asm volatile("s_waitcnt lgkmcnt(0)" : "+v"(fl), "+v"(pq[0]), "+v"(pq[1]), "+v"(pq[2]), "+v"(pq[3]), "+v"(pq[4]), "+v"(pq[5]), "+v"(pq[6]), "+v"(pq[7])::"memory");
    return __builtin_amdgcn_readfirstlane(fl) != 0u;
  };
  rdq();
  extra(ic<128>{});
  if (!there()) do rdq(); while (!there());
#pragma unroll
  for (int f = 0; f < 8; ++f) {
    asm volatile("" : "+v"(acc[0][f]));
    acc[0][f] += __builtin_bit_cast(f32x4, pq[f]);
  }
}

template <int MH, class WST>
DI void g4_phase(unsigned smem_base, int wid, const bf16* At, WST wst, const bf16* Yt,
                 bf16* __restrict__ C, float* xbuf, unsigned* flags) {
  using namespace g4;
  constexpr int KB = F_DIM / 32;
  const int lane = fresh_lane();
  constexpr int mh = MH;
  const int kq = wid & 3;
  const int xcd = blockIdx.x & 7, idx = blockIdx.x >> 3;
  const int kh = xcd & 1, bm = ((xcd >> 1) & 1) * 8 + (idx & 7), bn = (xcd >> 2) * 4 + (idx >> 3);
  const unsigned partner = blockIdx.x ^ 1;
  const int n0 = bn * 128, bm64 = bm * 2 + kh;
  const int fr = lane & 15, fq = lane >> 4;
  const unsigned voffL = lane * 16;
  const rsrc_t rsA = mk_rsrc((const char*)At + (size_t)(bm64 ^ mh ^ 1) * KB * 4096, KB * 4096);
  const rsrc_t rsEx = mk_rsrc(xbuf, 256 * 32768);
  const int kb0 = kh * (KB / 2);
  f32x4 acc[2][8];
  const int fo = ((idx & 7) * 2 + (xcd >> 2)) * 8 + (xcd & 1), fi = ((bm >> 1) * 2 + kh) * 8 + (idx >> 3) * 2;
  const unsigned pb = smem_base + (kq & 2 ? PX + (~kq & 1) * 8192 : 32768 + (~kq & 1) * 65536);
  unsigned pf = smem_base + CN + kq * 4;
  if constexpr (mh == 0) {
    g4_loop<0>(acc, smem_base, lane, wid, kb0, rsA, flags + (FL_B + fo) * 32, wst,
               [&] { wait_vm<8>(); __builtin_amdgcn_s_barrier(); return poll_t{0u, 0u}; }, [](auto) {});
    unsigned one = 1;
    poll_t pl = {0u, 0u};
    asm volatile("" : "+v"(pf), "+v"(one), "+v"(pl.a), "+v"(pl.b));
    static_for<4>([&](auto F) {
      constexpr int f = F.value;
      bstore_sc1<f * 1024>(pack8(acc[0][2 * f], acc[0][2 * f + 1]), voffL, rsEx, blockIdx.x * 32768u + kq * 8192u);
    });
    __builtin_amdgcn_s_barrier();
    __builtin_amdgcn_s_setprio(0);
    asm volatile("s_waitcnt vmcnt(0)\n\tbuffer_atomic_add %0, %1, %2, 0 offen sc1" ::"v"(one), "v"(voffL), "s"(mk_rsrc(flags + (FL_P + blockIdx.x) * 32 + kq, 4)) : "memory");
    unsigned spins = 0, t;
    asm volatile("buffer_atomic_add %0, %4, %5, 0 offen sc0 sc1\n\ts_sleep %6\n1:\n\t"
                 "buffer_atomic_add %1, %4, %5, 0 offen sc0 sc1\n\ts_waitcnt vmcnt(1)\n\tv_readfirstlane_b32 %3, %0\n\ts_nop 0\n\ts_cmp_lg_u32 %3, 0\n\ts_cbranch_scc1 2f\n\t"
                 "buffer_atomic_add %0, %4, %5, 0 offen sc0 sc1\n\ts_waitcnt vmcnt(1)\n\tv_readfirstlane_b32 %3, %1\n\ts_nop 0\n\ts_cmp_lg_u32 %3, 0\n\ts_cbranch_scc1 2f\n\t"
                 "s_add_u32 %2, %2, 1\n\ts_cmp_lt_u32 %2, 0x80000\n\ts_cbranch_scc1 1b\n2:"
                 : "+v"(pl.a), "+v"(pl.b), "+s"(spins), "=&s"(t) : "v"(voffL), "s"(mk_rsrc(flags + (FL_P + partner) * 32 + kq, 4)), "n"(PSLP) : "memory", "scc");
    static_for<4>([&](auto F) { bload_lds_sc1<F.value * 1024>(pb, voffL, rsEx, partner * 32768u + kq * 8192u); });
    wait_vm<0>();
    dsw32<128>(pf, one);
    poll_end(pl);
  } else {
  u32x4 yv[4];
  const rsrc_t rsX = mk_rsrc(Yt, S_LEN * D_DIM * 2);
  unsigned vx = (unsigned)(((bm64 >> 2) * 32 + bn * 4) * 16384 + ((bm64 & 3) * 4 + kq) * 1024 + ((((fq & 1) << 1) | (fq >> 1)) * 16 + fr) * 16);
  const unsigned oa = arrive<4, 4, 256>(smem_base + CN + 512, lane, flags + (FL_A + fo) * 32);
  asm volatile("" : "+v"(vx));
  u32x4 pv[4], gt[8], ct[2][2];
  f32x4 xr[8];
  unsigned ga = smem_base + GT + fq * 16, pa = pb + voffL;
  char* cp;
  unsigned fl;
  auto rdp = [&] {
    dsr32<128>(fl, pf);
    static_for<4>([&](auto F) { dsr128<F.value * 1024>(pv[F.value], pa); });
  };
  g4_loop<1>(acc, smem_base, lane, wid, kb0, rsA, flags + (FL_B + fi) * 32, wst, [&] { return await(oa, lane, flags + (FL_A + fi) * 32, 2, 8u); },
             [&](auto T) {
    constexpr int t = T.value, h = t >> 4, m = t & 15, c = h - 2;
    if constexpr (t < 0) {
      static_for<4>([&](auto J) { asm volatile("" : "=v"(yv[J.value])); bload_sc1<0>(yv[J.value], vx, rsX, J.value * 16384); });
      asm volatile("" : "+v"(ga), "+v"(pa), "+v"(pf));
    } else if constexpr (t == 128) rdp();
    else {
      if constexpr (t == X3) __builtin_amdgcn_s_setprio(1);
      if constexpr (c >= 0 && c < 4 && m == 7) {
        asm volatile("" : "+v"(yv[c]), "+v"(ct[c & 1][0]), "+v"(ct[c & 1][1]));
        const auto x0 = __builtin_amdgcn_permlane16_swap(yv[c][0], yv[c][2], false, false);
        const auto x1 = __builtin_amdgcn_permlane16_swap(yv[c][1], yv[c][3], false, false);
        yv[c] = u32x4{x0[0], x1[0], x0[1], x1[1]};
        asm volatile("" : "+v"(yv[c]));
      }
      if constexpr (c >= 0 && c < 4 && m >= 8) {
        constexpr int f = (m >> 1) & 1, e = (m & 1) * 2;
        if constexpr (m < 12) asm volatile("v_lshlrev_b32 %0, 16, %2\n\tv_and_b32 %1, 0xffff0000, %2" : "=&v"(xr[2 * c + f][e]), "=v"(xr[2 * c + f][e + 1]) : "v"(yv[c][m & 3]));
        else asm volatile("v_mul_f32 %0, %0, %2\n\tv_mul_f32 %1, %1, %3" : "+v"(xr[2 * c + f][e]), "+v"(xr[2 * c + f][e + 1]) : "v"(ct[c & 1][f][e]), "v"(ct[c & 1][f][e + 1]));
      }
      if constexpr (h >= 1 && h < 5 && (m == 8 || m == 9)) dsr128<512 + (2 * h + m - 10) * 64>(ct[(h - 1) & 1][m - 8], ga);
      if constexpr (t >= 108 && t < 124 && !(t & 1)) dsr128<(t - 108) * 32>(gt[(t - 108) / 2], ga);
      if constexpr (t == 20) {
        cp = (char*)C + (size_t)(bm64 * 64 + kq * 16 + (fr & 7)) * (D_DIM * 2) + n0 * 2 + (((fq & 1) << 1) | (fq >> 1)) * 16 + (fr >> 3) * 64;
        asm volatile("" : "+v"(cp));
      }
    }
  });
  asm volatile("" : "+v"(fl), "+v"(pv[0]), "+v"(pv[1]), "+v"(pv[2]), "+v"(pv[3]));
#pragma unroll
  for (int f = 0; f < 8; ++f) {
    asm volatile("" : "+v"(gt[f]));
    xr[f] += __builtin_bit_cast(f32x4, gt[f]) * acc[0][f];
    asm volatile("" : "+v"(xr[f]));
  }
  if (__builtin_amdgcn_readfirstlane(fl) == 0u) do {
    rdp();
    asm volatile("s_waitcnt lgkmcnt(0)" : "+v"(fl), "+v"(pv[0]), "+v"(pv[1]), "+v"(pv[2]), "+v"(pv[3])::"memory");
  } while (__builtin_amdgcn_readfirstlane(fl) == 0u);
  static_for<2>([&](auto L) {
    constexpr int l = L.value;
    u32x4 ov[2];
#pragma unroll
    for (int c = 0; c < 2; ++c) {
      const int f = 4 * l + 2 * c;
      const u32x4 p = pv[f >> 1];
      const f32x4 o0 = xr[f] + __builtin_bit_cast(f32x4, gt[f]) * f32x4{bf_lo(p[0]), bf_hi(p[0]), bf_lo(p[1]), bf_hi(p[1])};
      const f32x4 o1 = xr[f + 1] + __builtin_bit_cast(f32x4, gt[f + 1]) * f32x4{bf_lo(p[2]), bf_hi(p[2]), bf_lo(p[3]), bf_hi(p[3])};
      const auto s0 = __builtin_amdgcn_permlane16_swap(pk_bf16(o0[0], o0[1]), pk_bf16(o1[0], o1[1]), false, false);
      const auto s1 = __builtin_amdgcn_permlane16_swap(pk_bf16(o0[2], o0[3]), pk_bf16(o1[2], o1[3]), false, false);
      ov[c] = u32x4{s0[0], s1[0], s0[1], s1[1]};
    }
    u32x4 lo, hi;
#pragma unroll
    for (int e = 0; e < 4; ++e) {
      lo[e] = __builtin_amdgcn_update_dpp(ov[0][e], ov[1][e], 0x128, 0xf, 0xc, false);
      hi[e] = __builtin_amdgcn_update_dpp(ov[1][e], ov[0][e], 0x128, 0xf, 0x3, false);
    }
    gst128(cp + l * 128, lo);
    gst128(cp + 8 * (D_DIM * 2) + l * 128, hi);
  });
}
}

__global__ __launch_bounds__(512, 2) void block_kernel(char* ws, const bf16* X, const bf16* cnd, const bf16* wmod, const bf16* Wq,
                                                       const bf16* Wo, const bf16* Wg, const bf16* Wu, const bf16* Wd, bf16* C) {
  __shared__ __attribute__((aligned(1024))) char smem[Q_LDS];
  unsigned long long pc;
  asm volatile("s_getpc_b64 %0" : "=s"(pc));
  asm volatile("s_cmp_ge_u32 %0, 9\n\ts_cbranch_scc1 1f\n\ts_mov_b32 m0, %1\n\ts_nop 0\n\tbuffer_load_dword %2, %3, 0 offen lds\n1:"
               ::"s"((blockIdx.x >> 3) + __builtin_amdgcn_readfirstlane(threadIdx.x ^ 64)), "s"((unsigned)(uintptr_t)smem + QX_SINK), "v"((threadIdx.x & 63) * 128 + (blockIdx.x >> 3) * 8192),
                 "s"(mk_rsrc((const void*)(uintptr_t)(pc & ~255ull), CODEPF * 1024)) : "memory", "scc");
  asm volatile("" : "+s"(ws) : "s"(X), "s"(cnd), "s"(wmod), "s"(Wq), "s"(Wo), "s"(Wg), "s"(Wu), "s"(Wd), "s"(C));
  float* modp = (float*)ws;
  unsigned* ctab = (unsigned*)(ws + 212992);
  unsigned* flags = (unsigned*)(ws + 262144);
  float* stat = (float*)(ws + 524288);
  bf16* O = (bf16*)(ws + (1 << 20));
  bf16* Y = (bf16*)(ws + (9 << 20));
  bf16* H = (bf16*)(ws + (13 << 20));
  bf16* act = (bf16*)(ws + (17 << 20));
  float* xbuf = (float*)(ws + (32 << 20));
  const int wid = __builtin_amdgcn_readfirstlane(threadIdx.x >> 6);
  const int wm = wid & 3, wn = wid >> 2;
  const int xcd = blockIdx.x & 7, idx = blockIdx.x >> 3;
  if (idx >= 9 && idx < 12)
    asm volatile("s_mov_b32 m0, %0\n\ts_nop 0\n\tbuffer_load_dword %1, %2, 0 offen sc1 lds" ::"s"((unsigned)(uintptr_t)smem + QX_SINK),
                 "v"((((idx == 10 ? 512u : 0u) + threadIdx.x) << TSH) + xcd * 128 + 4096), "s"(idx == 11 ? mk_rsrc(C, 4 << 20) : mk_rsrc(ws + (1 << 20), 39 << 20)) : "memory");
  const int bm = idx & 7, bn = xcd * 4 + (idx >> 3);
  const unsigned smem_base = (unsigned)(uintptr_t)smem;
  const rsrc_t rsA = mk_rsrc((const char*)Y + (size_t)bm * 32 * 16384, 32 * 16384);
  const rsrc_t rsB = mk_rsrc(wn ? Wu : Wg, D_DIM * F_DIM * 2);
  const rsrc_t rsE = wid == 7 ? mk_rsrc((const void*)(uintptr_t)(pc & ~255ull), CODEPF * 1024) : mk_rsrc(stat, S_LEN * 128);
  const unsigned m0w = smem_base + wid * 2048;
  unsigned voffB0 = 0, voffB1 = 0;
  attn_phase(smem_base, wid, H, Wq, O, Wo, X, modp, Y, stat, flags, Wg, Wu, ctab, cnd, wmod, [&](auto S, auto I) {
    if constexpr (S.value == 0) {
      const int l2 = fresh_lane();
      const int r = (wm * 2 + I.value) * 4 + (l2 >> 4);
      const int sw = (r & 3) | (((r >> 3) & 1) << 2);
      const int c16 = l2 & 15;
      const int vb = (((bn & 3) * 224) & ~63) + ((c16 >> 1) ^ sw) * 32 + (c16 & 1) * 16;
      (I.value ? voffB1 : voffB0) = (unsigned)(r * (F_DIM * 2) + g3_line(bn >> 2, vb >> 7) * 128 + (vb & 127));
    }
    bload_lds<0>(m0w + S.value * 16384 + I.value * 1024, I.value ? voffB1 : voffB0, rsB, S.value * 229376u);
  });
  __builtin_amdgcn_s_setprio(0);
  const int lane = fresh_lane();
  const unsigned voffA = wid * 2048 + lane * 16;
  const unsigned oy = arrive<8, 8, 512>(smem_base + X_CN + 768, lane, flags + (FL_Y + xcd * 32 + (idx & 3)) * 32);
  auto pl = [&] { return await(oy, lane, flags + (FL_Y + bm * 32 + xcd * 4) * 32, 4, 8u); };
  const int n0 = ((xcd >> 2) * 4 + (idx >> 3)) * 128;
  const char* ta = lane >= 14 ? (const char*)Wd + blockIdx.x * 28672 + wid * 1024 + ((lane - 14) & 7) * 128
                              : (const char*)(lane < 7 ? Wg : Wu) + ((wid + WTD) * 32 + idx) * (F_DIM * 2) + xcd * 896 + (lane < 7 ? lane : lane - 7) * 128;
  unsigned vo = 0;
  const unsigned fsel = 0x80000u | (((xcd & 1) * 2 + (wm >> 1)) * 8);
  auto wsn = [&](auto WN, auto V, auto J) {
    constexpr int v = V.value, j = J.value, p = (4 * v + 2 * (j >> 1) + WN.value) * 2;
    if constexpr (v == 0 && j == 0) {
      const int l2 = fresh_lane();
      vo = (wid & 1) * 16384 + (l2 >> 4) * 2048 + ((((l2 & 15) >> 1) ^ ((l2 >> 4) | ((wid & 1) << 2))) * 32) + (l2 & 1) * 16;
    }
    unsigned so;
    asm volatile("s_bfe_u32 %0, %1, %2\n\ts_lshl_b32 %0, %0, 15\n\ts_or_b32 %0, %0, %3" : "=s"(so)
                 : "n"(act_frag(0, p) | act_frag(0, p + 1) << 8 | act_frag(1, p) << 16 | act_frag(1, p + 1) << 24), "s"(fsel), "n"((j & 1) * 8192) : "scc");
    bload_lds<0>(m0w + (j >> 1) * 65536 + (v & 3) * 16384 + (j & 1) * 1024, vo, mk_rsrc((const char*)Wd + n0 * 2, F_DIM * D_DIM * 2 - n0 * 2), so);
  };
  if (wn == 0) {
    auto wst = [&](auto V, auto J) { wsn(ic<0>{}, V, J); };
    g3_body<4>(smem_base, wm, wn, wid, lane, bm, bn, rsA, rsB, rsE, ta, voffA, voffB0, voffB1, act, wst, [](auto) {}, pl);
    g4_phase<0>(smem_base, wid, act, wst, Y, C, xbuf, flags);
  } else {
    auto wst = [&](auto V, auto J) { wsn(ic<1>{}, V, J); };
    u32x4 tv[8];
    f32x4 ps;
    float q0 = 0, q1 = 0;
    unsigned vs, gv;
    g3_body<3>(smem_base, wm, wn, wid, lane, bm, bn, rsA, rsB, rsE, ta, voffA, voffB0, voffB1, act, wst, [&](auto K) {
      constexpr int k = K.value;
      if constexpr (k <= 0) {
        if constexpr (k == 0) {
          const int l2 = fresh_lane();
          asm volatile("" : "=v"(gv));
          bload1_sc1(gv, (unsigned)((n0 + (wm & 1) * 64 + l2) * 4), mk_rsrc(modp + 49152 + (wm >= 2 ? 2048 : 3072), 4096));
          vs = (l2 >> 5) * 131072 + (bm * 256 + wm * 64) * 8 + (l2 & 31) * 16;
        }
        asm volatile("" : "=v"(tv[-k]));
        bload_sc1<0>(tv[-k], vs, mk_rsrc(stat, S_LEN * 128), -k * 16384);
      } else if constexpr (k <= 4) {
        if constexpr (k == 1) wait_vm<3>();
        asm volatile("" : "+v"(tv[2 * k - 2]), "+v"(tv[2 * k - 1]));
        const f32x4 t = __builtin_bit_cast(f32x4, tv[2 * k - 2]) + __builtin_bit_cast(f32x4, tv[2 * k - 1]);
        ps = k == 1 ? t : ps + t;
        asm volatile("" : "+v"(ps));
      } else if constexpr (k == 5) {
        const auto s0 = __builtin_amdgcn_permlane32_swap(__float_as_uint(ps[0]), __float_as_uint(ps[2]), false, false);
        const auto s1 = __builtin_amdgcn_permlane32_swap(__float_as_uint(ps[1]), __float_as_uint(ps[3]), false, false);
        ps[0] = (__uint_as_float(s0[0]) + __uint_as_float(s0[1])) * (1.0f / 16.0f);
        ps[1] = __uint_as_float(s1[0]) + __uint_as_float(s1[1]);
        const auto m = __builtin_amdgcn_permlane32_swap(__float_as_uint(ps[0]), __float_as_uint(ps[0]), false, false);
        ps[2] = __uint_as_float(m[0]); ps[3] = __uint_as_float(m[1]);
        asm volatile("" : "+v"(ps), "+v"(q0), "+v"(q1));
      } else if constexpr (k <= 13) {
        float d0, d1;
        asm volatile("v_sub_f32 %0, %4, %6\n\tv_sub_f32 %1, %5, %7\n\tv_fmac_f32 %2, %0, %0\n\tv_fmac_f32 %3, %1, %1"
                     : "=&v"(d0), "=&v"(d1), "+v"(q0), "+v"(q1) : "v"(tv[k - 6][0]), "v"(tv[k - 6][2]), "v"(ps[2]), "v"(ps[3]));
        if constexpr (k == 13) asm volatile("s_nop 1" : "+v"(q0), "+v"(q1));
      } else if constexpr (k == 14) {
        const auto q = __builtin_amdgcn_permlane32_swap(__float_as_uint(q0), __float_as_uint(q1), false, false);
        const float rstd = __builtin_amdgcn_rsqf((ps[1] + (__uint_as_float(q[0]) + __uint_as_float(q[1])) * (1.0f / 64.0f)) * (1.0f / D_DIM) + 1e-5f);
        const int l2 = fresh_lane();
        asm volatile("ds_write_b64 %0, %1" ::"v"(smem_base + 131072 + wm * 512 + (l2 & 31) * 16 + (l2 >> 5) * 8), "v"(f32x2{ps[0] * (-1.0f / 64.0f) * rstd, rstd}) : "memory");
      } else {
        asm volatile("" : "+v"(gv));
        dsw32<0>(smem_base + g4::GT + (wm * 64 + fresh_lane()) * 4, wm >= 2 ? __float_as_uint(__builtin_amdgcn_rcpf(__uint_as_float(gv))) : gv);
      }
    }, pl);
    g4_phase<1>(smem_base, wid, act, wst, Y, C, xbuf, flags);
  }
}

extern "C" void kernel_launch(void* const* d_in, const int* in_sizes, int n_in, void* d_out, int out_size,
                              void* d_ws, size_t ws_size, hipStream_t stream) {
  const bf16* x = (const bf16*)d_in[0];
  const bf16* wqkv = (const bf16*)d_in[1];
  const bf16* wo = (const bf16*)d_in[2];
  const bf16* wg = (const bf16*)d_in[4];
  const bf16* wu = (const bf16*)d_in[5];
  const bf16* wd = (const bf16*)d_in[6];
  const bf16* c = (const bf16*)d_in[7];
  const bf16* wmod = (const bf16*)d_in[8];
  bf16* out = (bf16*)d_out;

  block_kernel<<<256, 512, 0, stream>>>((char*)d_ws, x, c, wmod, wqkv, wo, wg, wu, wd, out);
}
```

```cpp
#include <hip/hip_runtime.h>
#include <hip/hip_bf16.h>
#include <stdint.h>

typedef __hip_bfloat16 bf16;
typedef __attribute__((ext_vector_type(8))) short bf16x8;
typedef __attribute__((ext_vector_type(4))) float f32x4;
typedef __attribute__((ext_vector_type(2))) float f32x2;
typedef __attribute__((ext_vector_type(4))) unsigned u32x4;
typedef __attribute__((ext_vector_type(2))) unsigned u32x2;
typedef __attribute__((ext_vector_type(2))) __bf16 bf16x2_t;
typedef __amdgpu_buffer_rsrc_t rsrc_t;

#define DI __device__ __forceinline__
constexpr int CODEPF = 68;

constexpr int S_LEN = 2048;
constexpr int D_DIM = 1024;
constexpr int F_DIM = 3584;
constexpr int QBAR = 2;
constexpr int NP1 = 5;
constexpr int PSLP = 4;
constexpr int NDLY = 20, TDLY = 15;
constexpr int TS = 12, TP = 6;
constexpr int WOS = 6;
constexpr int GDEP = 12, GS0 = 28;
constexpr int TPOS = 6, WTD = 6, CT3 = 24, ST3 = 16;

template <int V> struct ic { static constexpr int value = V; };
template <int N, int I = 0, class F>
DI void static_for(F&& f) {
  if constexpr (I < N) { f(ic<I>{}); static_for<N, I + 1>(f); }
}

DI float bf_lo(unsigned u) { return __uint_as_float(u << 16); }
DI float bf_hi(unsigned u) { return __uint_as_float(u & 0xffff0000u); }
DI unsigned pk_bf16(float lo, float hi) {
  const f32x2 v = {lo, hi};
  return __builtin_bit_cast(unsigned, __builtin_convertvector(v, bf16x2_t));
}
DI rsrc_t mk_rsrc(const void* p, unsigned bytes) { return __builtin_amdgcn_make_buffer_rsrc((void*)p, (short)0, (int)bytes, 0x00020000); }

template <int N>
DI void wait_vm() { asm volatile("s_waitcnt vmcnt(%0)" ::"n"(N > 63 ? 63 : N) : "memory"); }
#define WAIT_LGKM0() do { asm volatile("s_waitcnt lgkmcnt(0)" ::: "memory"); __builtin_amdgcn_sched_barrier(0); } while (0)
#define SCHED0() __builtin_amdgcn_sched_barrier(0)

#define MFMA_AV(C, B, A) asm volatile("v_mfma_f32_16x16x32_bf16 %0, %1, %2, %0" : "+v"(C) : "v"(B), "v"(A))
template <int OFF>
DI void bload_v(u32x4& d, unsigned voff, rsrc_t rs, unsigned soff) {
  asm volatile("buffer_load_dwordx4 %0, %1, %2, %3 offen offset:%4" : "+v"(d) : "v"(voff), "s"(rs), "s"(soff), "i"(OFF) : "memory");
}
template <int OFF>
DI void bload_lds(unsigned m0v, unsigned voff, rsrc_t rs, unsigned soff) {
  asm volatile("s_mov_b32 m0, %0\n\ts_nop 0\n\tbuffer_load_dwordx4 %1, %2, %3 offen offset:%4 lds" ::"s"(m0v), "v"(voff), "s"(rs), "s"(soff), "i"(OFF) : "memory");
}
DI void touch_lds(unsigned m0v, unsigned voff, rsrc_t rs, unsigned soff) {
  asm volatile("s_mov_b32 m0, %0\n\ts_nop 0\n\tbuffer_load_dword %1, %2, %3 offen lds" ::"s"(m0v), "v"(voff), "s"(rs), "s"(soff) : "memory");
}
template <int OFF>
DI void bload_lds_sc1(unsigned m0v, unsigned voff, rsrc_t rs, unsigned soff) {
  asm volatile("s_mov_b32 m0, %0\n\ts_nop 0\n\tbuffer_load_dwordx4 %1, %2, %3 offen offset:%4 sc1 lds" ::"s"(m0v), "v"(voff), "s"(rs), "s"(soff), "i"(OFF) : "memory");
}
template <int OFF>
DI void dsr128(u32x4& d, unsigned addr) { asm volatile("ds_read_b128 %0, %1 offset:%2" : "=v"(d) : "v"(addr), "i"(OFF) : "memory"); }
template <int OFF>
DI void dsr32(unsigned& d, unsigned addr) { asm volatile("ds_read_b32 %0, %1 offset:%2" : "=v"(d) : "v"(addr), "i"(OFF) : "memory"); }
DI void bload_nt(u32x4& d, unsigned voff, rsrc_t rs, unsigned soff) {
  asm volatile("buffer_load_dwordx4 %0, %1, %2, %3 offen nt" : "=v"(d) : "v"(voff), "s"(rs), "s"(soff) : "memory");
}
template <int OFF>
DI void dsr64(u32x2& d, unsigned addr) { asm volatile("ds_read_b64 %0, %1 offset:%2" : "=v"(d) : "v"(addr), "i"(OFF) : "memory"); }
template <int OFF>
DI void dsr_tr(u32x2& d, unsigned addr) { asm volatile("ds_read_b64_tr_b16 %0, %1 offset:%2" : "=v"(d) : "v"(addr), "i"(OFF) : "memory"); }
template <int OFF>
DI void dsw128(unsigned addr, u32x4 d) { asm volatile("ds_write_b128 %0, %1 offset:%2" ::"v"(addr), "v"(d), "i"(OFF) : "memory"); }
template <int OFF>
DI void dsw32(unsigned addr, unsigned d) { asm volatile("ds_write_b32 %0, %1 offset:%2" ::"v"(addr), "v"(d), "i"(OFF) : "memory"); }

DI void gst128(void* p, u32x4 v) {
  asm volatile("global_store_dwordx4 %0, %1, off sc1\n\ts_nop 1" ::"v"(p), "v"(v) : "memory");
}
DI int fresh_lane() {
  int l;
  asm volatile("v_mbcnt_lo_u32_b32 %0, -1, 0\n\tv_mbcnt_hi_u32_b32 %0, -1, %0" : "=v"(l));
  return l;
}
DI float hadd(f32x2 v) {
  float r;
  asm("v_add_f32 %0, %1, %2" : "=v"(r) : "v"(v[0]), "v"(v[1]));
  return r;
}
DI f32x2 lo2(f32x4 v) { return __builtin_shufflevector(v, v, 0, 1); }
DI f32x2 hi2(f32x4 v) { return __builtin_shufflevector(v, v, 2, 3); }
DI float rowsum4(float v) {
  const unsigned u = __float_as_uint(v);
  const auto a = __builtin_amdgcn_permlane16_swap(u, u, false, false);
  const unsigned t = __float_as_uint(__uint_as_float(a[0]) + __uint_as_float(a[1]));
  const auto b = __builtin_amdgcn_permlane32_swap(t, t, false, false);
  return __uint_as_float(b[0]) + __uint_as_float(b[1]);
}
DI float silu_f(float v) { return v * __builtin_amdgcn_rcpf(1.0f + __builtin_amdgcn_exp2f(v * -1.4426950408889634f)); }
DI float rbf(float v) { return bf_lo(pk_bf16(v, v)); }
DI float gain_nz(float m) {
  const float g = rbf(1.0f + rbf(m));
  return __builtin_fabsf(g) < 8.67361738e-19f ? 8.67361738e-19f : g;
}
template <int OFF>
DI void bload_sc1(u32x4& d, unsigned voff, rsrc_t rs, unsigned soff) {
  asm volatile("buffer_load_dwordx4 %0, %1, %2, %3 offen offset:%4 sc1" : "+v"(d) : "v"(voff), "s"(rs), "s"(soff), "i"(OFF) : "memory");
}
template <int OFF>
DI void bstore_sc1(u32x4 d, unsigned voff, rsrc_t rs, unsigned soff) {
  asm volatile("buffer_store_dwordx4 %0, %1, %2, %3 offen offset:%4 sc1\n\ts_nop 1" ::"v"(d), "v"(voff), "s"(rs), "s"(soff), "i"(OFF) : "memory");
}
DI void bload1_sc1(unsigned& d, unsigned voff, rsrc_t rs) {
  asm volatile("buffer_load_dword %0, %1, %2, 0 offen sc1" : "+v"(d) : "v"(voff), "s"(rs) : "memory");
}

constexpr int Q_SLOT = 21504, Q_NSL = 7;
constexpr int QX_WH = 4 * Q_SLOT;
constexpr int QX_HX = 65536;
constexpr int QX_GT = QX_WH + 65536;
constexpr int QX_CNT = QX_GT + 512;
constexpr int QX_SINK = QX_CNT + 512;
constexpr int G3_CT = QX_SINK + 1024;
constexpr int X_CN = G3_CT + 4096;
constexpr int Q_LDS = X_CN + 2048;
constexpr int P_TAB = 4 * Q_SLOT, P_RED = 5 * Q_SLOT, P_ST = P_RED + 4096;

DI unsigned lds_add(unsigned addr, unsigned v0) {
  unsigned o;
  asm volatile("ds_add_rtn_u32 %0, %1, %2\n\ts_waitcnt lgkmcnt(0)" : "=v"(o) : "v"(addr), "v"(v0) : "memory");
  return __builtin_amdgcn_readfirstlane(o);
}
template <int SLP>
DI void lds_wait(unsigned addr, unsigned n) {
  for (;;) {
    unsigned v;
    asm volatile("ds_read_b32 %0, %1\n\ts_waitcnt lgkmcnt(0)" : "=v"(v) : "v"(addr) : "memory");
    if (__builtin_amdgcn_readfirstlane(v) >= n) break;
    __builtin_amdgcn_s_sleep(SLP);
  }
}
struct poll_t { unsigned a, b; };
struct sig_t { rsrc_t rs; unsigned vo; };
DI sig_t drain(unsigned* out, int lane, int no, int so) {
  sig_t g = {mk_rsrc(out, (no - 1) * so + 4), (unsigned)(lane * so)};
  asm volatile("s_waitcnt vmcnt(0)" : "+v"(g.vo) : "s"(g.rs) : "memory");
  return g;
}
DI void signal(const sig_t& g) { asm volatile("buffer_atomic_add %0, %1, %2, 0 offen sc1" ::"v"(1u), "v"(g.vo), "s"(g.rs) : "memory"); }
#define POLL_RD(r) "v_mov_b32 " r ", 0\n\tbuffer_atomic_add " r ", %3, %4, 0 offen sc0 sc1\n\t"
#define POLL_CK(r) "s_waitcnt vmcnt(1)\n\tv_cmp_lt_u32 vcc, " r ", %5\n\ts_cbranch_vccz 2f\n\t"
DI poll_t poll(unsigned skip, unsigned* in, int lane, int nl, unsigned n) {
  poll_t r;
  asm volatile("s_cmp_lg_u32 %2, 0\n\ts_cbranch_scc1 2f\n\t" POLL_RD("%0") "s_sleep %6\n1:\n\t" POLL_RD("%1") POLL_CK("%0") POLL_RD("%0") POLL_CK("%1")
               "s_add_u32 %2, %2, 1\n\ts_cmp_lt_u32 %2, 0x80000\n\ts_cbranch_scc1 1b\n2:"
               : "=&v"(r.a), "=&v"(r.b), "+s"(skip) : "v"(lane * 128), "s"(mk_rsrc(in, nl * 128)), "v"(lane < nl ? n : 0u), "n"(PSLP) : "memory", "vcc", "scc");
  return r;
}
DI void walk(void* p, int mb, int lane) {
  asm volatile("buffer_atomic_add %0, %1, %2, 0 offen sc1" ::"v"(0u), "v"((lane << 20) + blockIdx.x * 128), "s"(mk_rsrc(p, mb << 20)) : "memory");
}
DI void poll_end(const poll_t& r) { asm volatile("" ::"v"(r.a), "v"(r.b)); }
template <int NW, int NO, int SO>
DI unsigned arrive(unsigned lcnt, int lane, unsigned* out) {
  asm volatile("" ::"v"(lcnt + lane * 4));
  const sig_t g = drain(out, lane, NO, SO);
  const unsigned o = lds_add(lcnt + lane * 4, 1u);
  if (o == NW - 1) {
    signal(g);
  }
  return o;
}
DI poll_t await(unsigned o, int lane, unsigned* in, int nl, unsigned n) {
  const poll_t r = poll(o, in, lane, nl, n);
  __builtin_amdgcn_s_barrier();
  return r;
}
constexpr int FL_M = 0, FL_H = 544, FL_O = 608, FL_P = 736, FL_Y = 992, FL_A = 1248, FL_B = 1376, FL_S = 1504;

DI f32x4 mfma0(u32x4 a, u32x4 b) {
  return __builtin_amdgcn_mfma_f32_16x16x32_bf16(__builtin_bit_cast(bf16x8, a), __builtin_bit_cast(bf16x8, b), f32x4{0, 0, 0, 0}, 0, 0, 0);
}
DI u32x4 pack8(const f32x4 a, const f32x4 b) { return u32x4{pk_bf16(a[0], a[1]), pk_bf16(a[2], a[3]), pk_bf16(b[0], b[1]), pk_bf16(b[2], b[3])}; }
template <class OP>
DI float rows4(const float (&v)[4], OP op) {
  const auto a = __builtin_amdgcn_permlane16_swap(__float_as_uint(v[0]), __float_as_uint(v[1]), false, false);
  const auto b = __builtin_amdgcn_permlane16_swap(__float_as_uint(v[2]), __float_as_uint(v[3]), false, false);
  const float t0 = op(__uint_as_float(a[0]), __uint_as_float(a[1])), t1 = op(__uint_as_float(b[0]), __uint_as_float(b[1]));
  const auto c = __builtin_amdgcn_permlane32_swap(__float_as_uint(t0), __float_as_uint(t1), false, false);
  return op(__uint_as_float(c[0]), __uint_as_float(c[1]));
}
DI void bcast4(float t, float (&v)[4]) {
  const auto c = __builtin_amdgcn_permlane32_swap(__float_as_uint(t), __float_as_uint(t), false, false);
  const auto a = __builtin_amdgcn_permlane16_swap(c[0], c[0], false, false);
  const auto b = __builtin_amdgcn_permlane16_swap(c[1], c[1], false, false);
  v[0] = __uint_as_float(a[0]); v[1] = __uint_as_float(a[1]); v[2] = __uint_as_float(b[0]); v[3] = __uint_as_float(b[1]);
}
DI float fmax2(float a, float b) { return __builtin_amdgcn_fmed3f(a, b, __builtin_inff()); }

DI float sum16(float v) {
  v += __builtin_bit_cast(float, __builtin_amdgcn_update_dpp(0, __builtin_bit_cast(int, v), 0x128, 0xf, 0xf, false));
  v += __builtin_bit_cast(float, __builtin_amdgcn_update_dpp(0, __builtin_bit_cast(int, v), 0x124, 0xf, 0xf, false));
  v += __builtin_bit_cast(float, __builtin_amdgcn_update_dpp(0, __builtin_bit_cast(int, v), 0x122, 0xf, 0xf, false));
  v += __builtin_bit_cast(float, __builtin_amdgcn_update_dpp(0, __builtin_bit_cast(int, v), 0x121, 0xf, 0xf, false));
  return v;
}
DI f32x4 sum4(const u32x4* p) {
  return (__builtin_bit_cast(f32x4, p[0]) + __builtin_bit_cast(f32x4, p[1])) + (__builtin_bit_cast(f32x4, p[2]) + __builtin_bit_cast(f32x4, p[3]));
}
template <int NB, int NMID, class MID>
DI void mod_gemv(unsigned red, int lane, int col0, int k0, const bf16* __restrict__ c, const bf16* __restrict__ wmod, MID mid) {
  const int fr = lane & 15, fq = lane >> 4;
  const rsrc_t rsW = mk_rsrc(wmod, 1024 * 6144 * 2);
  const unsigned vw = (unsigned)(((k0 + (fr >> 3) * 32 + fq * 8) * 6144 + col0 + (fr & 7) * 8) * 2);
  u32x4 raw[NB][8], cv[NB];
  static_for<NB>([&](auto B) {
    asm volatile("" : "=v"(cv[B.value]));
    bload_v<B.value * 128>(cv[B.value], (unsigned)((k0 + (fr & 1) * 32 + fq * 8) * 2), mk_rsrc(c, 2048), 0);
  });
  static_for<NB * 8>([&](auto N) {
    constexpr int b = N.value >> 3, j = N.value & 7;
    bload_nt(raw[b][j], vw, rsW, (b * 64 + j) * 12288);
  });
  __builtin_amdgcn_s_barrier();
  f32x4 acc[8];
#pragma unroll
  for (int n = 0; n < 8; ++n) acc[n] = f32x4{0, 0, 0, 0};
  u32x4 cf[NB];
  static_for<NB>([&](auto B) {
    constexpr int b = B.value;
    asm volatile("s_waitcnt vmcnt(%1)" : "+v"(cv[b]) : "n"(NB * 8) : "memory");
#pragma unroll
    for (int e = 0; e < 4; ++e) cf[b][e] = pk_bf16(silu_f(bf_lo(cv[b][e])), silu_f(bf_hi(cv[b][e])));
    asm volatile("" : "+v"(cf[b]));
  });
  static_for<NB>([&](auto B) {
    constexpr int b = B.value;
    if constexpr (b == 1) {
#pragma unroll
      for (int n = 0; n < 8; ++n) asm volatile("" : "+v"(acc[n]));
      mid();
    }
    asm volatile("s_waitcnt vmcnt(%8)" : "+v"(raw[b][0]), "+v"(raw[b][1]), "+v"(raw[b][2]), "+v"(raw[b][3]), "+v"(raw[b][4]), "+v"(raw[b][5]), "+v"(raw[b][6]), "+v"(raw[b][7])
                 : "n"(b ? NMID : (NB - 1) * 8) : "memory");
#pragma unroll
    for (int p = 0; p < 4; ++p) {
      u32x4 lo, hi;
#pragma unroll
      for (int t = 0; t < 4; ++t) {
        lo[t] = __builtin_amdgcn_perm(raw[b][2 * t + 1][p], raw[b][2 * t][p], 0x05040100u);
        hi[t] = __builtin_amdgcn_perm(raw[b][2 * t + 1][p], raw[b][2 * t][p], 0x07060302u);
      }
      acc[2 * p] = __builtin_amdgcn_mfma_f32_16x16x32_bf16(__builtin_bit_cast(bf16x8, lo), __builtin_bit_cast(bf16x8, cf[b]), acc[2 * p], 0, 0, 0);
      acc[2 * p + 1] = __builtin_amdgcn_mfma_f32_16x16x32_bf16(__builtin_bit_cast(bf16x8, hi), __builtin_bit_cast(bf16x8, cf[b]), acc[2 * p + 1], 0, 0, 0);
    }
  });
  if (fr == (fq >> 1)) {
    static_for<8>([&](auto R) {
      constexpr int r = R.value >> 1, h = (R.value & 1) * 4;
      dsw128<R.value * 16>(red + fq * 128, __builtin_bit_cast(u32x4, f32x4{acc[h][r], acc[h + 1][r], acc[h + 2][r], acc[h + 3][r]}));
    });
  }
}
template <int NR>
DI float red_sum(unsigned red, int lane) {
  unsigned v[NR];
  static_for<NR>([&](auto R) { dsr32<R.value * 256>(v[R.value], red + lane * 4); });
  asm volatile("s_waitcnt lgkmcnt(0)" ::: "memory");
  float s = 0;
#pragma unroll
  for (int r = 0; r < NR; r += 2) {
    asm volatile("" : "+v"(v[r]), "+v"(v[r + 1]));
    s += __uint_as_float(v[r]) + __uint_as_float(v[r + 1]);
  }
  return s;
}
DI unsigned cnt_read(unsigned* in, int lane, int nl) {
  unsigned t = 0;
  asm volatile("buffer_atomic_add %0, %1, %2, 0 offen sc0 sc1\n\ts_waitcnt vmcnt(0)" : "+v"(t) : "v"(lane * 128), "s"(mk_rsrc(in, nl * 128)) : "memory");
  return t;
}
DI bool words_set(unsigned* word, int lane) {
  unsigned t = 0;
  asm volatile("buffer_atomic_add %0, %1, %2, 0 offen sc0 sc1\n\ts_waitcnt vmcnt(0)" : "+v"(t) : "v"(lane * 8192), "s"(mk_rsrc(word, 7 * 8192 + 4)) : "memory");
  return (__builtin_amdgcn_ballot_w64(t == 0u) & 255u) == 0u;
}
DI void confirm(unsigned* word, int lane) {
#pragma unroll 1
  for (unsigned spins = 0; !words_set(word, lane) && ++spins < (1u << 20);) __builtin_amdgcn_s_sleep(2);
}
DI void set_words(unsigned* flags, int lane) {
  bstore_sc1<0>(u32x4{1u, 1u, 1u, 1u}, (unsigned)(lane * 128), mk_rsrc(flags + (FL_S + (blockIdx.x & 7) * 64) * 32, 64 * 128), 0);
}

template <class PRE>
DI void first_phase(unsigned smem_base, int wid, int lane, const bf16* __restrict__ x, const bf16* __restrict__ c, const bf16* __restrict__ wmod,
                    const bf16* __restrict__ W, float* modp, bf16* H, unsigned* flags, PRE prefill) {
  const int xcd = blockIdx.x & 7, idx = blockIdx.x >> 3;
  const int fr = lane & 15, fq = lane >> 4;
  const int u = xcd * 16 + (idx & 15), v = xcd * 8 + idx;
  unsigned* word = flags + __builtin_amdgcn_readfirstlane((FL_S + (blockIdx.x >> 2)) * 32 + (blockIdx.x & 3));
  const bool master = idx == 15;
  if (wid == (idx >= 16 ? 0 : 7)) {
    asm volatile("buffer_store_dword %0, %1, %2, 0 offen sc1" ::"v"(0u), "v"(lane * 8192), "s"(mk_rsrc(word, 7 * 8192 + 4)) : "memory");
  }
  if (master) {
    bstore_sc1<0>(u32x4{0u, 0u, 0u, 0u}, (unsigned)(wid * 2048 + lane * 16), mk_rsrc(flags + xcd * (FL_O / 8) * 32, FL_O / 8 * 128), 0);
    bstore_sc1<1024>(u32x4{0u, 0u, 0u, 0u}, (unsigned)(wid * 2048 + lane * 16), mk_rsrc(flags + xcd * (FL_O / 8) * 32, FL_O / 8 * 128), 0);
  }
  if (wid == 7) {
    dsw32<0>(smem_base + QX_CNT + lane * 4, 0u); dsw32<256>(smem_base + QX_CNT + lane * 4, 0u);
    dsw128<0>(smem_base + X_CN + lane * 16, u32x4{0u, 0u, 0u, 0u}); dsw128<1024>(smem_base + X_CN + lane * 16, u32x4{0u, 0u, 0u, 0u});
    asm volatile("s_waitcnt lgkmcnt(0)" ::: "memory");
  }
  if (__builtin_expect(idx >= 16, 1)) {
    unsigned o = 0;
    if (wid < 4) {
      const int col0 = (u >> 3) * 128, kp = u & 7;
      mod_gemv<1, 0>(smem_base + P_RED + wid * 512, lane, col0 + (wid & 1) * 64, kp * 128 + (wid >> 1) * 64, c, wmod, [] {});
      const sig_t g = {mk_rsrc(flags + (FL_M + (u & 15)) * 32, 16 * 4096 + 4), (unsigned)(lane * 4096)};
      asm volatile("" ::"s"(g.rs), "v"(g.vo));
      o = lds_add(smem_base + X_CN + 1280 + lane * 4, 1u);
      if (o == 3) {
        u32x2 r[4];
        static_for<4>([&](auto R) { dsr64<(R.value >> 1) * 1024 + (R.value & 1) * 256>(r[R.value], smem_base + P_RED + (lane >> 5) * 512 + (lane & 31) * 8); });
        asm volatile("s_waitcnt lgkmcnt(0)" : "+v"(r[0]), "+v"(r[1]), "+v"(r[2]), "+v"(r[3])::"memory");
        const f32x2 sum = (__builtin_bit_cast(f32x2, r[0]) + __builtin_bit_cast(f32x2, r[1])) + (__builtin_bit_cast(f32x2, r[2]) + __builtin_bit_cast(f32x2, r[3]));
        asm volatile("buffer_store_dwordx2 %0, %1, %2, 0 offen sc1" ::"v"(sum), "v"(lane * 8), "s"(mk_rsrc(modp + kp * 2048 + col0, 512)) : "memory");
        confirm(word, lane);
        signal(g);
      }
    } else {
      const rsrc_t rsX = mk_rsrc(x, S_LEN * D_DIM * 2);
      const int r0 = (wid - 4) * 4;
      u32x4 a[8];
      static_for<8>([&](auto N) {
        asm volatile("" : "=v"(a[N.value]));
        bload_v<(N.value & 1) * 1024>(a[N.value], (unsigned)((u * 16 + r0) * 2048 + lane * 16), rsX, (N.value >> 1) * 2048);
      });
      __builtin_amdgcn_s_barrier();
      asm volatile("s_waitcnt vmcnt(0)" : "+v"(a[0]), "+v"(a[1]), "+v"(a[2]), "+v"(a[3]), "+v"(a[4]), "+v"(a[5]), "+v"(a[6]), "+v"(a[7])::"memory");
      float sm[4], q[4];
      f32x2 xf[4][8];
#pragma unroll
      for (int i = 0; i < 4; ++i) {
        const unsigned ad = smem_base + (lane >> 5) * Q_SLOT + (lane & 31) * 256 + (((r0 + i) ^ (lane & 7)) * 16);
        dsw128<0>(ad, a[2 * i]); dsw128<2 * Q_SLOT>(ad, a[2 * i + 1]);
#pragma unroll
        for (int j = 0; j < 8; ++j) xf[i][j] = f32x2{bf_lo(a[2 * i + (j >> 2)][j & 3]), bf_hi(a[2 * i + (j >> 2)][j & 3])};
        const f32x2 t = ((xf[i][0] + xf[i][1]) + (xf[i][2] + xf[i][3])) + ((xf[i][4] + xf[i][5]) + (xf[i][6] + xf[i][7]));
        sm[i] = hadd(t);
      }
      const float mrow = sum16(rows4(sm, [](float p, float r) { return p + r; })) * (1.0f / D_DIM);
      bcast4(mrow, sm);
#pragma unroll
      for (int i = 0; i < 4; ++i) {
        f32x2 t0 = xf[i][0] - sm[i], t1 = xf[i][1] - sm[i];
        t0 *= t0; t1 *= t1;
#pragma unroll
        for (int j = 2; j < 8; j += 2) {
          const f32x2 d0 = xf[i][j] - sm[i], d1 = xf[i][j + 1] - sm[i];
          t0 += d0 * d0; t1 += d1 * d1;
        }
        t0 += t1;
        q[i] = hadd(t0);
      }
      const float rstd = __builtin_amdgcn_rsqf(sum16(rows4(q, [](float p, float r) { return p + r; })) * (1.0f / D_DIM) + 1e-5f);
      asm volatile("ds_write_b64 %0, %1\n\ts_waitcnt lgkmcnt(0)" ::"v"(smem_base + P_ST + (r0 + fq) * 8), "v"(f32x2{rstd, -mrow * rstd}) : "memory");
      prefill(wid - 4); prefill(wid);
    }
    u32x4 mp[8], xv[4], tb[4][4];
    u32x2 rr;
    const rsrc_t rsM = mk_rsrc(modp, 8 * 2048 * 4);
    const unsigned vm = (unsigned)((lane >> 5) * 4096 + wid * 512 + (lane & 31) * 16);
    asm volatile("" ::"s"(rsM), "v"(vm));
    const poll_t pm = poll(3 - o, flags + (FL_M + (1 + (u >> 3)) * 32) * 32, lane, 16, 8u);
    __builtin_amdgcn_s_barrier();
#pragma unroll
    for (int i = 0; i < 8; ++i) { asm volatile("" : "=v"(mp[i])); bload_sc1<0>(mp[i], vm, rsM, i * 8192); }
    const unsigned xa = smem_base + (wid >> 1) * Q_SLOT + (wid & 1) * 4096 + fq * 256 + ((fr ^ fq) * 16);
    dsr128<0>(xv[0], xa); dsr128<1024>(xv[1], xa ^ 64u); dsr128<2048>(xv[2], xa); dsr128<3072>(xv[3], xa ^ 64u);
    dsr64<0>(rr, smem_base + P_ST + fr * 8);
    asm volatile("s_waitcnt lgkmcnt(0)" : "+v"(xv[0]), "+v"(xv[1]), "+v"(xv[2]), "+v"(xv[3]), "+v"(rr)::"memory");
    f32x2 xn[4][4];
#pragma unroll
    for (int i = 0; i < 4; ++i)
#pragma unroll
      for (int d = 0; d < 4; ++d) {
        xn[i][d] = f32x2{bf_lo(xv[i][d]), bf_hi(xv[i][d])} * __uint_as_float(rr[0]) + __uint_as_float(rr[1]);
        asm volatile("" : "+v"(xn[i][d]));
      }
    char* hp = (char*)H + ((size_t)u * 32 + wid * 4) * 1024 + lane * 16;
    asm volatile("s_waitcnt vmcnt(0)" : "+v"(mp[0]), "+v"(mp[1]), "+v"(mp[2]), "+v"(mp[3]), "+v"(mp[4]), "+v"(mp[5]), "+v"(mp[6]), "+v"(mp[7]), "+v"(hp)::"memory");
    poll_end(pm);
    f32x4 mv = sum4(mp) + sum4(mp + 4);
#pragma unroll
    for (int e = 0; e < 4; ++e) {
      mv[e] = rbf(mv[e]);
      if (lane & 32) mv[e] = rbf(1.0f + mv[e]);
    }
    dsw128<0>(smem_base + P_TAB + wid * 1024 + lane * 16, __builtin_bit_cast(u32x4, mv));
    static_for<16>([&](auto N) {
      constexpr int i = N.value >> 2, j = N.value & 3;
      dsr128<i * 128 + (j >> 1) * 512 + (j & 1) * 16>(tb[i][j], smem_base + P_TAB + wid * 1024 + fq * 32);
    });
    static_for<4>([&](auto I) {
      constexpr int i = I.value;
      asm volatile("s_waitcnt lgkmcnt(%4)" : "+v"(tb[i][0]), "+v"(tb[i][1]), "+v"(tb[i][2]), "+v"(tb[i][3]) : "n"(12 - 4 * i) : "memory");
      u32x4 ho;
#pragma unroll
      for (int d = 0; d < 4; ++d) {
        const f32x4 gg = __builtin_bit_cast(f32x4, tb[i][2 + (d >> 1)]), ss = __builtin_bit_cast(f32x4, tb[i][d >> 1]);
        const f32x2 hv = xn[i][d] * ((d & 1) ? hi2(gg) : lo2(gg)) + ((d & 1) ? hi2(ss) : lo2(ss));
        ho[d] = pk_bf16(hv[0], hv[1]);
      }
      gst128(hp + i * 1024, ho);
    });
    const sig_t g = drain(flags + (FL_H + (u >> 3) * 4) * 32, lane, ((u & 7) == 7 && u != 127) ? 8 : 4, 128);
    if (lds_add(smem_base + X_CN + 1024 + lane * 4, 1u) == 7) {
      signal(g);
    }
  } else {
    const rsrc_t rsS = mk_rsrc(word, wid == 7 ? 7 * 8192 + 4 : 0);
    unsigned seen = 0;
    auto mid = [&] { asm volatile("buffer_atomic_add %0, %1, %2, 0 offen sc0 sc1" : "+v"(seen) : "v"(lane * 8192), "s"(rsS) : "memory"); };
    if (!master) {
      const int ql = (wid * 7 + idx - 8) * 64 + lane;
      if (idx >= 8 && wid < 3) touch_lds(smem_base + QX_SINK, (unsigned)((ql >> 1) * 2048 + (xcd & 3) * 512 + 256 + (ql & 1) * 128), mk_rsrc(W, D_DIM * 3 * D_DIM * 2), 0);
      prefill(wid);
    }
    if (idx < 8) {
      if (wid == 0)
        bstore_sc1<0>(u32x4{0u, 0u, 0u, 0u}, (unsigned)(lane * 128), mk_rsrc(flags + (FL_O + v * ((FL_S - FL_O) / 64)) * 32, (FL_S - FL_O) / 64 * 128), 0);
      __builtin_amdgcn_s_sleep(NDLY);
      mod_gemv<2, 1>(smem_base + P_RED + wid * 512, lane, 2048 + (xcd * 8 + wid) * 64, idx * 128, c, wmod, mid);
      const float sum = red_sum<2>(smem_base + P_RED + wid * 512, lane);
      asm volatile("buffer_store_dword %1, %2, %3, 0 offen sc1\n\ts_waitcnt vmcnt(1)" : "+v"(seen) : "v"(sum), "v"(lane * 4), "s"(mk_rsrc(modp + 16384 + ((xcd * 8 + wid) * 8 + idx) * 64, 256)) : "memory");
    } else if (!master) {
      __builtin_amdgcn_s_sleep(TDLY);
#pragma unroll
      for (int q = 0; q < 3; ++q) {
        const int ql = ((q * 8 + wid) * 7 + idx - 8) * 64 + lane + 224 * 12;
        touch_lds(smem_base + QX_SINK, (unsigned)((ql >> 2) * 2048 + (xcd & 3) * 512 + (ql & 3) * 128), mk_rsrc(W, D_DIM * 3 * D_DIM * 2), 0);
      }
      __builtin_amdgcn_s_barrier();
      wait_vm<3>();
      mid();
      asm volatile("s_waitcnt vmcnt(0)" : "+v"(seen)::"memory");
    } else {
      wait_vm<0>();
      __builtin_amdgcn_s_barrier();
      bstore_sc1<0>(u32x4{1u, 1u, 1u, 1u}, (unsigned)(wid * 1024 + lane * 16), mk_rsrc(flags + (FL_S + xcd * 64) * 32, 64 * 128), 0);
      unsigned spins = 0;
      if (wid == 7) {
#pragma unroll 1
        do set_words(flags, lane); while (!words_set(word, lane) && ++spins < (1u << 20));
      }
      __builtin_amdgcn_s_barrier();
      prefill(wid);
      if (wid == 7) {
#pragma unroll 1
        do set_words(flags, lane); while (__builtin_amdgcn_ballot_w64(cnt_read(flags + FL_M * 32, lane, 16) < (lane < 16 ? 8u : 0u)) != 0ull && ++spins < (1u << 20));
      }
      seen = 1;
    }
    if (wid != 7) wait_vm<0>();
    else if ((__builtin_amdgcn_ballot_w64(seen == 0u) & 255u) != 0u) confirm(word, lane);
  }
}
DI void wo_piece(unsigned smem_base, unsigned p, unsigned rot, unsigned voff, rsrc_t rs) {
  const unsigned pb = p >> 2, kb = (pb & 24u) | ((pb + rot) & 7u);
  bload_lds<0>(smem_base + (p < 64u ? QX_WH + p * 1024u : (p - 64u) * 1024u), voff, rs, kb * 65536u + (p & 3u) * 16384u);
}

DI void qkv_ldw(unsigned smem_base, int lane, int sw, int n0, rsrc_t rsW, unsigned so, unsigned u) {
#pragma unroll
  for (int i = 0; i < 4; ++i) {
    const int r = (i & 1) * 8 + (lane >> 3), ch = lane & 7;
    const int g = ((r >> 1) & 1) | (((r >> 3) & 1) << 1);
    bload_lds<0>(smem_base + 9216 + sw * 4096 + so + i * 1024, (unsigned)(r * 6144 + sw * 2048 + (n0 + (((ch >> 1) ^ g) * 16) + (ch & 1) * 8) * 2), rsW,
                 u * 196608u + (i >> 1) * 98304u);
  }
}
DI void qkv_loader(unsigned smem_base, int lane, int sw, int m0, int n0, const bf16* H, rsrc_t rsW,
                   unsigned rot, unsigned voffO, rsrc_t rsO) {
  const rsrc_t rsH = mk_rsrc(H, S_LEN * D_DIM * 2);
  int fb0 = (m0 >> 4) - 1 + 3 * sw; fb0 = fb0 < 0 ? 0 : fb0;
  const unsigned voffH0 = (unsigned)(fb0 * 32768 + lane * 16);
  const unsigned voffH1 = (unsigned)(((m0 >> 4) + 3 * sw) * 32768 + lane * 16);
  const unsigned m0a = smem_base + sw * 3072;
  auto ldh = [&](unsigned so, unsigned u) {
    bload_lds_sc1<0>(m0a + so, voffH0, rsH, u * 1024u);
    bload_lds_sc1<0>(m0a + so + 1024, voffH1, rsH, u * 1024u);
    bload_lds_sc1<0>(m0a + so + 2048, voffH1, rsH, u * 1024u + 32768u);
  };
  auto ldw = [&](unsigned so, unsigned u) { qkv_ldw(smem_base, lane, sw, n0, rsW, so, u); };
  asm volatile("" ::"v"(voffH0), "v"(voffH1), "s"(rsH), "s"(m0a));
  __builtin_amdgcn_s_barrier();
#pragma unroll
  for (int u = 1; u <= NP1; ++u) ldh(u * Q_SLOT, u);
  wait_vm<3 * (NP1 - 1)>();
  __builtin_amdgcn_s_barrier();
#pragma unroll
  for (int u = NP1 + 1; u < Q_NSL; ++u) ldh(u * Q_SLOT, u);
  static_for<Q_NSL - 2, 1>([&](auto U) {
    constexpr int u = U.value;
    ldh((u - 1) * Q_SLOT, u + Q_NSL - 1); ldw((u - 1) * Q_SLOT, u + Q_NSL - 1);
    wait_vm<3 * (Q_NSL - 2 - u) + 7 * u>();
    __builtin_amdgcn_s_barrier();
  });
  unsigned sb = (Q_NSL - 3) * Q_SLOT;
  for (int u = Q_NSL - 2; u < 32 - (Q_NSL - 1); ++u) {
    ldh(sb, u + Q_NSL - 1); ldw(sb, u + Q_NSL - 1);
    sb = (sb == (Q_NSL - 1) * Q_SLOT) ? 0u : sb + Q_SLOT;
    wait_vm<(Q_NSL - 2) * 7>();
    __builtin_amdgcn_s_barrier();
  }
  for (int t = 0; t < 6; ++t) {
    const unsigned p0 = (unsigned)(t * 21 + (t >= 3 ? 1 : 0) + sw * 7);
#pragma unroll
    for (int i = 0; i < 7; ++i) wo_piece(smem_base, p0 + i, rot, voffO, rsO);
    if (t < 5) wait_vm<(Q_NSL - 2) * 7>();
    __builtin_amdgcn_s_barrier();
  }
}

template <int WM>
DI void qkv_mfma(unsigned smem_base, int lane, int wn, int bm, int bn, const bf16* H, bf16* __restrict__ O) {
  const int fr = lane & 15, fq = lane >> 4;
  unsigned bb0, bb1;
  const unsigned ab = smem_base + lane * 16;
  {
    const int q = (lane >> 2) & 3, p = lane & 3;
    const int r = 8 * fq + q;
    const int g = ((r >> 1) & 1) | (((r >> 3) & 1) << 1);
    bb0 = smem_base + 9216 + r * 128 + (((wn * 2) ^ g) * 32) + 8 * p;
    bb1 = bb0 ^ 32u;
  }
  const bool par = fr & 1, c0 = 4 * fq + (fr & 1) <= fr, c1 = 4 * fq + 2 + (fr & 1) <= fr;
  const unsigned s0 = par ? 0x01000c0cu : 0x0c0c0100u, s1 = par ? 0x03020c0cu : 0x0c0c0302u;
  u32x4 sel = {c0 ? s0 : 0x0c0c0c0cu, c1 ? s1 : 0x0c0c0c0cu, c0 ? 0x0c0c0c0cu : s0, c1 ? 0x0c0c0c0cu : s1};
  unsigned hx = ab + QX_HX + wn * 3072;
  asm volatile("" : "+v"(sel), "+v"(hx));
  const rsrc_t rsO = mk_rsrc((char*)O + (size_t)((bm * 2 + WM) * 32 + bn * 2 + wn) * 4096, 4096);
  f32x4 acc[4][6], acch[2];
#pragma unroll
  for (int i = 0; i < 4; ++i)
#pragma unroll
    for (int j = 0; j < 6; ++j) { acc[i][j] = f32x4{0, 0, 0, 0}; asm volatile("" : "+v"(acc[i][j])); }
#pragma unroll
  for (int j = 0; j < 2; ++j) { acch[j] = f32x4{0, 0, 0, 0}; asm volatile("" : "+v"(acch[j])); }
  __builtin_amdgcn_s_setprio(2);
  u32x4 aX[5], aY[5];
  u32x2 bXl[6], bXh[6], bYl[6], bYh[6];
  unsigned ra = ab, rb0 = bb0, rb1 = bb1, so = 0;
#define Q_READ(AR, BL, BH, R)                                                                                    \
  do {                                                                                                           \
    if ((R) < 12) {                                                                                              \
      constexpr int jb = ((R) < 12 ? (R) : 0) >> 1;                                                              \
      if ((R) & 1) dsr_tr<(jb >> 1) * 4096 + 512>(BH[jb], (jb & 1) ? rb1 : rb0);                                 \
      else dsr_tr<(jb >> 1) * 4096>(BL[jb], (jb & 1) ? rb1 : rb0);                                               \
    } else if ((R) < 16) {                                                                                       \
      constexpr int ia = ((R) >= 12 && (R) < 16) ? (R) - 12 : 0;                                                 \
      dsr128<1024 + WM * 4096 + ia * 1024>(AR[ia], ra);                                                          \
    } else {                                                                                                     \
      dsr128<0>(AR[4], ra);                                                                                      \
    }                                                                                                            \
  } while (0)
#define Q_MFMA(AR, BB, N)                                                                                        \
  do {                                                                                                           \
    if ((N) < 16) MFMA_AV(acc[(N) & 3][((N) < 24 ? (N) : 0) >> 2], BB[((N) < 24 ? (N) : 0) >> 2], AR[(N) & 3]);  \
    else if ((N) < 24) MFMA_AV(acc[(N) & 3][((N) < 24 ? (N) : 0) >> 2], AR[(N) & 3], BB[((N) < 24 ? (N) : 0) >> 2]); \
    else if (WM == 0) MFMA_AV(acch[(N) & 1], BB[2 + ((N) & 1)], AR[4]);                                          \
    else MFMA_AV(acch[(N) & 1], AR[4], BB[4 + ((N) & 1)]);                                                       \
  } while (0)
#define Q_ADV(K)                                                                                                 \
  do {                                                                                                           \
    if ((K) == 0) asm volatile("s_add_u32 %0, %0, %1\n\ts_cmp_eq_u32 %0, %2\n\ts_cselect_b32 %0, 0, %0" : "+s"(so) : "n"(Q_SLOT), "n"(Q_NSL * Q_SLOT) : "scc"); \
    else if ((K) == 1) asm volatile("v_add_u32 %0, %1, %2" : "=v"(rb0) : "s"(so), "v"(bb0));                     \
    else if ((K) == 2) asm volatile("v_add_u32 %0, %1, %2" : "=v"(rb1) : "s"(so), "v"(bb1));                     \
    else asm volatile("v_add_u32 %0, %1, %2" : "=v"(ra) : "s"(so), "v"(ab));                                     \
  } while (0)
#define Q_UNIT(AR, BL, BH, NAR, NBL, NBH)                                                                        \
  do {                                                                                                           \
    WAIT_LGKM0();                                                                                                \
    u32x4 bb[6];                                                                                                 \
    _Pragma("unroll") for (int j = 0; j < 6; ++j) bb[j] = u32x4{BL[j][0], BL[j][1], BH[j][0], BH[j][1]};         \
    SCHED0();                                                                                                    \
    static_for<26>([&](auto N) {                                                                                 \
      constexpr int n = N.value;                                                                                 \
      if constexpr (n == QBAR) __builtin_amdgcn_s_barrier();                                                     \
      Q_MFMA(AR, bb, n);                                                                                         \
      if constexpr (n >= QBAR && n < QBAR + 17) Q_READ(NAR, NBL, NBH, n - QBAR);                                 \
      if constexpr (n >= QBAR + 18 && n < QBAR + 22) Q_ADV(n - QBAR - 18);                                       \
    });                                                                                                          \
    SCHED0();                                                                                                    \
  } while (0)
  {
    const rsrc_t rsH = mk_rsrc(H, S_LEN * D_DIM * 2);
    unsigned vh = lane * 16 + (bm * 8 + WM * 4) * 32768, vl = lane * 16 + (bm ? bm * 8 - 1 : 0) * 32768;
    asm volatile("" : "+v"(vh), "+v"(vl), "+v"(rb0), "+v"(rb1) : "s"(rsH));
    __builtin_amdgcn_s_barrier();
    static_for<5>([&](auto I) { asm volatile("" : "=v"(aX[I.value])); bload_sc1<0>(aX[I.value], I.value < 4 ? vh : vl, rsH, (I.value & 3) * 32768); });
    static_for<12>([&](auto R) { Q_READ(aX, bXl, bXh, R.value); });
    static_for<4>([&](auto K) { Q_ADV(K.value); });
    asm volatile("s_waitcnt vmcnt(0)" : "+v"(aX[0]), "+v"(aX[1]), "+v"(aX[2]), "+v"(aX[3]), "+v"(aX[4])::"memory");
  }
  for (int u = 0; u < 32; u += 2) {
    Q_UNIT(aX, bXl, bXh, aY, bYl, bYh);
    Q_UNIT(aY, bYl, bYh, aX, bXl, bXh);
  }
  asm volatile("s_waitcnt lgkmcnt(0)\n\ts_nop 7" ::: "memory");
#pragma unroll
  for (int i = 0; i < 5; ++i) asm volatile("" : "+v"(aX[i]));
#pragma unroll
  for (int j = 0; j < 6; ++j) asm volatile("" : "+v"(bXl[j]), "+v"(bXh[j]));
#pragma unroll
  for (int i = 0; i < 4; ++i)
#pragma unroll
    for (int j = 0; j < 6; ++j) asm volatile("" : "+v"(acc[i][j]));
  asm volatile("" : "+v"(acch[0]), "+v"(acch[1]));
  SCHED0();
  u32x4 kk[5], vv[5];
  if (WM == 0) {
    kk[4] = pack8(acc[3][2], acc[3][3]); vv[4] = pack8(acc[3][4], acc[3][5]);
    dsw128<0>(hx, kk[4]); dsw128<1024>(hx, vv[4]);
  } else {
    dsw128<2048>(hx, pack8(acch[0], acch[1]));
  }
  asm volatile("s_waitcnt lgkmcnt(0)" ::: "memory");
  __builtin_amdgcn_s_barrier();
  if (WM == 0) dsr128<2048>(vv[0], hx);
  else { dsr128<0>(kk[0], hx); dsr128<1024>(vv[0], hx); }
  SCHED0();
  if (WM == 0) kk[0] = pack8(acch[0], acch[1]);
#pragma unroll
  for (int i = 0; i < (WM == 0 ? 3 : 4); ++i) kk[i + 1] = pack8(acc[i][2], acc[i][3]);
  const float cs = 0.17677669529663687f * 1.4426950408889634f;
  float l0[4], l1[4], tm[4];
#pragma unroll
  for (int ii = 1; ii < 5; ++ii) {
    const int i = ii & 3;
    const u32x4 q = pack8(acc[i][0], acc[i][1]);
    if (WM == 1 && i == 0) asm volatile("s_waitcnt lgkmcnt(0)" : "+v"(kk[0]), "+v"(vv[0])::"memory");
    const f32x4 sc = mfma0(kk[i + 1], q), sp = mfma0(kk[i], q);
    float p0 = par ? sp[1] : sp[0], p1 = par ? sp[3] : sp[2];
    if (WM == 0 && i == 0 && bm == 0) { p0 = -5.656854249e9f; p1 = -5.656854249e9f; }
    l0[i] = c0 ? (par ? sc[1] : sc[0]) : p0;
    l1[i] = c1 ? (par ? sc[3] : sc[2]) : p1;
    tm[i] = fmax2(l0[i], l1[i]);
  }
#pragma unroll
  for (int i = 0; i < (WM == 0 ? 3 : 4); ++i) vv[i + 1] = pack8(acc[i][4], acc[i][5]);
  bcast4(rows4(tm, [](float a, float b) { return fmax2(a, b); }), tm);
#pragma unroll
  for (int i = 0; i < 4; ++i) {
    l0[i] = __builtin_amdgcn_exp2f((l0[i] - tm[i]) * cs);
    l1[i] = __builtin_amdgcn_exp2f((l1[i] - tm[i]) * cs);
    tm[i] = l0[i] + l1[i];
  }
  bcast4(__builtin_amdgcn_rcpf(rows4(tm, [](float a, float b) { return a + b; })), tm);
  if (WM == 0) asm volatile("s_waitcnt lgkmcnt(0)" : "+v"(vv[0])::"memory");
#pragma unroll
  for (int i = 0; i < 4; ++i) {
    const unsigned pp = pk_bf16(l0[i] * tm[i], l1[i] * tm[i]);
    const u32x4 pr = {__builtin_amdgcn_perm(pp, pp, sel[0]), __builtin_amdgcn_perm(pp, pp, sel[1]),
                      __builtin_amdgcn_perm(pp, pp, sel[2]), __builtin_amdgcn_perm(pp, pp, sel[3])};
    const u32x4 ov = pack8(mfma0(u32x4{vv[i + 1][0], vv[i + 1][1], vv[i][0], vv[i][1]}, pr), mfma0(u32x4{vv[i + 1][2], vv[i + 1][3], vv[i][2], vv[i][3]}, pr));
    asm volatile("buffer_store_dwordx4 %0, %1, %2, 0 offen offset:%3 sc1\n\ts_nop 1" ::"v"(ov), "v"(lane * 16), "s"(rsO), "i"(i * 1024) : "memory");
  }
}

template <class GST>
DI void attn_phase(unsigned smem_base, int wid, bf16* H, const bf16* __restrict__ W, bf16* O,
                   const bf16* __restrict__ wo, const bf16* __restrict__ X, float* modp, bf16* Y, float* stat,
                   unsigned* flags, const bf16* __restrict__ wg, const bf16* __restrict__ wu,
                   unsigned* ctab, const bf16* __restrict__ cnd, const bf16* __restrict__ wmod, GST gst) {
  const int tid = threadIdx.x, lane = tid & 63;
  const int xcd = blockIdx.x & 7, idx = blockIdx.x >> 3;
  const int bm = (xcd >> 2) * 8 + (idx & 7), bn = (xcd & 3) * 4 + (idx >> 3);
  const int rb = xcd * 2 + (idx & 1), cg = idx >> 1;
  const unsigned rot = __builtin_amdgcn_readfirstlane((cg >> 2) * 2);
  const unsigned cnt = smem_base + QX_CNT, cntl = cnt + lane * 4;
  const rsrc_t rsW = mk_rsrc(W, D_DIM * 3 * D_DIM * 2);
  first_phase(smem_base, wid, lane, X, cnd, wmod, W, modp, H, flags, [&](int w) {
#pragma unroll
    for (int i = 0; i < 3; ++i) {
      const int p = w + 8 * i < 21 ? w + 8 * i : 20, u = (p * 11) >> 5;
      qkv_ldw(smem_base, lane, p - 3 * u, bn * 64, rsW, u * Q_SLOT, u);
    }
  });
  if (wid < 4) {
    if (wid & 1) qkv_mfma<1>(smem_base, lane, wid >> 1, bm, bn, H, O);
    else qkv_mfma<0>(smem_base, lane, wid >> 1, bm, bn, H, O);
  } else {
    const rsrc_t rsO = mk_rsrc(wo, D_DIM * D_DIM * 2);
    const unsigned voffO = (unsigned)((lane >> 3) * 2048 + cg * 128 + ((((lane & 7) >> 1) ^ (lane >> 4)) * 32) + (lane & 1) * 16);
    if (wid < 7) qkv_loader(smem_base, lane, wid - 4, bm * 128, bn * 64, H, rsW, rot, voffO, rsO);
    else {
      const unsigned vgu = idx < 16 ? 0x40000000u : (unsigned)((((idx - 16) * 64 + lane) / 7) * (F_DIM * 2) + xcd * 896 + (((idx - 16) * 64 + lane) % 7) * 128);
      const poll_t ph = poll(0u, flags + (FL_H + bm * 4 + (xcd & 3)) * 32, lane, 1, bm ? 9u : 8u);
      asm volatile("s_barrier" ::: "memory");
      SCHED0();
      unsigned* r1 = flags + (FL_M + (1 + xcd) * 32 + 24) * 32;
      const sig_t gi = drain(idx < 8 ? r1 : flags + (FL_M + 24 + xcd) * 32, lane, idx < 16 ? 1 : 0, 4);
      poll_end(ph);
      signal(gi);
      if (idx < 8) {
        const int v = xcd * 8 + idx;
        unsigned t = 0;
        for (int i = 0; i < 4; ++i) __builtin_amdgcn_s_barrier();
        asm volatile("buffer_atomic_add %0, %1, %2, 0 offen sc0 sc1\n\ts_barrier\n\ts_barrier\n\ts_barrier\n\ts_barrier\n\ts_waitcnt vmcnt(0)" : "+v"(t) : "v"(lane * 128), "s"(mk_rsrc(r1, 128)) : "memory");
#pragma unroll 1
        for (unsigned spins = 0; __builtin_amdgcn_readfirstlane(t) < 8u && spins < (1u << 20); ++spins) t = cnt_read(r1, lane, 1);
        static_for<2>([&](auto I) { bload_lds_sc1<I.value * 1024>(smem_base + G3_CT, lane * 16, mk_rsrc(modp + 16384 + v * 512, 2048), 0); });
        for (int i = 0; i < 4; ++i) __builtin_amdgcn_s_barrier();
        wait_vm<0>();
        float sum = red_sum<8>(smem_base + G3_CT, lane);
        sum = (v >> 4) == 2 ? gain_nz(sum) : rbf(sum);
        asm volatile("buffer_store_dword %0, %1, %2, 0 offen sc1" ::"v"(sum), "v"(lane * 4), "s"(mk_rsrc(modp + 49152 + v * 64, 256)) : "memory");
        asm volatile("buffer_store_short_d16_hi %0, %1, %2, 0 offen sc1" ::"v"(sum), "v"((lane >> 5) * 128 + (lane & 31) * 2),
                     "s"(mk_rsrc(ctab + (v & 15) * 64 + (v & 16), (v >> 4) == 1 || (v >> 4) == 2 ? 256 : 0)) : "memory");
        for (int i = 0; i < 4; ++i) __builtin_amdgcn_s_barrier();
        signal(drain(flags + (FL_M + 16 + idx) * 32, lane, 9, 4096));
      }
      {
        int nxt = TS + idx % TP, ev = 0;
#pragma unroll 1
        for (int i = idx < 8 ? 16 : 0; i < 27; ++i) {
          if (i == WOS && idx >= 16) {
            touch_lds(smem_base + QX_SINK, (blockIdx.x - 128) * 16384 + lane * 128, rsO, 0);
            touch_lds(smem_base + QX_SINK, (blockIdx.x - 128) * 16384 + lane * 128, rsO, 8192);
          }
          if (i == nxt && ev < 2) {
            touch_lds(smem_base + QX_SINK, vgu, mk_rsrc(ev ? wu : wg, D_DIM * F_DIM * 2), 0);
            nxt += TP; ++ev;
          }
          __builtin_amdgcn_s_barrier();
        }
      }
      unsigned* cr = flags + (FL_M + (idx == 15 ? 0 : 1 + xcd) * 32 + 16) * 32;
      const int nl = idx == 15 ? 16 : 8;
      unsigned t = 0;
      asm volatile("buffer_atomic_add %0, %1, %2, 0 offen sc0 sc1" : "+v"(t) : "v"(lane * 128), "s"(mk_rsrc(cr, nl * 128)) : "memory");
      wo_piece(smem_base, 63u, rot, voffO, rsO);
      __builtin_amdgcn_s_barrier();
      __builtin_amdgcn_s_barrier();
      asm volatile("s_waitcnt vmcnt(1)" : "+v"(t)::"memory");
      for (unsigned spins = 0; __builtin_amdgcn_ballot_w64(t < (lane < nl ? 8u : 0u)) != 0ull && spins < (1u << 20); ++spins) {
        if (idx == 15) set_words(flags, lane);
        t = cnt_read(cr, lane, nl);
      }
      static_for<4>([&](auto I) { bload_lds_sc1<I.value * 1024>(smem_base + G3_CT, lane * 16, mk_rsrc(ctab, 4096), 0); });
#pragma unroll
      for (int i = 0; i < 2; ++i)
        asm volatile("s_mov_b32 m0, %0\n\ts_nop 0\n\tbuffer_load_dword %1, %2, 0 offen sc1 lds" ::"s"(smem_base + QX_GT + i * 256), "v"(lane * 4),
                     "s"(mk_rsrc(modp + 49152 + i * 2048 + cg * 64, 256)) : "memory");
      for (int i = 30; i < 33; ++i) __builtin_amdgcn_s_barrier();
      wo_piece(smem_base, 127u, rot, voffO, rsO);
    }
    __builtin_amdgcn_s_barrier();
    wait_vm<0>();
    lds_add(cntl, 1u);
  }
  const int fr = lane & 15, fq = lane >> 4;
  const int kq = wid & 3, rg = rb * 2 + (wid >> 2);
  const int ko = (kq + cg) & 3;
  lds_wait<2>(cnt, 4);
  u32x2 wl[8][4], wh[8][4];
  {
    const int q = (lane >> 2) & 3, p = lane & 3, r = 4 * fq + q;
    const unsigned wb = smem_base + (kq < 2 ? QX_WH + kq * 32768 : (kq - 2) * 32768) + r * 128 + (((r >> 1) & 3) * 32) + 8 * p;
    unsigned wa[4];
#pragma unroll
    for (int f = 0; f < 4; ++f) wa[f] = wb ^ (f * 32);
    static_for<32>([&](auto E) {
      constexpr int s = E.value >> 2, f = E.value & 3;
      dsr_tr<s * 4096>(wl[s][f], wa[f]);
      dsr_tr<s * 4096 + 2048>(wh[s][f], wa[f]);
    });
  }
  const rsrc_t rsA = mk_rsrc((const char*)O + (size_t)rg * 131072, 131072);
  unsigned voffA[4], hs[8];
#pragma unroll
  for (int i = 0; i < 4; ++i) { voffA[i] = lane * 16 + ((ko + 1 + i) & 3) * 1024; asm volatile("" : "+v"(voffA[i])); }
#pragma unroll
  for (int i = 0; i < 8; ++i) { hs[i] = __builtin_amdgcn_readfirstlane((kq * 8 + ((i + rot) & 7)) * 4096); asm volatile("" : "+s"(hs[i])); }
  asm volatile("" ::"s"(rsA));
  if (wid < 4) {
    const sig_t g = drain(flags + (FL_O + bm * 8 + (bn & 1)) * 32, lane, 4, 256);
    if (lds_add(cntl + 256, 1u) == 3u) signal(g);
  }
  const poll_t pl = poll(wid - 7, flags + (FL_O + rb * 8 + (cg & 3) * 2) * 32, lane, 2, 8u);
  asm volatile("s_waitcnt lgkmcnt(0)" ::: "memory");
  __builtin_amdgcn_s_barrier();
  u32x4 wf[8][4];
#pragma unroll
  for (int s = 0; s < 8; ++s)
#pragma unroll
    for (int f = 0; f < 4; ++f) {
      asm volatile("" : "+v"(wl[s][f]), "+v"(wh[s][f]));
      wf[s][f] = u32x4{wl[s][f][0], wl[s][f][1], wh[s][f][0], wh[s][f][1]};
    }
  const unsigned voffL = lane * 16;
  u32x4 af[32], xv[2], pr[3][4], gt[4], ct[4];
  f32x4 acc[4][4];
  auto aload = [&](auto N) {
    constexpr int n = N.value;
    asm volatile("" : "=v"(af[n]));
    bload_sc1<0>(af[n], voffA[n >> 3], rsA, hs[n & 7]);
  };
  static_for<GDEP>([&](auto N) { aload(N); });
  const unsigned sl = smem_base + 49152 + voffL + (wid >> 2) * 49152;
  const int sw = ((fq & 1) << 1) | (fq >> 1);
  SCHED0();
  static_for<32>([&](auto N) {
    constexpr int n = N.value, ps = n >> 3, s = n & 7, XG = 32 - GDEP;
    constexpr int last = n - 1 + GDEP < 31 ? n - 1 + GDEP : 31;
    asm volatile("s_waitcnt vmcnt(%1)" : "+v"(af[n]) : "n"(last - n + (n > XG ? 2 : 0) + (n > GS0 + 3 ? 6 : (n > GS0 ? 2 * (n - GS0) : 0))) : "memory");
    if constexpr (n == 0) poll_end(pl);
    if constexpr (n == 28) { WAIT_LGKM0(); __builtin_amdgcn_s_barrier(); }
    static_for<4>([&](auto F) {
      constexpr int f = F.value;
      if constexpr (s == 0) asm volatile("v_mfma_f32_16x16x32_bf16 %0, %1, %2, 0" : "=&v"(acc[ps][f]) : "v"(wf[s][f]), "v"(af[n]));
      else MFMA_AV(acc[ps][f], wf[s][f], af[n]);
      if constexpr (f == 0 && n + GDEP < 32) aload(ic<(n + GDEP < 32 ? n + GDEP : 0)>{});
      if constexpr (f == 0 && n == XG) {
        const rsrc_t rsX = mk_rsrc(X, S_LEN * D_DIM * 2);
        const unsigned vx = (unsigned)(((rg * 64 + ko * 16 + fr) * D_DIM + cg * 64 + sw * 8) * 2);
        asm volatile("" : "=v"(xv[0]), "=v"(xv[1]));
        bload_v<0>(xv[0], vx, rsX, 0); bload_v<64>(xv[1], vx, rsX, 0);
      }
      if constexpr ((f & 1) && n >= GS0 && n < GS0 + 3) gst(ic<(n >= GS0 ? n - GS0 : 0)>{}, ic<(f >> 1)>{});
      if constexpr (ps > 0 && (s == 2 || s == 3) && (f & 1)) {
        constexpr int e = (s - 2) * 2 + (f >> 1);
        dsw128<(ps - 1) * 4096 + e * 1024>(sl + ((kq + ps) & 3) * 12288, __builtin_bit_cast(u32x4, acc[ps - 1][e]));
      }
      if constexpr (n >= 28 && n < 31) dsr128<(n - 28) * 4096 + f * 1024>(pr[n - 28][f], sl + kq * 12288);
      if constexpr (n == 31) { dsr128<f * 64>(gt[f], smem_base + QX_GT + fq * 16); dsr128<256 + f * 64>(ct[f], smem_base + QX_GT + fq * 16); }
    });
  });
  asm volatile("s_waitcnt vmcnt(6)" : "+v"(xv[0]), "+v"(xv[1])::"memory");
  asm volatile("s_waitcnt lgkmcnt(0)\n\ts_nop 4" ::: "memory");
  f32x4 fin[4], yv[4];
#pragma unroll
  for (int f = 0; f < 4; ++f) asm volatile("" : "+v"(acc[3][f]), "+v"(pr[0][f]), "+v"(pr[1][f]), "+v"(pr[2][f]), "+v"(gt[f]), "+v"(ct[f]));
#pragma unroll
  for (int c = 0; c < 2; ++c) {
    const auto x0 = __builtin_amdgcn_permlane16_swap(xv[c][0], xv[c][2], false, false);
    const auto x1 = __builtin_amdgcn_permlane16_swap(xv[c][1], xv[c][3], false, false);
#pragma unroll
    for (int h = 0; h < 2; ++h) {
      const int f = 2 * c + h;
      const f32x4 a = (acc[3][f] + __builtin_bit_cast(f32x4, pr[0][f])) + (__builtin_bit_cast(f32x4, pr[1][f]) + __builtin_bit_cast(f32x4, pr[2][f]));
      const f32x4 xf = {bf_lo(x0[h]), bf_hi(x0[h]), bf_lo(x1[h]), bf_hi(x1[h])};
      fin[f] = xf + __builtin_bit_cast(f32x4, gt[f]) * a;
      yv[f] = fin[f] * __builtin_bit_cast(f32x4, ct[f]);
    }
    const auto y0 = __builtin_amdgcn_permlane16_swap(pk_bf16(yv[2 * c][0], yv[2 * c][1]), pk_bf16(yv[2 * c + 1][0], yv[2 * c + 1][1]), false, false);
    const auto y1 = __builtin_amdgcn_permlane16_swap(pk_bf16(yv[2 * c][2], yv[2 * c][3]), pk_bf16(yv[2 * c + 1][2], yv[2 * c + 1][3]), false, false);
    gst128((char*)Y + ((size_t)(rg >> 2) * 32 + cg * 2 + c) * 16384 + ((rg & 3) * 4 + ko) * 1024 + (sw * 16 + fr) * 16,
           u32x4{y0[0], y1[0], y0[1], y1[1]});
  }
  const f32x4 s4 = (fin[0] + fin[1]) + (fin[2] + fin[3]);
  const float s = rowsum4(hadd(lo2(s4) + hi2(s4)));
  const float mb = s * (1.0f / 64.0f);
  f32x4 q4 = {0, 0, 0, 0};
#pragma unroll
  for (int f = 0; f < 4; ++f) { const f32x4 d = fin[f] - mb; q4 += d * d; }
  const float m2 = rowsum4(hadd(lo2(q4) + hi2(q4)));
  asm volatile("buffer_store_dwordx2 %0, %1, %2, 0 offen sc1" ::"v"(f32x2{s, m2}), "v"(lane * 8),
               "s"(mk_rsrc(stat + ((size_t)cg * S_LEN + rg * 64 + ko * 16) * 2, 128)) : "memory");
}

#define G3_STAGE(SLOT, P, U)                                                                               \
  do {                                                                                                     \
    if ((P) == 0) bload_lds_sc1<0>(m0w + 65536 + (SLOT) * 16384, voffA, rsA, (unsigned)(U) * 16384u);      \
    else if ((P) == 1) bload_lds_sc1<1024>(m0w + 65536 + (SLOT) * 16384, voffA, rsA, (unsigned)(U) * 16384u);  \
    else bload_lds<0>(m0w + (SLOT) * 16384 + ((P) - 2) * 1024, (P) == 2 ? voffB0 : voffB1, rsB, (unsigned)(U) * 229376u);  \
  } while (0)

DI constexpr int g3_line(int gx, int p) {
  const int t = p + ((3 + gx) & 7);
  return 7 * gx + (t >= 7 ? t - 7 : t);
}
constexpr unsigned act_frag(int kh, int p) {
  const int tl = p < 48 ? p / 3 : (p - 48) >> 2, jv = (tl & 3) * 7 + (p < 48 ? 4 + p % 3 : p & 3);
  return g3_line(kh * 4 + (tl >> 2), jv >> 2) * 4 + (jv & 3);
}
template <int FN, class WST, class HOOK, class PRE>
DI void g3_body(unsigned smem_base, int wm, int wn, int wid, int lane, int bm, int bn, rsrc_t rsA, rsrc_t rsB,
                rsrc_t rsE, const char* ta, unsigned voffA, unsigned voffB0, unsigned voffB1, bf16* act, WST wst, HOOK hook, PRE pre) {
  constexpr int NB = 2 * FN, NM = 4 * NB, RD = 5 + 2 * NB;
  const int fr = lane & 15, fq = lane >> 4;
  f32x4 acc[4][NB], accC[2];
  accC[0] = f32x4{0, 0, 0, 0}; accC[1] = f32x4{0, 0, 0, 0};
  asm volatile("" : "+v"(accC[0]), "+v"(accC[1]));
  const int rot = wm % FN;
  unsigned cad = fr >= 2 || wm >= FN ? smem_base + X_CN + 1536 : smem_base + G3_CT + (fr & 1) * 64 + fq * 16;
  unsigned cin = fr >= 2 || wm >= FN ? 0u : 512u;
  asm volatile("" : "+v"(cin));
#pragma unroll
  for (int i = 0; i < 4; ++i)
#pragma unroll
    for (int j = 0; j < NB; ++j) { acc[i][j] = f32x4{0, 0, 0, 0}; asm volatile("" : "+v"(acc[i][j])); }
  unsigned aad = smem_base + 65536 + wm * 4096 + lane * 16;
  unsigned bad[FN];
  {
    const int q = (lane >> 2) & 3, p = lane & 3;
    const int r = 8 * fq + q;
    const int sw = (r & 3) | (((r >> 3) & 1) << 2);
#pragma unroll
    for (int j = 0; j < FN; ++j) {
      const int gi = wn * 4 + (j + rot) % FN + (bn & 1);
      bad[j] = smem_base + r * 256 + ((gi ^ sw) * 32) + 8 * p;
    }
  }
  u32x4 aX[5], aY[5];
  u32x2 bXl[NB], bXh[NB], bYl[NB], bYh[NB];
  const unsigned m0w = smem_base + wid * 2048;
  const int ut = wid == 7 ? CT3 : wid == 6 ? ST3 : -1;
  const unsigned ve = wid == 7 ? lane * 32 + (blockIdx.x >> 3) * 2048u + 8192u : ((bn & 3) * 4 + (lane >> 4)) * 16384 + bm * 2048 + (lane & 15) * 128;
  unsigned tinc = lane < 14 ? 8 * 229376 : 8192;
  asm volatile("" : "+v"(ta), "+v"(tinc));
#define G3_READ(AR, BL, BH, SLOT, R)                                                                       \
  do {                                                                                                     \
    if ((R) < 2 * NB) {                                                                                    \
      constexpr int jb = ((R) < 2 * NB ? (R) : 0) >> 1;                                                    \
      constexpr int off = (SLOT) * 16384 + (jb / FN) * 8192 + ((R) & 1) * 1024;                            \
      if ((R) & 1) dsr_tr<off>(BH[jb], bad[jb % FN]);                                                       \
      else dsr_tr<off>(BL[jb], bad[jb % FN]);                                                               \
    } else if ((R) < 2 * NB + 4) {                                                                         \
      constexpr int ia = ((R) >= 2 * NB && (R) < 2 * NB + 4) ? (R) - 2 * NB : 0;                           \
      dsr128<(SLOT) * 16384 + ia * 1024>(AR[ia], aad);                                                      \
    } else {                                                                                               \
      if ((SLOT) == 0) cad += cin;                                       \
      dsr128<(SLOT) * 128>(AR[4], cad);                                                                    \
    }                                                                                                      \
  } while (0)
  constexpr int RS = 131072, CC = RS + 2048;
  unsigned ca[FN], pa, ra, sa;
  int l2;
  auto prep = [&](auto N) {
    constexpr int n = N.value;
    if constexpr (n == 0) {
      l2 = fresh_lane();
#pragma unroll
      for (int j = 0; j < FN; ++j) { ca[j] = smem_base + CC + (wn * 4 + (j + rot) % FN) * 64 + (l2 & 48); asm volatile("" : "+v"(ca[j])); }
    }
    if constexpr (n == 1) {
      pa = ca[0] + (l2 & 15) * 512;
      sa = smem_base + QX_SINK + l2 * 16;
      asm volatile("" : "+v"(pa), "+v"(sa), "+v"(l2));
    }
    if constexpr (n == 2) {
      pa = (l2 & 15) < (wm < FN ? 2 : 0) ? pa : sa;
      ra = smem_base + RS + (wm * 64 + (l2 & 15)) * 8;
      asm volatile("" : "+v"(pa), "+v"(ra));
    }
    if constexpr (n == 3) {
      asm volatile("" : "+v"(l2));
      sa = (l2 & 16) * 64 + (l2 & 32) * 8 + (l2 & 15) * 16;
      asm volatile("" : "+v"(sa));
    }
  };
  auto rd = [&](auto SL, auto RR, u32x4 (&AR)[5], u32x2 (&BL)[NB], u32x2 (&BH)[NB]) { G3_READ(AR, BL, BH, SL.value, RR.value); };
#define G3_UNIT(AR, BL, BH, NAR, NBL, NBH, SLOT, U, LI)                                                    \
  do {                                                                                                     \
    WAIT_LGKM0();                                                                                          \
    u32x4 bb[NB];                                                                                          \
    _Pragma("unroll") for (int j = 0; j < NB; ++j) bb[j] = u32x4{BL[j][0], BL[j][1], BH[j][0], BH[j][1]};  \
    SCHED0();                                                                                              \
    asm volatile("s_nop 1");                                                                               \
    static_for<NM>([&](auto N) {                                                                           \
      constexpr int n = N.value;                                                                           \
      if constexpr (n == NM / 2) {                                                                         \
        wait_vm<(LI && (SLOT) < 2 && FN == 3) ? 13 + 4 * (SLOT) : 8>(); __builtin_amdgcn_s_barrier();      \
      }                                                                                                    \
      if constexpr (n == NM / 2 + TPOS && !LI) {                                                           \
          \
        if (((U) & 7) == wid) {                                                                            \
          asm volatile("s_mov_b32 m0, %2\n\ts_mov_b64 exec, %1\n\tglobal_load_lds_dword %0, off\n\ts_mov_b64 exec, -1"      \
                       ::"v"(ta), "s"((U) + WTD < 32 ? 0x3fffffull : 0x3fc000ull), "s"(smem_base + 131072 + 4096) : "memory"); \
          ta += tinc;                                                                                      \
        }                                                                                                  \
        if (FN == 3 && (SLOT) == 0 && (U) == ut) touch_lds(smem_base + 131072 + 4096, ve, rsE, 0);         \
      }                                                                                                    \
      MFMA_AV(acc[n & 3][n >> 2], bb[n >> 2], AR[n & 3]);                                                  \
      if constexpr (LI && (SLOT) == 0 && n < 12 && n % 3 == 0) hook(ic<-n / 3>{});                         \
      if constexpr (LI && (SLOT) == 0 && (n == 15 || n == 21)) hook(ic<-(n + 9) / 6>{});                   \
      if constexpr (LI && (SLOT) == 1 && (n == 0 || n == 3)) hook(ic<-6 - n / 3>{});                       \
      if constexpr (LI && (SLOT) == 1 && n % 6 == 2 && n < 24) prep(ic<n / 6>{});                          \
      if constexpr (LI && (SLOT) == 2 && n >= 21 && n < 24) hook(ic<n - 8>{});                             \
      if constexpr (n < NM / 2) {                                                                          \
        if constexpr (LI && (SLOT) == 2 && n < 12) hook(ic<n + 1>{});                                      \
        if constexpr (n % 3 == 1 && n / 3 < 4) {                                                           \
          if constexpr (LI && (SLOT) > 0) wst(ic<((SLOT) + 3) & 3>{}, ic<(n / 3) & 3>{});                  \
          else G3_STAGE(((SLOT) + 3) & 3, n / 3, (U) + 3);                                                 \
        }                                                                                                  \
      } else {                                                                                             \
        constexpr int r0 = (n - NM / 2) * 2;                                                               \
        if constexpr (r0 < RD) rd(ic<((SLOT) + 1) & 3>{}, ic<(r0 < RD ? r0 : 0)>{}, NAR, NBL, NBH);        \
        if constexpr (r0 + 1 < RD) rd(ic<((SLOT) + 1) & 3>{}, ic<(r0 + 1 < RD ? r0 + 1 : 0)>{}, NAR, NBL, NBH); \
      }                                                                                                    \
    });                                                                                                    \
    MFMA_AV(accC[0], bb[0], AR[4]);                                                                        \
    MFMA_AV(accC[1], bb[FN], AR[4]);                                                                       \
    SCHED0();                                                                                              \
  } while (0)

  unsigned v0 = wm * 4096 + lane * 16;
#pragma unroll
  for (int j = 0; j < FN; ++j) asm volatile("" : "+v"(bad[j]));
  asm volatile("" : "+v"(aad), "+v"(cad), "+v"(voffA), "+v"(v0) : "s"(rsA), "s"(m0w));
  const poll_t pl = pre();
  static_for<4>([&](auto I) { asm volatile("" : "=v"(aX[I.value])); bload_sc1<I.value * 1024>(aX[I.value], v0, rsA, 0); });
  static_for<2 * NB>([&](auto RR) { rd(ic<0>{}, RR, aX, bXl, bXh); });
  dsr128<0>(aX[4], cad);
  G3_STAGE(1, 0, 1); G3_STAGE(1, 1, 1); G3_STAGE(2, 0, 2); G3_STAGE(2, 1, 2);
#pragma unroll
  for (int i = 0; i < 2; ++i) bload_lds<0>(smem_base + 131072 + 4096, voffA, rsA, 0x40000000u);
  asm volatile("s_waitcnt vmcnt(6)" : "+v"(aX[0]), "+v"(aX[1]), "+v"(aX[2]), "+v"(aX[3])::"memory");
  poll_end(pl);
  for (int u = 0; u < 28; u += 4) {
    G3_UNIT(aX, bXl, bXh, aY, bYl, bYh, 0, u, false);
    G3_UNIT(aY, bYl, bYh, aX, bXl, bXh, 1, u + 1, false);
    G3_UNIT(aX, bXl, bXh, aY, bYl, bYh, 2, u + 2, false);
    G3_UNIT(aY, bYl, bYh, aX, bXl, bXh, 3, u + 3, false);
  }
  G3_UNIT(aX, bXl, bXh, aY, bYl, bYh, 0, 28, true);
  G3_UNIT(aY, bYl, bYh, aX, bXl, bXh, 1, 29, true);
  if (FN == 3) __builtin_amdgcn_s_setprio(1);
  G3_UNIT(aX, bXl, bXh, aY, bYl, bYh, 2, 30, true);
  constexpr float KG = -1.4426950408889634f, KU = -0.6931471805599453f;
  constexpr int EB = 7;
  u32x2 rs[4];
  u32x4 cc[2][4];
  auto rdc = [&](auto J, auto E) { dsr128<E.value * 512>(cc[J.value & 1][E.value], ca[J.value]); };
  {
    WAIT_LGKM0();
    u32x4 bb[NB];
#pragma unroll
    for (int j = 0; j < NB; ++j) bb[j] = u32x4{bYl[j][0], bYl[j][1], bYh[j][0], bYh[j][1]};
    SCHED0();
    asm volatile("s_nop 1");
    static_for<NM>([&](auto N) {
      constexpr int n = N.value, k = n - EB;
      if constexpr (n == EB) {
        asm volatile("s_waitcnt lgkmcnt(0)" ::: "memory"); __builtin_amdgcn_s_barrier();
      }
      if constexpr (n == 0) { MFMA_AV(accC[0], bb[0], aY[4]); MFMA_AV(accC[1], bb[FN], aY[4]); }
      MFMA_AV(acc[n & 3][n >> 2], bb[n >> 2], aY[n & 3]);
      if constexpr (n % 3 == 1 && n / 3 < 4) wst(ic<2>{}, ic<(n / 3) & 3>{});
      if constexpr (n == 3) {
        asm volatile("" : "+v"(accC[0]), "+v"(accC[1]));
        dsw128<0>(pa, __builtin_bit_cast(u32x4, accC[0] * KG));
        dsw128<1024>(pa, __builtin_bit_cast(u32x4, accC[1] * KU));
      }
      if constexpr (k == 0 || k == 1) { dsr64<k * 256>(rs[2 * k], ra); dsr64<k * 256 + 128>(rs[2 * k + 1], ra); }
      if constexpr (k >= 2 && k < 6) { rdc(ic<(k - 2) / 2>{}, ic<(k & 1) * 2>{}); rdc(ic<(k - 2) / 2>{}, ic<(k & 1) * 2 + 1>{}); }
      if constexpr (k >= 6 && k < 10) wst(ic<3>{}, ic<(k - 6) & 3>{});
    });
    asm volatile("s_nop 7" ::: "memory");
    SCHED0();
  }
  const rsrc_t rsO = mk_rsrc((char*)act + (size_t)(bm * 4 + wm) * (F_DIM / 32) * 4096, F_DIM / 32 * 4096);
  asm volatile("s_waitcnt lgkmcnt(4)" : "+v"(rs[0]), "+v"(rs[1]), "+v"(rs[2]), "+v"(rs[3]), "+v"(cc[0][0]), "+v"(cc[0][1]), "+v"(cc[0][2]), "+v"(cc[0][3])::"memory");
  float rg[4], ru[4];
#pragma unroll
  for (int i = 0; i < 4; ++i) { rg[i] = __uint_as_float(rs[i][1]) * KG; ru[i] = __uint_as_float(rs[i][1]) * KU; }
  auto frag = [&](auto JJ) {
    constexpr int j = JJ.value;
    auto half = [](auto v, int n) { const f32x4 f = __builtin_bit_cast(f32x4, v); return (n & 1) ? hi2(f) : lo2(f); };
    f32x2 g[8], uu[8], e[8];
    u32x4(&c)[4] = cc[j & 1];
#define STAGE(X) do { SCHED0(); _Pragma("unroll") for (int n = 0; n < 8; ++n) { X; } } while (0)
    STAGE(g[n] = __uint_as_float(rs[n >> 1][0]) * half(c[0], n) + half(c[1], n));
    STAGE(g[n] = rg[n >> 1] * half(acc[n >> 1][j], n) + g[n]);
    STAGE(e[n] = (f32x2{__builtin_amdgcn_exp2f(g[n][0]), __builtin_amdgcn_exp2f(g[n][1])}));
    STAGE(uu[n] = __uint_as_float(rs[n >> 1][0]) * half(c[2], n) + half(c[3], n));
    SCHED0();
    if constexpr (j + 2 < FN) static_for<4>([&](auto E) { rdc(ic<j + 2>{}, E); });
    STAGE(e[n] = e[n] + 1.0f);
    STAGE(e[n] = (f32x2{__builtin_amdgcn_rcpf(e[n][0]), __builtin_amdgcn_rcpf(e[n][1])}));
    STAGE(uu[n] = ru[n >> 1] * half(acc[n >> 1][FN + j], n) + uu[n]);
    STAGE(g[n] = g[n] * uu[n]);
    STAGE(g[n] = g[n] * e[n]);
    unsigned pk[8];
    STAGE(pk[n] = pk_bf16(g[n][0], g[n][1]));
    SCHED0();
    const int J = (bn >> 4) * 112 + (FN == 4 ? 48 : 0) + (bn & 15) * FN + (j + rot) % FN;
    static_for<2>([&](auto I) {
      constexpr int i = I.value * 4;
      const auto s0 = __builtin_amdgcn_permlane16_swap(pk[i], pk[i + 2], false, false);
      const auto s1 = __builtin_amdgcn_permlane16_swap(pk[i + 1], pk[i + 3], false, false);
      bstore_sc1<I.value * 2048>(u32x4{s0[0], s1[0], s0[1], s1[1]}, sa, rsO, (unsigned)((J >> 1) * 4096 + (J & 1) * 512));
    });
    if constexpr (j + 1 < FN) {
      u32x4(&d)[4] = cc[(j + 1) & 1];
      asm volatile("s_waitcnt lgkmcnt(%4)" : "+v"(d[0]), "+v"(d[1]), "+v"(d[2]), "+v"(d[3]) : "n"(j + 2 < FN ? 4 : 0) : "memory");
    }
    SCHED0();
  };
  static_for<FN>(frag);
}

namespace g4 {
constexpr int NU = 14, SL = 4, AD = 3;
constexpr int CN = X_CN;
constexpr int GT = 131072 + 5120;
constexpr int CU = 1;
constexpr int TU = NU - SL;
constexpr int PX = 137216;
constexpr int XP = 30, X3 = 16;
constexpr int na(int mh, int t) { return t < (mh ? TU - 2 : TU + 1) ? 4 : 0; }
constexpr int nb(int mh, int t) { return t < 0 || t >= TU ? 0 : t == TU - 1 && !mh ? 8 : 4 + (t == (mh ? CU : 0)); }
constexpr int behind(int mh, int t0, int u, int b) {
  int s = b ? na(mh, u) : 0;
  for (int t = t0 + 1; t < u; ++t) s += na(mh, t) + nb(mh, t);
  return s;
}
constexpr int vm(int mh, int u, int b) {
  if (mh && u >= TU) return u == TU ? 18 - 2 * b : u == TU + 1 ? 8 : 4 * (NU - 1 - u) + 2 - 2 * b;
  const int wa = u < NU - 2 ? nb(mh, u - AD + 1) + behind(mh, u - AD + 1, u, b) : behind(mh, u + 1 - SL, u, b);
  const int wb = u + 1 < SL ? 63 : u < TU - 1 ? behind(mh, u + 1 - SL, u, b) : u == TU - 1 ? behind(mh, u + 2 - SL, u, b) : u == TU + 1 ? behind(mh, TU - 1, u, b) : 63;
  return b == 0 ? wa + 2 : (wb < wa ? wb : wa);
}
constexpr int as(int mh, int v) { return v < NU - 2 ? v % AD : (v == NU - 2) == !mh ? AD : (TU - 1) % AD; }
}

template <int MH, class WST, class PRE, class EXTRA>
DI void g4_loop(f32x4 (&acc)[2][8], unsigned smem_base, int lane, int wid, int kb0, rsrc_t rsA, unsigned* fb, WST ldb, PRE pre, EXTRA extra) {
  using namespace g4;
  const int fq = lane >> 4, k2 = wid & 1;
  unsigned voffA[2], bad[8];
#pragma unroll
  for (int i = 0; i < 2; ++i) voffA[i] = lane * 16 + ((wid & 3) ^ i) * 1024;
  {
    const int q = (lane >> 2) & 3, r = 8 * fq + q, sw = q | ((fq & 1) << 2);
#pragma unroll
    for (int f = 0; f < 8; ++f) bad[f] = smem_base + k2 * 65536 + r * 256 + ((f ^ sw) * 32) + 8 * (lane & 3);
  }
  const unsigned sA = (unsigned)(kb0 + 2 * k2) * 4096u;
  u32x4 af[AD + 1][4];
  u32x2 bl[2][8], bh[2][8];
  auto lda = [&](auto V, auto J) {
    constexpr int v = V.value, j = J.value;
    asm volatile("" : "=v"(af[as(MH, v)][j]));
    bload_sc1<0>(af[as(MH, v)][j], voffA[j & 1], rsA, sA + (4 * v + (j >> 1)) * 4096u);
  };
  auto rdb = [&](auto V, auto B, auto E) {
    constexpr int o = (V.value & 3) * 16384 + B.value * 8192, f = E.value >> 1;
    if constexpr (E.value & 1) dsr_tr<o + 1024>(bh[B.value][f], bad[f]);
    else dsr_tr<o>(bl[B.value][f], bad[f]);
  };
  const unsigned wo = wid >> 1 == 1 ? 8192u : 65536u;
  unsigned wp = smem_base + (wid >> 1 == 1 ? PX : 32768 + (wid >> 1) * 8192) + k2 * wo + lane * 16;
#pragma unroll
  for (int f = 0; f < 8; ++f) asm volatile("" : "+v"(bad[f]));
  asm volatile("" : "+v"(voffA[0]), "+v"(voffA[1]), "+v"(wp) : "s"(rsA), "s"(sA));
  unsigned cv = 0, rp, fa, fb2, one = 1;
  const unsigned wf = smem_base + CN + wid * 4;
  const poll_t pl = pre();
  if (MH == 1) __builtin_amdgcn_s_setprio(0);
  static_for<4 * (AD - 1)>([&](auto J) { lda(ic<J.value / 4>{}, ic<J.value % 4>{}); });
  static_for<16>([&](auto E) { rdb(ic<0>{}, ic<0>{}, E); });
  static_for<NU>([&](auto U) {
    constexpr int u = U.value;
    constexpr bool last = u == NU - 1;
    static_for<32>([&](auto N) {
      constexpr int n = N.value, b = last ? n >= 24 || (n >= 12 && n < 20) : n >> 4, m = last ? (n == 12 ? 0 : n) : n & 15;
      constexpr int f = last ? (n >= 12 && n < 20 ? n - 12 : n & 7) : m >> 1, i = last ? n < 8 || (n >= 12 && n < 20) : m & 1;
      if constexpr (MH == 1 && u == TU && n == 0) {
        asm volatile("s_waitcnt vmcnt(0) lgkmcnt(0)\n\ts_barrier" ::: "memory");
        static_for<16>([&](auto J) {
          if constexpr (J.value == 8) extra(ic<-1>{});
          lda(ic<TU + J.value / 4>{}, ic<J.value % 4>{});
        });
        __builtin_amdgcn_s_barrier();
      }
      if constexpr (MH ? u == TU && n == X3 : last && n == XP) __builtin_amdgcn_s_barrier();
      if constexpr (m == 0) {
        asm volatile("s_waitcnt vmcnt(%2) lgkmcnt(0)" : "+v"(af[as(MH, u)][2 * b]), "+v"(af[as(MH, u)][2 * b + 1])
                     : "n"(vm(MH, u, b)) : "memory");
        if constexpr (u == 0 && b == 0 && MH == 1) poll_end(pl);
#pragma unroll
        for (int g = 0; g < 8; ++g) asm volatile("" : "+v"(bl[b][g]), "+v"(bh[b][g]));
        if constexpr (b == 1 && (u < TU || (u == TU + 1 && !MH))) __builtin_amdgcn_s_barrier();
        if constexpr (b == 1 && u == TU - 1 && MH == 0) __builtin_amdgcn_s_setprio(2);
      }
      const u32x4 bb = {bl[b][f][0], bl[b][f][1], bh[b][f][0], bh[b][f][1]};
      if constexpr (u == 0 && b == 0) asm volatile("v_mfma_f32_16x16x32_bf16 %0, %1, %2, 0" : "=&v"(acc[i][f]) : "v"(bb), "v"(af[0][i]));
      else MFMA_AV(acc[i][f], bb, af[as(MH, u)][2 * b + i]);
      if constexpr (m < 8 && !(last && b)) {
        rdb(ic<u + b>{}, ic<1 - b>{}, ic<2 * m>{});
        rdb(ic<u + b>{}, ic<1 - b>{}, ic<2 * m + 1>{});
      }
      if constexpr ((m & 3) == 2) {
        if constexpr (b == 0 && na(MH, u) == 4) lda(ic<(u < TU ? u + AD - 1 : NU - 1)>{}, ic<(m >> 2)>{});
        if constexpr (b == 1 && u + SL < NU) ldb(ic<u + SL>{}, ic<(m >> 2)>{});
      }
      if constexpr (MH == 0 && u == TU - 1 && b == 1 && (m & 3) == 0) lda(ic<NU - 2>{}, ic<(m >> 2)>{});
      if constexpr (MH == 0 && u == 0 && n == 0)
        asm volatile("ds_add_rtn_u32 %0, %1, %2" : "=v"(cv) : "v"(smem_base + CN + 256 + lane * 4), "v"(1u) : "memory");
      if constexpr (MH == 0 && u == 0 && n == 20) {
        asm volatile("" : "+v"(cv));
        signal(sig_t{mk_rsrc(fb, __builtin_amdgcn_readfirstlane(cv) == 3u ? 3 * 256 + 4 : 0), (unsigned)(lane * 256)});
      }
      if constexpr (MH == 1 && u == CU && n == 31)
        asm volatile("buffer_atomic_add %0, %1, %2, 0 offen sc0 sc1" : "+v"(cv) : "v"(lane * 128), "s"(mk_rsrc(fb, wid == 4 ? 256 : 0)) : "memory");
      if constexpr (MH == 1 && u == 6 - AD && n == 8) {
        asm volatile("s_waitcnt vmcnt(%1)" : "+v"(cv) : "n"(behind(1, CU, u, 0) + 2) : "memory");
        if (__builtin_expect(((unsigned)__builtin_amdgcn_ballot_w64(cv < 8u) & (wid == 4 ? 3u : 0u)) != 0u, 0)) {
          const poll_t p = poll(0u, fb, lane, 2, 8u);
          wait_vm<0>();
          poll_end(p);
        }
      }
      if constexpr (u >= TU) extra(ic<(u - TU) * 32 + n>{});
      if constexpr (MH && u == TU + 1 && n == 24) dsw32<64>(wf, 1u);
      if constexpr (MH && u == NU - 2 && n == 24) dsr32<64>(cv, wf ^ 8u);
      if constexpr (MH && last && n == 9) {
        asm volatile("" : "+v"(cv));
        if (__builtin_expect(__builtin_amdgcn_readfirstlane(cv) == 0u, 0)) lds_wait<1>((wf ^ 8u) + 64, 1u);
      }
      if constexpr (last && n == 10) {
        rp = wp + wo - k2 * 2 * wo; fa = wf; fb2 = wf ^ 4u;
        asm volatile("" : "+v"(rp), "+v"(fa), "+v"(fb2), "+v"(one));
      }
      if constexpr (last && n >= 15 && n < 31 && (n & 1)) dsw128<(n - 15) / 2 * 1024>(wp, __builtin_bit_cast(u32x4, acc[1][(n - 15) / 2 & 7]));
      if constexpr (last && n == 29) dsw32<0>(fa, one);
    });
  });
  u32x4 pq[8];
  unsigned fl;
  auto rdq = [&] {
    dsr32<0>(fl, fb2);
    static_for<8>([&](auto F) { dsr128<F.value * 1024>(pq[F.value], rp); });
  };
  auto there = [&] {
    asm volatile("s_waitcnt lgkmcnt(0)" : "+v"(fl), "+v"(pq[0]), "+v"(pq[1]), "+v"(pq[2]), "+v"(pq[3]), "+v"(pq[4]), "+v"(pq[5]), "+v"(pq[6]), "+v"(pq[7])::"memory");
    return __builtin_amdgcn_readfirstlane(fl) != 0u;
  };
  rdq();
  extra(ic<128>{});
  if (!there()) do rdq(); while (!there());
#pragma unroll
  for (int f = 0; f < 8; ++f) {
    asm volatile("" : "+v"(acc[0][f]));
    acc[0][f] += __builtin_bit_cast(f32x4, pq[f]);
  }
}

template <int MH, class WST>
DI void g4_phase(unsigned smem_base, int wid, const bf16* At, WST wst, const bf16* Yt,
                 bf16* __restrict__ C, float* xbuf, unsigned* flags) {
  using namespace g4;
  constexpr int KB = F_DIM / 32;
  const int lane = fresh_lane();
  constexpr int mh = MH;
  const int kq = wid & 3;
  const int xcd = blockIdx.x & 7, idx = blockIdx.x >> 3;
  const int kh = xcd & 1, bm = ((xcd >> 1) & 1) * 8 + (idx & 7), bn = (xcd >> 2) * 4 + (idx >> 3);
  const unsigned partner = blockIdx.x ^ 1;
  const int n0 = bn * 128, bm64 = bm * 2 + kh;
  const int fr = lane & 15, fq = lane >> 4;
  const unsigned voffL = lane * 16;
  const rsrc_t rsA = mk_rsrc((const char*)At + (size_t)(bm64 ^ mh ^ 1) * KB * 4096, KB * 4096);
  const rsrc_t rsEx = mk_rsrc(xbuf, 256 * 32768);
  const int kb0 = kh * (KB / 2);
  f32x4 acc[2][8];
  const int fo = ((idx & 7) * 2 + (xcd >> 2)) * 8 + (xcd & 1), fi = ((bm >> 1) * 2 + kh) * 8 + (idx >> 3) * 2;
  const unsigned pb = smem_base + (kq & 2 ? PX + (~kq & 1) * 8192 : 32768 + (~kq & 1) * 65536);
  unsigned pf = smem_base + CN + kq * 4;
  if constexpr (mh == 0) {
    g4_loop<0>(acc, smem_base, lane, wid, kb0, rsA, flags + (FL_B + fo) * 32, wst,
               [&] { wait_vm<8>(); __builtin_amdgcn_s_barrier(); return poll_t{0u, 0u}; }, [](auto) {});
    unsigned one = 1;
    poll_t pl = {0u, 0u};
    asm volatile("" : "+v"(pf), "+v"(one), "+v"(pl.a), "+v"(pl.b));
    static_for<4>([&](auto F) {
      constexpr int f = F.value;
      bstore_sc1<f * 1024>(pack8(acc[0][2 * f], acc[0][2 * f + 1]), voffL, rsEx, blockIdx.x * 32768u + kq * 8192u);
    });
    __builtin_amdgcn_s_barrier();
    __builtin_amdgcn_s_setprio(0);
    asm volatile("s_waitcnt vmcnt(0)\n\tbuffer_atomic_add %0, %1, %2, 0 offen sc1" ::"v"(one), "v"(voffL), "s"(mk_rsrc(flags + (FL_P + blockIdx.x) * 32 + kq, 4)) : "memory");
    unsigned spins = 0, t;
    asm volatile("buffer_atomic_add %0, %4, %5, 0 offen sc0 sc1\n\ts_sleep %6\n1:\n\t"
                 "buffer_atomic_add %1, %4, %5, 0 offen sc0 sc1\n\ts_waitcnt vmcnt(1)\n\tv_readfirstlane_b32 %3, %0\n\ts_nop 0\n\ts_cmp_lg_u32 %3, 0\n\ts_cbranch_scc1 2f\n\t"
                 "buffer_atomic_add %0, %4, %5, 0 offen sc0 sc1\n\ts_waitcnt vmcnt(1)\n\tv_readfirstlane_b32 %3, %1\n\ts_nop 0\n\ts_cmp_lg_u32 %3, 0\n\ts_cbranch_scc1 2f\n\t"
                 "s_add_u32 %2, %2, 1\n\ts_cmp_lt_u32 %2, 0x80000\n\ts_cbranch_scc1 1b\n2:"
                 : "+v"(pl.a), "+v"(pl.b), "+s"(spins), "=&s"(t) : "v"(voffL), "s"(mk_rsrc(flags + (FL_P + partner) * 32 + kq, 4)), "n"(PSLP) : "memory", "scc");
    static_for<4>([&](auto F) { bload_lds_sc1<F.value * 1024>(pb, voffL, rsEx, partner * 32768u + kq * 8192u); });
    wait_vm<0>();
    dsw32<128>(pf, one);
    asm volatile("s_waitcnt lgkmcnt(0)" ::: "memory");
    poll_end(pl);
  } else {
  u32x4 yv[4];
  const rsrc_t rsX = mk_rsrc(Yt, S_LEN * D_DIM * 2);
  unsigned vx = (unsigned)(((bm64 >> 2) * 32 + bn * 4) * 16384 + ((bm64 & 3) * 4 + kq) * 1024 + ((((fq & 1) << 1) | (fq >> 1)) * 16 + fr) * 16);
  const unsigned oa = arrive<4, 4, 256>(smem_base + CN + 512, lane, flags + (FL_A + fo) * 32);
  asm volatile("" : "+v"(vx));
  u32x4 pv[4], gt[8], ct[2][2];
  f32x4 xr[8];
  unsigned ga = smem_base + GT + fq * 16, pa = pb + voffL;
  char* cp;
  unsigned fl;
  auto rdp = [&] {
    dsr32<128>(fl, pf);
    static_for<4>([&](auto F) { dsr128<F.value * 1024>(pv[F.value], pa); });
  };
  g4_loop<1>(acc, smem_base, lane, wid, kb0, rsA, flags + (FL_B + fi) * 32, wst, [&] { return await(oa, lane, flags + (FL_A + fi) * 32, 2, 8u); },
             [&](auto T) {
    constexpr int t = T.value, h = t >> 4, m = t & 15, c = h - 2;
    if constexpr (t == -1) {
      static_for<4>([&](auto J) { asm volatile("" : "=v"(yv[J.value])); bload_sc1<0>(yv[J.value], vx, rsX, J.value * 16384); });
      asm volatile("" : "+v"(ga), "+v"(pa), "+v"(pf));
    } else if constexpr (t == 128) rdp();
    else {
      if constexpr (t == X3) __builtin_amdgcn_s_setprio(1);
      if constexpr (c >= 0 && c < 4 && m == 7) {
        asm volatile("" : "+v"(yv[c]), "+v"(ct[c & 1][0]), "+v"(ct[c & 1][1]));
        const auto x0 = __builtin_amdgcn_permlane16_swap(yv[c][0], yv[c][2], false, false);
        const auto x1 = __builtin_amdgcn_permlane16_swap(yv[c][1], yv[c][3], false, false);
        yv[c] = u32x4{x0[0], x1[0], x0[1], x1[1]};
        asm volatile("" : "+v"(yv[c]));
      }
      if constexpr (c >= 0 && c < 4 && m >= 8) {
        constexpr int f = (m >> 1) & 1, e = (m & 1) * 2;
        if constexpr (m < 12) asm volatile("v_lshlrev_b32 %0, 16, %2\n\tv_and_b32 %1, 0xffff0000, %2" : "=&v"(xr[2 * c + f][e]), "=v"(xr[2 * c + f][e + 1]) : "v"(yv[c][m & 3]));
        else asm volatile("v_mul_f32 %0, %0, %2\n\tv_mul_f32 %1, %1, %3" : "+v"(xr[2 * c + f][e]), "+v"(xr[2 * c + f][e + 1]) : "v"(ct[c & 1][f][e]), "v"(ct[c & 1][f][e + 1]));
      }
      if constexpr (h >= 1 && h < 5 && (m == 8 || m == 9)) dsr128<512 + (2 * h + m - 10) * 64>(ct[(h - 1) & 1][m - 8], ga);
      if constexpr (t >= 108 && t < 124 && !(t & 1)) dsr128<(t - 108) * 32>(gt[(t - 108) / 2], ga);
      if constexpr (t == 20) {
        cp = (char*)C + (size_t)(bm64 * 64 + kq * 16 + (fr & 7)) * (D_DIM * 2) + n0 * 2 + (((fq & 1) << 1) | (fq >> 1)) * 16 + (fr >> 3) * 64;
        asm volatile("" : "+v"(cp));
      }
    }
  });
  asm volatile("" : "+v"(fl), "+v"(pv[0]), "+v"(pv[1]), "+v"(pv[2]), "+v"(pv[3]));
#pragma unroll
  for (int f = 0; f < 8; ++f) {
    asm volatile("" : "+v"(gt[f]));
    xr[f] += __builtin_bit_cast(f32x4, gt[f]) * acc[0][f];
    asm volatile("" : "+v"(xr[f]));
  }
  if (__builtin_amdgcn_readfirstlane(fl) == 0u) do {
    rdp();
    asm volatile("s_waitcnt lgkmcnt(0)" : "+v"(fl), "+v"(pv[0]), "+v"(pv[1]), "+v"(pv[2]), "+v"(pv[3])::"memory");
  } while (__builtin_amdgcn_readfirstlane(fl) == 0u);
  static_for<2>([&](auto L) {
    constexpr int l = L.value;
    u32x4 ov[2];
#pragma unroll
    for (int c = 0; c < 2; ++c) {
      const int f = 4 * l + 2 * c;
      const u32x4 p = pv[f >> 1];
      const f32x4 o0 = xr[f] + __builtin_bit_cast(f32x4, gt[f]) * f32x4{bf_lo(p[0]), bf_hi(p[0]), bf_lo(p[1]), bf_hi(p[1])};
      const f32x4 o1 = xr[f + 1] + __builtin_bit_cast(f32x4, gt[f + 1]) * f32x4{bf_lo(p[2]), bf_hi(p[2]), bf_lo(p[3]), bf_hi(p[3])};
      const auto s0 = __builtin_amdgcn_permlane16_swap(pk_bf16(o0[0], o0[1]), pk_bf16(o1[0], o1[1]), false, false);
      const auto s1 = __builtin_amdgcn_permlane16_swap(pk_bf16(o0[2], o0[3]), pk_bf16(o1[2], o1[3]), false, false);
      ov[c] = u32x4{s0[0], s1[0], s0[1], s1[1]};
    }
    u32x4 lo, hi;
#pragma unroll
    for (int e = 0; e < 4; ++e) {
      lo[e] = __builtin_amdgcn_update_dpp(ov[0][e], ov[1][e], 0x128, 0xf, 0xc, false);
      hi[e] = __builtin_amdgcn_update_dpp(ov[1][e], ov[0][e], 0x128, 0xf, 0x3, false);
    }
    gst128(cp + l * 128, lo);
    gst128(cp + 8 * (D_DIM * 2) + l * 128, hi);
  });
}
}

__global__ __launch_bounds__(512, 2) void block_kernel(char* ws, const bf16* X, const bf16* cnd, const bf16* wmod, const bf16* Wq,
                                                       const bf16* Wo, const bf16* Wg, const bf16* Wu, const bf16* Wd, bf16* C) {
  __shared__ __attribute__((aligned(1024))) char smem[Q_LDS];
  unsigned long long pc;
  asm volatile("s_getpc_b64 %0" : "=s"(pc));
  asm volatile("s_cmp_ge_u32 %0, 9\n\ts_cbranch_scc1 1f\n\ts_mov_b32 m0, %1\n\ts_nop 0\n\tbuffer_load_dword %2, %3, 0 offen lds\n1:"
               ::"s"((blockIdx.x >> 3) + __builtin_amdgcn_readfirstlane(threadIdx.x ^ 64)), "s"((unsigned)(uintptr_t)smem + QX_SINK), "v"((threadIdx.x & 63) * 128 + (blockIdx.x >> 3) * 8192),
                 "s"(mk_rsrc((const void*)(uintptr_t)(pc & ~255ull), CODEPF * 1024)) : "memory", "scc");
  asm volatile("" : "+s"(ws) : "s"(X), "s"(cnd), "s"(wmod), "s"(Wq), "s"(Wo), "s"(Wg), "s"(Wu), "s"(Wd), "s"(C));
  float* modp = (float*)ws;
  unsigned* ctab = (unsigned*)(ws + 212992);
  unsigned* flags = (unsigned*)(ws + 262144);
  float* stat = (float*)(ws + 524288);
  bf16* O = (bf16*)(ws + (1 << 20));
  bf16* Y = (bf16*)(ws + (9 << 20));
  bf16* H = (bf16*)(ws + (13 << 20));
  bf16* act = (bf16*)(ws + (17 << 20));
  float* xbuf = (float*)(ws + (32 << 20));
  const int wid = __builtin_amdgcn_readfirstlane(threadIdx.x >> 6);
  const int wm = wid & 3, wn = wid >> 2;
  const int xcd = blockIdx.x & 7, idx = blockIdx.x >> 3;
  const int bm = idx & 7, bn = xcd * 4 + (idx >> 3);
  const unsigned smem_base = (unsigned)(uintptr_t)smem;
  if (idx == 9) {
    if (wid < 2) walk(wid ? (void*)C : O, wid ? 4 : 39, threadIdx.x & 63);
    else if (wid < 6) touch_lds(smem_base + QX_SINK, (threadIdx.x & 63) << 20, mk_rsrc(wid == 2 ? Wo : wid == 3 ? Wg : wid == 4 ? Wu : Wd, wid == 2 ? 2 << 20 : 7 << 20), 0);
  }
  const rsrc_t rsA = mk_rsrc((const char*)Y + (size_t)bm * 32 * 16384, 32 * 16384);
  const rsrc_t rsB = mk_rsrc(wn ? Wu : Wg, D_DIM * F_DIM * 2);
  const rsrc_t rsE = wid == 7 ? mk_rsrc((const void*)(uintptr_t)(pc & ~255ull), CODEPF * 1024) : mk_rsrc(stat, S_LEN * 128);
  const unsigned m0w = smem_base + wid * 2048;
  unsigned voffB0 = 0, voffB1 = 0;
  attn_phase(smem_base, wid, H, Wq, O, Wo, X, modp, Y, stat, flags, Wg, Wu, ctab, cnd, wmod, [&](auto S, auto I) {
    if constexpr (S.value == 0) {
      const int l2 = fresh_lane();
      const int r = (wm * 2 + I.value) * 4 + (l2 >> 4);
      const int sw = (r & 3) | (((r >> 3) & 1) << 2);
      const int c16 = l2 & 15;
      const int vb = (((bn & 3) * 224) & ~63) + ((c16 >> 1) ^ sw) * 32 + (c16 & 1) * 16;
      (I.value ? voffB1 : voffB0) = (unsigned)(r * (F_DIM * 2) + g3_line(bn >> 2, vb >> 7) * 128 + (vb & 127));
    }
    bload_lds<0>(m0w + S.value * 16384 + I.value * 1024, I.value ? voffB1 : voffB0, rsB, S.value * 229376u);
  });
  __builtin_amdgcn_s_setprio(0);
  const int lane = fresh_lane();
  const unsigned voffA = wid * 2048 + lane * 16;
  const unsigned oy = arrive<8, 8, 512>(smem_base + X_CN + 768, lane, flags + (FL_Y + xcd * 32 + (idx & 3)) * 32);
  auto pl = [&] { return await(oy, lane, flags + (FL_Y + bm * 32 + xcd * 4) * 32, 4, 8u); };
  const int n0 = ((xcd >> 2) * 4 + (idx >> 3)) * 128;
  const char* ta = lane >= 14 ? (const char*)Wd + blockIdx.x * 28672 + wid * 1024 + ((lane - 14) & 7) * 128
                              : (const char*)(lane < 7 ? Wg : Wu) + ((wid + WTD) * 32 + idx) * (F_DIM * 2) + xcd * 896 + (lane < 7 ? lane : lane - 7) * 128;
  unsigned vo = 0;
  const unsigned fsel = 0x80000u | (((xcd & 1) * 2 + (wm >> 1)) * 8);
  auto wsn = [&](auto WN, auto V, auto J) {
    constexpr int v = V.value, j = J.value, p = (4 * v + 2 * (j >> 1) + WN.value) * 2;
    if constexpr (v == 0 && j == 0) {
      const int l2 = fresh_lane();
      vo = (wid & 1) * 16384 + (l2 >> 4) * 2048 + ((((l2 & 15) >> 1) ^ ((l2 >> 4) | ((wid & 1) << 2))) * 32) + (l2 & 1) * 16;
    }
    unsigned so;
    asm volatile("s_bfe_u32 %0, %1, %2\n\ts_lshl_b32 %0, %0, 15\n\ts_or_b32 %0, %0, %3" : "=s"(so)
                 : "n"(act_frag(0, p) | act_frag(0, p + 1) << 8 | act_frag(1, p) << 16 | act_frag(1, p + 1) << 24), "s"(fsel), "n"((j & 1) * 8192) : "scc");
    bload_lds<0>(m0w + (j >> 1) * 65536 + (v & 3) * 16384 + (j & 1) * 1024, vo, mk_rsrc((const char*)Wd + n0 * 2, F_DIM * D_DIM * 2 - n0 * 2), so);
  };
  if (wn == 0) {
    auto wst = [&](auto V, auto J) { wsn(ic<0>{}, V, J); };
    g3_body<4>(smem_base, wm, wn, wid, lane, bm, bn, rsA, rsB, rsE, ta, voffA, voffB0, voffB1, act, wst, [](auto) {}, pl);
    g4_phase<0>(smem_base, wid, act, wst, Y, C, xbuf, flags);
  } else {
    auto wst = [&](auto V, auto J) { wsn(ic<1>{}, V, J); };
    u32x4 tv[8];
    f32x4 ps;
    float q0 = 0, q1 = 0;
    unsigned vs, gv;
    g3_body<3>(smem_base, wm, wn, wid, lane, bm, bn, rsA, rsB, rsE, ta, voffA, voffB0, voffB1, act, wst, [&](auto K) {
      constexpr int k = K.value;
      if constexpr (k <= 0) {
        if constexpr (k == 0) {
          const int l2 = fresh_lane();
          asm volatile("" : "=v"(gv));
          bload1_sc1(gv, (unsigned)((n0 + (wm & 1) * 64 + l2) * 4), mk_rsrc(modp + 49152 + (wm >= 2 ? 2048 : 3072), 4096));
          vs = (l2 >> 5) * 131072 + (bm * 256 + wm * 64) * 8 + (l2 & 31) * 16;
        }
        asm volatile("" : "=v"(tv[-k]));
        bload_sc1<0>(tv[-k], vs, mk_rsrc(stat, S_LEN * 128), -k * 16384);
      } else if constexpr (k <= 4) {
        if constexpr (k == 1) wait_vm<3>();
        asm volatile("" : "+v"(tv[2 * k - 2]), "+v"(tv[2 * k - 1]));
        const f32x4 t = __builtin_bit_cast(f32x4, tv[2 * k - 2]) + __builtin_bit_cast(f32x4, tv[2 * k - 1]);
        ps = k == 1 ? t : ps + t;
        asm volatile("" : "+v"(ps));
      } else if constexpr (k == 5) {
        const auto s0 = __builtin_amdgcn_permlane32_swap(__float_as_uint(ps[0]), __float_as_uint(ps[2]), false, false);
        const auto s1 = __builtin_amdgcn_permlane32_swap(__float_as_uint(ps[1]), __float_as_uint(ps[3]), false, false);
        ps[0] = (__uint_as_float(s0[0]) + __uint_as_float(s0[1])) * (1.0f / 16.0f);
        ps[1] = __uint_as_float(s1[0]) + __uint_as_float(s1[1]);
        const auto m = __builtin_amdgcn_permlane32_swap(__float_as_uint(ps[0]), __float_as_uint(ps[0]), false, false);
        ps[2] = __uint_as_float(m[0]); ps[3] = __uint_as_float(m[1]);
        asm volatile("" : "+v"(ps), "+v"(q0), "+v"(q1));
      } else if constexpr (k <= 13) {
        float d0, d1;
        asm volatile("v_sub_f32 %0, %4, %6\n\tv_sub_f32 %1, %5, %7\n\tv_fmac_f32 %2, %0, %0\n\tv_fmac_f32 %3, %1, %1"
                     : "=&v"(d0), "=&v"(d1), "+v"(q0), "+v"(q1) : "v"(tv[k - 6][0]), "v"(tv[k - 6][2]), "v"(ps[2]), "v"(ps[3]));
        if constexpr (k == 13) asm volatile("s_nop 1" : "+v"(q0), "+v"(q1));
      } else if constexpr (k == 14) {
        const auto q = __builtin_amdgcn_permlane32_swap(__float_as_uint(q0), __float_as_uint(q1), false, false);
        const float rstd = __builtin_amdgcn_rsqf((ps[1] + (__uint_as_float(q[0]) + __uint_as_float(q[1])) * (1.0f / 64.0f)) * (1.0f / D_DIM) + 1e-5f);
        const int l2 = fresh_lane();
        asm volatile("ds_write_b64 %0, %1" ::"v"(smem_base + 131072 + wm * 512 + (l2 & 31) * 16 + (l2 >> 5) * 8), "v"(f32x2{ps[0] * (-1.0f / 64.0f) * rstd, rstd}) : "memory");
      } else {
        asm volatile("" : "+v"(gv));
        dsw32<0>(smem_base + g4::GT + (wm * 64 + fresh_lane()) * 4, wm >= 2 ? __float_as_uint(__builtin_amdgcn_rcpf(__uint_as_float(gv))) : gv);
      }
    }, pl);
    g4_phase<1>(smem_base, wid, act, wst, Y, C, xbuf, flags);
  }
}

extern "C" void kernel_launch(void* const* d_in, const int* in_sizes, int n_in, void* d_out, int out_size,
                              void* d_ws, size_t ws_size, hipStream_t stream) {
  const bf16* x = (const bf16*)d_in[0];
  const bf16* wqkv = (const bf16*)d_in[1];
  const bf16* wo = (const bf16*)d_in[2];
  const bf16* wg = (const bf16*)d_in[4];
  const bf16* wu = (const bf16*)d_in[5];
  const bf16* wd = (const bf16*)d_in[6];
  const bf16* c = (const bf16*)d_in[7];
  const bf16* wmod = (const bf16*)d_in[8];
  bf16* out = (bf16*)d_out;

  block_kernel<<<256, 512, 0, stream>>>((char*)d_ws, x, c, wmod, wqkv, wo, wg, wu, wd, out);
}
```
